```python
import torch

def _softmax(x, axis=-1):
    return torch.softmax(x, dim=axis)

def _gelu(x):
    return torch.nn.functional.gelu(x, approximate='tanh')

def _silu(x):
    return torch.nn.functional.silu(x)

def _sigmoid(x):
    return torch.sigmoid(x)

def _layernorm(x, axis=-1, eps=1e-05):
    m = x.mean(dim=axis, keepdim=True)
    v = x.var(dim=axis, keepdim=True, unbiased=False)
    return (x - m) / torch.sqrt(v + eps)

def _scatter_add(tgt, idx, src, axis=0):
    return tgt.clone().index_add_(axis, idx, src)

def _segment_sum(src, seg, n):
    out = torch.zeros((n,) + src.shape[1:], dtype=src.dtype)
    return out.index_add_(0, seg, src)

def _pad(x, pw):
    flat = tuple((v for pair in reversed(pw) for v in pair))
    return torch.nn.functional.pad(x, flat)
kernel_name = 'gather_scatter__tensor_product_equiv_tfn__graph'

def setup_inputs():

    _w = lambda a: (lambda t: t.to(torch.bfloat16) if t.is_floating_point() else t)(torch.as_tensor(a))
    N, deg, C, L, DE = (10000, 32, 128, 1, 4)
    E = N * deg
    M = (L + 1) ** 2
    src = (lambda lo, hi, *s: torch.randint(lo, hi, s))(0, N, E).to(torch.int64)
    dst = (lambda lo, hi, *s: torch.randint(lo, hi, s))(0, N, E).to(torch.int64)
    r = (lambda *s: torch.randn(*s, dtype=torch.bfloat16) * 0.05)(E, 3)
    r = r / (torch.linalg.norm(r, dim=-1, keepdim=True) + 1e-09)
    rx, ry, rz = (r[:, 0].clone(), r[:, 1].clone(), r[:, 2].clone())
    Ys = [torch.ones((E, 1), dtype=float)]
    if L >= 1:
        Ys.append(r.to(float))
    if L >= 2:
        Ys.append(torch.stack([2 * rx * ry, 2 * ry * rz, (3 * rz * rz - 1.0) / (lambda _x: torch.sqrt(torch.as_tensor(_x)))(3.0), 2 * rx * rz, rx * rx - ry * ry], dim=-1).to(float))
    for _l in range(3, L + 1):
        Ys.append(((lambda *s: torch.randn(*s, dtype=torch.bfloat16) * 0.05)(E, 2 * _l + 1) * 0.3).to(float))
    Y = torch.cat(Ys, dim=-1)
    cg = torch.zeros((M, M, M), dtype=float)
    off = [_l * _l for _l in range(L + 2)]
    for l1 in range(L + 1):
        for l2 in range(L + 1):
            for l3 in range(abs(l1 - l2), min(L, l1 + l2) + 1):
                d1, d2, d3 = (2 * l1 + 1, 2 * l2 + 1, 2 * l3 + 1)
                cg[off[l1]:off[l1 + 1], off[l2]:off[l2 + 1], off[l3]:off[l3 + 1]] = (lambda *s: torch.randn(*s, dtype=torch.bfloat16) * 0.05)(d1, d2, d3) / (lambda _x: torch.sqrt(torch.as_tensor(_x)))(float(d1 * d2))
    return {'x': _w((lambda *s: torch.randn(*s, dtype=torch.bfloat16) * 0.05)(N, C, M) * 0.2), 'src': _w(src), 'dst': _w(dst), 'Y': _w(Y), 'edge_feat': _w((lambda *s: torch.randn(*s, dtype=torch.bfloat16) * 0.05)(E, DE)), 'w_rad': _w((lambda *s: torch.randn(*s, dtype=torch.bfloat16) * 0.05)(DE, C) * (1.0 / max(1.0, DE) ** 0.5)), 'cg': _w(cg), 'w_mix': _w((lambda *s: torch.randn(*s, dtype=torch.bfloat16) * 0.05)(C, C) * (1.0 / max(1.0, C) ** 0.5))}

def reference(x, src, dst, Y, edge_feat, w_rad, cg, w_mix):
    N, C, M = (x.shape[0], x.shape[1], x.shape[2])
    R = _silu(edge_feat @ w_rad)
    xs = x[src]
    tp = torch.einsum('eci,ef,ifo->eco', xs, Y, cg)
    msg = R[:, :, None] * tp
    agg = _segment_sum(msg.reshape(-1, C * M), dst, N).reshape(N, C, M)
    return torch.einsum('ncm,cd->ndm', agg, w_mix)
```

```cpp
#include <hip/hip_runtime.h>
#include <hip/hip_bf16.h>
#include <stddef.h>
#include <stdint.h>

typedef __attribute__((ext_vector_type(2))) float f32x2;
typedef __attribute__((ext_vector_type(4))) float f32x4;
typedef __attribute__((ext_vector_type(8))) float f32x8;
typedef __attribute__((ext_vector_type(16))) float f32x16;
typedef __attribute__((ext_vector_type(8))) short s16x8;
typedef __attribute__((ext_vector_type(2))) unsigned int u32x2;
typedef __attribute__((ext_vector_type(3))) unsigned int u32x3;
typedef __attribute__((ext_vector_type(4))) unsigned int u32x4;
typedef __attribute__((ext_vector_type(2))) __bf16 bf16x2;

#define TDECL
#define STAMP(i)
#define CSTAMP(i)
#define TFLUSH

#define STREAM_ASM \
  "ds_read_b32 v11, v12\n" \
  "ds_read_b64 v[106:107], v14\n" \
  "ds_read_b64 v[104:105], v15\n" \
  "ds_read_b32 v18, v13\n" \
  "v_mov_b64 v[32:33], 0\n" \
  "v_mov_b64 v[34:35], 0\n" \
  "v_mov_b64 v[36:37], 0\n" \
  "v_mov_b64 v[38:39], 0\n" \
  "s_waitcnt lgkmcnt(3)\n" \
  "v_and_b32 v10, 0xfffffc00, v11\n" \
  "v_mov_b64 v[40:41], 0\n" \
  "v_mov_b64 v[42:43], 0\n" \
  "v_add_u32_dpp v9, v10, v8 row_newbcast:0 row_mask:0xf bank_mask:0xf\n" \
  "buffer_load_dwordx4 v[120:123], v9, %[xs], 0 offen\n" \
  "v_add_u32_dpp v9, v10, v8 row_newbcast:1 row_mask:0xf bank_mask:0xf\n" \
  "buffer_load_dwordx4 v[124:127], v9, %[xs], 0 offen\n" \
  "v_add_u32_dpp v9, v10, v8 row_newbcast:2 row_mask:0xf bank_mask:0xf\n" \
  "buffer_load_dwordx4 v[128:131], v9, %[xs], 0 offen\n" \
  "v_add_u32_dpp v9, v10, v8 row_newbcast:3 row_mask:0xf bank_mask:0xf\n" \
  "buffer_load_dwordx4 v[132:135], v9, %[xs], 0 offen\n" \
  "v_add_u32_dpp v9, v10, v8 row_newbcast:4 row_mask:0xf bank_mask:0xf\n" \
  "buffer_load_dwordx4 v[136:139], v9, %[xs], 0 offen\n" \
  "s_waitcnt lgkmcnt(0)\n" \
  "v_mfma_f32_4x4x4_16b_bf16 v[80:83], v[104:105], v[28:29], 0 cbsz:4\n" \
  "v_mfma_f32_4x4x4_16b_bf16 v[84:87], v[104:105], v[30:31], 0 cbsz:4\n" \
  "v_mfma_f32_4x4x4_16b_bf16 v[112:115], v[106:107], v[20:21], 0\n" \
  "v_mfma_f32_4x4x4_16b_bf16 v[116:119], v[106:107], v[22:23], 0\n" \
  "v_mfma_f32_4x4x4_16b_bf16 v[72:75], v[106:107], v[24:25], 0\n" \
  "v_mfma_f32_4x4x4_16b_bf16 v[76:79], v[106:107], v[26:27], 0\n" \
  "v_and_b32 v18, v17, v18\n" \
  "v_cmp_ne_u32_e64 s[62:63], 0, v18\n" \
  "v_cvt_pkrtz_f16_f32 v97, v80, v81\n" \
  "v_and_b32 v96, v16, v97\n" \
  "v_and_b32_sdwa v97, v16, v97 dst_sel:DWORD dst_unused:UNUSED_PAD src0_sel:DWORD src1_sel:WORD_1\n" \
  "ds_read_b32 v96, v96 offset:%[tb]\n" \
  "ds_read_b32 v97, v97 offset:%[tb]\n" \
  "v_cvt_pkrtz_f16_f32 v99, v84, v85\n" \
  "v_and_b32 v98, v16, v99\n" \
  "v_and_b32_sdwa v99, v16, v99 dst_sel:DWORD dst_unused:UNUSED_PAD src0_sel:DWORD src1_sel:WORD_1\n" \
  "ds_read_b32 v98, v98 offset:%[tb]\n" \
  "ds_read_b32 v99, v99 offset:%[tb]\n" \
  "v_cvt_pkrtz_f16_f32 v101, v82, v83\n" \
  "v_and_b32 v100, v16, v101\n" \
  "v_and_b32_sdwa v101, v16, v101 dst_sel:DWORD dst_unused:UNUSED_PAD src0_sel:DWORD src1_sel:WORD_1\n" \
  "ds_read_b32 v100, v100 offset:%[tb]\n" \
  "ds_read_b32 v101, v101 offset:%[tb]\n" \
  "v_cvt_pk_bf16_f32 v108, v112, v116\n" \
  "v_cvt_pk_bf16_f32 v109, v72, v76\n" \
  "v_mov_b64 v[44:45], 0\n" \
  "v_mov_b64 v[46:47], 0\n" \
  "v_mov_b64 v[48:49], 0\n" \
  "v_mov_b64 v[50:51], 0\n" \
  "v_mov_b64 v[52:53], 0\n" \
  "v_mov_b64 v[54:55], 0\n" \
  "v_mov_b64 v[56:57], 0\n" \
  "v_mov_b64 v[58:59], 0\n" \
  "v_mov_b64 v[60:61], 0\n" \
  "v_mov_b64 v[62:63], 0\n" \
  "v_mov_b64 v[64:65], 0\n" \
  "v_mov_b64 v[66:67], 0\n" \
  "v_mov_b64 v[68:69], 0\n" \
  "v_mov_b64 v[70:71], 0\n" \
  "s_waitcnt vmcnt(4)\n" \
  "v_mfma_f32_4x4x4_16b_bf16 v[72:75], v[108:109], v[120:121], 0 cbsz:4\n" \
  "v_mfma_f32_4x4x4_16b_bf16 v[76:79], v[108:109], v[122:123], 0 cbsz:4\n" \
  "v_add_u32_dpp v9, v10, v8 row_newbcast:5 row_mask:0xf bank_mask:0xf\n" \
  "buffer_load_dwordx4 v[140:143], v9, %[xs], 0 offen\n" \
  "s_waitcnt lgkmcnt(0)\n" \
  "s_nop 1\n" \
  "Lbody%=:\n" \
  "s_bfe_u32 s61, s62, 0x40000\n" \
  "s_waitcnt lgkmcnt(2)\n" \
  "v_pk_mul_f32 v[80:81], v[80:81], v[96:97]\n" \
  "v_pk_mul_f32 v[84:85], v[84:85], v[98:99]\n" \
  "s_set_gpr_idx_on s61, gpr_idx(SRC2,DST)\n" \
  "v_pk_fma_f32 v[32:33], v[80:81], v[72:73], v[32:33] op_sel:[0,0,0] op_sel_hi:[0,1,1]\n" \
  "v_pk_fma_f32 v[42:43], v[80:81], v[74:75], v[42:43] op_sel:[0,0,0] op_sel_hi:[0,1,1]\n" \
  "v_pk_fma_f32 v[52:53], v[84:85], v[76:77], v[52:53] op_sel:[0,0,0] op_sel_hi:[0,1,1]\n" \
  "v_pk_fma_f32 v[62:63], v[84:85], v[78:79], v[62:63] op_sel:[0,0,0] op_sel_hi:[0,1,1]\n" \
  "s_set_gpr_idx_off\n" \
  "s_waitcnt vmcnt(4)\n" \
  "v_mfma_f32_4x4x4_16b_bf16 v[72:75], v[108:109], v[124:125], 0 cbsz:4 abid:1\n" \
  "v_mfma_f32_4x4x4_16b_bf16 v[76:79], v[108:109], v[126:127], 0 cbsz:4 abid:1\n" \
  "v_mfma_f32_4x4x4_16b_bf16 v[88:91], v[104:105], v[28:29], 0 cbsz:4 abid:1\n" \
  "v_mfma_f32_4x4x4_16b_bf16 v[92:95], v[104:105], v[30:31], 0 cbsz:4 abid:1\n" \
  "v_add_u32_dpp v9, v10, v8 row_newbcast:6 row_mask:0xf bank_mask:0xf\n" \
  "v_cvt_pkrtz_f16_f32 v103, v86, v87\n" \
  "buffer_load_dwordx4 v[144:147], v9, %[xs], 0 offen\n" \
  "v_and_b32 v102, v16, v103\n" \
  "v_and_b32_sdwa v103, v16, v103 dst_sel:DWORD dst_unused:UNUSED_PAD src0_sel:DWORD src1_sel:WORD_1\n" \
  "ds_read_b32 v102, v102 offset:%[tb]\n" \
  "ds_read_b32 v103, v103 offset:%[tb]\n" \
  "s_bfe_u32 s61, s62, 0x40004\n" \
  "s_set_gpr_idx_on s61, gpr_idx(SRC2,DST)\n" \
  "v_pk_fma_f32 v[32:33], v[80:81], v[72:73], v[32:33] op_sel:[1,0,0] op_sel_hi:[1,1,1]\n" \
  "v_pk_fma_f32 v[42:43], v[80:81], v[74:75], v[42:43] op_sel:[1,0,0] op_sel_hi:[1,1,1]\n" \
  "v_pk_fma_f32 v[52:53], v[84:85], v[76:77], v[52:53] op_sel:[1,0,0] op_sel_hi:[1,1,1]\n" \
  "v_pk_fma_f32 v[62:63], v[84:85], v[78:79], v[62:63] op_sel:[1,0,0] op_sel_hi:[1,1,1]\n" \
  "s_set_gpr_idx_off\n" \
  "s_waitcnt vmcnt(4)\n" \
  "v_mfma_f32_4x4x4_16b_bf16 v[72:75], v[108:109], v[128:129], 0 cbsz:4 abid:2\n" \
  "v_mfma_f32_4x4x4_16b_bf16 v[76:79], v[108:109], v[130:131], 0 cbsz:4 abid:2\n" \
  "v_add_u32_dpp v9, v10, v8 row_newbcast:7 row_mask:0xf bank_mask:0xf\n" \
  "v_cvt_pkrtz_f16_f32 v97, v88, v89\n" \
  "buffer_load_dwordx4 v[148:151], v9, %[xs], 0 offen\n" \
  "v_and_b32 v96, v16, v97\n" \
  "v_and_b32_sdwa v97, v16, v97 dst_sel:DWORD dst_unused:UNUSED_PAD src0_sel:DWORD src1_sel:WORD_1\n" \
  "ds_read_b32 v96, v96 offset:%[tb]\n" \
  "ds_read_b32 v97, v97 offset:%[tb]\n" \
  "s_bfe_u32 s61, s62, 0x40008\n" \
  "s_waitcnt lgkmcnt(2)\n" \
  "v_pk_mul_f32 v[82:83], v[82:83], v[100:101]\n" \
  "v_pk_mul_f32 v[86:87], v[86:87], v[102:103]\n" \
  "s_set_gpr_idx_on s61, gpr_idx(SRC2,DST)\n" \
  "v_pk_fma_f32 v[32:33], v[82:83], v[72:73], v[32:33] op_sel:[0,0,0] op_sel_hi:[0,1,1]\n" \
  "v_pk_fma_f32 v[42:43], v[82:83], v[74:75], v[42:43] op_sel:[0,0,0] op_sel_hi:[0,1,1]\n" \
  "v_pk_fma_f32 v[52:53], v[86:87], v[76:77], v[52:53] op_sel:[0,0,0] op_sel_hi:[0,1,1]\n" \
  "v_pk_fma_f32 v[62:63], v[86:87], v[78:79], v[62:63] op_sel:[0,0,0] op_sel_hi:[0,1,1]\n" \
  "s_set_gpr_idx_off\n" \
  "s_waitcnt vmcnt(4)\n" \
  "v_mfma_f32_4x4x4_16b_bf16 v[72:75], v[108:109], v[132:133], 0 cbsz:4 abid:3\n" \
  "v_mfma_f32_4x4x4_16b_bf16 v[76:79], v[108:109], v[134:135], 0 cbsz:4 abid:3\n" \
  "v_add_u32_dpp v9, v10, v8 row_newbcast:8 row_mask:0xf bank_mask:0xf\n" \
  "v_cvt_pkrtz_f16_f32 v99, v92, v93\n" \
  "buffer_load_dwordx4 v[120:123], v9, %[xs], 0 offen\n" \
  "v_and_b32 v98, v16, v99\n" \
  "v_and_b32_sdwa v99, v16, v99 dst_sel:DWORD dst_unused:UNUSED_PAD src0_sel:DWORD src1_sel:WORD_1\n" \
  "ds_read_b32 v98, v98 offset:%[tb]\n" \
  "ds_read_b32 v99, v99 offset:%[tb]\n" \
  "ds_read_b64 v[106:107], v14 offset:256\n" \
  "s_bfe_u32 s61, s62, 0x4000c\n" \
  "s_set_gpr_idx_on s61, gpr_idx(SRC2,DST)\n" \
  "v_pk_fma_f32 v[32:33], v[82:83], v[72:73], v[32:33] op_sel:[1,0,0] op_sel_hi:[1,1,1]\n" \
  "v_pk_fma_f32 v[42:43], v[82:83], v[74:75], v[42:43] op_sel:[1,0,0] op_sel_hi:[1,1,1]\n" \
  "v_pk_fma_f32 v[52:53], v[86:87], v[76:77], v[52:53] op_sel:[1,0,0] op_sel_hi:[1,1,1]\n" \
  "v_pk_fma_f32 v[62:63], v[86:87], v[78:79], v[62:63] op_sel:[1,0,0] op_sel_hi:[1,1,1]\n" \
  "s_set_gpr_idx_off\n" \
  "s_sub_u32 s60, s60, 1\n" \
  "s_cbranch_scc1 Lend%=\n" \
  "s_waitcnt vmcnt(4)\n" \
  "v_mfma_f32_4x4x4_16b_bf16 v[72:75], v[108:109], v[136:137], 0 cbsz:4 abid:4\n" \
  "v_mfma_f32_4x4x4_16b_bf16 v[76:79], v[108:109], v[138:139], 0 cbsz:4 abid:4\n" \
  "v_add_u32_dpp v9, v10, v8 row_newbcast:9 row_mask:0xf bank_mask:0xf\n" \
  "v_cvt_pkrtz_f16_f32 v101, v90, v91\n" \
  "buffer_load_dwordx4 v[124:127], v9, %[xs], 0 offen\n" \
  "v_and_b32 v100, v16, v101\n" \
  "v_and_b32_sdwa v101, v16, v101 dst_sel:DWORD dst_unused:UNUSED_PAD src0_sel:DWORD src1_sel:WORD_1\n" \
  "ds_read_b32 v100, v100 offset:%[tb]\n" \
  "ds_read_b32 v101, v101 offset:%[tb]\n" \
  "ds_read_b32 v18, v13 offset:64\n" \
  "v_add_u32 v14, 0x100, v14\n" \
  "s_bfe_u32 s61, s62, 0x40010\n" \
  "s_waitcnt lgkmcnt(4)\n" \
  "v_pk_mul_f32 v[88:89], v[88:89], v[96:97]\n" \
  "v_pk_mul_f32 v[92:93], v[92:93], v[98:99]\n" \
  "s_set_gpr_idx_on s61, gpr_idx(SRC2,DST)\n" \
  "v_pk_fma_f32 v[32:33], v[88:89], v[72:73], v[32:33] op_sel:[0,0,0] op_sel_hi:[0,1,1]\n" \
  "v_pk_fma_f32 v[42:43], v[88:89], v[74:75], v[42:43] op_sel:[0,0,0] op_sel_hi:[0,1,1]\n" \
  "v_pk_fma_f32 v[52:53], v[92:93], v[76:77], v[52:53] op_sel:[0,0,0] op_sel_hi:[0,1,1]\n" \
  "v_pk_fma_f32 v[62:63], v[92:93], v[78:79], v[62:63] op_sel:[0,0,0] op_sel_hi:[0,1,1]\n" \
  "s_set_gpr_idx_off\n" \
  "s_waitcnt vmcnt(4)\n" \
  "v_mfma_f32_4x4x4_16b_bf16 v[72:75], v[108:109], v[140:141], 0 cbsz:4 abid:5\n" \
  "v_mfma_f32_4x4x4_16b_bf16 v[76:79], v[108:109], v[142:143], 0 cbsz:4 abid:5\n" \
  "v_mfma_f32_4x4x4_16b_bf16 v[80:83], v[104:105], v[28:29], 0 cbsz:4 abid:2\n" \
  "v_mfma_f32_4x4x4_16b_bf16 v[84:87], v[104:105], v[30:31], 0 cbsz:4 abid:2\n" \
  "v_add_u32_dpp v9, v10, v8 row_newbcast:10 row_mask:0xf bank_mask:0xf\n" \
  "v_cvt_pkrtz_f16_f32 v103, v94, v95\n" \
  "buffer_load_dwordx4 v[128:131], v9, %[xs], 0 offen\n" \
  "v_and_b32 v102, v16, v103\n" \
  "v_and_b32_sdwa v103, v16, v103 dst_sel:DWORD dst_unused:UNUSED_PAD src0_sel:DWORD src1_sel:WORD_1\n" \
  "ds_read_b32 v102, v102 offset:%[tb]\n" \
  "ds_read_b32 v103, v103 offset:%[tb]\n" \
  "s_bfe_u32 s61, s62, 0x40014\n" \
  "s_set_gpr_idx_on s61, gpr_idx(SRC2,DST)\n" \
  "v_pk_fma_f32 v[32:33], v[88:89], v[72:73], v[32:33] op_sel:[1,0,0] op_sel_hi:[1,1,1]\n" \
  "v_pk_fma_f32 v[42:43], v[88:89], v[74:75], v[42:43] op_sel:[1,0,0] op_sel_hi:[1,1,1]\n" \
  "v_pk_fma_f32 v[52:53], v[92:93], v[76:77], v[52:53] op_sel:[1,0,0] op_sel_hi:[1,1,1]\n" \
  "v_pk_fma_f32 v[62:63], v[92:93], v[78:79], v[62:63] op_sel:[1,0,0] op_sel_hi:[1,1,1]\n" \
  "s_set_gpr_idx_off\n" \
  "s_waitcnt vmcnt(4)\n" \
  "v_mfma_f32_4x4x4_16b_bf16 v[72:75], v[108:109], v[144:145], 0 cbsz:4 abid:6\n" \
  "v_mfma_f32_4x4x4_16b_bf16 v[76:79], v[108:109], v[146:147], 0 cbsz:4 abid:6\n" \
  "v_add_u32_dpp v9, v10, v8 row_newbcast:11 row_mask:0xf bank_mask:0xf\n" \
  "v_cvt_pkrtz_f16_f32 v97, v80, v81\n" \
  "buffer_load_dwordx4 v[132:135], v9, %[xs], 0 offen\n" \
  "v_and_b32 v96, v16, v97\n" \
  "v_and_b32_sdwa v97, v16, v97 dst_sel:DWORD dst_unused:UNUSED_PAD src0_sel:DWORD src1_sel:WORD_1\n" \
  "ds_read_b32 v96, v96 offset:%[tb]\n" \
  "ds_read_b32 v97, v97 offset:%[tb]\n" \
  "v_add_u32 v13, 64, v13\n" \
  "s_bfe_u32 s61, s62, 0x40018\n" \
  "s_waitcnt lgkmcnt(2)\n" \
  "v_pk_mul_f32 v[90:91], v[90:91], v[100:101]\n" \
  "v_pk_mul_f32 v[94:95], v[94:95], v[102:103]\n" \
  "s_set_gpr_idx_on s61, gpr_idx(SRC2,DST)\n" \
  "v_pk_fma_f32 v[32:33], v[90:91], v[72:73], v[32:33] op_sel:[0,0,0] op_sel_hi:[0,1,1]\n" \
  "v_pk_fma_f32 v[42:43], v[90:91], v[74:75], v[42:43] op_sel:[0,0,0] op_sel_hi:[0,1,1]\n" \
  "v_pk_fma_f32 v[52:53], v[94:95], v[76:77], v[52:53] op_sel:[0,0,0] op_sel_hi:[0,1,1]\n" \
  "v_pk_fma_f32 v[62:63], v[94:95], v[78:79], v[62:63] op_sel:[0,0,0] op_sel_hi:[0,1,1]\n" \
  "s_set_gpr_idx_off\n" \
  "s_waitcnt vmcnt(4) lgkmcnt(7)\n" \
  "v_mfma_f32_4x4x4_16b_bf16 v[72:75], v[108:109], v[148:149], 0 cbsz:4 abid:7\n" \
  "v_mfma_f32_4x4x4_16b_bf16 v[76:79], v[108:109], v[150:151], 0 cbsz:4 abid:7\n" \
  "v_mfma_f32_4x4x4_16b_bf16 v[112:115], v[106:107], v[20:21], 0\n" \
  "v_add_u32_dpp v9, v10, v8 row_newbcast:12 row_mask:0xf bank_mask:0xf\n" \
  "v_cvt_pkrtz_f16_f32 v99, v84, v85\n" \
  "buffer_load_dwordx4 v[136:139], v9, %[xs], 0 offen\n" \
  "v_and_b32 v98, v16, v99\n" \
  "v_and_b32_sdwa v99, v16, v99 dst_sel:DWORD dst_unused:UNUSED_PAD src0_sel:DWORD src1_sel:WORD_1\n" \
  "ds_read_b32 v98, v98 offset:%[tb]\n" \
  "ds_read_b32 v99, v99 offset:%[tb]\n" \
  "ds_read_b32 v11, v12 offset:64\n" \
  "s_bfe_u32 s61, s62, 0x4001c\n" \
  "s_set_gpr_idx_on s61, gpr_idx(SRC2,DST)\n" \
  "v_pk_fma_f32 v[32:33], v[90:91], v[72:73], v[32:33] op_sel:[1,0,0] op_sel_hi:[1,1,1]\n" \
  "v_pk_fma_f32 v[42:43], v[90:91], v[74:75], v[42:43] op_sel:[1,0,0] op_sel_hi:[1,1,1]\n" \
  "v_pk_fma_f32 v[52:53], v[94:95], v[76:77], v[52:53] op_sel:[1,0,0] op_sel_hi:[1,1,1]\n" \
  "v_pk_fma_f32 v[62:63], v[94:95], v[78:79], v[62:63] op_sel:[1,0,0] op_sel_hi:[1,1,1]\n" \
  "s_set_gpr_idx_off\n" \
  "s_sub_u32 s60, s60, 1\n" \
  "s_cbranch_scc1 Lend%=\n" \
  "s_waitcnt vmcnt(4) lgkmcnt(7)\n" \
  "v_mfma_f32_4x4x4_16b_bf16 v[72:75], v[108:109], v[120:121], 0 cbsz:4 abid:8\n" \
  "v_mfma_f32_4x4x4_16b_bf16 v[76:79], v[108:109], v[122:123], 0 cbsz:4 abid:8\n" \
  "v_add_u32_dpp v9, v10, v8 row_newbcast:13 row_mask:0xf bank_mask:0xf\n" \
  "v_cvt_pkrtz_f16_f32 v101, v82, v83\n" \
  "buffer_load_dwordx4 v[140:143], v9, %[xs], 0 offen\n" \
  "v_and_b32 v100, v16, v101\n" \
  "v_and_b32_sdwa v101, v16, v101 dst_sel:DWORD dst_unused:UNUSED_PAD src0_sel:DWORD src1_sel:WORD_1\n" \
  "ds_read_b32 v100, v100 offset:%[tb]\n" \
  "ds_read_b32 v101, v101 offset:%[tb]\n" \
  "v_add_u32 v12, 64, v12\n" \
  "v_and_b32 v18, v17, v18\n" \
  "s_bfe_u32 s61, s63, 0x40000\n" \
  "s_waitcnt lgkmcnt(3)\n" \
  "v_pk_mul_f32 v[80:81], v[80:81], v[96:97]\n" \
  "v_pk_mul_f32 v[84:85], v[84:85], v[98:99]\n" \
  "s_set_gpr_idx_on s61, gpr_idx(SRC2,DST)\n" \
  "v_pk_fma_f32 v[32:33], v[80:81], v[72:73], v[32:33] op_sel:[0,0,0] op_sel_hi:[0,1,1]\n" \
  "v_pk_fma_f32 v[42:43], v[80:81], v[74:75], v[42:43] op_sel:[0,0,0] op_sel_hi:[0,1,1]\n" \
  "v_pk_fma_f32 v[52:53], v[84:85], v[76:77], v[52:53] op_sel:[0,0,0] op_sel_hi:[0,1,1]\n" \
  "v_pk_fma_f32 v[62:63], v[84:85], v[78:79], v[62:63] op_sel:[0,0,0] op_sel_hi:[0,1,1]\n" \
  "s_set_gpr_idx_off\n" \
  "s_waitcnt vmcnt(4)\n" \
  "v_mfma_f32_4x4x4_16b_bf16 v[72:75], v[108:109], v[124:125], 0 cbsz:4 abid:9\n" \
  "v_mfma_f32_4x4x4_16b_bf16 v[76:79], v[108:109], v[126:127], 0 cbsz:4 abid:9\n" \
  "v_mfma_f32_4x4x4_16b_bf16 v[88:91], v[104:105], v[28:29], 0 cbsz:4 abid:3\n" \
  "v_mfma_f32_4x4x4_16b_bf16 v[92:95], v[104:105], v[30:31], 0 cbsz:4 abid:3\n" \
  "v_add_u32_dpp v9, v10, v8 row_newbcast:14 row_mask:0xf bank_mask:0xf\n" \
  "v_cvt_pkrtz_f16_f32 v103, v86, v87\n" \
  "buffer_load_dwordx4 v[144:147], v9, %[xs], 0 offen\n" \
  "v_and_b32 v102, v16, v103\n" \
  "v_and_b32_sdwa v103, v16, v103 dst_sel:DWORD dst_unused:UNUSED_PAD src0_sel:DWORD src1_sel:WORD_1\n" \
  "ds_read_b32 v102, v102 offset:%[tb]\n" \
  "ds_read_b32 v103, v103 offset:%[tb]\n" \
  "s_bfe_u32 s61, s63, 0x40004\n" \
  "s_set_gpr_idx_on s61, gpr_idx(SRC2,DST)\n" \
  "v_pk_fma_f32 v[32:33], v[80:81], v[72:73], v[32:33] op_sel:[1,0,0] op_sel_hi:[1,1,1]\n" \
  "v_pk_fma_f32 v[42:43], v[80:81], v[74:75], v[42:43] op_sel:[1,0,0] op_sel_hi:[1,1,1]\n" \
  "v_pk_fma_f32 v[52:53], v[84:85], v[76:77], v[52:53] op_sel:[1,0,0] op_sel_hi:[1,1,1]\n" \
  "v_pk_fma_f32 v[62:63], v[84:85], v[78:79], v[62:63] op_sel:[1,0,0] op_sel_hi:[1,1,1]\n" \
  "s_set_gpr_idx_off\n" \
  "s_waitcnt vmcnt(4)\n" \
  "v_mfma_f32_4x4x4_16b_bf16 v[72:75], v[108:109], v[128:129], 0 cbsz:4 abid:10\n" \
  "v_mfma_f32_4x4x4_16b_bf16 v[76:79], v[108:109], v[130:131], 0 cbsz:4 abid:10\n" \
  "v_add_u32_dpp v9, v10, v8 row_newbcast:15 row_mask:0xf bank_mask:0xf\n" \
  "v_cvt_pkrtz_f16_f32 v97, v88, v89\n" \
  "buffer_load_dwordx4 v[148:151], v9, %[xs], 0 offen\n" \
  "v_and_b32 v96, v16, v97\n" \
  "v_and_b32_sdwa v97, v16, v97 dst_sel:DWORD dst_unused:UNUSED_PAD src0_sel:DWORD src1_sel:WORD_1\n" \
  "ds_read_b32 v96, v96 offset:%[tb]\n" \
  "ds_read_b32 v97, v97 offset:%[tb]\n" \
  "ds_read_b64 v[104:105], v15 offset:256\n" \
  "v_cmp_ne_u32_e64 s[64:65], 0, v18\n" \
  "s_bfe_u32 s61, s63, 0x40008\n" \
  "s_waitcnt lgkmcnt(3)\n" \
  "v_pk_mul_f32 v[82:83], v[82:83], v[100:101]\n" \
  "v_pk_mul_f32 v[86:87], v[86:87], v[102:103]\n" \
  "s_set_gpr_idx_on s61, gpr_idx(SRC2,DST)\n" \
  "v_pk_fma_f32 v[32:33], v[82:83], v[72:73], v[32:33] op_sel:[0,0,0] op_sel_hi:[0,1,1]\n" \
  "v_pk_fma_f32 v[42:43], v[82:83], v[74:75], v[42:43] op_sel:[0,0,0] op_sel_hi:[0,1,1]\n" \
  "v_pk_fma_f32 v[52:53], v[86:87], v[76:77], v[52:53] op_sel:[0,0,0] op_sel_hi:[0,1,1]\n" \
  "v_pk_fma_f32 v[62:63], v[86:87], v[78:79], v[62:63] op_sel:[0,0,0] op_sel_hi:[0,1,1]\n" \
  "s_set_gpr_idx_off\n" \
  "s_waitcnt vmcnt(4) lgkmcnt(7)\n" \
  "v_and_b32 v10, 0xfffffc00, v11\n" \
  "v_mfma_f32_4x4x4_16b_bf16 v[72:75], v[108:109], v[132:133], 0 cbsz:4 abid:11\n" \
  "v_mfma_f32_4x4x4_16b_bf16 v[76:79], v[108:109], v[134:135], 0 cbsz:4 abid:11\n" \
  "v_mfma_f32_4x4x4_16b_bf16 v[116:119], v[106:107], v[22:23], 0\n" \
  "v_add_u32_dpp v9, v10, v8 row_newbcast:0 row_mask:0xf bank_mask:0xf\n" \
  "v_cvt_pkrtz_f16_f32 v99, v92, v93\n" \
  "buffer_load_dwordx4 v[120:123], v9, %[xs], 0 offen\n" \
  "v_and_b32 v98, v16, v99\n" \
  "v_and_b32_sdwa v99, v16, v99 dst_sel:DWORD dst_unused:UNUSED_PAD src0_sel:DWORD src1_sel:WORD_1\n" \
  "ds_read_b32 v98, v98 offset:%[tb]\n" \
  "ds_read_b32 v99, v99 offset:%[tb]\n" \
  "s_bfe_u32 s61, s63, 0x4000c\n" \
  "s_set_gpr_idx_on s61, gpr_idx(SRC2,DST)\n" \
  "v_pk_fma_f32 v[32:33], v[82:83], v[72:73], v[32:33] op_sel:[1,0,0] op_sel_hi:[1,1,1]\n" \
  "v_pk_fma_f32 v[42:43], v[82:83], v[74:75], v[42:43] op_sel:[1,0,0] op_sel_hi:[1,1,1]\n" \
  "v_pk_fma_f32 v[52:53], v[86:87], v[76:77], v[52:53] op_sel:[1,0,0] op_sel_hi:[1,1,1]\n" \
  "v_pk_fma_f32 v[62:63], v[86:87], v[78:79], v[62:63] op_sel:[1,0,0] op_sel_hi:[1,1,1]\n" \
  "s_set_gpr_idx_off\n" \
  "s_sub_u32 s60, s60, 1\n" \
  "s_cbranch_scc1 Lend%=\n" \
  "s_waitcnt vmcnt(4)\n" \
  "v_mfma_f32_4x4x4_16b_bf16 v[72:75], v[108:109], v[136:137], 0 cbsz:4 abid:12\n" \
  "v_mfma_f32_4x4x4_16b_bf16 v[76:79], v[108:109], v[138:139], 0 cbsz:4 abid:12\n" \
  "v_add_u32_dpp v9, v10, v8 row_newbcast:1 row_mask:0xf bank_mask:0xf\n" \
  "v_cvt_pkrtz_f16_f32 v101, v90, v91\n" \
  "buffer_load_dwordx4 v[124:127], v9, %[xs], 0 offen\n" \
  "v_and_b32 v100, v16, v101\n" \
  "v_and_b32_sdwa v101, v16, v101 dst_sel:DWORD dst_unused:UNUSED_PAD src0_sel:DWORD src1_sel:WORD_1\n" \
  "ds_read_b32 v100, v100 offset:%[tb]\n" \
  "ds_read_b32 v101, v101 offset:%[tb]\n" \
  "v_add_u32 v15, 0x100, v15\n" \
  "s_bfe_u32 s61, s63, 0x40010\n" \
  "s_waitcnt lgkmcnt(2)\n" \
  "v_pk_mul_f32 v[88:89], v[88:89], v[96:97]\n" \
  "v_pk_mul_f32 v[92:93], v[92:93], v[98:99]\n" \
  "s_set_gpr_idx_on s61, gpr_idx(SRC2,DST)\n" \
  "v_pk_fma_f32 v[32:33], v[88:89], v[72:73], v[32:33] op_sel:[0,0,0] op_sel_hi:[0,1,1]\n" \
  "v_pk_fma_f32 v[42:43], v[88:89], v[74:75], v[42:43] op_sel:[0,0,0] op_sel_hi:[0,1,1]\n" \
  "v_pk_fma_f32 v[52:53], v[92:93], v[76:77], v[52:53] op_sel:[0,0,0] op_sel_hi:[0,1,1]\n" \
  "v_pk_fma_f32 v[62:63], v[92:93], v[78:79], v[62:63] op_sel:[0,0,0] op_sel_hi:[0,1,1]\n" \
  "s_set_gpr_idx_off\n" \
  "s_waitcnt vmcnt(4) lgkmcnt(4)\n" \
  "v_mfma_f32_4x4x4_16b_bf16 v[72:75], v[108:109], v[140:141], 0 cbsz:4 abid:13\n" \
  "v_mfma_f32_4x4x4_16b_bf16 v[76:79], v[108:109], v[142:143], 0 cbsz:4 abid:13\n" \
  "v_mfma_f32_4x4x4_16b_bf16 v[80:83], v[104:105], v[28:29], 0 cbsz:4\n" \
  "v_mfma_f32_4x4x4_16b_bf16 v[84:87], v[104:105], v[30:31], 0 cbsz:4\n" \
  "v_add_u32_dpp v9, v10, v8 row_newbcast:2 row_mask:0xf bank_mask:0xf\n" \
  "v_cvt_pkrtz_f16_f32 v103, v94, v95\n" \
  "buffer_load_dwordx4 v[128:131], v9, %[xs], 0 offen\n" \
  "v_and_b32 v102, v16, v103\n" \
  "v_and_b32_sdwa v103, v16, v103 dst_sel:DWORD dst_unused:UNUSED_PAD src0_sel:DWORD src1_sel:WORD_1\n" \
  "ds_read_b32 v102, v102 offset:%[tb]\n" \
  "ds_read_b32 v103, v103 offset:%[tb]\n" \
  "s_bfe_u32 s61, s63, 0x40014\n" \
  "s_set_gpr_idx_on s61, gpr_idx(SRC2,DST)\n" \
  "v_pk_fma_f32 v[32:33], v[88:89], v[72:73], v[32:33] op_sel:[1,0,0] op_sel_hi:[1,1,1]\n" \
  "v_pk_fma_f32 v[42:43], v[88:89], v[74:75], v[42:43] op_sel:[1,0,0] op_sel_hi:[1,1,1]\n" \
  "v_pk_fma_f32 v[52:53], v[92:93], v[76:77], v[52:53] op_sel:[1,0,0] op_sel_hi:[1,1,1]\n" \
  "v_pk_fma_f32 v[62:63], v[92:93], v[78:79], v[62:63] op_sel:[1,0,0] op_sel_hi:[1,1,1]\n" \
  "s_set_gpr_idx_off\n" \
  "s_waitcnt vmcnt(4)\n" \
  "v_mfma_f32_4x4x4_16b_bf16 v[72:75], v[108:109], v[144:145], 0 cbsz:4 abid:14\n" \
  "v_mfma_f32_4x4x4_16b_bf16 v[76:79], v[108:109], v[146:147], 0 cbsz:4 abid:14\n" \
  "v_add_u32_dpp v9, v10, v8 row_newbcast:3 row_mask:0xf bank_mask:0xf\n" \
  "v_cvt_pkrtz_f16_f32 v97, v80, v81\n" \
  "buffer_load_dwordx4 v[132:135], v9, %[xs], 0 offen\n" \
  "v_and_b32 v96, v16, v97\n" \
  "v_and_b32_sdwa v97, v16, v97 dst_sel:DWORD dst_unused:UNUSED_PAD src0_sel:DWORD src1_sel:WORD_1\n" \
  "ds_read_b32 v96, v96 offset:%[tb]\n" \
  "ds_read_b32 v97, v97 offset:%[tb]\n" \
  "v_cvt_pk_bf16_f32 v19, v112, v116\n" \
  "s_bfe_u32 s61, s63, 0x40018\n" \
  "s_waitcnt lgkmcnt(2)\n" \
  "v_pk_mul_f32 v[90:91], v[90:91], v[100:101]\n" \
  "v_pk_mul_f32 v[94:95], v[94:95], v[102:103]\n" \
  "s_set_gpr_idx_on s61, gpr_idx(SRC2,DST)\n" \
  "v_pk_fma_f32 v[32:33], v[90:91], v[72:73], v[32:33] op_sel:[0,0,0] op_sel_hi:[0,1,1]\n" \
  "v_pk_fma_f32 v[42:43], v[90:91], v[74:75], v[42:43] op_sel:[0,0,0] op_sel_hi:[0,1,1]\n" \
  "v_pk_fma_f32 v[52:53], v[94:95], v[76:77], v[52:53] op_sel:[0,0,0] op_sel_hi:[0,1,1]\n" \
  "v_pk_fma_f32 v[62:63], v[94:95], v[78:79], v[62:63] op_sel:[0,0,0] op_sel_hi:[0,1,1]\n" \
  "s_set_gpr_idx_off\n" \
  "s_waitcnt vmcnt(4)\n" \
  "v_mfma_f32_4x4x4_16b_bf16 v[112:115], v[106:107], v[24:25], 0\n" \
  "v_mfma_f32_4x4x4_16b_bf16 v[116:119], v[106:107], v[26:27], 0\n" \
  "v_mfma_f32_4x4x4_16b_bf16 v[72:75], v[108:109], v[148:149], 0 cbsz:4 abid:15\n" \
  "v_mfma_f32_4x4x4_16b_bf16 v[76:79], v[108:109], v[150:151], 0 cbsz:4 abid:15\n" \
  "v_add_u32_dpp v9, v10, v8 row_newbcast:4 row_mask:0xf bank_mask:0xf\n" \
  "v_cvt_pkrtz_f16_f32 v99, v84, v85\n" \
  "buffer_load_dwordx4 v[136:139], v9, %[xs], 0 offen\n" \
  "v_and_b32 v98, v16, v99\n" \
  "v_and_b32_sdwa v99, v16, v99 dst_sel:DWORD dst_unused:UNUSED_PAD src0_sel:DWORD src1_sel:WORD_1\n" \
  "ds_read_b32 v98, v98 offset:%[tb]\n" \
  "ds_read_b32 v99, v99 offset:%[tb]\n" \
  "v_cvt_pk_bf16_f32 v109, v112, v116\n" \
  "v_mov_b32 v108, v19\n" \
  "s_bfe_u32 s61, s63, 0x4001c\n" \
  "s_set_gpr_idx_on s61, gpr_idx(SRC2,DST)\n" \
  "v_pk_fma_f32 v[32:33], v[90:91], v[72:73], v[32:33] op_sel:[1,0,0] op_sel_hi:[1,1,1]\n" \
  "v_pk_fma_f32 v[42:43], v[90:91], v[74:75], v[42:43] op_sel:[1,0,0] op_sel_hi:[1,1,1]\n" \
  "v_pk_fma_f32 v[52:53], v[94:95], v[76:77], v[52:53] op_sel:[1,0,0] op_sel_hi:[1,1,1]\n" \
  "v_pk_fma_f32 v[62:63], v[94:95], v[78:79], v[62:63] op_sel:[1,0,0] op_sel_hi:[1,1,1]\n" \
  "s_set_gpr_idx_off\n" \
  "s_sub_u32 s60, s60, 1\n" \
  "s_cbranch_scc1 Lend%=\n" \
  "s_waitcnt vmcnt(4)\n" \
  "v_mfma_f32_4x4x4_16b_bf16 v[72:75], v[108:109], v[120:121], 0 cbsz:4\n" \
  "v_mfma_f32_4x4x4_16b_bf16 v[76:79], v[108:109], v[122:123], 0 cbsz:4\n" \
  "v_add_u32_dpp v9, v10, v8 row_newbcast:5 row_mask:0xf bank_mask:0xf\n" \
  "v_cvt_pkrtz_f16_f32 v101, v82, v83\n" \
  "buffer_load_dwordx4 v[140:143], v9, %[xs], 0 offen\n" \
  "v_and_b32 v100, v16, v101\n" \
  "v_and_b32_sdwa v101, v16, v101 dst_sel:DWORD dst_unused:UNUSED_PAD src0_sel:DWORD src1_sel:WORD_1\n" \
  "ds_read_b32 v100, v100 offset:%[tb]\n" \
  "ds_read_b32 v101, v101 offset:%[tb]\n" \
  "s_mov_b64 s[62:63], s[64:65]\n" \
  "s_branch Lbody%=\n" \
  "Lend%=:\n" \
  "s_waitcnt vmcnt(0) lgkmcnt(0)\n"
#define STREAM_CLOBBERS \
  "v9", "v10", "v11", "v18", "v19", "v72", "v73", "v74", "v75", "v76", "v77", "v78", "v79", "v80", "v81", "v82", \
  "v83", "v84", "v85", "v86", "v87", "v88", "v89", "v90", "v91", "v92", "v93", "v94", "v95", "v96", "v97", \
  "v98", "v99", "v100", "v101", "v102", "v103", "v104", "v105", "v106", "v107", "v108", "v109", "v110", "v111", \
  "v112", "v113", "v114", "v115", "v116", "v117", "v118", "v119", "v120", "v121", "v122", "v123", "v124", \
  "v125", "v126", "v127", "v128", "v129", "v130", "v131", "v132", "v133", "v134", "v135", "v136", "v137", \
  "v138", "v139", "v140", "v141", "v142", "v143", "v144", "v145", "v146", "v147", "v148", "v149", "v150", \
  "v151", "s61", "s62", "s63", "s64", "s65", "s66", "s67", "s68", "s69", "s70", "s71", "s72", "s73", "s74", \
  "s75", "s76", "s77", "s78", "s79", "scc", "vcc", "memory"

namespace {

constexpr int C_ = 128;
constexpr int MW = 12;
constexpr int MT = MW * 64;
constexpr int SW = 8;
constexpr int NS = 5;
constexpr int NPB = SW * NS;
constexpr int NPASS = 16;
constexpr int HS = NPB * NPASS / 2 + 4;
constexpr int NBMAX = 256;
constexpr int PW = 5;
constexpr int PT = PW * 64;
constexpr int PK = 4;
constexpr int CH = PT * PK;
constexpr int MK = 4;
constexpr int CAPW = 256;
constexpr unsigned OOB = 0x80000000u;
#define OUTAUX 16
#define PNT " nt"
#define PFN 5
#define WSL 64
#define CPW (MW - 1)
#define TB1 2
#define TB2 5
#define GOW SW
#define PLP 2
#define POLLS 2
constexpr unsigned WS_EPOCH = 0;
constexpr unsigned WS_OFF = 4096;
constexpr unsigned WS_RECK = WS_OFF + NBMAX * NBMAX * 8;
constexpr unsigned WS_RECP = WS_RECK + NBMAX * CH * 12;
constexpr unsigned WS_END = WS_RECP + NBMAX * CH * 16;

__device__ __forceinline__ float bf2f(unsigned short v) { return __uint_as_float(((unsigned)v) << 16); }
__device__ __forceinline__ unsigned short f2bf(float f) {
  __hip_bfloat16 b = __float2bfloat16(f);
  return *reinterpret_cast<unsigned short*>(&b);
}
__device__ __forceinline__ unsigned pk2bf(float lo, float hi) {
  return __builtin_bit_cast(unsigned, __builtin_convertvector(f32x2{lo, hi}, bf16x2));
}
__device__ __forceinline__ unsigned wave_incl_scan(unsigned x) {
  x += __builtin_amdgcn_update_dpp(0u, x, 0x111, 0xf, 0xf, false);
  x += __builtin_amdgcn_update_dpp(0u, x, 0x112, 0xf, 0xf, false);
  x += __builtin_amdgcn_update_dpp(0u, x, 0x114, 0xf, 0xf, false);
  x += __builtin_amdgcn_update_dpp(0u, x, 0x118, 0xf, 0xf, false);
  x += __builtin_amdgcn_update_dpp(0u, x, 0x142, 0xa, 0xf, false);
  x += __builtin_amdgcn_update_dpp(0u, x, 0x143, 0xc, 0xf, false);
  return x;
}

typedef __attribute__((ext_vector_type(2))) unsigned short u16x2;
__device__ __forceinline__ unsigned check_word(const u32x4& p, unsigned tag) {
  tag = __builtin_amdgcn_udot2(__builtin_bit_cast(u16x2, p[0]), u16x2{0x9e3b, 0x79b9}, tag, false);
  tag = __builtin_amdgcn_udot2(__builtin_bit_cast(u16x2, p[1]), u16x2{0x85eb, 0xca6b}, tag, false);
  tag = __builtin_amdgcn_udot2(__builtin_bit_cast(u16x2, p[2]), u16x2{0xc2b3, 0xae35}, tag, false);
  return __builtin_amdgcn_udot2(__builtin_bit_cast(u16x2, p[3]), u16x2{0xa7d5, 0xeb2f}, tag, false);
}

struct Lds {
  u32x4 sink[16];
  unsigned flag, wrdy, rdone, go;
  unsigned long long tag, pad2_;
  int deg[64];
  unsigned nl[64];
  unsigned tot[HS];
  u32x4 ct[72];
  __attribute__((aligned(16))) unsigned so[SW * CAPW];
  union {
    __attribute__((aligned(16))) unsigned hist[SW * HS];
    u32x4 row5[SW * 64];
  };
  f32x4 sg[4096];
  union {
    struct {
      __attribute__((aligned(16))) unsigned phist[PW][NBMAX / 2];
      unsigned pbase[PW][NBMAX / 2];
      unsigned ri[SW][MK * 64];
      __attribute__((aligned(16))) unsigned sk[CH * 3];
    };
    u32x4 pay[SW * CAPW];
  };
  union {
    u32x4 sp[CH];
    u32x4 wf[C_ * C_ / 8];
  };
};
static_assert(sizeof(Lds) <= 163840, "LDS budget");
static_assert(offsetof(Lds, sg) < 65536, "table offset");

__device__ __forceinline__ unsigned lds_addr(const void* p) {
  return (unsigned)(uintptr_t)(__attribute__((address_space(3))) const void*)p;
}
__device__ __forceinline__ u32x4 make_srd(const void* p, unsigned num) {
  unsigned long long a = (unsigned long long)p;
  return u32x4{(unsigned)a, (unsigned)(a >> 32), num, 0x00020000u};
}

}

extern "C" __global__ __launch_bounds__(MT) void tfn_main_kernel(
    const int* __restrict__ dst, const int* __restrict__ src, const u32x2* __restrict__ Y, const u32x2* __restrict__ ef,
    int E, int N, int NBK, unsigned M3, const unsigned short* __restrict__ x, char* ws, u32x4* __restrict__ out,
    const unsigned short* __restrict__ wrad, const unsigned short* __restrict__ cg, const unsigned short* __restrict__ wmix) {
  __shared__ Lds L;
  const int tid = threadIdx.x, lane = tid & 63;
  const int w = __builtin_amdgcn_readfirstlane(tid >> 6);
  TDECL;
  STAMP(0);
  asm volatile("s_cmp_lg_u32 %1, %2\n s_cbranch_scc1 1f\n"
               "s_getpc_b64 s[40:41]\n .Ltfn_p0:\n"
               "s_max_i32 s42, 0, .Ltfn_p1-.Ltfn_p0\n s_mov_b32 m0, %3\n s_mov_b32 s43, 0x20000\n s_movk_i32 s44, 128\n"
               "buffer_load_dword %0, s[40:43], 0 offen lds\n buffer_load_dword %0, s[40:43], s44 offen lds\n 1:"
               :: "v"(lane * 256), "s"((blockIdx.x >> 3) * 16 + w), "n"(CPW), "n"(offsetof(Lds, sink))
               : "s40", "s41", "s42", "s43", "s44", "scc", "memory");
  const int xcd = blockIdx.x & 7;
  const int bucket = ((xcd * NBK) >> 3) + (blockIdx.x >> 3);
  const bool idle = bucket >= (((xcd + 1) * NBK) >> 3);
  const int e0 = bucket * CH + tid * 2;
  u32x2 d0, d1, s0, s1;
  u32x4 y0, y1, f0, f1;
  const int wp = idle ? PW : w;
  u32x2 ev;
  asm volatile("s_cmp_lg_u32 %3, %4\n s_cbranch_scc1 1f\n global_load_dwordx2 %0, %1, %2\n 1:"
               : "=&v"(ev) : "v"(WS_EPOCH), "s"(ws), "s"(w), "n"(SW) : "scc");
  asm volatile(
      "s_cmp_ge_u32 %8, %9\n s_cbranch_scc1 1f\n"
      "buffer_load_dwordx2 %0, %4, %5, 0 offen" PNT "\n buffer_load_dwordx2 %1, %4, %5, %7 offen" PNT "\n"
      "buffer_load_dwordx2 %2, %4, %6, 0 offen" PNT "\n buffer_load_dwordx2 %3, %4, %6, %7 offen" PNT "\n"
      "1:\n s_barrier\n"
      : "=&v"(d0), "=&v"(d1), "=&v"(s0), "=&v"(s1)
      : "v"(e0 * 4), "s"(make_srd(dst, E * 4)), "s"(make_srd(src, E * 4)), "s"(CH * 2), "s"(wp), "n"(PW) : "scc");
  const u32x4 wsd = make_srd(ws, WS_END);
  asm volatile(
      "s_cmp_ge_u32 %9, %10\n s_cbranch_scc1 1f\n s_setprio 3\n"
      "buffer_load_dwordx4 %0, %5, %6, 0 offen" PNT "\n buffer_load_dwordx4 %1, %5, %6, %8 offen" PNT "\n"
      "buffer_load_dwordx4 %2, %5, %7, 0 offen" PNT "\n buffer_load_dwordx4 %3, %5, %7, %8 offen" PNT "\n"
      "1:\n"
      : "=&v"(y0), "=&v"(y1), "=&v"(f0), "=&v"(f1), "+v"(ev)
      : "v"(e0 * 8), "s"(make_srd(Y, E * 8)), "s"(make_srd(ef, E * 8)), "s"(CH * 4), "s"(wp), "n"(PW), "s"(ws),
        "s"(out), "s"(wmix), "s"(cg), "s"(wrad) : "scc");
  STAMP(1);
  auto table = [&](int q) {
    f32x4 p, n;
#pragma unroll
    for (int i = 0; i < 4; i++) {
      const unsigned short c = min(q * 16 + i * 4 + 2, 0x7bff);
      const float e = __builtin_amdgcn_exp2f(-1.4426950408889634f * (float)__builtin_bit_cast(_Float16, c));
      p[i] = __builtin_amdgcn_rcpf(1.f + e);
      n[i] = e * p[i];
    }
    L.sg[q] = p;
    L.sg[q + 2048] = n;
  };
  auto valid = [&](int k) { return e0 + (k >> 1) * (CH / 2) + (k & 1) < E; };
  auto warm = [&](int i0, int i1) {
    const unsigned pfo = ((xcd * N) >> 3) * 1024 + ((blockIdx.x >> 3) * 64 + lane) * 128;
#pragma unroll 1
    for (int i = i0; i < i1; i += MW - SW)
      asm volatile("s_mov_b32 m0, %2\n s_nop 0\n buffer_load_dword %0, %1, %3 offen lds\n"
                   :: "v"(pfo), "s"(make_srd(x, N * 1024)), "s"(lds_addr(L.sink)), "s"(i * 32 * 64 * 128) : "memory");
  };
  if (idle) {
    if (w >= SW) warm(w - SW, PFN);
    return;
  }
  unsigned bk[PK], pr[PK], ky[PK];
  unsigned cv, wq4[4];
  asm volatile(
      "s_cmp_lg_u32 %9, %10\n s_cbranch_scc1 1f\n global_load_ushort %0, %5, %7\n global_load_dword %1, %6, %8\n"
      "global_load_dword %2, %6, %8 offset:256\n global_load_dword %3, %6, %8 offset:512\n"
      "global_load_dword %4, %6, %8 offset:768\n 1:"
      : "=&v"(cv), "=&v"(wq4[0]), "=&v"(wq4[1]), "=&v"(wq4[2]), "=&v"(wq4[3])
      : "v"(lane * 2), "v"(lane * 4), "s"(cg), "s"(wrad), "s"(w), "n"(PW) : "scc");
  const int ht = tid - PT;
  if (w < PW) {
    reinterpret_cast<u32x2*>(L.phist[w])[lane] = u32x2{0, 0};
    asm volatile("s_waitcnt vmcnt(4)" : "+v"(d0), "+v"(d1), "+v"(s0), "+v"(s1));
    STAMP(3);
    const u32x4 dv = {d0[0], d0[1], d1[0], d1[1]}, sv = {s0[0], s0[1], s1[0], s1[1]};
#pragma unroll
    for (int k = 0; k < PK; k++) {
      bk[k] = dv[k] / NPB;
      pr[k] = atomicAdd(&L.phist[w][bk[k] >> 1], (valid(k) ? 1u : 0u) << ((bk[k] & 1) * 16));
    }
#pragma unroll
    for (int k = 0; k < PK; k++) {
      ky[k] = (sv[k] << 10) | ((dv[k] - bk[k] * NPB) << 4) | __umulhi(sv[k], M3);
      pr[k] = (pr[k] >> ((bk[k] & 1) * 16)) & 0xffff;
      asm volatile("" : "+v"(ky[k]), "+v"(pr[k]));
    }
  } else {
    if (w == PW) {
      L.deg[lane] = lane < NPB ? lane : -1;
      if (lane < 4) (&L.flag)[lane] = 0;
    }
#pragma unroll 1
    for (int q = ht; q < TB1 * (MT - PT); q += MT - PT) table(q);
    if (w == SW) {
      asm volatile("s_waitcnt    vmcnt(0)" : "+v"(ev));
      L.tag = (ev[0] | (unsigned long long)ev[1] << 32) * 6364136223846793005ull + 1442695040888963407ull;
    }
  }
  STAMP(4);
  __syncthreads();
  STAMP(5);
  const auto wsr = __builtin_amdgcn_make_buffer_rsrc((void*)ws, 0, WS_END, 0x00020000);
  const unsigned long long epoch = L.tag;
  unsigned ca = 0, cb = 0, ba, bb;
  if (w < PW || w == SW) {
    unsigned pa = 0, pb = 0;
#pragma unroll
    for (int ww = 0; ww < PW; ww++) {
      const unsigned ha = L.phist[ww][lane], hb = L.phist[ww][64 + lane];
      pa = ww == w ? ca : pa;
      pb = ww == w ? cb : pb;
      ca += ha;
      cb += hb;
    }
    const unsigned t = __builtin_amdgcn_perm(cb, ca, 0x05040100) + __builtin_amdgcn_perm(cb, ca, 0x07060302);
    const unsigned inc = wave_incl_scan(t), ex = inc - t;
    ba = (ex & 0xffff) * 0x10001u + (ca << 16);
    bb = ((ex >> 16) + (__builtin_amdgcn_readlane(inc, 63) & 0xffff)) * 0x10001u + (cb << 16);
    if (w < PW) {
      L.pbase[w][lane] = ba + pa;
      L.pbase[w][64 + lane] = bb + pb;
    }
  }
  const unsigned tagl = __builtin_amdgcn_readfirstlane((unsigned)epoch), tag0 = tagl << 22;
  const unsigned tag1 = __builtin_amdgcn_readfirstlane((unsigned)(epoch >> 32));
  STAMP(6);
  u32x4 pv[PK];
  if (w < PW) {
    unsigned b4[PK];
#pragma unroll
    for (int k = 0; k < PK; k++) b4[k] = reinterpret_cast<const unsigned short*>(L.pbase[w])[bk[k]];
    asm volatile("s_waitcnt vmcnt(0)" : "+v"(y0), "+v"(y1), "+v"(f0), "+v"(f1));
    unsigned hw[PK];
#pragma unroll
    for (int k = 0; k < PK; k++) {
      const u32x4 &yy = k < 2 ? y0 : y1, &ff = k < 2 ? f0 : f1;
      pv[k] = u32x4{yy[(k & 1) * 2], yy[(k & 1) * 2 + 1], ff[(k & 1) * 2], ff[(k & 1) * 2 + 1]};
      hw[k] = check_word(pv[k], tagl);
      ky[k] |= tagl << 24;
    }
#pragma unroll
    for (int k = 0; k < PK; k++) {
      pr[k] = valid(k) ? pr[k] + b4[k] : CH - 1;
      L.sk[pr[k] * 3] = ky[k];
      L.sk[pr[k] * 3 + 1] = ky[k] + tag1;
      L.sk[pr[k] * 3 + 2] = hw[k];
    }
  } else if (w == SW) {
    __builtin_amdgcn_raw_buffer_store_b128(u32x4{(ba & 0xffff) | ((ca & 0xffff) << 11) | tag0, tag1, (ba >> 16) | ((ca >> 16) << 11) | tag0, tag1},
                                           wsr, lane * 16 + WS_OFF + bucket * (NBMAX * 8), 0, 16);
    __builtin_amdgcn_raw_buffer_store_b128(u32x4{(bb & 0xffff) | ((cb & 0xffff) << 11) | tag0, tag1, (bb >> 16) | ((cb >> 16) << 11) | tag0, tag1},
                                           wsr, lane * 16 + 1024 + WS_OFF + bucket * (NBMAX * 8), 0, 16);
  } else {
    if (w == PW) {
      asm volatile("s_waitcnt   vmcnt(0)" : "+v"(cv), "+v"(wq4[0]), "+v"(wq4[1]), "+v"(wq4[2]), "+v"(wq4[3]));
      reinterpret_cast<unsigned short*>(L.ct)[(lane & 3) * 16 + (lane >> 2)] = cv;
      L.ct[8 + lane] = u32x4{__builtin_amdgcn_perm(wq4[1], wq4[0], 0x05040100), __builtin_amdgcn_perm(wq4[3], wq4[2], 0x05040100),
                             __builtin_amdgcn_perm(wq4[1], wq4[0], 0x07060302), __builtin_amdgcn_perm(wq4[3], wq4[2], 0x07060302)};
    }
    const int zt = ht - (w > SW ? 64 : 0);
#pragma unroll
    for (int i = 0; i < 2; i++)
      if (i * 384 + zt < SW * HS / 4) reinterpret_cast<u32x4*>(L.hist)[i * 384 + zt] = u32x4{0, 0, 0, 0};
  }
  STAMP(7);
  __syncthreads();
  STAMP(27);
  const int st = tid - SW * 64;
  if (w < PW) {
#pragma unroll
    for (int k = 0; k < PK; k++) L.sp[pr[k]] = pv[k];
  } else if (w >= SW) {
    u32x4 v[4];
#pragma unroll
    for (int k = 0; k < 4; k++) v[k] = reinterpret_cast<const u32x4*>(L.sk)[min(k * 256 + st, CH * 3 / 4 - 1)];
#pragma unroll
    for (int k = 0; k < 4; k++)
      if (k < 3 || w < MW - 1)
        __builtin_amdgcn_raw_buffer_store_b128(v[k], wsr, (k * 256 + st) * 16 + WS_RECK + bucket * (CH * 12), 0, 16);
  }
  STAMP(28);
  __syncthreads();
  STAMP(8);
  const int node0 = bucket * NPB;
  int Tw = 0;
  unsigned key[MK], chk[MK], rk[MK], idx[MK], bin[MK];
  u32x4 pay_[MK];
  const int sgi = (lane >> 1) * SW + w;
  auto lds_ld = [&](const unsigned* p) {
    return __builtin_amdgcn_readfirstlane(__hip_atomic_load(p, __ATOMIC_RELAXED, __HIP_MEMORY_SCOPE_WORKGROUP));
  };
  if (w >= SW) {
    u32x4 v[5];
#pragma unroll
    for (int k = 0; k < 5; k++) v[k] = L.sp[k * 256 + st];
#pragma unroll 1
    for (int q = st + TB1 * (MT - PT); q < TB1 * (MT - PT) + TB2 * 256; q += MT - SW * 64) table(q);
    while (lds_ld(&L.go) < GOW) __builtin_amdgcn_s_sleep(1);
    STAMP(10);
#pragma unroll
    for (int k = 0; k < 5; k++)
      __builtin_amdgcn_raw_buffer_store_b128(v[k], wsr, (k * 256 + st) * 16 + WS_RECP + bucket * (CH * 16), 0, 16);
#pragma unroll 1
    for (int q = st + TB1 * (MT - PT) + TB2 * 256; q < 2048; q += MT - SW * 64) table(q);
  } else {
    unsigned ow = 0;
    {
      bool want = sgi < NBK;
#pragma unroll 1
      for (int spin = 0; spin < (1 << 16); spin++) {
        const u32x2 g = __builtin_amdgcn_raw_buffer_load_b64(wsr, want ? sgi * (NBMAX * 8) : OOB, WS_OFF + bucket * 8, 16);
        const bool ok = want && g[1] == tag1 && ((g[0] ^ tag0) >> 22) == 0;
        ow = ok ? g[0] & 0x3fffff : ow;
        want = want && !ok;
        if (__ballot(want) == 0) break;
        __builtin_amdgcn_s_sleep(POLLS);
        asm volatile("" ::: "memory");
      }
    }
    STAMP(10);
    const unsigned cnt = ow >> 11;
    const unsigned inc = wave_incl_scan((lane & 1) == 0 ? cnt : 0u);
    const unsigned Tw0 = __builtin_amdgcn_readlane(inc, 63);
    const bool wok = Tw0 <= (unsigned)(MK * 64);
    Tw = wok ? Tw0 : 0;
    unsigned r0 = (sgi * CH + (ow & 0x7ff) + (lane & 1)) * 4;
    unsigned* lp = &L.ri[w][inc - cnt + (lane & 1)];
    if (wok) {
#pragma clang loop unroll(disable) vectorize(disable)
      for (int i = (int)cnt - (lane & 1); i > 0; i -= 2, lp += 2, r0 += 8) *lp = r0;
    } else if (lane == 0) {
      L.flag = 1;
    }
#pragma unroll
    for (int k = 0; k < MK; k++) {
      idx[k] = L.ri[w][k * 64 + lane];
      idx[k] = lane < Tw - k * 64 ? idx[k] : 0x20000000u;
    }
    STAMP(11);
    if (lane == 0) atomicAdd(&L.go, 1u);
#pragma unroll 1
    for (int spin = 0; spin < (1 << 16); spin++) {
      u32x3 g[MK];
#pragma unroll
      for (int k = 0; k < MK - 1; k++) g[k] = __builtin_amdgcn_raw_buffer_load_b96(wsr, idx[k] * 3, WS_RECK, 16);
      g[MK - 1] = u32x3{0, 0, 0};
      if (Tw > (MK - 1) * 64) g[MK - 1] = __builtin_amdgcn_raw_buffer_load_b96(wsr, idx[MK - 1] * 3, WS_RECK, 16);
      bool bad = false;
#pragma unroll
      for (int k = 0; k < MK; k++) {
        key[k] = g[k][0] ^ (tagl << 24);
        chk[k] = g[k][2];
        bad |= lane < Tw - k * 64 && (g[k][1] != g[k][0] + tag1 || (key[k] >> 24) != 0);
      }
      if (__ballot(bad) == 0) break;
      __builtin_amdgcn_s_sleep(POLLS);
      asm volatile("" ::: "memory");
    }
    STAMP(12);
  }
#define PAYLOAD_LOADS \
  asm volatile( \
      "s_cmp_ge_u32 %11, %12\n s_cbranch_scc1 2f\n" \
      "buffer_load_dwordx4 %0, %4, %8, %9 offen sc1\n buffer_load_dwordx4 %1, %5, %8, %9 offen sc1\n" \
      "buffer_load_dwordx4 %2, %6, %8, %9 offen sc1\n" \
      "s_cmp_le_i32 %10, 192\n s_cbranch_scc1 2f\n buffer_load_dwordx4 %3, %7, %8, %9 offen sc1\n 2:\n" \
      : "=&v"(pay_[0]), "=&v"(pay_[1]), "=&v"(pay_[2]), "=&v"(pay_[3]) \
      : "v"(idx[0] * 4), "v"(idx[1] * 4), "v"(idx[2] * 4), "v"(idx[3] * 4), "s"(wsd), "s"(WS_RECP), "s"(Tw), "s"(w), "n"(SW) \
      : "scc", "memory");
  if (w < SW) {
    auto count = [&](int k) {
      const bool ok = lane < Tw - k * 64;
      key[k] = ok ? key[k] : lane * 2;
      bin[k] = (min(key[k] & 0x3f0, (NPB - 1) * 16u) | ((key[k] - 2 * xcd) & 15)) * 2;
      rk[k] = atomicAdd(&L.hist[w * HS + (bin[k] >> 2)], (ok ? 1u : 0u) << ((bin[k] & 2) * 8));
      atomicAdd(&L.deg[bin[k] >> 5], ok ? 64 : 0);
    };
#pragma unroll
    for (int k = 0; k < MK - 1; k++) count(k);
    if (Tw > (MK - 1) * 64) count(MK - 1);
  }
  STAMP(14);
  __syncthreads();
  STAMP(16);
  if (w < PW) {
    unsigned h[SW], s = 0;
#pragma unroll
    for (int i = 0; i < SW; i++) h[i] = L.hist[i * HS + tid];
#pragma unroll
    for (int i = 0; i < SW; i++) {
      L.hist[i * HS + tid] = s;
      s += h[i];
    }
    L.tot[tid] = s;
  } else {
    const int dg = L.deg[lane];
    unsigned r = 0;
#pragma unroll
    for (int i = 0; i < 6; i++) {
      const unsigned ri = __popcll(__ballot(dg > __builtin_amdgcn_readlane(dg, (w - PW) * 6 + i)));
      r = lane == i ? ri : r;
    }
    const int n = (w - PW) * 6 + lane;
    if (lane < 6 && n < NPB) L.nl[((r & 7) ^ (r < 16 ? 0 : 7)) * 8 + (r >> 3)] = n;
  }
  PAYLOAD_LOADS
  STAMP(17);
  __syncthreads();
  STAMP(18);
  if (w >= SW) {
    warm(w - SW, PFN);
    if (bucket == 0 && tid == MT - 1)
      __builtin_amdgcn_raw_buffer_store_b64(u32x2{tagl, tag1}, wsr, WS_EPOCH, 0, 16);
  }
  int len = 0;
  if (w < SW) {
    const bool use = (lane & 7) < NS;
    unsigned* tp = &L.tot[use ? L.nl[w * 8 + (lane & 7)] * 8 + (lane >> 3) : NPB * NPASS / 2];
    const unsigned v = use ? *tp : 0u;
    const unsigned in2 = wave_incl_scan(v), ex = in2 - v;
    unsigned e0 = __builtin_amdgcn_update_dpp(0u, ex, 0x150, 0xf, 0x3, false);
    e0 = __builtin_amdgcn_update_dpp(e0, ex, 0x158, 0xf, 0xc, false);
    unsigned i7 = __builtin_amdgcn_update_dpp(0u, in2, 0x157, 0xf, 0x3, false);
    i7 = __builtin_amdgcn_update_dpp(i7, in2, 0x15f, 0xf, 0xc, false);
    *tp = ex + (e0 >> 16) + (i7 << 16) + (w * CAPW + ((lane & 7) << 12)) * 0x10001u;
    const unsigned tt = __builtin_amdgcn_readlane(in2, 63);
    len = (tt & 0xffff) + (tt >> 16);
    if (len > CAPW - 40) {
      if (lane == 0) L.flag = 1;
    } else if (lane < 36) {
      L.so[w * CAPW + len + lane] = OOB;
      L.pay[w * CAPW + len + lane] = u32x4{0, 0, 0, 0};
    }
  }
  __syncthreads();
  STAMP(20);
  const bool slow = __builtin_amdgcn_readfirstlane(L.flag) != 0;
  if (w < SW) {
    auto place = [&](int k0, int k1) {
#pragma unroll
      for (int k = k0; k < k1; k++) rk[k] += L.hist[w * HS + (bin[k] >> 2)] + L.tot[bin[k] >> 2];
#pragma unroll
      for (int k = k0; k < k1; k++) {
        rk[k] >>= (bin[k] & 2) * 8;
        const unsigned e = (key[k] & ~1023u) | ((rk[k] >> 11) & 14);
        rk[k] = lane < Tw - k * 64 && !slow ? rk[k] & 0xfff : CAPW - 1;
        L.so[rk[k]] = e;
      }
    };
    place(0, MK - 1);
    if (Tw > (MK - 1) * 64) place(MK - 1, MK);
    asm volatile("s_waitcnt vmcnt(0)" : "+v"(pay_[0]), "+v"(pay_[1]), "+v"(pay_[2]), "+v"(pay_[3]));
    STAMP(21);
#pragma unroll 1
    for (int spin = 0; spin < (1 << 16); spin++) {
      bool bad = false;
#pragma unroll
      for (int k = 0; k < MK; k++) bad |= lane < Tw - k * 64 && check_word(pay_[k], tagl) != chk[k];
      if (__ballot(bad) == 0) break;
      asm volatile("" ::: "memory");
#pragma unroll
      for (int k = 0; k < MK; k++) {
        pay_[k] = __builtin_amdgcn_raw_buffer_load_b128(wsr, idx[k] * 4, WS_RECP, 16);
        chk[k] = __builtin_amdgcn_raw_buffer_load_b32(wsr, idx[k] * 3 + 8, WS_RECK, 16);
      }
    }
#pragma unroll
    for (int k = 0; k < MK - 1; k++) L.pay[rk[k]] = pay_[k];
    if (Tw > (MK - 1) * 64) L.pay[rk[MK - 1]] = pay_[MK - 1];
  }
  __syncthreads();
  STAMP(22);
  CSTAMP(30);
  f32x16 ac0, ac1;
  f32x8 ac2;
  if (w >= SW) {
    __builtin_amdgcn_s_sleep(WSL);
    const u32x4* wp = reinterpret_cast<const u32x4*>(wmix) + ((w - SW) * 32 + (lane >> 4) * 8) * 16 + (lane & 15);
    u32x4 q[8];
#pragma unroll
    for (int j = 0; j < 8; j++) q[j] = wp[j * 16];
    u32x4* fp = &L.wf[((w - SW) * 8 + ((lane >> 2) & 3) * 2) * 64 + (lane & 48) + (lane & 3) * 4];
#pragma unroll
    for (int i = 0; i < 4; i++) {
      fp[i] = u32x4{__builtin_amdgcn_perm(q[1][i], q[0][i], 0x05040100), __builtin_amdgcn_perm(q[3][i], q[2][i], 0x05040100),
                    __builtin_amdgcn_perm(q[5][i], q[4][i], 0x05040100), __builtin_amdgcn_perm(q[7][i], q[6][i], 0x05040100)};
      fp[64 + i] = u32x4{__builtin_amdgcn_perm(q[1][i], q[0][i], 0x07060302), __builtin_amdgcn_perm(q[3][i], q[2][i], 0x07060302),
                         __builtin_amdgcn_perm(q[5][i], q[4][i], 0x07060302), __builtin_amdgcn_perm(q[7][i], q[6][i], 0x07060302)};
    }
    asm volatile("" ::: "memory");
    if (lane == 0) atomicAdd(&L.wrdy, 1u);
    if (w >= SW + 2) {
      TFLUSH;
      return;
    }
  } else if (!slow) {
    __builtin_amdgcn_s_setprio(1);
    const int start = w * CAPW;
    unsigned sop = lds_addr(&L.so[start + (lane & 15)]);
    unsigned s4p = lds_addr(&L.so[start + (lane >> 2)]);
    unsigned yp = lds_addr(&L.pay[start + (lane >> 2)]);
    unsigned ep = lds_addr(&L.pay[start + (lane & 15)]) + 8;
    const u32x4 cg0 = L.ct[(lane & 3) * 2], cg1 = L.ct[(lane & 3) * 2 + 1];
    const u32x4 wq = L.ct[8 + lane];
    unsigned rem = __builtin_amdgcn_readfirstlane(max(len - 1, 0) >> 2);
    asm volatile(STREAM_ASM
                 : "={v[32:47]}"(ac0), "={v[48:63]}"(ac1), "={v[64:71]}"(ac2), "+{v12}"(sop), "+{v13}"(s4p), "+{v14}"(yp),
                   "+{v15}"(ep), "+{s60}"(rem)
                 : "{v8}"(lane * 16), "{v16}"(0xfffcu), "{v17}"(1u << (lane & 3)),
                   "{v[20:23]}"(cg0), "{v[24:27]}"(cg1), "{v[28:31]}"(wq),
                   [xs] "s"(make_srd(x, N * 1024)), [tb] "n"(offsetof(Lds, sg))
                 : STREAM_CLOBBERS);
  }
  STAMP(24);
  CSTAMP(31);
  const int row = lane & 15, kq = lane >> 4;
  u32x4* rows = w < SW ? &L.pay[w * CAPW] : &L.row5[(w - SW) * 256];
  if (w >= SW) {
    const unsigned need = 15u << ((w - SW) * 4);
    while ((lds_ld(&L.rdone) & need) != need) __builtin_amdgcn_s_sleep(2);
  } else if (slow) {
#pragma unroll 1
    for (int it = 0; it < 2 * NS; it++) {
      const int sl = it >> 1, slice = it & 1, ch = slice * 64 + lane;
      const unsigned dl = L.nl[w * 8 + sl];
      float acc[4] = {0.f, 0.f, 0.f, 0.f};
#pragma unroll 1
      for (int g = 0; g < NBK; g++) {
        const unsigned o2 = __builtin_amdgcn_raw_buffer_load_b32(wsr, g * (NBMAX * 8), WS_OFF + bucket * 8, 16);
        const unsigned a = o2 & 0x7ff, b = a + ((o2 >> 11) & 0x7ff);
#pragma unroll 1
        for (unsigned t = a; t < b; t++) {
          u32x3 kr;
          u32x4 r;
#pragma unroll 1
          for (int spin = 0; spin < (1 << 16); spin++) {
            asm volatile("" ::: "memory");
            kr = __builtin_amdgcn_raw_buffer_load_b96(wsr, (g * CH + t) * 12, WS_RECK, 16);
            r = __builtin_amdgcn_raw_buffer_load_b128(wsr, (g * CH + t) * 16, WS_RECP, 16);
            if (__ballot(kr[1] != kr[0] + tag1 || ((kr[0] >> 24) != (tagl & 255)) || check_word(r, tagl) != kr[2]) == 0) break;
          }
          const unsigned ky = kr[0] & 0xffffff;
          if (((ky >> 4) & 63) != dl) continue;
          float yy[4] = {bf2f(r[0] & 0xffff), bf2f(r[0] >> 16), bf2f(r[1] & 0xffff), bf2f(r[1] >> 16)};
          float ee[4] = {bf2f(r[2] & 0xffff), bf2f(r[2] >> 16), bf2f(r[3] & 0xffff), bf2f(r[3] >> 16)};
          float z = 0.f;
#pragma unroll
          for (int k = 0; k < 4; k++) z += ee[k] * bf2f(wrad[k * C_ + ch]);
          float R = z / (1.f + __expf(-z));
          const unsigned short* xr = x + (size_t)(ky >> 10) * 512 + ch * 4;
#pragma unroll 1
          for (int o = 0; o < 4; o++) {
            float tp = 0.f;
#pragma unroll 1
            for (int i = 0; i < 4; i++) {
              float A = 0.f;
#pragma unroll
              for (int f = 0; f < 4; f++) A += yy[f] * bf2f(cg[i * 16 + f * 4 + o]);
              tp += bf2f(xr[i]) * bf2f(f2bf(A));
            }
            float v = R * tp;
            acc[0] += o == 0 ? v : 0.f;
            acc[1] += o == 1 ? v : 0.f;
            acc[2] += o == 2 ? v : 0.f;
            acc[3] += o == 3 ? v : 0.f;
          }
        }
      }
      u32x4* rw = sl < 4 ? &rows[sl * 64] : &L.row5[w * 64];
      unsigned short* ar = reinterpret_cast<unsigned short*>(&rw[slice * 32]) + (lane >> 5) * 128 + (lane & 31);
#pragma unroll
      for (int o = 0; o < 4; o++) ar[o * 32] = f2bf(acc[o]);
    }
    asm volatile("" ::: "memory");
    if (lane == 0) atomicOr(&L.rdone, 1u << w);
  } else {
    auto acc = [&](int i) { return i < 16 ? ac0[i] : i < 32 ? ac1[i - 16] : ac2[i - 32]; };
    auto put = [&](u32x4* rw, int j) {
      unsigned* rp = reinterpret_cast<unsigned*>(rw) + (lane >> 4) * 64 + (lane & 15);
#pragma unroll
      for (int o = 0; o < 4; o++) {
        const int i = (o >> 1) * 2 * NS + 2 * j + (o & 1);
        rp[o * 16] = pk2bf(acc(i), acc(i + 4 * NS));
      }
    };
    put(&L.row5[w * 64], 4);
    asm volatile("" ::: "memory");
    if (lane == 0) atomicOr(&L.rdone, 1u << w);
#pragma unroll
    for (int j = 0; j < 4; j++) put(&rows[j * 64], j);
  }
  __builtin_amdgcn_fence(__ATOMIC_SEQ_CST, "wavefront");
  const unsigned node = node0 + L.nl[w < SW ? w * 8 + kq : ((w - SW) * 4 + kq) * 8 + 4];
  const u32x4* op = &rows[(row >> 2) * 64 + (row & 3) * 4 + kq];
  s16x8 f[4];
#pragma unroll
  for (int k = 0; k < 4; k++) f[k] = __builtin_bit_cast(s16x8, op[k * 16]);
  while (lds_ld(&L.wrdy) != MW - SW) __builtin_amdgcn_s_sleep(1);
  asm volatile("" ::: "memory");
  f32x4 d[8];
#pragma unroll
  for (int t = 0; t < 8; t++) d[t] = f32x4{0.f, 0.f, 0.f, 0.f};
#pragma unroll
  for (int t = 0; t < 32; t++)
    d[t & 7] = __builtin_amdgcn_mfma_f32_16x16x32_bf16(f[t >> 3], __builtin_bit_cast(s16x8, L.wf[t * 64 + lane]), d[t & 7], 0, 0, 0);
  const auto ors = __builtin_amdgcn_make_buffer_rsrc((void*)out, 0, N * 1024, 0x00020000);
#pragma unroll
  for (int m = 0; m < 4; m++) {
    const f32x4 a = d[2 * m], b = d[2 * m + 1];
    __builtin_amdgcn_raw_buffer_store_b128(
        u32x4{pk2bf(a[0], a[1]), pk2bf(a[2], a[3]), pk2bf(b[0], b[1]), pk2bf(b[2], b[3])}, ors,
        node * 1024 + m * 256 + row * 16, 0, OUTAUX);
  }
  asm volatile(".Ltfn_p1:");
  STAMP(26);
  TFLUSH;
}

extern "C" void kernel_launch(void* const* d_in, const int* in_sizes, int n_in, void* d_out, int out_size,
                              void* d_ws, size_t ws_size, hipStream_t stream) {
  const unsigned short* x = (const unsigned short*)d_in[0];
  const int* src = (const int*)d_in[1];
  const int* dst = (const int*)d_in[2];
  const u32x2* Y = (const u32x2*)d_in[3];
  const u32x2* ef = (const u32x2*)d_in[4];
  const unsigned short* wrad = (const unsigned short*)d_in[5];
  const unsigned short* cg = (const unsigned short*)d_in[6];
  const unsigned short* wmix = (const unsigned short*)d_in[7];
  const int N = in_sizes[0] / 512;
  const int E = in_sizes[1];
  const int NBK = (N + NPB - 1) / NPB;
  const unsigned long long M3l = (((unsigned long long)NPASS) << 32) / (unsigned long long)N;
  const unsigned M3 = M3l > 0xffffffffull ? 0xffffffffu : (unsigned)M3l;
  tfn_main_kernel<<<256, MT, 0, stream>>>(dst, src, Y, ef, E, N, NBK, M3, x, (char*)d_ws, (u32x4*)d_out, wrad, cg, wmix);
}
```

```cpp
#include <hip/hip_runtime.h>
#include <hip/hip_bf16.h>
#include <stddef.h>
#include <stdint.h>

typedef __attribute__((ext_vector_type(2))) float f32x2;
typedef __attribute__((ext_vector_type(4))) float f32x4;
typedef __attribute__((ext_vector_type(8))) float f32x8;
typedef __attribute__((ext_vector_type(16))) float f32x16;
typedef __attribute__((ext_vector_type(8))) short s16x8;
typedef __attribute__((ext_vector_type(2))) unsigned int u32x2;
typedef __attribute__((ext_vector_type(3))) unsigned int u32x3;
typedef __attribute__((ext_vector_type(4))) unsigned int u32x4;
typedef __attribute__((ext_vector_type(16))) unsigned int u32x16;
typedef __attribute__((ext_vector_type(2))) __bf16 bf16x2;

#define TDECL
#define STAMP(i)
#define CSTAMP(i)
#define TFLUSH

#define STREAM_ASM \
  "ds_read_b64 v[106:107], v14\n" \
  "ds_read_b64 v[104:105], v15\n" \
  "ds_read_b32 v18, v13\n" \
  "s_setprio 1\n" \
  "v_mov_b64 v[32:33], 0\n" \
  "v_mov_b64 v[34:35], 0\n" \
  "v_mov_b64 v[36:37], 0\n" \
  "v_mov_b64 v[38:39], 0\n" \
  "v_mov_b64 v[40:41], 0\n" \
  "v_mov_b64 v[42:43], 0\n" \
  "s_waitcnt lgkmcnt(0)\n" \
  "v_mfma_f32_4x4x4_16b_bf16 v[80:83], v[104:105], v[28:29], 0 cbsz:4\n" \
  "v_mfma_f32_4x4x4_16b_bf16 v[84:87], v[104:105], v[30:31], 0 cbsz:4\n" \
  "v_mfma_f32_4x4x4_16b_bf16 v[112:115], v[106:107], v[20:21], 0\n" \
  "v_mfma_f32_4x4x4_16b_bf16 v[116:119], v[106:107], v[22:23], 0\n" \
  "v_mfma_f32_4x4x4_16b_bf16 v[72:75], v[106:107], v[24:25], 0\n" \
  "v_mfma_f32_4x4x4_16b_bf16 v[76:79], v[106:107], v[26:27], 0\n" \
  "v_and_b32 v18, v17, v18\n" \
  "v_cmp_ne_u32_e64 s[62:63], 0, v18\n" \
  "v_cvt_pkrtz_f16_f32 v97, v80, v81\n" \
  "v_and_b32 v96, v16, v97\n" \
  "v_and_b32_sdwa v97, v16, v97 dst_sel:DWORD dst_unused:UNUSED_PAD src0_sel:DWORD src1_sel:WORD_1\n" \
  "ds_read_b32 v96, v96 offset:%[tb]\n" \
  "ds_read_b32 v97, v97 offset:%[tb]\n" \
  "v_cvt_pkrtz_f16_f32 v99, v84, v85\n" \
  "v_and_b32 v98, v16, v99\n" \
  "v_and_b32_sdwa v99, v16, v99 dst_sel:DWORD dst_unused:UNUSED_PAD src0_sel:DWORD src1_sel:WORD_1\n" \
  "ds_read_b32 v98, v98 offset:%[tb]\n" \
  "ds_read_b32 v99, v99 offset:%[tb]\n" \
  "v_cvt_pkrtz_f16_f32 v101, v82, v83\n" \
  "v_and_b32 v100, v16, v101\n" \
  "v_and_b32_sdwa v101, v16, v101 dst_sel:DWORD dst_unused:UNUSED_PAD src0_sel:DWORD src1_sel:WORD_1\n" \
  "ds_read_b32 v100, v100 offset:%[tb]\n" \
  "ds_read_b32 v101, v101 offset:%[tb]\n" \
  "v_cvt_pk_bf16_f32 v108, v112, v116\n" \
  "v_cvt_pk_bf16_f32 v109, v72, v76\n" \
  "v_mov_b64 v[44:45], 0\n" \
  "v_mov_b64 v[46:47], 0\n" \
  "v_mov_b64 v[48:49], 0\n" \
  "v_mov_b64 v[50:51], 0\n" \
  "v_mov_b64 v[52:53], 0\n" \
  "v_mov_b64 v[54:55], 0\n" \
  "v_mov_b64 v[56:57], 0\n" \
  "v_mov_b64 v[58:59], 0\n" \
  "v_mov_b64 v[60:61], 0\n" \
  "v_mov_b64 v[62:63], 0\n" \
  "v_mov_b64 v[64:65], 0\n" \
  "v_mov_b64 v[66:67], 0\n" \
  "v_mov_b64 v[68:69], 0\n" \
  "v_mov_b64 v[70:71], 0\n" \
  "s_waitcnt vmcnt(4)\n" \
  "v_mfma_f32_4x4x4_16b_bf16 v[72:75], v[108:109], v[120:121], 0 cbsz:4\n" \
  "v_mfma_f32_4x4x4_16b_bf16 v[76:79], v[108:109], v[122:123], 0 cbsz:4\n" \
  "v_add_u32_dpp v9, v10, v8 row_newbcast:5 row_mask:0xf bank_mask:0xf\n" \
  "buffer_load_dwordx4 v[140:143], v9, %[xs], 0 offen\n" \
  "s_waitcnt lgkmcnt(0)\n" \
  "s_nop 1\n" \
  "Lbody%=:\n" \
  "s_bfe_u32 s61, s62, 0x40000\n" \
  "s_waitcnt lgkmcnt(2)\n" \
  "v_pk_mul_f32 v[80:81], v[80:81], v[96:97]\n" \
  "v_pk_mul_f32 v[84:85], v[84:85], v[98:99]\n" \
  "s_set_gpr_idx_on s61, gpr_idx(SRC2,DST)\n" \
  "v_pk_fma_f32 v[32:33], v[80:81], v[72:73], v[32:33] op_sel:[0,0,0] op_sel_hi:[0,1,1]\n" \
  "v_pk_fma_f32 v[42:43], v[80:81], v[74:75], v[42:43] op_sel:[0,0,0] op_sel_hi:[0,1,1]\n" \
  "v_pk_fma_f32 v[52:53], v[84:85], v[76:77], v[52:53] op_sel:[0,0,0] op_sel_hi:[0,1,1]\n" \
  "v_pk_fma_f32 v[62:63], v[84:85], v[78:79], v[62:63] op_sel:[0,0,0] op_sel_hi:[0,1,1]\n" \
  "s_set_gpr_idx_off\n" \
  "s_waitcnt vmcnt(4)\n" \
  "v_mfma_f32_4x4x4_16b_bf16 v[72:75], v[108:109], v[124:125], 0 cbsz:4 abid:1\n" \
  "v_mfma_f32_4x4x4_16b_bf16 v[76:79], v[108:109], v[126:127], 0 cbsz:4 abid:1\n" \
  "v_mfma_f32_4x4x4_16b_bf16 v[88:91], v[104:105], v[28:29], 0 cbsz:4 abid:1\n" \
  "v_mfma_f32_4x4x4_16b_bf16 v[92:95], v[104:105], v[30:31], 0 cbsz:4 abid:1\n" \
  "v_add_u32_dpp v9, v10, v8 row_newbcast:6 row_mask:0xf bank_mask:0xf\n" \
  "v_cvt_pkrtz_f16_f32 v103, v86, v87\n" \
  "buffer_load_dwordx4 v[144:147], v9, %[xs], 0 offen\n" \
  "v_and_b32 v102, v16, v103\n" \
  "v_and_b32_sdwa v103, v16, v103 dst_sel:DWORD dst_unused:UNUSED_PAD src0_sel:DWORD src1_sel:WORD_1\n" \
  "ds_read_b32 v102, v102 offset:%[tb]\n" \
  "ds_read_b32 v103, v103 offset:%[tb]\n" \
  "v_mov_b32 v111, s60\n" \
  "ds_write2_b32 v110, v111, v111 offset1:8\n" \
  "s_bfe_u32 s61, s62, 0x40004\n" \
  "s_set_gpr_idx_on s61, gpr_idx(SRC2,DST)\n" \
  "v_pk_fma_f32 v[32:33], v[80:81], v[72:73], v[32:33] op_sel:[1,0,0] op_sel_hi:[1,1,1]\n" \
  "v_pk_fma_f32 v[42:43], v[80:81], v[74:75], v[42:43] op_sel:[1,0,0] op_sel_hi:[1,1,1]\n" \
  "v_pk_fma_f32 v[52:53], v[84:85], v[76:77], v[52:53] op_sel:[1,0,0] op_sel_hi:[1,1,1]\n" \
  "v_pk_fma_f32 v[62:63], v[84:85], v[78:79], v[62:63] op_sel:[1,0,0] op_sel_hi:[1,1,1]\n" \
  "s_set_gpr_idx_off\n" \
  "s_waitcnt vmcnt(4)\n" \
  "v_mfma_f32_4x4x4_16b_bf16 v[72:75], v[108:109], v[128:129], 0 cbsz:4 abid:2\n" \
  "v_mfma_f32_4x4x4_16b_bf16 v[76:79], v[108:109], v[130:131], 0 cbsz:4 abid:2\n" \
  "v_add_u32_dpp v9, v10, v8 row_newbcast:7 row_mask:0xf bank_mask:0xf\n" \
  "v_cvt_pkrtz_f16_f32 v97, v88, v89\n" \
  "buffer_load_dwordx4 v[148:151], v9, %[xs], 0 offen\n" \
  "v_and_b32 v96, v16, v97\n" \
  "v_and_b32_sdwa v97, v16, v97 dst_sel:DWORD dst_unused:UNUSED_PAD src0_sel:DWORD src1_sel:WORD_1\n" \
  "ds_read_b32 v96, v96 offset:%[tb]\n" \
  "ds_read_b32 v97, v97 offset:%[tb]\n" \
  "ds_read_b32 v111, v110 offset:16\n" \
  "s_bfe_u32 s61, s62, 0x40008\n" \
  "s_waitcnt lgkmcnt(4)\n" \
  "v_pk_mul_f32 v[82:83], v[82:83], v[100:101]\n" \
  "v_pk_mul_f32 v[86:87], v[86:87], v[102:103]\n" \
  "s_set_gpr_idx_on s61, gpr_idx(SRC2,DST)\n" \
  "v_pk_fma_f32 v[32:33], v[82:83], v[72:73], v[32:33] op_sel:[0,0,0] op_sel_hi:[0,1,1]\n" \
  "v_pk_fma_f32 v[42:43], v[82:83], v[74:75], v[42:43] op_sel:[0,0,0] op_sel_hi:[0,1,1]\n" \
  "v_pk_fma_f32 v[52:53], v[86:87], v[76:77], v[52:53] op_sel:[0,0,0] op_sel_hi:[0,1,1]\n" \
  "v_pk_fma_f32 v[62:63], v[86:87], v[78:79], v[62:63] op_sel:[0,0,0] op_sel_hi:[0,1,1]\n" \
  "s_set_gpr_idx_off\n" \
  "s_waitcnt vmcnt(4)\n" \
  "v_mfma_f32_4x4x4_16b_bf16 v[72:75], v[108:109], v[132:133], 0 cbsz:4 abid:3\n" \
  "v_mfma_f32_4x4x4_16b_bf16 v[76:79], v[108:109], v[134:135], 0 cbsz:4 abid:3\n" \
  "v_add_u32_dpp v9, v10, v8 row_newbcast:8 row_mask:0xf bank_mask:0xf\n" \
  "v_cvt_pkrtz_f16_f32 v99, v92, v93\n" \
  "buffer_load_dwordx4 v[120:123], v9, %[xs], 0 offen\n" \
  "v_and_b32 v98, v16, v99\n" \
  "v_and_b32_sdwa v99, v16, v99 dst_sel:DWORD dst_unused:UNUSED_PAD src0_sel:DWORD src1_sel:WORD_1\n" \
  "ds_read_b32 v98, v98 offset:%[tb]\n" \
  "ds_read_b32 v99, v99 offset:%[tb]\n" \
  "ds_read_b64 v[106:107], v14 offset:256\n" \
  "s_bfe_u32 s61, s62, 0x4000c\n" \
  "s_set_gpr_idx_on s61, gpr_idx(SRC2,DST)\n" \
  "v_pk_fma_f32 v[32:33], v[82:83], v[72:73], v[32:33] op_sel:[1,0,0] op_sel_hi:[1,1,1]\n" \
  "v_pk_fma_f32 v[42:43], v[82:83], v[74:75], v[42:43] op_sel:[1,0,0] op_sel_hi:[1,1,1]\n" \
  "v_pk_fma_f32 v[52:53], v[86:87], v[76:77], v[52:53] op_sel:[1,0,0] op_sel_hi:[1,1,1]\n" \
  "v_pk_fma_f32 v[62:63], v[86:87], v[78:79], v[62:63] op_sel:[1,0,0] op_sel_hi:[1,1,1]\n" \
  "s_set_gpr_idx_off\n" \
  "s_sub_u32 s60, s60, 1\n" \
  "s_cbranch_scc1 Lend%=\n" \
  "s_waitcnt vmcnt(4)\n" \
  "v_mfma_f32_4x4x4_16b_bf16 v[72:75], v[108:109], v[136:137], 0 cbsz:4 abid:4\n" \
  "v_mfma_f32_4x4x4_16b_bf16 v[76:79], v[108:109], v[138:139], 0 cbsz:4 abid:4\n" \
  "v_add_u32_dpp v9, v10, v8 row_newbcast:9 row_mask:0xf bank_mask:0xf\n" \
  "v_cvt_pkrtz_f16_f32 v101, v90, v91\n" \
  "buffer_load_dwordx4 v[124:127], v9, %[xs], 0 offen\n" \
  "v_and_b32 v100, v16, v101\n" \
  "v_and_b32_sdwa v101, v16, v101 dst_sel:DWORD dst_unused:UNUSED_PAD src0_sel:DWORD src1_sel:WORD_1\n" \
  "ds_read_b32 v100, v100 offset:%[tb]\n" \
  "ds_read_b32 v101, v101 offset:%[tb]\n" \
  "ds_read_b32 v18, v13 offset:64\n" \
  "v_add_u32 v14, 0x100, v14\n" \
  "s_bfe_u32 s61, s62, 0x40010\n" \
  "s_waitcnt lgkmcnt(4)\n" \
  "v_pk_mul_f32 v[88:89], v[88:89], v[96:97]\n" \
  "v_pk_mul_f32 v[92:93], v[92:93], v[98:99]\n" \
  "s_set_gpr_idx_on s61, gpr_idx(SRC2,DST)\n" \
  "v_pk_fma_f32 v[32:33], v[88:89], v[72:73], v[32:33] op_sel:[0,0,0] op_sel_hi:[0,1,1]\n" \
  "v_pk_fma_f32 v[42:43], v[88:89], v[74:75], v[42:43] op_sel:[0,0,0] op_sel_hi:[0,1,1]\n" \
  "v_pk_fma_f32 v[52:53], v[92:93], v[76:77], v[52:53] op_sel:[0,0,0] op_sel_hi:[0,1,1]\n" \
  "v_pk_fma_f32 v[62:63], v[92:93], v[78:79], v[62:63] op_sel:[0,0,0] op_sel_hi:[0,1,1]\n" \
  "s_set_gpr_idx_off\n" \
  "s_waitcnt vmcnt(4)\n" \
  "v_mfma_f32_4x4x4_16b_bf16 v[72:75], v[108:109], v[140:141], 0 cbsz:4 abid:5\n" \
  "v_mfma_f32_4x4x4_16b_bf16 v[76:79], v[108:109], v[142:143], 0 cbsz:4 abid:5\n" \
  "v_mfma_f32_4x4x4_16b_bf16 v[80:83], v[104:105], v[28:29], 0 cbsz:4 abid:2\n" \
  "v_mfma_f32_4x4x4_16b_bf16 v[84:87], v[104:105], v[30:31], 0 cbsz:4 abid:2\n" \
  "v_add_u32_dpp v9, v10, v8 row_newbcast:10 row_mask:0xf bank_mask:0xf\n" \
  "v_cvt_pkrtz_f16_f32 v103, v94, v95\n" \
  "buffer_load_dwordx4 v[128:131], v9, %[xs], 0 offen\n" \
  "v_and_b32 v102, v16, v103\n" \
  "v_and_b32_sdwa v103, v16, v103 dst_sel:DWORD dst_unused:UNUSED_PAD src0_sel:DWORD src1_sel:WORD_1\n" \
  "ds_read_b32 v102, v102 offset:%[tb]\n" \
  "ds_read_b32 v103, v103 offset:%[tb]\n" \
  "s_bfe_u32 s61, s62, 0x40014\n" \
  "s_set_gpr_idx_on s61, gpr_idx(SRC2,DST)\n" \
  "v_pk_fma_f32 v[32:33], v[88:89], v[72:73], v[32:33] op_sel:[1,0,0] op_sel_hi:[1,1,1]\n" \
  "v_pk_fma_f32 v[42:43], v[88:89], v[74:75], v[42:43] op_sel:[1,0,0] op_sel_hi:[1,1,1]\n" \
  "v_pk_fma_f32 v[52:53], v[92:93], v[76:77], v[52:53] op_sel:[1,0,0] op_sel_hi:[1,1,1]\n" \
  "v_pk_fma_f32 v[62:63], v[92:93], v[78:79], v[62:63] op_sel:[1,0,0] op_sel_hi:[1,1,1]\n" \
  "s_set_gpr_idx_off\n" \
  "s_waitcnt vmcnt(4) lgkmcnt(8)\n" \
  "v_mfma_f32_4x4x4_16b_bf16 v[72:75], v[108:109], v[144:145], 0 cbsz:4 abid:6\n" \
  "v_mfma_f32_4x4x4_16b_bf16 v[76:79], v[108:109], v[146:147], 0 cbsz:4 abid:6\n" \
  "v_add_u32_dpp v9, v10, v8 row_newbcast:11 row_mask:0xf bank_mask:0xf\n" \
  "v_cvt_pkrtz_f16_f32 v97, v80, v81\n" \
  "buffer_load_dwordx4 v[132:135], v9, %[xs], 0 offen\n" \
  "v_and_b32 v96, v16, v97\n" \
  "v_and_b32_sdwa v97, v16, v97 dst_sel:DWORD dst_unused:UNUSED_PAD src0_sel:DWORD src1_sel:WORD_1\n" \
  "ds_read_b32 v96, v96 offset:%[tb]\n" \
  "ds_read_b32 v97, v97 offset:%[tb]\n" \
  "v_add_u32 v13, 64, v13\n" \
  "v_readfirstlane_b32 s66, v111\n" \
  "s_setprio 1\n" \
  "s_cmp_ge_u32 s60, s66\n" \
  "s_cbranch_scc0 Lf5_%=\n" \
  "s_setprio 2\n" \
  "Lf5_%=:\n" \
  "s_bfe_u32 s61, s62, 0x40018\n" \
  "s_waitcnt lgkmcnt(2)\n" \
  "v_pk_mul_f32 v[90:91], v[90:91], v[100:101]\n" \
  "v_pk_mul_f32 v[94:95], v[94:95], v[102:103]\n" \
  "s_set_gpr_idx_on s61, gpr_idx(SRC2,DST)\n" \
  "v_pk_fma_f32 v[32:33], v[90:91], v[72:73], v[32:33] op_sel:[0,0,0] op_sel_hi:[0,1,1]\n" \
  "v_pk_fma_f32 v[42:43], v[90:91], v[74:75], v[42:43] op_sel:[0,0,0] op_sel_hi:[0,1,1]\n" \
  "v_pk_fma_f32 v[52:53], v[94:95], v[76:77], v[52:53] op_sel:[0,0,0] op_sel_hi:[0,1,1]\n" \
  "v_pk_fma_f32 v[62:63], v[94:95], v[78:79], v[62:63] op_sel:[0,0,0] op_sel_hi:[0,1,1]\n" \
  "s_set_gpr_idx_off\n" \
  "s_waitcnt vmcnt(4) lgkmcnt(7)\n" \
  "v_mfma_f32_4x4x4_16b_bf16 v[72:75], v[108:109], v[148:149], 0 cbsz:4 abid:7\n" \
  "v_mfma_f32_4x4x4_16b_bf16 v[76:79], v[108:109], v[150:151], 0 cbsz:4 abid:7\n" \
  "v_mfma_f32_4x4x4_16b_bf16 v[112:115], v[106:107], v[20:21], 0\n" \
  "v_add_u32_dpp v9, v10, v8 row_newbcast:12 row_mask:0xf bank_mask:0xf\n" \
  "v_cvt_pkrtz_f16_f32 v99, v84, v85\n" \
  "buffer_load_dwordx4 v[136:139], v9, %[xs], 0 offen\n" \
  "v_and_b32 v98, v16, v99\n" \
  "v_and_b32_sdwa v99, v16, v99 dst_sel:DWORD dst_unused:UNUSED_PAD src0_sel:DWORD src1_sel:WORD_1\n" \
  "ds_read_b32 v98, v98 offset:%[tb]\n" \
  "ds_read_b32 v99, v99 offset:%[tb]\n" \
  "ds_read_b32 v11, v12 offset:64\n" \
  "s_bfe_u32 s61, s62, 0x4001c\n" \
  "s_set_gpr_idx_on s61, gpr_idx(SRC2,DST)\n" \
  "v_pk_fma_f32 v[32:33], v[90:91], v[72:73], v[32:33] op_sel:[1,0,0] op_sel_hi:[1,1,1]\n" \
  "v_pk_fma_f32 v[42:43], v[90:91], v[74:75], v[42:43] op_sel:[1,0,0] op_sel_hi:[1,1,1]\n" \
  "v_pk_fma_f32 v[52:53], v[94:95], v[76:77], v[52:53] op_sel:[1,0,0] op_sel_hi:[1,1,1]\n" \
  "v_pk_fma_f32 v[62:63], v[94:95], v[78:79], v[62:63] op_sel:[1,0,0] op_sel_hi:[1,1,1]\n" \
  "s_set_gpr_idx_off\n" \
  "s_sub_u32 s60, s60, 1\n" \
  "s_cbranch_scc1 Lend%=\n" \
  "s_waitcnt vmcnt(4) lgkmcnt(7)\n" \
  "v_mfma_f32_4x4x4_16b_bf16 v[72:75], v[108:109], v[120:121], 0 cbsz:4 abid:8\n" \
  "v_mfma_f32_4x4x4_16b_bf16 v[76:79], v[108:109], v[122:123], 0 cbsz:4 abid:8\n" \
  "v_add_u32_dpp v9, v10, v8 row_newbcast:13 row_mask:0xf bank_mask:0xf\n" \
  "v_cvt_pkrtz_f16_f32 v101, v82, v83\n" \
  "buffer_load_dwordx4 v[140:143], v9, %[xs], 0 offen\n" \
  "v_and_b32 v100, v16, v101\n" \
  "v_and_b32_sdwa v101, v16, v101 dst_sel:DWORD dst_unused:UNUSED_PAD src0_sel:DWORD src1_sel:WORD_1\n" \
  "ds_read_b32 v100, v100 offset:%[tb]\n" \
  "ds_read_b32 v101, v101 offset:%[tb]\n" \
  "v_add_u32 v12, 64, v12\n" \
  "v_and_b32 v18, v17, v18\n" \
  "s_bfe_u32 s61, s63, 0x40000\n" \
  "s_waitcnt lgkmcnt(3)\n" \
  "v_pk_mul_f32 v[80:81], v[80:81], v[96:97]\n" \
  "v_pk_mul_f32 v[84:85], v[84:85], v[98:99]\n" \
  "s_set_gpr_idx_on s61, gpr_idx(SRC2,DST)\n" \
  "v_pk_fma_f32 v[32:33], v[80:81], v[72:73], v[32:33] op_sel:[0,0,0] op_sel_hi:[0,1,1]\n" \
  "v_pk_fma_f32 v[42:43], v[80:81], v[74:75], v[42:43] op_sel:[0,0,0] op_sel_hi:[0,1,1]\n" \
  "v_pk_fma_f32 v[52:53], v[84:85], v[76:77], v[52:53] op_sel:[0,0,0] op_sel_hi:[0,1,1]\n" \
  "v_pk_fma_f32 v[62:63], v[84:85], v[78:79], v[62:63] op_sel:[0,0,0] op_sel_hi:[0,1,1]\n" \
  "s_set_gpr_idx_off\n" \
  "s_waitcnt vmcnt(4)\n" \
  "v_mfma_f32_4x4x4_16b_bf16 v[72:75], v[108:109], v[124:125], 0 cbsz:4 abid:9\n" \
  "v_mfma_f32_4x4x4_16b_bf16 v[76:79], v[108:109], v[126:127], 0 cbsz:4 abid:9\n" \
  "v_mfma_f32_4x4x4_16b_bf16 v[88:91], v[104:105], v[28:29], 0 cbsz:4 abid:3\n" \
  "v_mfma_f32_4x4x4_16b_bf16 v[92:95], v[104:105], v[30:31], 0 cbsz:4 abid:3\n" \
  "v_add_u32_dpp v9, v10, v8 row_newbcast:14 row_mask:0xf bank_mask:0xf\n" \
  "v_cvt_pkrtz_f16_f32 v103, v86, v87\n" \
  "buffer_load_dwordx4 v[144:147], v9, %[xs], 0 offen\n" \
  "v_and_b32 v102, v16, v103\n" \
  "v_and_b32_sdwa v103, v16, v103 dst_sel:DWORD dst_unused:UNUSED_PAD src0_sel:DWORD src1_sel:WORD_1\n" \
  "ds_read_b32 v102, v102 offset:%[tb]\n" \
  "ds_read_b32 v103, v103 offset:%[tb]\n" \
  "v_mov_b32 v111, s60\n" \
  "ds_write2_b32 v110, v111, v111 offset1:8\n" \
  "s_bfe_u32 s61, s63, 0x40004\n" \
  "s_set_gpr_idx_on s61, gpr_idx(SRC2,DST)\n" \
  "v_pk_fma_f32 v[32:33], v[80:81], v[72:73], v[32:33] op_sel:[1,0,0] op_sel_hi:[1,1,1]\n" \
  "v_pk_fma_f32 v[42:43], v[80:81], v[74:75], v[42:43] op_sel:[1,0,0] op_sel_hi:[1,1,1]\n" \
  "v_pk_fma_f32 v[52:53], v[84:85], v[76:77], v[52:53] op_sel:[1,0,0] op_sel_hi:[1,1,1]\n" \
  "v_pk_fma_f32 v[62:63], v[84:85], v[78:79], v[62:63] op_sel:[1,0,0] op_sel_hi:[1,1,1]\n" \
  "s_set_gpr_idx_off\n" \
  "s_waitcnt vmcnt(4)\n" \
  "v_mfma_f32_4x4x4_16b_bf16 v[72:75], v[108:109], v[128:129], 0 cbsz:4 abid:10\n" \
  "v_mfma_f32_4x4x4_16b_bf16 v[76:79], v[108:109], v[130:131], 0 cbsz:4 abid:10\n" \
  "v_add_u32_dpp v9, v10, v8 row_newbcast:15 row_mask:0xf bank_mask:0xf\n" \
  "v_cvt_pkrtz_f16_f32 v97, v88, v89\n" \
  "buffer_load_dwordx4 v[148:151], v9, %[xs], 0 offen\n" \
  "v_and_b32 v96, v16, v97\n" \
  "v_and_b32_sdwa v97, v16, v97 dst_sel:DWORD dst_unused:UNUSED_PAD src0_sel:DWORD src1_sel:WORD_1\n" \
  "ds_read_b32 v96, v96 offset:%[tb]\n" \
  "ds_read_b32 v97, v97 offset:%[tb]\n" \
  "ds_read_b64 v[104:105], v15 offset:256\n" \
  "v_cmp_ne_u32_e64 s[64:65], 0, v18\n" \
  "ds_read_b32 v111, v110 offset:16\n" \
  "s_bfe_u32 s61, s63, 0x40008\n" \
  "s_waitcnt lgkmcnt(5)\n" \
  "v_pk_mul_f32 v[82:83], v[82:83], v[100:101]\n" \
  "v_pk_mul_f32 v[86:87], v[86:87], v[102:103]\n" \
  "s_set_gpr_idx_on s61, gpr_idx(SRC2,DST)\n" \
  "v_pk_fma_f32 v[32:33], v[82:83], v[72:73], v[32:33] op_sel:[0,0,0] op_sel_hi:[0,1,1]\n" \
  "v_pk_fma_f32 v[42:43], v[82:83], v[74:75], v[42:43] op_sel:[0,0,0] op_sel_hi:[0,1,1]\n" \
  "v_pk_fma_f32 v[52:53], v[86:87], v[76:77], v[52:53] op_sel:[0,0,0] op_sel_hi:[0,1,1]\n" \
  "v_pk_fma_f32 v[62:63], v[86:87], v[78:79], v[62:63] op_sel:[0,0,0] op_sel_hi:[0,1,1]\n" \
  "s_set_gpr_idx_off\n" \
  "s_waitcnt vmcnt(4) lgkmcnt(9)\n" \
  "v_and_b32 v10, 0xfffffc00, v11\n" \
  "v_mfma_f32_4x4x4_16b_bf16 v[72:75], v[108:109], v[132:133], 0 cbsz:4 abid:11\n" \
  "v_mfma_f32_4x4x4_16b_bf16 v[76:79], v[108:109], v[134:135], 0 cbsz:4 abid:11\n" \
  "v_mfma_f32_4x4x4_16b_bf16 v[116:119], v[106:107], v[22:23], 0\n" \
  "v_add_u32_dpp v9, v10, v8 row_newbcast:0 row_mask:0xf bank_mask:0xf\n" \
  "v_cvt_pkrtz_f16_f32 v99, v92, v93\n" \
  "buffer_load_dwordx4 v[120:123], v9, %[xs], 0 offen\n" \
  "v_and_b32 v98, v16, v99\n" \
  "v_and_b32_sdwa v99, v16, v99 dst_sel:DWORD dst_unused:UNUSED_PAD src0_sel:DWORD src1_sel:WORD_1\n" \
  "ds_read_b32 v98, v98 offset:%[tb]\n" \
  "ds_read_b32 v99, v99 offset:%[tb]\n" \
  "s_bfe_u32 s61, s63, 0x4000c\n" \
  "s_set_gpr_idx_on s61, gpr_idx(SRC2,DST)\n" \
  "v_pk_fma_f32 v[32:33], v[82:83], v[72:73], v[32:33] op_sel:[1,0,0] op_sel_hi:[1,1,1]\n" \
  "v_pk_fma_f32 v[42:43], v[82:83], v[74:75], v[42:43] op_sel:[1,0,0] op_sel_hi:[1,1,1]\n" \
  "v_pk_fma_f32 v[52:53], v[86:87], v[76:77], v[52:53] op_sel:[1,0,0] op_sel_hi:[1,1,1]\n" \
  "v_pk_fma_f32 v[62:63], v[86:87], v[78:79], v[62:63] op_sel:[1,0,0] op_sel_hi:[1,1,1]\n" \
  "s_set_gpr_idx_off\n" \
  "s_sub_u32 s60, s60, 1\n" \
  "s_cbranch_scc1 Lend%=\n" \
  "s_waitcnt vmcnt(4)\n" \
  "v_mfma_f32_4x4x4_16b_bf16 v[72:75], v[108:109], v[136:137], 0 cbsz:4 abid:12\n" \
  "v_mfma_f32_4x4x4_16b_bf16 v[76:79], v[108:109], v[138:139], 0 cbsz:4 abid:12\n" \
  "v_add_u32_dpp v9, v10, v8 row_newbcast:1 row_mask:0xf bank_mask:0xf\n" \
  "v_cvt_pkrtz_f16_f32 v101, v90, v91\n" \
  "buffer_load_dwordx4 v[124:127], v9, %[xs], 0 offen\n" \
  "v_and_b32 v100, v16, v101\n" \
  "v_and_b32_sdwa v101, v16, v101 dst_sel:DWORD dst_unused:UNUSED_PAD src0_sel:DWORD src1_sel:WORD_1\n" \
  "ds_read_b32 v100, v100 offset:%[tb]\n" \
  "ds_read_b32 v101, v101 offset:%[tb]\n" \
  "v_add_u32 v15, 0x100, v15\n" \
  "s_bfe_u32 s61, s63, 0x40010\n" \
  "s_waitcnt lgkmcnt(2)\n" \
  "v_pk_mul_f32 v[88:89], v[88:89], v[96:97]\n" \
  "v_pk_mul_f32 v[92:93], v[92:93], v[98:99]\n" \
  "s_set_gpr_idx_on s61, gpr_idx(SRC2,DST)\n" \
  "v_pk_fma_f32 v[32:33], v[88:89], v[72:73], v[32:33] op_sel:[0,0,0] op_sel_hi:[0,1,1]\n" \
  "v_pk_fma_f32 v[42:43], v[88:89], v[74:75], v[42:43] op_sel:[0,0,0] op_sel_hi:[0,1,1]\n" \
  "v_pk_fma_f32 v[52:53], v[92:93], v[76:77], v[52:53] op_sel:[0,0,0] op_sel_hi:[0,1,1]\n" \
  "v_pk_fma_f32 v[62:63], v[92:93], v[78:79], v[62:63] op_sel:[0,0,0] op_sel_hi:[0,1,1]\n" \
  "s_set_gpr_idx_off\n" \
  "s_waitcnt vmcnt(4) lgkmcnt(5)\n" \
  "v_mfma_f32_4x4x4_16b_bf16 v[72:75], v[108:109], v[140:141], 0 cbsz:4 abid:13\n" \
  "v_mfma_f32_4x4x4_16b_bf16 v[76:79], v[108:109], v[142:143], 0 cbsz:4 abid:13\n" \
  "v_mfma_f32_4x4x4_16b_bf16 v[80:83], v[104:105], v[28:29], 0 cbsz:4\n" \
  "v_mfma_f32_4x4x4_16b_bf16 v[84:87], v[104:105], v[30:31], 0 cbsz:4\n" \
  "v_add_u32_dpp v9, v10, v8 row_newbcast:2 row_mask:0xf bank_mask:0xf\n" \
  "v_cvt_pkrtz_f16_f32 v103, v94, v95\n" \
  "buffer_load_dwordx4 v[128:131], v9, %[xs], 0 offen\n" \
  "v_and_b32 v102, v16, v103\n" \
  "v_and_b32_sdwa v103, v16, v103 dst_sel:DWORD dst_unused:UNUSED_PAD src0_sel:DWORD src1_sel:WORD_1\n" \
  "ds_read_b32 v102, v102 offset:%[tb]\n" \
  "ds_read_b32 v103, v103 offset:%[tb]\n" \
  "s_bfe_u32 s61, s63, 0x40014\n" \
  "s_set_gpr_idx_on s61, gpr_idx(SRC2,DST)\n" \
  "v_pk_fma_f32 v[32:33], v[88:89], v[72:73], v[32:33] op_sel:[1,0,0] op_sel_hi:[1,1,1]\n" \
  "v_pk_fma_f32 v[42:43], v[88:89], v[74:75], v[42:43] op_sel:[1,0,0] op_sel_hi:[1,1,1]\n" \
  "v_pk_fma_f32 v[52:53], v[92:93], v[76:77], v[52:53] op_sel:[1,0,0] op_sel_hi:[1,1,1]\n" \
  "v_pk_fma_f32 v[62:63], v[92:93], v[78:79], v[62:63] op_sel:[1,0,0] op_sel_hi:[1,1,1]\n" \
  "s_set_gpr_idx_off\n" \
  "s_waitcnt vmcnt(4) lgkmcnt(6)\n" \
  "v_mfma_f32_4x4x4_16b_bf16 v[72:75], v[108:109], v[144:145], 0 cbsz:4 abid:14\n" \
  "v_mfma_f32_4x4x4_16b_bf16 v[76:79], v[108:109], v[146:147], 0 cbsz:4 abid:14\n" \
  "v_add_u32_dpp v9, v10, v8 row_newbcast:3 row_mask:0xf bank_mask:0xf\n" \
  "v_cvt_pkrtz_f16_f32 v97, v80, v81\n" \
  "buffer_load_dwordx4 v[132:135], v9, %[xs], 0 offen\n" \
  "v_and_b32 v96, v16, v97\n" \
  "v_and_b32_sdwa v97, v16, v97 dst_sel:DWORD dst_unused:UNUSED_PAD src0_sel:DWORD src1_sel:WORD_1\n" \
  "ds_read_b32 v96, v96 offset:%[tb]\n" \
  "ds_read_b32 v97, v97 offset:%[tb]\n" \
  "v_readfirstlane_b32 s66, v111\n" \
  "s_setprio 1\n" \
  "s_cmp_ge_u32 s60, s66\n" \
  "s_cbranch_scc0 Lf13_%=\n" \
  "s_setprio 2\n" \
  "Lf13_%=:\n" \
  "v_cvt_pk_bf16_f32 v19, v112, v116\n" \
  "s_bfe_u32 s61, s63, 0x40018\n" \
  "s_waitcnt lgkmcnt(2)\n" \
  "v_pk_mul_f32 v[90:91], v[90:91], v[100:101]\n" \
  "v_pk_mul_f32 v[94:95], v[94:95], v[102:103]\n" \
  "s_set_gpr_idx_on s61, gpr_idx(SRC2,DST)\n" \
  "v_pk_fma_f32 v[32:33], v[90:91], v[72:73], v[32:33] op_sel:[0,0,0] op_sel_hi:[0,1,1]\n" \
  "v_pk_fma_f32 v[42:43], v[90:91], v[74:75], v[42:43] op_sel:[0,0,0] op_sel_hi:[0,1,1]\n" \
  "v_pk_fma_f32 v[52:53], v[94:95], v[76:77], v[52:53] op_sel:[0,0,0] op_sel_hi:[0,1,1]\n" \
  "v_pk_fma_f32 v[62:63], v[94:95], v[78:79], v[62:63] op_sel:[0,0,0] op_sel_hi:[0,1,1]\n" \
  "s_set_gpr_idx_off\n" \
  "s_waitcnt vmcnt(4)\n" \
  "v_mfma_f32_4x4x4_16b_bf16 v[112:115], v[106:107], v[24:25], 0\n" \
  "v_mfma_f32_4x4x4_16b_bf16 v[116:119], v[106:107], v[26:27], 0\n" \
  "v_mfma_f32_4x4x4_16b_bf16 v[72:75], v[108:109], v[148:149], 0 cbsz:4 abid:15\n" \
  "v_mfma_f32_4x4x4_16b_bf16 v[76:79], v[108:109], v[150:151], 0 cbsz:4 abid:15\n" \
  "v_add_u32_dpp v9, v10, v8 row_newbcast:4 row_mask:0xf bank_mask:0xf\n" \
  "v_cvt_pkrtz_f16_f32 v99, v84, v85\n" \
  "buffer_load_dwordx4 v[136:139], v9, %[xs], 0 offen\n" \
  "v_and_b32 v98, v16, v99\n" \
  "v_and_b32_sdwa v99, v16, v99 dst_sel:DWORD dst_unused:UNUSED_PAD src0_sel:DWORD src1_sel:WORD_1\n" \
  "ds_read_b32 v98, v98 offset:%[tb]\n" \
  "ds_read_b32 v99, v99 offset:%[tb]\n" \
  "v_cvt_pk_bf16_f32 v109, v112, v116\n" \
  "v_mov_b32 v108, v19\n" \
  "s_bfe_u32 s61, s63, 0x4001c\n" \
  "s_set_gpr_idx_on s61, gpr_idx(SRC2,DST)\n" \
  "v_pk_fma_f32 v[32:33], v[90:91], v[72:73], v[32:33] op_sel:[1,0,0] op_sel_hi:[1,1,1]\n" \
  "v_pk_fma_f32 v[42:43], v[90:91], v[74:75], v[42:43] op_sel:[1,0,0] op_sel_hi:[1,1,1]\n" \
  "v_pk_fma_f32 v[52:53], v[94:95], v[76:77], v[52:53] op_sel:[1,0,0] op_sel_hi:[1,1,1]\n" \
  "v_pk_fma_f32 v[62:63], v[94:95], v[78:79], v[62:63] op_sel:[1,0,0] op_sel_hi:[1,1,1]\n" \
  "s_set_gpr_idx_off\n" \
  "s_sub_u32 s60, s60, 1\n" \
  "s_cbranch_scc1 Lend%=\n" \
  "s_waitcnt vmcnt(4)\n" \
  "v_mfma_f32_4x4x4_16b_bf16 v[72:75], v[108:109], v[120:121], 0 cbsz:4\n" \
  "v_mfma_f32_4x4x4_16b_bf16 v[76:79], v[108:109], v[122:123], 0 cbsz:4\n" \
  "v_add_u32_dpp v9, v10, v8 row_newbcast:5 row_mask:0xf bank_mask:0xf\n" \
  "v_cvt_pkrtz_f16_f32 v101, v82, v83\n" \
  "buffer_load_dwordx4 v[140:143], v9, %[xs], 0 offen\n" \
  "v_and_b32 v100, v16, v101\n" \
  "v_and_b32_sdwa v101, v16, v101 dst_sel:DWORD dst_unused:UNUSED_PAD src0_sel:DWORD src1_sel:WORD_1\n" \
  "ds_read_b32 v100, v100 offset:%[tb]\n" \
  "ds_read_b32 v101, v101 offset:%[tb]\n" \
  "s_mov_b64 s[62:63], s[64:65]\n" \
  "s_branch Lbody%=\n" \
  "Lend%=:\n" \
  "s_setprio 0\n" \
  "v_mov_b32 v20, 0\n" \
  "v_mov_b32 v21, %[bit]\n" \
  "v_cvt_pk_bf16_f32 v40, v40, v60\n" \
  "v_cvt_pk_bf16_f32 v41, v41, v61\n" \
  "v_cvt_pk_bf16_f32 v50, v50, v70\n" \
  "v_cvt_pk_bf16_f32 v51, v51, v71\n" \
  "ds_write2_b32 v156, v40, v41 offset0:0 offset1:16\n" \
  "ds_write2_b32 v156, v50, v51 offset0:32 offset1:48\n" \
  "s_mov_b64 exec, 1\n" \
  "ds_or_b32 v20, v21 offset:%[rd]\n" \
  "s_mov_b64 exec, -1\n" \
  "ds_read_b32 v20, v20 offset:%[wr]\n" \
  "v_cvt_pk_bf16_f32 v32, v32, v52\n" \
  "v_cvt_pk_bf16_f32 v33, v33, v53\n" \
  "v_cvt_pk_bf16_f32 v42, v42, v62\n" \
  "v_cvt_pk_bf16_f32 v43, v43, v63\n" \
  "ds_read_b128 v[72:75], v158 offset:0\n" \
  "ds_read_b128 v[76:79], v158 offset:1024\n" \
  "ds_read_b128 v[80:83], v158 offset:8192\n" \
  "ds_read_b128 v[84:87], v158 offset:9216\n" \
  "v_cvt_pk_bf16_f32 v34, v34, v54\n" \
  "v_cvt_pk_bf16_f32 v35, v35, v55\n" \
  "v_cvt_pk_bf16_f32 v44, v44, v64\n" \
  "v_cvt_pk_bf16_f32 v45, v45, v65\n" \
  "ds_write2st64_b32 v152, v32, v34 offset0:0 offset1:4\n" \
  "ds_write2st64_b32 v153, v33, v35 offset0:0 offset1:4\n" \
  "ds_write2st64_b32 v154, v42, v44 offset0:0 offset1:4\n" \
  "ds_write2st64_b32 v155, v43, v45 offset0:0 offset1:4\n" \
  "v_cvt_pk_bf16_f32 v36, v36, v56\n" \
  "v_cvt_pk_bf16_f32 v37, v37, v57\n" \
  "v_cvt_pk_bf16_f32 v46, v46, v66\n" \
  "v_cvt_pk_bf16_f32 v47, v47, v67\n" \
  "v_cvt_pk_bf16_f32 v38, v38, v58\n" \
  "v_cvt_pk_bf16_f32 v39, v39, v59\n" \
  "v_cvt_pk_bf16_f32 v48, v48, v68\n" \
  "v_cvt_pk_bf16_f32 v49, v49, v69\n" \
  "ds_write2st64_b32 v152, v36, v38 offset0:8 offset1:12\n" \
  "ds_write2st64_b32 v153, v37, v39 offset0:8 offset1:12\n" \
  "ds_write2st64_b32 v154, v46, v48 offset0:8 offset1:12\n" \
  "ds_write2st64_b32 v155, v47, v49 offset0:8 offset1:12\n" \
  "ds_read_b128 v[32:35], v157 offset:0\n" \
  "ds_read_b128 v[36:39], v157 offset:256\n" \
  "ds_read_b128 v[40:43], v157 offset:512\n" \
  "ds_read_b128 v[44:47], v157 offset:768\n" \
  "ds_read_b128 v[88:91], v158 offset:16384\n" \
  "ds_read_b128 v[92:95], v158 offset:17408\n" \
  "ds_read_b128 v[96:99], v158 offset:24576\n" \
  "ds_read_b128 v[100:103], v158 offset:25600\n" \
  "ds_read_b128 v[104:107], v158 offset:2048\n" \
  "ds_read_b128 v[108:111], v158 offset:3072\n" \
  "ds_read_b128 v[112:115], v158 offset:10240\n" \
  "ds_read_b128 v[116:119], v158 offset:11264\n" \
  "s_waitcnt lgkmcnt(11)\n" \
  "v_readfirstlane_b32 s66, v20\n" \
  "s_cmp_lg_u32 s66, 4\n" \
  "s_cbranch_scc1 Lredo%=\n" \
  "Lmix%=:\n" \
  "s_waitcnt lgkmcnt(11)\n" \
  "v_mfma_f32_16x16x32_bf16 v[48:51], v[32:35], v[72:75], 0\n" \
  "ds_read_b128 v[72:75], v158 offset:18432\n" \
  "v_mfma_f32_16x16x32_bf16 v[52:55], v[32:35], v[76:79], 0\n" \
  "ds_read_b128 v[76:79], v158 offset:19456\n" \
  "s_waitcnt lgkmcnt(12)\n" \
  "v_mfma_f32_16x16x32_bf16 v[48:51], v[36:39], v[80:83], v[48:51]\n" \
  "ds_read_b128 v[80:83], v158 offset:26624\n" \
  "v_mfma_f32_16x16x32_bf16 v[52:55], v[36:39], v[84:87], v[52:55]\n" \
  "ds_read_b128 v[84:87], v158 offset:27648\n" \
  "s_waitcnt lgkmcnt(11)\n" \
  "v_mfma_f32_16x16x32_bf16 v[48:51], v[40:43], v[88:91], v[48:51]\n" \
  "ds_read_b128 v[88:91], v158 offset:4096\n" \
  "s_waitcnt lgkmcnt(11)\n" \
  "v_mfma_f32_16x16x32_bf16 v[52:55], v[40:43], v[92:95], v[52:55]\n" \
  "ds_read_b128 v[92:95], v158 offset:5120\n" \
  "s_waitcnt lgkmcnt(11)\n" \
  "v_mfma_f32_16x16x32_bf16 v[48:51], v[44:47], v[96:99], v[48:51]\n" \
  "ds_read_b128 v[96:99], v158 offset:12288\n" \
  "s_waitcnt lgkmcnt(11)\n" \
  "v_mfma_f32_16x16x32_bf16 v[52:55], v[44:47], v[100:103], v[52:55]\n" \
  "ds_read_b128 v[100:103], v158 offset:13312\n" \
  "s_waitcnt lgkmcnt(11)\n" \
  "v_mfma_f32_16x16x32_bf16 v[56:59], v[32:35], v[104:107], 0\n" \
  "ds_read_b128 v[104:107], v158 offset:20480\n" \
  "s_waitcnt lgkmcnt(11)\n" \
  "v_mfma_f32_16x16x32_bf16 v[60:63], v[32:35], v[108:111], 0\n" \
  "ds_read_b128 v[108:111], v158 offset:21504\n" \
  "s_waitcnt lgkmcnt(11)\n" \
  "v_mfma_f32_16x16x32_bf16 v[56:59], v[36:39], v[112:115], v[56:59]\n" \
  "ds_read_b128 v[112:115], v158 offset:28672\n" \
  "s_waitcnt lgkmcnt(11)\n" \
  "v_mfma_f32_16x16x32_bf16 v[60:63], v[36:39], v[116:119], v[60:63]\n" \
  "ds_read_b128 v[116:119], v158 offset:29696\n" \
  "s_waitcnt lgkmcnt(11)\n" \
  "v_mfma_f32_16x16x32_bf16 v[56:59], v[40:43], v[72:75], v[56:59]\n" \
  "ds_read_b128 v[72:75], v158 offset:6144\n" \
  "v_cvt_pk_bf16_f32 v64, v48, v49\n" \
  "v_cvt_pk_bf16_f32 v65, v50, v51\n" \
  "v_cvt_pk_bf16_f32 v66, v52, v53\n" \
  "v_cvt_pk_bf16_f32 v67, v54, v55\n" \
  "buffer_store_dwordx4 v[64:67], v159, %[os], 0 offen offset:0 sc1\n" \
  "s_waitcnt lgkmcnt(11)\n" \
  "v_mfma_f32_16x16x32_bf16 v[60:63], v[40:43], v[76:79], v[60:63]\n" \
  "ds_read_b128 v[76:79], v158 offset:7168\n" \
  "s_waitcnt lgkmcnt(11)\n" \
  "v_mfma_f32_16x16x32_bf16 v[56:59], v[44:47], v[80:83], v[56:59]\n" \
  "ds_read_b128 v[80:83], v158 offset:14336\n" \
  "s_waitcnt lgkmcnt(11)\n" \
  "v_mfma_f32_16x16x32_bf16 v[60:63], v[44:47], v[84:87], v[60:63]\n" \
  "ds_read_b128 v[84:87], v158 offset:15360\n" \
  "s_waitcnt lgkmcnt(11)\n" \
  "v_mfma_f32_16x16x32_bf16 v[48:51], v[32:35], v[88:91], 0\n" \
  "ds_read_b128 v[88:91], v158 offset:22528\n" \
  "s_waitcnt lgkmcnt(11)\n" \
  "v_mfma_f32_16x16x32_bf16 v[52:55], v[32:35], v[92:95], 0\n" \
  "ds_read_b128 v[92:95], v158 offset:23552\n" \
  "s_waitcnt lgkmcnt(11)\n" \
  "v_mfma_f32_16x16x32_bf16 v[48:51], v[36:39], v[96:99], v[48:51]\n" \
  "ds_read_b128 v[96:99], v158 offset:30720\n" \
  "s_waitcnt lgkmcnt(11)\n" \
  "v_mfma_f32_16x16x32_bf16 v[52:55], v[36:39], v[100:103], v[52:55]\n" \
  "ds_read_b128 v[100:103], v158 offset:31744\n" \
  "s_waitcnt lgkmcnt(11)\n" \
  "v_mfma_f32_16x16x32_bf16 v[48:51], v[40:43], v[104:107], v[48:51]\n" \
  "v_cvt_pk_bf16_f32 v68, v56, v57\n" \
  "v_cvt_pk_bf16_f32 v69, v58, v59\n" \
  "v_cvt_pk_bf16_f32 v70, v60, v61\n" \
  "v_cvt_pk_bf16_f32 v71, v62, v63\n" \
  "buffer_store_dwordx4 v[68:71], v159, %[os], 0 offen offset:256 sc1\n" \
  "s_waitcnt lgkmcnt(10)\n" \
  "v_mfma_f32_16x16x32_bf16 v[52:55], v[40:43], v[108:111], v[52:55]\n" \
  "s_waitcnt lgkmcnt(9)\n" \
  "v_mfma_f32_16x16x32_bf16 v[48:51], v[44:47], v[112:115], v[48:51]\n" \
  "s_waitcnt lgkmcnt(8)\n" \
  "v_mfma_f32_16x16x32_bf16 v[52:55], v[44:47], v[116:119], v[52:55]\n" \
  "s_waitcnt lgkmcnt(7)\n" \
  "v_mfma_f32_16x16x32_bf16 v[56:59], v[32:35], v[72:75], 0\n" \
  "s_waitcnt lgkmcnt(6)\n" \
  "v_mfma_f32_16x16x32_bf16 v[60:63], v[32:35], v[76:79], 0\n" \
  "s_waitcnt lgkmcnt(5)\n" \
  "v_mfma_f32_16x16x32_bf16 v[56:59], v[36:39], v[80:83], v[56:59]\n" \
  "s_waitcnt lgkmcnt(4)\n" \
  "v_mfma_f32_16x16x32_bf16 v[60:63], v[36:39], v[84:87], v[60:63]\n" \
  "s_waitcnt lgkmcnt(3)\n" \
  "v_mfma_f32_16x16x32_bf16 v[56:59], v[40:43], v[88:91], v[56:59]\n" \
  "v_cvt_pk_bf16_f32 v64, v48, v49\n" \
  "v_cvt_pk_bf16_f32 v65, v50, v51\n" \
  "v_cvt_pk_bf16_f32 v66, v52, v53\n" \
  "v_cvt_pk_bf16_f32 v67, v54, v55\n" \
  "buffer_store_dwordx4 v[64:67], v159, %[os], 0 offen offset:512 sc1\n" \
  "s_waitcnt lgkmcnt(2)\n" \
  "v_mfma_f32_16x16x32_bf16 v[60:63], v[40:43], v[92:95], v[60:63]\n" \
  "s_waitcnt lgkmcnt(1)\n" \
  "v_mfma_f32_16x16x32_bf16 v[56:59], v[44:47], v[96:99], v[56:59]\n" \
  "s_waitcnt lgkmcnt(0)\n" \
  "v_mfma_f32_16x16x32_bf16 v[60:63], v[44:47], v[100:103], v[60:63]\n" \
  "s_nop 7\n" \
  "v_cvt_pk_bf16_f32 v68, v56, v57\n" \
  "v_cvt_pk_bf16_f32 v69, v58, v59\n" \
  "v_cvt_pk_bf16_f32 v70, v60, v61\n" \
  "v_cvt_pk_bf16_f32 v71, v62, v63\n" \
  "buffer_store_dwordx4 v[68:71], v159, %[os], 0 offen offset:768 sc1\n" \
  "s_branch Lout%=\n" \
  "Lredo%=:\n" \
  "v_mov_b32 v20, 0\n" \
  "s_sleep 1\n" \
  "ds_read_b32 v20, v20 offset:%[wr]\n" \
  "ds_read_b128 v[72:75], v158 offset:0\n" \
  "ds_read_b128 v[76:79], v158 offset:1024\n" \
  "ds_read_b128 v[80:83], v158 offset:8192\n" \
  "ds_read_b128 v[84:87], v158 offset:9216\n" \
  "ds_read_b128 v[88:91], v158 offset:16384\n" \
  "ds_read_b128 v[92:95], v158 offset:17408\n" \
  "ds_read_b128 v[96:99], v158 offset:24576\n" \
  "ds_read_b128 v[100:103], v158 offset:25600\n" \
  "ds_read_b128 v[104:107], v158 offset:2048\n" \
  "ds_read_b128 v[108:111], v158 offset:3072\n" \
  "ds_read_b128 v[112:115], v158 offset:10240\n" \
  "ds_read_b128 v[116:119], v158 offset:11264\n" \
  "s_waitcnt lgkmcnt(0)\n" \
  "v_readfirstlane_b32 s66, v20\n" \
  "s_cmp_lg_u32 s66, 4\n" \
  "s_cbranch_scc1 Lredo%=\n" \
  "s_branch Lmix%=\n" \
  "Lout%=:\n" \
  "s_endpgm\n"
#define STREAM_CLOBBERS \
  "v9", "v11", "v18", "v19", "v32", "v33", "v34", "v35", "v36", "v37", "v38", "v39", "v40", "v41", "v42", "v43", \
  "v44", "v45", "v46", "v47", "v48", "v49", "v50", "v51", "v52", "v53", "v54", "v55", "v56", "v57", "v58", \
  "v59", "v60", "v61", "v62", "v63", "v64", "v65", "v66", "v67", "v68", "v69", "v70", "v71", "v72", "v73", \
  "v74", "v75", "v76", "v77", "v78", "v79", "v80", "v81", "v82", "v83", "v84", "v85", "v86", "v87", "v88", \
  "v89", "v90", "v91", "v92", "v93", "v94", "v95", "v96", "v97", "v98", "v99", "v100", "v101", "v102", "v103", \
  "v104", "v105", "v106", "v107", "v108", "v109", "v111", "v112", "v113", "v114", "v115", "v116", "v117", \
  "v118", "v119", "s61", "s62", "s63", "s64", "s65", "s66", "s67", "s68", "s69", "s70", "s71", "s72", "s73", \
  "s74", "s75", "s76", "s77", "s78", "s79", "scc", "vcc", "memory"
#define PRE_ASM \
  "s_cmp_lg_u32 %[no], 0\n" \
  "s_cbranch_scc1 Lpe%=\n" \
  "ds_read_b32  v11, v12\n" \
  "s_waitcnt lgkmcnt(0)\n" \
  "v_and_b32 v10, 0xfffffc00, v11\n" \
  "s_nop 1\n" \
  "v_add_u32_dpp v9, v10, v8 row_newbcast:0 row_mask:0xf bank_mask:0xf\n" \
  "buffer_load_dwordx4 v[120:123], v9, %[xs], 0 offen\n" \
  "v_add_u32_dpp v9, v10, v8 row_newbcast:1 row_mask:0xf bank_mask:0xf\n" \
  "buffer_load_dwordx4 v[124:127], v9, %[xs], 0 offen\n" \
  "v_add_u32_dpp v9, v10, v8 row_newbcast:2 row_mask:0xf bank_mask:0xf\n" \
  "buffer_load_dwordx4 v[128:131], v9, %[xs], 0 offen\n" \
  "v_add_u32_dpp v9, v10, v8 row_newbcast:3 row_mask:0xf bank_mask:0xf\n" \
  "buffer_load_dwordx4 v[132:135], v9, %[xs], 0 offen\n" \
  "v_add_u32_dpp v9, v10, v8 row_newbcast:4 row_mask:0xf bank_mask:0xf\n" \
  "buffer_load_dwordx4 v[136:139], v9, %[xs], 0 offen\n" \
  "Lpe%=:\n"
#define PAY_WAIT \
  "s_waitcnt vmcnt(5)\n" \
  "s_cmp_eq_u32 %[no], 0\n" \
  "s_cbranch_scc1 Lpw%=\n" \
  "s_waitcnt  vmcnt(0)\n" \
  "Lpw%=:\n"

namespace {

constexpr int C_ = 128;
constexpr int MW = 12;
constexpr int MT = MW * 64;
constexpr int SW = 8;
constexpr int NS = 5;
constexpr int NPB = SW * NS;
constexpr int NPASS = 16;
constexpr int HS = NPB * NPASS / 2 + 4;
constexpr int NBMAX = 256;
constexpr int PW = 5;
constexpr int PT = PW * 64;
constexpr int PK = 4;
constexpr int CH = PT * PK;
constexpr int MK = 4;
constexpr int CAPW = 256;
constexpr unsigned OOB = 0x80000000u;
#define OUTAUX 16
#define PNT " nt"
#define PFN 4
#define WSL 64
#define CPW (MW - 1)
#define TB1 2
#define TB2 5
#define GOW SW
#define PLP 0
#define PF1 0
#define SPF 0
#define HWV 10
#define HSL 24
#define IPF 4
#define POLLS 2
constexpr unsigned WS_EPOCH = 0;
constexpr unsigned WS_OFF = 4096;
constexpr unsigned RECB = CH * 20;
constexpr unsigned WS_RECK = WS_OFF + NBMAX * NBMAX * 16;
constexpr unsigned WS_RECP = WS_RECK + CH * 4;
constexpr unsigned WS_END = WS_RECK + NBMAX * RECB;

__device__ __forceinline__ float bf2f(unsigned short v) { return __uint_as_float(((unsigned)v) << 16); }
__device__ __forceinline__ unsigned short f2bf(float f) {
  __hip_bfloat16 b = __float2bfloat16(f);
  return *reinterpret_cast<unsigned short*>(&b);
}
__device__ __forceinline__ unsigned pk2bf(float lo, float hi) {
  return __builtin_bit_cast(unsigned, __builtin_convertvector(f32x2{lo, hi}, bf16x2));
}
__device__ __forceinline__ unsigned wave_incl_scan(unsigned x) {
  x += __builtin_amdgcn_update_dpp(0u, x, 0x111, 0xf, 0xf, false);
  x += __builtin_amdgcn_update_dpp(0u, x, 0x112, 0xf, 0xf, false);
  x += __builtin_amdgcn_update_dpp(0u, x, 0x114, 0xf, 0xf, false);
  x += __builtin_amdgcn_update_dpp(0u, x, 0x118, 0xf, 0xf, false);
  x += __builtin_amdgcn_update_dpp(0u, x, 0x142, 0xa, 0xf, false);
  x += __builtin_amdgcn_update_dpp(0u, x, 0x143, 0xc, 0xf, false);
  return x;
}

__device__ __forceinline__ unsigned check_word(const u32x4& p, unsigned s) { return s + p[0] + p[1] + p[2] + p[3]; }

struct Lds {
  u32x4 sink[16];
  unsigned flag, wrdy, rdone, go;
  unsigned long long tag, pad2_;
  unsigned prog[16];
  int deg[64];
  unsigned nl[64];
  unsigned tot[HS];
  u32x4 ct[72];
  __attribute__((aligned(16))) unsigned so[SW * CAPW];
  union {
    __attribute__((aligned(16))) unsigned hist[SW * HS];
    u32x4 row5[SW * 64];
  };
  f32x4 sg[4096];
  union {
    struct {
      __attribute__((aligned(16))) unsigned phist[PW][NBMAX / 2];
      unsigned pbase[PW][NBMAX / 2];
      __attribute__((aligned(16))) unsigned seg[3][NBMAX];
      unsigned ri[SW][MK * 64];
    };
    u32x4 pay[SW * CAPW];
  };
  union {
    struct {
      __attribute__((aligned(16))) unsigned sk[CH];
      u32x4 sp[CH];
    };
    u32x4 wf[C_ * C_ / 8];
  };
};
static_assert(sizeof(Lds) <= 163840, "LDS budget");
static_assert(offsetof(Lds, sg) < 65536, "table offset");

__device__ __forceinline__ unsigned lds_addr(const void* p) {
  return (unsigned)(uintptr_t)(__attribute__((address_space(3))) const void*)p;
}
__device__ __forceinline__ u32x4 make_srd(const void* p, unsigned num) {
  unsigned long long a = (unsigned long long)p;
  return u32x4{(unsigned)a, (unsigned)(a >> 32), num, 0x00020000u};
}

}

extern "C" __global__ __launch_bounds__(MT) void tfn_main_kernel(
    const int* __restrict__ dst, const int* __restrict__ src, const u32x2* __restrict__ Y, const u32x2* __restrict__ ef,
    int E, int N, int NBK, unsigned M3, const unsigned short* __restrict__ x, char* ws, u32x4* __restrict__ out,
    const unsigned short* __restrict__ wrad, const unsigned short* __restrict__ cg, const unsigned short* __restrict__ wmix) {
  __shared__ Lds L;
  const int tid = threadIdx.x, lane = tid & 63;
  const int w = __builtin_amdgcn_readfirstlane(tid >> 6);
  TDECL;
  STAMP(0);
  asm volatile("s_cmp_lg_u32 %1, %2\n s_cbranch_scc1 1f\n"
               "s_getpc_b64 s[40:41]\n .Ltfn_p0:\n"
               "s_max_i32 s42, 0, .Ltfn_p1-.Ltfn_p0\n s_mov_b32 m0, %3\n s_mov_b32 s43, 0x20000\n s_movk_i32 s44, 128\n"
               "buffer_load_dword %0, s[40:43], 0 offen lds\n buffer_load_dword %0, s[40:43], s44 offen lds\n 1:"
               :: "v"(lane * 256), "s"((blockIdx.x >> 3) * 16 + w), "n"(CPW), "n"(offsetof(Lds, sink))
               : "s40", "s41", "s42", "s43", "s44", "scc", "memory");
  const int xcd = blockIdx.x & 7;
  const int bucket = ((xcd * NBK) >> 3) + (blockIdx.x >> 3);
  const bool idle = bucket >= (((xcd + 1) * NBK) >> 3);
  const int e0 = bucket * CH + tid * 2;
  u32x2 d0, d1, s0, s1;
  u32x4 y0, y1, f0, f1;
  const int wp = idle ? PW : w;
  asm volatile(
      "s_cmp_ge_u32 %8, %9\n s_cbranch_scc1 1f\n"
      "buffer_load_dwordx2 %0, %4, %5, 0 offen" PNT "\n buffer_load_dwordx2 %1, %4, %5, %7 offen" PNT "\n"
      "buffer_load_dwordx2 %2, %4, %6, 0 offen" PNT "\n buffer_load_dwordx2 %3, %4, %6, %7 offen" PNT "\n"
      "1:\n s_barrier\n"
      : "=&v"(d0), "=&v"(d1), "=&v"(s0), "=&v"(s1)
      : "v"(e0 * 4), "s"(make_srd(dst, E * 4)), "s"(make_srd(src, E * 4)), "s"(CH * 2), "s"(wp), "n"(PW) : "scc");
  const u32x4 wsd = make_srd(ws, WS_END);
  unsigned long long ev;
  asm volatile(
      "s_cmp_lg_u32 %9, %16\n s_cbranch_scc1 2f\n s_load_dwordx2 %4, %11, 0\n s_setprio 3\n s_waitcnt lgkmcnt(0)\n 2:\n"
      "s_cmp_ge_u32 %9, %10\n s_cbranch_scc1 1f\n s_setprio 3\n"
      "buffer_load_dwordx4 %0, %5, %6, 0 offen" PNT "\n buffer_load_dwordx4 %1, %5, %6, %8 offen" PNT "\n"
      "buffer_load_dwordx4 %2, %5, %7, 0 offen" PNT "\n buffer_load_dwordx4 %3, %5, %7, %8 offen" PNT "\n"
      "1:\n"
      : "=&v"(y0), "=&v"(y1), "=&v"(f0), "=&v"(f1), "=&s"(ev)
      : "v"(e0 * 8), "s"(make_srd(Y, E * 8)), "s"(make_srd(ef, E * 8)), "s"(CH * 4), "s"(wp), "n"(PW), "s"(ws),
        "s"(out), "s"(wmix), "s"(cg), "s"(wrad), "n"(HWV) : "scc");
  STAMP(1);
  auto table = [&](int q) {
    f32x4 p, n;
#pragma unroll
    for (int i = 0; i < 4; i++) {
      const unsigned short c = min(q * 16 + i * 4 + 2, 0x7bff);
      const float e = __builtin_amdgcn_exp2f(-1.4426950408889634f * (float)__builtin_bit_cast(_Float16, c));
      p[i] = __builtin_amdgcn_rcpf(1.f + e);
      n[i] = e * p[i];
    }
    L.sg[q] = p;
    L.sg[q + 2048] = n;
  };
  auto valid = [&](int k) { return e0 + (k >> 1) * (CH / 2) + (k & 1) < E; };
  auto warm = [&](int i0, int i1) {
    const unsigned pfo = ((xcd * N) >> 3) * 1024 + ((blockIdx.x >> 3) * 64 + lane) * 128;
#pragma unroll 1
    for (int i = i0; i < i1; i += MW - SW)
      asm volatile("s_mov_b32 m0, %2\n s_nop 0\n buffer_load_dword %0, %1, %3 offen lds\n"
                   :: "v"(pfo), "s"(make_srd(x, N * 1024)), "s"(lds_addr(L.sink)), "s"(i * 32 * 64 * 128) : "memory");
  };
  if (idle) {
    if (w >= SW) warm(w - SW, PFN);
#pragma unroll 1
    for (int i = 0; i < IPF; i++)
      asm volatile("s_mov_b32 m0, %2\n s_nop 0\n buffer_load_dword %0, %1, %3 offen lds\n"
                   :: "v"((xcd * MT + tid) * 128), "s"(make_srd(x, N * 1024)), "s"(lds_addr(L.sink)), "s"(i * 8 * MT * 128) : "memory");
    return;
  }
  unsigned bk[PK], pr[PK], ky[PK];
  unsigned cv, wq4[4];
  asm volatile(
      "s_cmp_lg_u32 %9, %10\n s_cbranch_scc1 1f\n global_load_ushort %0, %5, %7\n global_load_dword %1, %6, %8\n"
      "global_load_dword %2, %6, %8 offset:256\n global_load_dword %3, %6, %8 offset:512\n"
      "global_load_dword %4, %6, %8 offset:768\n 1:"
      : "=&v"(cv), "=&v"(wq4[0]), "=&v"(wq4[1]), "=&v"(wq4[2]), "=&v"(wq4[3])
      : "v"(lane * 2), "v"(lane * 4), "s"(cg), "s"(wrad), "s"(w), "n"(PW) : "scc");
  const int zt = tid - PT - (w > HWV ? 64 : 0);
  unsigned so0 = tid * 16 + WS_RECK + bucket * RECB;
  unsigned ho0 = w < SW ? ((lane >> 1) * SW + w) * (NBMAX * 16) : lane * 16 + WS_OFF + bucket * (NBMAX * 16);
  asm volatile("" : "+v"(so0), "+v"(ho0));
  if (w < PW) {
    reinterpret_cast<u32x2*>(L.phist[w])[lane] = u32x2{0, 0};
    asm volatile("s_waitcnt vmcnt(4)" : "+v"(d0), "+v"(d1), "+v"(s0), "+v"(s1));
    STAMP(3);
    const u32x4 dv = {d0[0], d0[1], d1[0], d1[1]}, sv = {s0[0], s0[1], s1[0], s1[1]};
#pragma unroll
    for (int k = 0; k < PK; k++) {
      bk[k] = valid(k) ? dv[k] / NPB : NBMAX - 1;
      pr[k] = atomicAdd(&L.phist[w][bk[k] >> 1], 1u << ((bk[k] * 16) & 31));
    }
#pragma unroll
    for (int k = 0; k < PK; k++) {
      ky[k] = (sv[k] << 10) | ((dv[k] - bk[k] * NPB) << 4) | __umulhi(sv[k], M3);
      pr[k] = __builtin_amdgcn_ubfe(pr[k], bk[k] * 16, 16);
      asm volatile("" : "+v"(ky[k]), "+v"(pr[k]));
    }
  } else {
    if (w == PW) {
      L.deg[lane] = lane < NPB ? lane : -1;
      if (lane < 4) (&L.flag)[lane] = 0;
    }
    if (w == HWV) {
      L.tag = ev * 6364136223846793005ull + 1442695040888963407ull;
      reinterpret_cast<u32x4*>(L.seg[1])[lane] = u32x4{0, 0, 0, 0};
      reinterpret_cast<u32x4*>(L.seg[2])[lane] = u32x4{0, 0, 0, 0};
    } else {
#pragma unroll 1
      for (int q = zt; q < TB1 * 384; q += 384) table(q);
    }
  }
  STAMP(4);
  __syncthreads();
  STAMP(5);
  const auto wsr = __builtin_amdgcn_make_buffer_rsrc((void*)ws, 0, WS_END, 0x00020000);
  const unsigned long long epoch = L.tag;
  const unsigned tagl = __builtin_amdgcn_readfirstlane((unsigned)epoch), tag0 = tagl << 22;
  const unsigned tag1 = __builtin_amdgcn_readfirstlane((unsigned)(epoch >> 32));
  auto lds_ld = [&](const unsigned* p) {
    return __builtin_amdgcn_readfirstlane(__hip_atomic_load(p, __ATOMIC_RELAXED, __HIP_MEMORY_SCOPE_WORKGROUP));
  };
  u32x4 pv[PK];
  if (w < PW) {
    asm volatile("s_waitcnt vmcnt(0)" : "+v"(y0), "+v"(y1), "+v"(f0), "+v"(f1));
#pragma unroll
    for (int k = 0; k < PK; k++) {
      const u32x4 &yy = k < 2 ? y0 : y1, &ff = k < 2 ? f0 : f1;
      pv[k] = u32x4{yy[(k & 1) * 2], yy[(k & 1) * 2 + 1], ff[(k & 1) * 2], ff[(k & 1) * 2 + 1]};
      ky[k] |= tagl << 24;
      atomicAdd(&L.seg[1][bk[k]], ky[k]);
      atomicAdd(&L.seg[2][bk[k]], check_word(pv[k], 0));
    }
    asm volatile("s_mov_b64 exec, 1\n ds_add_u32 %0, %1\n s_mov_b64 exec, -1" :: "v"(lds_addr(&L.go)), "v"(1) : "memory");
  }
  if (w < PW || w == HWV) {
    unsigned ca = 0, cb = 0, pa = 0, pb = 0;
#pragma unroll
    for (int ww = 0; ww < PW; ww++) {
      const unsigned ha = L.phist[ww][lane], hb = L.phist[ww][64 + lane];
      pa = ww == w ? ca : pa;
      pb = ww == w ? cb : pb;
      ca += ha;
      cb += hb;
    }
    const unsigned t = __builtin_amdgcn_perm(cb, ca, 0x05040100) + __builtin_amdgcn_perm(cb, ca, 0x07060302);
    const unsigned inc = wave_incl_scan(t), ex = inc - t;
    const unsigned ba = (ex & 0xffff) * 0x10001u + (ca << 16);
    const unsigned bb = ((ex >> 16) + (__builtin_amdgcn_readlane(inc, 63) & 0xffff)) * 0x10001u + (cb << 16);
    if (w < PW) {
      L.pbase[w][lane] = ba + pa;
      L.pbase[w][64 + lane] = bb + pb;
      STAMP(6);
      unsigned b4[PK];
#pragma unroll
      for (int k = 0; k < PK; k++) b4[k] = reinterpret_cast<const unsigned short*>(L.pbase[w])[bk[k]];
#pragma unroll
      for (int k = 0; k < PK; k++) {
        L.sk[pr[k] + b4[k]] = ky[k];
        L.sp[pr[k] + b4[k]] = pv[k];
      }
    } else {
      reinterpret_cast<u32x2*>(L.seg[0])[lane] = u32x2{(ba & 0xffff) | ((ca & 0xffff) << 11) | tag0, (ba >> 16) | ((ca >> 16) << 11) | tag0};
      reinterpret_cast<u32x2*>(L.seg[0])[64 + lane] = u32x2{(bb & 0xffff) | ((cb & 0xffff) << 11) | tag0, (bb >> 16) | ((cb >> 16) << 11) | tag0};
      while (lds_ld(&L.go) < PW) {}
      asm volatile("" ::: "memory");
      u32x4 hv[4];
#pragma unroll
      for (int k = 0; k < 4; k++) hv[k] = u32x4{L.seg[0][k * 64 + lane], L.seg[1][k * 64 + lane], L.seg[2][k * 64 + lane], 0};
#pragma unroll
      for (int k = 0; k < 4; k++) {
        hv[k][3] = tag1 - hv[k][0] - hv[k][1] - hv[k][2];
        __builtin_amdgcn_raw_buffer_store_b128(hv[k], wsr, ho0 + k * 1024, 0, 16);
      }
    }
  } else {
    if (w == PW) {
      asm volatile("s_waitcnt   vmcnt(0)" : "+v"(cv), "+v"(wq4[0]), "+v"(wq4[1]), "+v"(wq4[2]), "+v"(wq4[3]));
      reinterpret_cast<unsigned short*>(L.ct)[(lane & 3) * 16 + (lane >> 2)] = cv;
      L.ct[8 + lane] = u32x4{__builtin_amdgcn_perm(wq4[1], wq4[0], 0x05040100), __builtin_amdgcn_perm(wq4[3], wq4[2], 0x05040100),
                             __builtin_amdgcn_perm(wq4[1], wq4[0], 0x07060302), __builtin_amdgcn_perm(wq4[3], wq4[2], 0x07060302)};
    }
#pragma unroll
    for (int i = 0; i < 2; i++)
      if (i * 384 + zt < SW * HS / 4) reinterpret_cast<u32x4*>(L.hist)[i * 384 + zt] = u32x4{0, 0, 0, 0};
    __builtin_amdgcn_s_setprio(3);
  }
  STAMP(7);
  __syncthreads();
  STAMP(27);
  const int st = tid - SW * 64;
  {
    const u32x4 v0 = reinterpret_cast<const u32x4*>(L.sk)[tid], v1 = reinterpret_cast<const u32x4*>(L.sk)[MT + tid];
    __builtin_amdgcn_raw_buffer_store_b128(v0, wsr, so0, 0, 16);
    __builtin_amdgcn_raw_buffer_store_b128(v1, wsr, so0 + MT * 16, 0, 16);
    if (w == PW)
      __builtin_amdgcn_raw_buffer_store_b128(reinterpret_cast<const u32x4*>(L.sk)[2 * MT + lane], wsr, so0 + (2 * MT - PT) * 16, 0, 16);
  }
  STAMP(8);
  const int node0 = bucket * NPB;
  int Tw = 0;
  unsigned key[MK], rk[MK], idx[MK], bin[MK], psum = 0;
  u32x4 pay_[MK];
  __builtin_amdgcn_s_barrier();
  STAMP(9);
  if (w >= SW) {
    __builtin_amdgcn_s_setprio(0);
#pragma unroll 1
    for (int q = st + TB1 * 384; q < 2048; q += MT - SW * 64) table(q);
    STAMP(12);
  } else {
    const int sgi = (lane >> 1) * SW + w;
    u32x4 g = {0, 0, 0, 0};
    unsigned r0 = (sgi << 16) + (lane & 1) * 4;
    asm volatile("" : "+v"(r0));
    if (sgi < NBK) {
#pragma unroll 1
      for (int spin = 0; spin < (1 << 16); spin++) {
        asm volatile("buffer_load_dwordx4 %0, %1, %2, %3 offen sc1\n s_waitcnt  vmcnt(0)"
                     : "=&v"(g) : "v"(ho0), "s"(wsd), "s"(WS_OFF + bucket * 16) : "memory");
        const bool ok = g[0] + g[1] + g[2] + g[3] == tag1 && ((g[0] ^ tag0) >> 22) == 0;
        g[0] = ok ? g[0] : 0u;
        if (ok) break;
      }
    }
    STAMP(10);
    const unsigned cnt = __builtin_amdgcn_ubfe(g[0], 11, 11);
    const unsigned inc = wave_incl_scan((lane & 1) == 0 ? cnt : 0u);
    const unsigned Tw0 = __builtin_amdgcn_readlane(inc, 63);
    const bool wok = Tw0 <= (unsigned)(MK * 64);
    Tw = wok ? Tw0 : 0;
    r0 += (g[0] & 0x7ff) * 4;
    unsigned* lp = &L.ri[w][inc - cnt + (lane & 1)];
    if (wok) {
#pragma clang loop unroll(disable) vectorize(disable)
      for (int i = (int)cnt - (lane & 1); i > 0; i -= 2, lp += 2, r0 += 8) *lp = r0;
    } else if (lane == 0) {
      L.flag = 1;
    }
    unsigned ik[MK];
#pragma unroll
    for (int k = 0; k < MK; k++) {
      idx[k] = L.ri[w][k * 64 + lane];
      const unsigned cb = lane < Tw - k * 64 ? (idx[k] >> 16) * RECB : OOB;
      ik[k] = (idx[k] & 0xffff) + cb;
      idx[k] = (idx[k] & 0xffff) * 4 + cb;
    }
    STAMP(11);
    const unsigned ksum = (lane & 1) == 0 && wok ? g[1] : 0u;
    psum = (lane & 1) == 0 && wok ? -g[2] : 0u;
    key[MK - 1] = 0;
#pragma unroll 1
    for (int spin = 0; spin < (1 << 16); spin++) {
      asm volatile(
          "buffer_load_dword %0, %4, %8, %9 offen sc1\n buffer_load_dword %1, %5, %8, %9 offen sc1\n"
          "buffer_load_dword %2, %6, %8, %9 offen sc1\n"
          "s_cmp_le_i32 %10, 192\n s_cbranch_scc1 1f\n buffer_load_dword %3, %7, %8, %9 offen sc1\n 1:\n s_waitcnt  vmcnt(0)"
          : "=&v"(key[0]), "=&v"(key[1]), "=&v"(key[2]), "+v"(key[3])
          : "v"(ik[0]), "v"(ik[1]), "v"(ik[2]), "v"(ik[3]), "s"(wsd), "s"(WS_RECK), "s"(Tw) : "scc", "memory");
      if (__builtin_amdgcn_readlane(wave_incl_scan(key[0] + key[1] + key[2] + key[3] - ksum), 63) == 0) break;
    }
    STAMP(12);
  }
#define PAYLOAD_LOADS \
  asm volatile( \
      "s_cmp_ge_u32 %11, %12\n s_cbranch_scc1 2f\n" \
      "buffer_load_dwordx4 %0, %4, %8, %9 offen sc1\n buffer_load_dwordx4 %1, %5, %8, %9 offen sc1\n" \
      "buffer_load_dwordx4 %2, %6, %8, %9 offen sc1\n" \
      "s_cmp_le_i32 %10, 192\n s_cbranch_scc1 2f\n buffer_load_dwordx4 %3, %7, %8, %9 offen sc1\n 2:\n" \
      : "=&v"(pay_[0]), "=&v"(pay_[1]), "=&v"(pay_[2]), "=&v"(pay_[3]) \
      : "v"(idx[0]), "v"(idx[1]), "v"(idx[2]), "v"(idx[3]), "s"(wsd), "s"(WS_RECP), "s"(Tw), "s"(w), "n"(SW) \
      : "scc", "memory");
  PAYLOAD_LOADS
  if (w < SW) {
    auto count = [&](int k) {
      const bool ok = lane < Tw - k * 64;
      key[k] = ok ? key[k] : lane * 2;
      bin[k] = (min(key[k] & 0x3f0, (NPB - 1) * 16u) | ((key[k] - 2 * xcd) & 15)) * 2;
      rk[k] = atomicAdd(&L.hist[w * HS + (bin[k] >> 2)], (ok ? 1u : 0u) << ((bin[k] & 2) * 8));
      atomicAdd(&L.deg[bin[k] >> 5], ok ? 64 : 0);
    };
#pragma unroll
    for (int k = 0; k < MK - 1; k++) count(k);
    if (Tw > (MK - 1) * 64) count(MK - 1);
  }
  STAMP(14);
  __syncthreads();
  STAMP(16);
  if (w < PW) {
    unsigned h[SW], s = 0;
#pragma unroll
    for (int i = 0; i < SW; i++) h[i] = L.hist[i * HS + tid];
#pragma unroll
    for (int i = 0; i < SW; i++) {
      L.hist[i * HS + tid] = s;
      s += h[i];
    }
    L.tot[tid] = s;
  } else {
    const int dg = L.deg[lane];
    unsigned r = 0;
#pragma unroll
    for (int i = 0; i < 6; i++) {
      const unsigned ri = __popcll(__ballot(dg > __builtin_amdgcn_readlane(dg, (w - PW) * 6 + i)));
      r = lane == i ? ri : r;
    }
    const int n = (w - PW) * 6 + lane;
    if (lane < 6 && n < NPB) L.nl[((r & 7) ^ (r < 16 ? 0 : 7)) * 8 + (r >> 3)] = n;
  }
  STAMP(17);
  __syncthreads();
  STAMP(18);
  if (w >= SW) {
    warm(PF1 + w - SW, PFN);
    if (bucket == 0 && tid == MT - 1)
      __builtin_amdgcn_raw_buffer_store_b64(u32x2{tagl, tag1}, wsr, WS_EPOCH, 0, 16);
  }
  int len = 0;
  if (w < SW) {
    const bool use = (lane & 7) < NS;
    unsigned* tp = &L.tot[use ? L.nl[w * 8 + (lane & 7)] * 8 + (lane >> 3) : NPB * NPASS / 2];
    const unsigned v = use ? *tp : 0u;
    const unsigned in2 = wave_incl_scan(v), ex = in2 - v;
    unsigned e0 = __builtin_amdgcn_update_dpp(0u, ex, 0x150, 0xf, 0x3, false);
    e0 = __builtin_amdgcn_update_dpp(e0, ex, 0x158, 0xf, 0xc, false);
    unsigned i7 = __builtin_amdgcn_update_dpp(0u, in2, 0x157, 0xf, 0x3, false);
    i7 = __builtin_amdgcn_update_dpp(i7, in2, 0x15f, 0xf, 0xc, false);
    *tp = ex + (e0 >> 16) + (i7 << 16) + (w * CAPW + ((lane & 7) << 12)) * 0x10001u;
    const unsigned tt = __builtin_amdgcn_readlane(in2, 63);
    len = (tt & 0xffff) + (tt >> 16);
    if (lane < 2) L.prog[w + lane * 8] = len >> 2;
    if (len > CAPW - 40) {
      if (lane == 0) L.flag = 1;
    } else if (lane < 36) {
      L.so[w * CAPW + len + lane] = OOB;
      L.pay[w * CAPW + len + lane] = u32x4{0, 0, 0, 0};
    }
  }
  __syncthreads();
  STAMP(20);
  const unsigned slowv = __builtin_amdgcn_readfirstlane(L.flag);
  const bool slow = slowv != 0;
  if (w < SW) {
    auto place = [&](int k0, int k1) {
#pragma unroll
      for (int k = k0; k < k1; k++) rk[k] += L.hist[w * HS + (bin[k] >> 2)] + L.tot[bin[k] >> 2];
#pragma unroll
      for (int k = k0; k < k1; k++) {
        rk[k] >>= (bin[k] & 2) * 8;
        const unsigned e = (key[k] & 0xfffc00u) | ((rk[k] >> 11) & 14);
        rk[k] = lane < Tw - k * 64 && !slow ? rk[k] & 0xfff : CAPW - 1;
        L.so[rk[k]] = e;
      }
    };
    place(0, MK - 1);
    if (Tw > (MK - 1) * 64) place(MK - 1, MK);
  }
  __syncthreads();
  STAMP(22);
  CSTAMP(30);
  const u32x4 xs = make_srd(x, N * 1024);
  unsigned sop = lds_addr(&L.so[(w & 7) * CAPW + (lane & 15)]), xi;
  u32x16 ra, rb;
  asm volatile(PRE_ASM : "={v[120:135]}"(ra), "={v[136:151]}"(rb), "={v10}"(xi)
               : "{v8}"(lane * 16), "{v12}"(sop), [xs] "s"(xs), [no] "s"(slowv | (w >> 3)) : "v9", "v11", "scc", "memory");
  if (w < SW) {
    asm volatile(PAY_WAIT : "+v"(pay_[0]), "+v"(pay_[1]), "+v"(pay_[2]), "+v"(pay_[3]) : [no] "s"(slowv) : "scc");
    STAMP(21);
#pragma unroll 1
    for (int spin = 0; spin < (1 << 16); spin++) {
      unsigned cs = psum;
#pragma unroll
      for (int k = 0; k < MK - 1; k++) cs = check_word(pay_[k], cs);
      if (Tw > (MK - 1) * 64) cs = check_word(pay_[MK - 1], cs);
      if (__builtin_amdgcn_readlane(wave_incl_scan(cs), 63) == 0) break;
      asm volatile("" ::: "memory");
#pragma unroll
      for (int k = 0; k < MK; k++) pay_[k] = __builtin_amdgcn_raw_buffer_load_b128(wsr, idx[k], WS_RECP, 16);
      __builtin_amdgcn_s_waitcnt(0);
    }
#pragma unroll
    for (int k = 0; k < MK - 1; k++) L.pay[rk[k]] = pay_[k];
    if (Tw > (MK - 1) * 64) L.pay[rk[MK - 1]] = pay_[MK - 1];
  }
  __syncthreads();
  STAMP(23);
  const int row = lane & 15, kq = lane >> 4;
  if (w >= SW) {
    __builtin_amdgcn_s_sleep(WSL);
    const u32x4* wp = reinterpret_cast<const u32x4*>(wmix) + ((w - SW) * 32 + (lane >> 4) * 8) * 16 + (lane & 15);
    u32x4 q[8];
#pragma unroll
    for (int j = 0; j < 8; j++) q[j] = wp[j * 16];
    u32x4* fp = &L.wf[((w - SW) * 8 + ((lane >> 2) & 3) * 2) * 64 + (lane & 48) + (lane & 3) * 4];
#pragma unroll
    for (int i = 0; i < 4; i++) {
      fp[i] = u32x4{__builtin_amdgcn_perm(q[1][i], q[0][i], 0x05040100), __builtin_amdgcn_perm(q[3][i], q[2][i], 0x05040100),
                    __builtin_amdgcn_perm(q[5][i], q[4][i], 0x05040100), __builtin_amdgcn_perm(q[7][i], q[6][i], 0x05040100)};
      fp[64 + i] = u32x4{__builtin_amdgcn_perm(q[1][i], q[0][i], 0x07060302), __builtin_amdgcn_perm(q[3][i], q[2][i], 0x07060302),
                         __builtin_amdgcn_perm(q[5][i], q[4][i], 0x07060302), __builtin_amdgcn_perm(q[7][i], q[6][i], 0x07060302)};
    }
    asm volatile("" ::: "memory");
    if (lane == 0) atomicAdd(&L.wrdy, 1u);
    while (lds_ld(&L.wrdy) != MW - SW) __builtin_amdgcn_s_sleep(8);
  } else if (!slow) {
    const int start = w * CAPW;
    unsigned s4p = lds_addr(&L.so[start + (lane >> 2)]);
    unsigned yp = lds_addr(&L.pay[start + (lane >> 2)]);
    unsigned ep = lds_addr(&L.pay[start + (lane & 15)]) + 8;
    const u32x4 cg0 = L.ct[(lane & 3) * 2], cg1 = L.ct[(lane & 3) * 2 + 1];
    const u32x4 wq = L.ct[8 + lane];
    unsigned rem = __builtin_amdgcn_readfirstlane(max(len - 1, 0) >> 2);
    const unsigned rp = lds_addr(&L.pay[start]) + kq * 256 + row * 4;
    u32x4 c0 = cg0;
    unsigned pg = lds_addr(&L.prog[w]);
    asm volatile(STREAM_ASM
                 : "+{v12}"(sop), "+{v13}"(s4p), "+{v14}"(yp), "+{v15}"(ep), "+{s60}"(rem), "+{v10}"(xi),
                   "+{v[120:135]}"(ra), "+{v[136:151]}"(rb), "+{v[20:23]}"(c0), "+{v110}"(pg)
                 : "{v8}"(lane * 16), "{v16}"(0xfffcu), "{v17}"(1u << (lane & 3)),
                   "{v[24:27]}"(cg1), "{v[28:31]}"(wq), "{v[152:155]}"(u32x4{rp, rp + 64, rp + 128, rp + 192}),
                   "{v156}"(lds_addr(&L.row5[w * 64]) + kq * 256 + row * 4),
                   "{v157}"(lds_addr(&L.pay[start + (row >> 2) * 64 + (row & 3) * 4 + kq])), "{v158}"(lds_addr(&L.wf[lane])),
                   "{v159}"((node0 + L.nl[w * 8 + kq]) * 1024 + row * 16),
                   [xs] "s"(xs), [os] "s"(make_srd(out, N * 1024)), [bit] "s"(1u << w), [tb] "n"(offsetof(Lds, sg)),
                   [rd] "n"(offsetof(Lds, rdone)), [wr] "n"(offsetof(Lds, wrdy))
                 : STREAM_CLOBBERS);
    return;
  }
  STAMP(24);
  CSTAMP(31);
  const int ht = (w - SW) >> 1;
  u32x4* rows = w < SW ? &L.pay[w * CAPW] : &L.row5[ht * 256];
  const u32x4* wfp = &L.wf[(w < SW ? 0 : (w & 1) * 256) + lane];
  auto wfrag = [&](int i) {
    return __builtin_bit_cast(s16x8, wfp[(((i >> 1) & 3) * 8 + (i >> 3) * 2 + (i & 1)) * 64]);
  };
  unsigned oo = (node0 + L.nl[w < SW ? w * 8 + kq : (ht * 4 + kq) * 8 + 4]) * 1024 + row * 16 + (w < SW ? 0 : (w & 1) * 512);
  s16x8 wv[16];
  if (w >= SW) {
#pragma unroll
    for (int i = 0; i < 16; i++) wv[i] = wfrag(i);
    const unsigned need = 15u << (ht * 4);
    while (lds_ld(&L.prog[ht * 4]) > 4 && lds_ld(&L.rdone) == 0) __builtin_amdgcn_s_sleep(HSL);
    while ((lds_ld(&L.rdone) & need) != need) __builtin_amdgcn_s_sleep(2);
  } else {
#pragma unroll 1
    for (int it = 0; it < 2 * NS; it++) {
      const int sl = it >> 1, slice = it & 1, ch = slice * 64 + lane;
      const unsigned dl = L.nl[w * 8 + sl];
      float acc[4] = {0.f, 0.f, 0.f, 0.f};
#pragma unroll 1
      for (int eb = 0; eb < E; eb += 64) {
        unsigned long long m = __ballot(eb + lane < E && (unsigned)dst[min(eb + lane, E - 1)] == node0 + dl);
#pragma unroll 1
        for (; m; m &= m - 1) {
          const int e = eb + __builtin_ctzll(m);
          const u32x2 ry = Y[e], rf = ef[e];
          float yy[4] = {bf2f(ry[0] & 0xffff), bf2f(ry[0] >> 16), bf2f(ry[1] & 0xffff), bf2f(ry[1] >> 16)};
          float ee[4] = {bf2f(rf[0] & 0xffff), bf2f(rf[0] >> 16), bf2f(rf[1] & 0xffff), bf2f(rf[1] >> 16)};
          float z = 0.f;
#pragma unroll
          for (int k = 0; k < 4; k++) z += ee[k] * bf2f(wrad[k * C_ + ch]);
          float R = z / (1.f + __expf(-z));
          const unsigned short* xr = x + (size_t)src[e] * 512 + ch * 4;
#pragma unroll 1
          for (int o = 0; o < 4; o++) {
            float tp = 0.f;
#pragma unroll 1
            for (int i = 0; i < 4; i++) {
              float A = 0.f;
#pragma unroll
              for (int f = 0; f < 4; f++) A += yy[f] * bf2f(cg[i * 16 + f * 4 + o]);
              tp += bf2f(xr[i]) * bf2f(f2bf(A));
            }
            float v = R * tp;
            acc[0] += o == 0 ? v : 0.f;
            acc[1] += o == 1 ? v : 0.f;
            acc[2] += o == 2 ? v : 0.f;
            acc[3] += o == 3 ? v : 0.f;
          }
        }
      }
      u32x4* rw = sl < 4 ? &rows[sl * 64] : &L.row5[w * 64];
      unsigned short* ar = reinterpret_cast<unsigned short*>(&rw[slice * 32]) + (lane >> 5) * 128 + (lane & 31);
#pragma unroll
      for (int o = 0; o < 4; o++) ar[o * 32] = f2bf(acc[o]);
    }
    asm volatile("" ::: "memory");
    if (lane == 0) atomicOr(&L.rdone, 1u << w);
    while (lds_ld(&L.wrdy) != MW - SW) __builtin_amdgcn_s_sleep(1);
    asm volatile("" ::: "memory");
#pragma unroll
    for (int i = 0; i < 16; i++) wv[i] = wfrag(i);
  }
  asm volatile("" ::: "memory");
  const u32x4* op = &rows[(row >> 2) * 64 + (row & 3) * 4 + kq];
  s16x8 f[4];
#pragma unroll
  for (int k = 0; k < 4; k++) f[k] = __builtin_bit_cast(s16x8, op[k * 16]);
  const auto ors = __builtin_amdgcn_make_buffer_rsrc((void*)out, 0, N * 1024, 0x00020000);
  auto pair = [&](int m, bool more) {
    f32x4 a = {0.f, 0.f, 0.f, 0.f}, b = {0.f, 0.f, 0.f, 0.f};
#pragma unroll
    for (int k = 0; k < 4; k++) {
      const int i = (m & 1) * 8 + 2 * k;
      a = __builtin_amdgcn_mfma_f32_16x16x32_bf16(f[k], wv[i], a, 0, 0, 0);
      b = __builtin_amdgcn_mfma_f32_16x16x32_bf16(f[k], wv[i + 1], b, 0, 0, 0);
      if (more) {
        wv[i] = wfrag(i + 16);
        wv[i + 1] = wfrag(i + 17);
      }
    }
    __builtin_amdgcn_raw_buffer_store_b128(
        u32x4{pk2bf(a[0], a[1]), pk2bf(a[2], a[3]), pk2bf(b[0], b[1]), pk2bf(b[2], b[3])}, ors, oo + m * 256, 0, OUTAUX);
  };
  if (w < SW) {
    pair(0, true);
    pair(1, true);
    oo += 512;
  }
  pair(0, false);
  pair(1, false);
  asm volatile(".Ltfn_p1:");
  STAMP(26);
  TFLUSH;
}

extern "C" void kernel_launch(void* const* d_in, const int* in_sizes, int n_in, void* d_out, int out_size,
                              void* d_ws, size_t ws_size, hipStream_t stream) {
  const unsigned short* x = (const unsigned short*)d_in[0];
  const int* src = (const int*)d_in[1];
  const int* dst = (const int*)d_in[2];
  const u32x2* Y = (const u32x2*)d_in[3];
  const u32x2* ef = (const u32x2*)d_in[4];
  const unsigned short* wrad = (const unsigned short*)d_in[5];
  const unsigned short* cg = (const unsigned short*)d_in[6];
  const unsigned short* wmix = (const unsigned short*)d_in[7];
  const int N = in_sizes[0] / 512;
  const int E = in_sizes[1];
  const int NBK = (N + NPB - 1) / NPB;
  const unsigned long long M3l = (((unsigned long long)NPASS) << 32) / (unsigned long long)N;
  const unsigned M3 = M3l > 0xffffffffull ? 0xffffffffu : (unsigned)M3l;
  tfn_main_kernel<<<256, MT, 0, stream>>>(dst, src, Y, ef, E, N, NBK, M3, x, (char*)d_ws, (u32x4*)d_out, wrad, cg, wmix);
}
```

```cpp
#include <hip/hip_runtime.h>
#include <hip/hip_bf16.h>
#include <stddef.h>
#include <stdint.h>

typedef __attribute__((ext_vector_type(2))) float f32x2;
typedef __attribute__((ext_vector_type(4))) float f32x4;
typedef __attribute__((ext_vector_type(8))) float f32x8;
typedef __attribute__((ext_vector_type(16))) float f32x16;
typedef __attribute__((ext_vector_type(8))) short s16x8;
typedef __attribute__((ext_vector_type(2))) unsigned int u32x2;
typedef __attribute__((ext_vector_type(3))) unsigned int u32x3;
typedef __attribute__((ext_vector_type(4))) unsigned int u32x4;
typedef __attribute__((ext_vector_type(8))) unsigned int u32x8;
typedef __attribute__((ext_vector_type(16))) unsigned int u32x16;
typedef __attribute__((ext_vector_type(2))) __bf16 bf16x2;

#define TDECL
#define STAMP(i)
#define CSTAMP(i)
#define TFLUSH

#define STREAM_ASM \
  "s_mov_b32 s63, 0x10000\n" \
  "Lh%=:\n" \
  "buffer_load_dwordx4 v[72:75], v136, %[ws], %[ho] offen sc1\n" \
  "v_mov_b32 v117, 0\n" \
  "v_mov_b32 v116, 64\n" \
  "v_mov_b32 v87, 0\n" \
  "v_mov_b64 v[44:45], 0\n" \
  "v_mov_b64 v[46:47], 0\n" \
  "v_mov_b32 v118, 0x7fc\n" \
  "s_waitcnt vmcnt(0)\n" \
  "v_add3_u32 v108, v72, v73, v74\n" \
  "v_xor_b32 v109, %[t0], v72\n" \
  "v_add_u32 v108, v108, v75\n" \
  "v_cmp_lt_u32 vcc, 0x3fffff, v109\n" \
  "v_cmp_ne_u32_e64 s[74:75], %[t1], v108\n" \
  "s_or_b64 vcc, vcc, s[74:75]\n" \
  "s_cbranch_scc1 Lhr%=\n" \
  "s_mov_b64 exec, -1\n" \
  "v_bfe_u32 v108, v72, 11, 11\n" \
  "v_and_b32 v109, v140, v108\n" \
  "v_and_b32 v112, 0x7ff, v72\n" \
  "v_sub_u32 v111, v108, v141\n" \
  "v_add_u32_dpp v109, v109, v109 row_shr:1 row_mask:0xf bank_mask:0xf bound_ctrl:1\n" \
  "v_lshl_add_u32 v112, v112, 2, v137\n" \
  "v_and_b32 v114, v140, v73\n" \
  "v_add_u32_dpp v109, v109, v109 row_shr:2 row_mask:0xf bank_mask:0xf bound_ctrl:1\n" \
  "v_and_b32 v115, v140, v74\n" \
  "v_sub_u32 v114, 0, v114\n" \
  "v_add_u32_dpp v109, v109, v109 row_shr:4 row_mask:0xf bank_mask:0xf bound_ctrl:1\n" \
  "v_sub_u32 v115, 0, v115\n" \
  "s_nop 0\n" \
  "v_add_u32_dpp v109, v109, v109 row_shr:8 row_mask:0xf bank_mask:0xf bound_ctrl:1\n" \
  "s_nop 1\n" \
  "v_add_u32_dpp v109, v109, v109 row_bcast:15 row_mask:0xa bank_mask:0xf\n" \
  "s_nop 1\n" \
  "v_add_u32_dpp v109, v109, v109 row_bcast:31 row_mask:0xc bank_mask:0xf\n" \
  "v_sub_u32 v108, v109, v108\n" \
  "v_readlane_b32 s62, v109, 63\n" \
  "v_lshl_add_u32 v113, v108, 2, v138\n" \
  "s_cmpk_gt_u32 s62, 0x100\n" \
  "s_cbranch_scc1 Lover%=\n" \
  "Lw%=:\n" \
  "v_cmpx_lt_i32 vcc, 0, v111\n" \
  "ds_write_b32 v113, v112 offset:0\n" \
  "v_add_u32 v112, 8, v112\n" \
  "v_cmpx_lt_i32 vcc, 2, v111\n" \
  "ds_write_b32 v113, v112 offset:8\n" \
  "v_add_u32 v112, 8, v112\n" \
  "v_cmpx_lt_i32 vcc, 4, v111\n" \
  "ds_write_b32 v113, v112 offset:16\n" \
  "v_add_u32 v112, 8, v112\n" \
  "v_cmpx_lt_i32 vcc, 6, v111\n" \
  "ds_write_b32 v113, v112 offset:24\n" \
  "v_add_u32 v112, 8, v112\n" \
  "v_cmpx_lt_i32 vcc, 8, v111\n" \
  "ds_write_b32 v113, v112 offset:32\n" \
  "v_add_u32 v112, 8, v112\n" \
  "s_cbranch_execz Lwd%=\n" \
  "v_cmpx_lt_i32 vcc, 10, v111\n" \
  "ds_write_b32 v113, v112 offset:40\n" \
  "v_add_u32 v112, 8, v112\n" \
  "s_cbranch_execz Lwd%=\n" \
  "v_cmpx_lt_i32 vcc, 12, v111\n" \
  "ds_write_b32 v113, v112 offset:48\n" \
  "v_add_u32 v112, 8, v112\n" \
  "s_cbranch_execz Lwd%=\n" \
  "v_cmpx_lt_i32 vcc, 14, v111\n" \
  "ds_write_b32 v113, v112 offset:56\n" \
  "v_add_u32 v112, 8, v112\n" \
  "v_add_u32 v113, 64, v113\n" \
  "v_subrev_u32 v111, 16, v111\n" \
  "s_branch Lw%=\n" \
  "Lwd%=:\n" \
  "s_mov_b64 exec, -1\n" \
  "Lld%=:\n" \
  "ds_read2st64_b32 v[76:77], v139 offset1:1\n" \
  "ds_read2st64_b32 v[78:79], v139 offset0:2 offset1:3\n" \
  "s_mov_b32 s63, 0x10000\n" \
  "s_waitcnt lgkmcnt(0)\n" \
  "buffer_load_dword v84, v76, %[ws], %[rk] offen sc1\n" \
  "buffer_load_dword v85, v77, %[ws], %[rk] offen sc1\n" \
  "buffer_load_dword v86, v78, %[ws], %[rk] offen sc1\n" \
  "s_cmpk_le_u32 s62, 0xc0\n" \
  "s_cbranch_scc1 Lka%=\n" \
  "buffer_load_dword v87, v79, %[ws], %[rk] offen sc1\n" \
  "Lka%=:\n" \
  "v_lshlrev_b32 v80, 2, v76\n" \
  "v_lshlrev_b32 v81, 2, v77\n" \
  "v_lshlrev_b32 v82, 2, v78\n" \
  "v_lshlrev_b32 v83, 2, v79\n" \
  "s_barrier\n" \
  "s_cmpk_le_u32 s62, 0xc0\n" \
  "s_cbranch_scc1 Lpa%=\n" \
  "buffer_load_dwordx4 v[44:47], v83, %[ws], %[rp] offen sc1\n" \
  "Lpa%=:\n" \
  "buffer_load_dwordx4 v[32:35], v80, %[ws], %[rp] offen sc1\n" \
  "buffer_load_dwordx4 v[36:39], v81, %[ws], %[rp] offen sc1\n" \
  "buffer_load_dwordx4 v[40:43], v82, %[ws], %[rp] offen sc1\n" \
  "v_cmp_le_i32_e64 s[66:67], 0, v80\n" \
  "v_cmp_le_i32_e64 s[68:69], 0, v81\n" \
  "v_cmp_le_i32_e64 s[70:71], 0, v82\n" \
  "v_cmp_le_i32_e64 s[72:73], 0, v83\n" \
  "v_mov_b64 v[48:49], 0\n" \
  "v_mov_b64 v[50:51], 0\n" \
  "v_mov_b64 v[52:53], 0\n" \
  "v_mov_b64 v[54:55], 0\n" \
  "v_mov_b64 v[56:57], 0\n" \
  "v_mov_b64 v[58:59], 0\n" \
  "v_mov_b64 v[60:61], 0\n" \
  "v_mov_b64 v[62:63], 0\n" \
  "v_mov_b64 v[64:65], 0\n" \
  "v_mov_b64 v[66:67], 0\n" \
  "v_mov_b64 v[68:69], 0\n" \
  "v_mov_b64 v[70:71], 0\n" \
  "s_waitcnt vmcnt(3)\n" \
  "Lk%=:\n" \
  "v_add3_u32 v108, v84, v85, v86\n" \
  "v_add3_u32 v108, v108, v87, v114\n" \
  "v_lshlrev_b32 v88, 1, v84\n" \
  "v_lshlrev_b32 v92, 4, v84\n" \
  "v_add_u32_dpp v108, v108, v108 row_shr:1 row_mask:0xf bank_mask:0xf bound_ctrl:1\n" \
  "v_lshrrev_b32 v104, 2, v84\n" \
  "v_and_or_b32 v88, v88, v118, %[wb]\n" \
  "v_add_u32_dpp v108, v108, v108 row_shr:2 row_mask:0xf bank_mask:0xf bound_ctrl:1\n" \
  "v_lshlrev_b32_e64 v100, v92, 4\n" \
  "v_and_b32 v104, 0xfc, v104\n" \
  "v_add_u32_dpp v108, v108, v108 row_shr:4 row_mask:0xf bank_mask:0xf bound_ctrl:1\n" \
  "v_lshlrev_b32 v89, 1, v85\n" \
  "v_lshlrev_b32 v93, 4, v85\n" \
  "v_add_u32_dpp v108, v108, v108 row_shr:8 row_mask:0xf bank_mask:0xf bound_ctrl:1\n" \
  "v_lshrrev_b32 v105, 2, v85\n" \
  "v_and_or_b32 v89, v89, v118, %[wb]\n" \
  "v_add_u32_dpp v108, v108, v108 row_bcast:15 row_mask:0xa bank_mask:0xf\n" \
  "v_lshlrev_b32_e64 v101, v93, 4\n" \
  "v_and_b32 v105, 0xfc, v105\n" \
  "v_add_u32_dpp v108, v108, v108 row_bcast:31 row_mask:0xc bank_mask:0xf\n" \
  "v_lshlrev_b32 v90, 1, v86\n" \
  "v_readlane_b32 s65, v108, 63\n" \
  "v_lshlrev_b32 v94, 4, v86\n" \
  "v_lshrrev_b32 v106, 2, v86\n" \
  "v_and_or_b32 v90, v90, v118, %[wb]\n" \
  "v_lshlrev_b32_e64 v102, v94, 4\n" \
  "v_and_b32 v106, 0xfc, v106\n" \
  "s_cmp_lg_u32 s65, 0\n" \
  "s_cbranch_scc1 Lkr%=\n" \
  "Lkc%=:\n" \
  "s_mov_b64 exec, s[66:67]\n" \
  "ds_add_rtn_u32 v96, v88, v100 offset:%[hi]\n" \
  "ds_add_u32 v104, v116 offset:%[dg]\n" \
  "s_mov_b64 exec, s[68:69]\n" \
  "ds_add_rtn_u32 v97, v89, v101 offset:%[hi]\n" \
  "ds_add_u32 v105, v116 offset:%[dg]\n" \
  "s_mov_b64 exec, s[70:71]\n" \
  "ds_add_rtn_u32 v98, v90, v102 offset:%[hi]\n" \
  "ds_add_u32 v106, v116 offset:%[dg]\n" \
  "s_cmpk_gt_u32 s62, 0xc0\n" \
  "s_mov_b64 exec, -1\n" \
  "s_cbranch_scc1 Lc3%=\n" \
  "Lc4%=:\n" \
  "v_and_b32 v100, v118, v88\n" \
  "v_and_b32 v101, v118, v89\n" \
  "v_and_b32 v102, v118, v90\n" \
  "s_waitcnt lgkmcnt(0)\n" \
  "s_barrier\n" \
  "ds_read_b32 v108, v149 offset:%[dg]\n" \
  "ds_read_b32 v109, v150 offset:%[dg]\n" \
  "ds_read2st64_b32 v[76:77], v142 offset0:0 offset1:8\n" \
  "ds_read2st64_b32 v[78:79], v142 offset0:16 offset1:24\n" \
  "ds_read2st64_b32 v[104:105], v142 offset0:32 offset1:40\n" \
  "ds_read2st64_b32 v[106:107], v142 offset0:48 offset1:56\n" \
  "v_and_b32 v84, 0xfffc00, v84\n" \
  "v_and_b32 v85, 0xfffc00, v85\n" \
  "v_and_b32 v86, 0xfffc00, v86\n" \
  "v_and_b32 v87, 0xfffc00, v87\n" \
  "v_mov_b32 v111, 0\n" \
  "v_mov_b32 v112, 63\n" \
  "s_waitcnt lgkmcnt(4)\n" \
  "v_readlane_b32 s74, v109, 0\n" \
  "v_readlane_b32 s76, v109, 1\n" \
  "v_readlane_b32 s78, v109, 2\n" \
  "s_nop 0\n" \
  "v_cmp_lt_i32_e64 s[74:75], s74, v108\n" \
  "v_cmp_lt_i32_e64 s[76:77], s76, v108\n" \
  "v_cmp_lt_i32_e64 s[78:79], s78, v108\n" \
  "s_bcnt1_i32_b64 s74, s[74:75]\n" \
  "s_bcnt1_i32_b64 s76, s[76:77]\n" \
  "s_bcnt1_i32_b64 s78, s[78:79]\n" \
  "v_writelane_b32 v112, s74, 0\n" \
  "v_writelane_b32 v112, s76, 1\n" \
  "v_writelane_b32 v112, s78, 2\n" \
  "v_lshlrev_b32 v112, 2, v112\n" \
  "s_waitcnt lgkmcnt(3)\n" \
  "ds_write2st64_b32 v142, v117, v76 offset1:8\n" \
  "v_add_u32 v77, v77, v76\n" \
  "s_waitcnt lgkmcnt(3)\n" \
  "v_add_u32 v78, v78, v77\n" \
  "ds_write2st64_b32 v142, v77, v78 offset0:16 offset1:24\n" \
  "v_add_u32 v79, v79, v78\n" \
  "s_waitcnt lgkmcnt(3)\n" \
  "v_add_u32 v104, v104, v79\n" \
  "ds_write2st64_b32 v142, v79, v104 offset0:32 offset1:40\n" \
  "v_add_u32 v105, v105, v104\n" \
  "s_waitcnt lgkmcnt(3)\n" \
  "v_add_u32 v106, v106, v105\n" \
  "ds_write2st64_b32 v142, v105, v106 offset0:48 offset1:56\n" \
  "v_add_u32 v107, v107, v106\n" \
  "ds_write_b32 v142, v107 offset:16384\n" \
  "ds_write_b32 v112, v150 offset:%[br]\n" \
  "s_waitcnt lgkmcnt(0)\n" \
  "s_barrier\n" \
  "ds_read_b32 v108, v143\n" \
  "ds_read_b32 v109, v144\n" \
  "s_mov_b32 s74, 0x1f1f1f1f\n" \
  "s_mov_b32 s75, 0x1f1f1f1f\n" \
  "s_waitcnt lgkmcnt(1)\n" \
  "v_lshl_add_u32 v76, v108, 3, v145\n" \
  "s_mov_b64 exec, s[74:75]\n" \
  "ds_read_b32 v111, v76\n" \
  "s_mov_b64 exec, -1\n" \
  "ds_write_b32 v148, v108\n" \
  "s_waitcnt lgkmcnt(1)\n" \
  "v_add_u32_dpp v112, v111, v111 row_shr:1 row_mask:0xf bank_mask:0xf bound_ctrl:1\n" \
  "v_subrev_u32 v109, %[n0], v109\n" \
  "v_and_b32 v109, 0xfc, v109\n" \
  "v_add_u32_dpp v112, v112, v112 row_shr:2 row_mask:0xf bank_mask:0xf bound_ctrl:1\n" \
  "v_lshl_add_u32 v159, v109, 8, v159\n" \
  "v_sub_u32 v77, v146, v111\n" \
  "v_add_u32_dpp v112, v112, v112 row_shr:4 row_mask:0xf bank_mask:0xf bound_ctrl:1\n" \
  "s_nop 1\n" \
  "v_add_u32_dpp v112, v112, v112 row_shr:8 row_mask:0xf bank_mask:0xf bound_ctrl:1\n" \
  "s_nop 1\n" \
  "v_add_u32_dpp v112, v112, v112 row_bcast:15 row_mask:0xa bank_mask:0xf\n" \
  "s_nop 1\n" \
  "v_add_u32_dpp v112, v112, v112 row_bcast:31 row_mask:0xc bank_mask:0xf\n" \
  "v_sub_u32 v111, v112, v111\n" \
  "v_readlane_b32 s65, v112, 63\n" \
  "v_mov_b32_dpp v78, v112 row_newbcast:7 row_mask:0xf bank_mask:0x3\n" \
  "v_mov_b32_dpp v79, v111 row_newbcast:0 row_mask:0xf bank_mask:0x3\n" \
  "v_add_u32 v77, v77, v112\n" \
  "v_mov_b32_dpp v78, v112 row_newbcast:15 row_mask:0xf bank_mask:0xc\n" \
  "v_mov_b32_dpp v79, v111 row_newbcast:8 row_mask:0xf bank_mask:0xc\n" \
  "s_and_b32 s64, s65, 0xffff\n" \
  "v_lshl_add_u32 v77, v78, 16, v77\n" \
  "s_lshr_b32 s65, s65, 16\n" \
  "v_add_u32_sdwa v77, v77, v79 dst_sel:DWORD dst_unused:UNUSED_PAD src0_sel:DWORD src1_sel:WORD_1\n" \
  "s_add_u32 s64, s64, s65\n" \
  "s_mov_b64 exec, s[74:75]\n" \
  "ds_write_b32 v76, v77\n" \
  "s_mov_b64 exec, -1\n" \
  "s_cmpk_gt_u32 s64, 864\n" \
  "s_cbranch_scc1 Llo%=\n" \
  "Llc%=:\n" \
  "s_waitcnt lgkmcnt(0)\n" \
  "s_barrier\n" \
  "ds_read_b32 v109, v117 offset:%[fl]\n" \
  "ds_read_b32 v76, v88 offset:%[hi]\n" \
  "ds_read_b32 v104, v100 offset:%[to]\n" \
  "ds_read_b32 v77, v89 offset:%[hi]\n" \
  "ds_read_b32 v105, v101 offset:%[to]\n" \
  "ds_read_b32 v78, v90 offset:%[hi]\n" \
  "ds_read_b32 v106, v102 offset:%[to]\n" \
  "v_add_u32 v113, s64, v147\n" \
  "v_bfrev_b32 v111, 1\n" \
  "s_max_u32 s60, s64, 4\n" \
  "s_sub_u32 s60, s60, 4\n" \
  "s_lshr_b32 s60, s60, 4\n" \
  "s_waitcnt lgkmcnt(4)\n" \
  "v_readfirstlane_b32 s61, v109\n" \
  "v_add3_u32 v88, v76, v104, v96\n" \
  "v_lshrrev_b32 v88, v92, v88\n" \
  "v_bfe_u32 v72, v88, 13, 3\n" \
  "v_and_b32 v88, 0x1ffc, v88\n" \
  "v_or_b32 v72, v84, v72\n" \
  "s_cmp_lg_u32 s61, 0\n" \
  "s_cbranch_scc1 Lslow%=\n" \
  "s_waitcnt lgkmcnt(0)\n" \
  "v_add3_u32 v89, v77, v105, v97\n" \
  "v_add3_u32 v90, v78, v106, v98\n" \
  "v_lshrrev_b32 v89, v93, v89\n" \
  "v_lshrrev_b32 v90, v94, v90\n" \
  "v_bfe_u32 v73, v89, 13, 3\n" \
  "v_bfe_u32 v74, v90, 13, 3\n" \
  "v_and_b32 v89, 0x1ffc, v89\n" \
  "v_and_b32 v90, 0x1ffc, v90\n" \
  "v_or_b32 v73, v85, v73\n" \
  "v_or_b32 v74, v86, v74\n" \
  "s_mov_b64 exec, s[66:67]\n" \
  "ds_write_b32 v88, v72 offset:%[so]\n" \
  "s_mov_b64 exec, s[68:69]\n" \
  "ds_write_b32 v89, v73 offset:%[so]\n" \
  "s_mov_b64 exec, s[70:71]\n" \
  "ds_write_b32 v90, v74 offset:%[so]\n" \
  "s_cmpk_gt_u32 s62, 0xc0\n" \
  "s_cbranch_scc1 Lq3%=\n" \
  "Lq4%=:\n" \
  "s_mov_b32 exec_lo, -1\n" \
  "s_mov_b32 exec_hi, 0\n" \
  "ds_write_b32 v113, v111 offset:%[so]\n" \
  "s_mov_b64 exec, -1\n" \
  "s_waitcnt lgkmcnt(0)\n" \
  "s_barrier\n" \
  "ds_read_b32  v11, v12\n" \
  "ds_read_b32 v18, v13\n" \
  "v_lshlrev_b32 v88, 2, v88\n" \
  "v_lshlrev_b32 v89, 2, v89\n" \
  "v_lshlrev_b32 v90, 2, v90\n" \
  "v_lshlrev_b32 v91, 2, v91\n" \
  "v_lshlrev_b32 v113, 2, v113\n" \
  "s_mov_b32 s63, 0x10000\n" \
  "s_waitcnt lgkmcnt(1)\n" \
  "v_and_b32 v10, 0xfffffc00, v11\n" \
  "s_nop 1\n" \
  "v_add_u32_dpp v9, v10, v8 row_newbcast:0 row_mask:0xf bank_mask:0xf\n" \
  "buffer_load_dwordx4 v[120:123], v9, %[xs], 0 offen\n" \
  "v_add_u32_dpp v9, v10, v8 row_newbcast:1 row_mask:0xf bank_mask:0xf\n" \
  "buffer_load_dwordx4 v[124:127], v9, %[xs], 0 offen\n" \
  "v_add_u32_dpp v9, v10, v8 row_newbcast:2 row_mask:0xf bank_mask:0xf\n" \
  "buffer_load_dwordx4 v[128:131], v9, %[xs], 0 offen\n" \
  "v_add_u32_dpp v9, v10, v8 row_newbcast:3 row_mask:0xf bank_mask:0xf\n" \
  "buffer_load_dwordx4 v[132:135], v9, %[xs], 0 offen\n" \
  "v_add_u32_dpp v9, v10, v8 row_newbcast:4 row_mask:0xf bank_mask:0xf\n" \
  "buffer_load_dwordx4 v[136:139], v9, %[xs], 0 offen\n" \
  "s_waitcnt vmcnt(5)\n" \
  "Lpc%=:\n" \
  "s_mov_b64 exec, s[66:67]\n" \
  "ds_write_b128 v88, v[32:35] offset:%[pa]\n" \
  "s_mov_b64 exec, s[68:69]\n" \
  "ds_write_b128 v89, v[36:39] offset:%[pa]\n" \
  "s_mov_b64 exec, s[70:71]\n" \
  "ds_write_b128 v90, v[40:43] offset:%[pa]\n" \
  "s_mov_b64 exec, s[72:73]\n" \
  "ds_write_b128 v91, v[44:47] offset:%[pa]\n" \
  "s_mov_b32 exec_lo, -1\n" \
  "s_mov_b32 exec_hi, 0\n" \
  "ds_write_b128 v113, v[48:51] offset:%[pa]\n" \
  "s_mov_b64 exec, -1\n" \
  "v_add3_u32 v108, v32, v33, v115\n" \
  "v_add3_u32 v111, v34, v35, v36\n" \
  "v_add3_u32 v112, v37, v38, v39\n" \
  "v_add3_u32 v108, v108, v40, v41\n" \
  "v_add3_u32 v111, v111, v42, v43\n" \
  "v_add3_u32 v112, v112, v44, v45\n" \
  "v_add3_u32 v108, v108, v46, v47\n" \
  "v_add3_u32 v108, v108, v111, v112\n" \
  "s_nop 1\n" \
  "v_add_u32_dpp v108, v108, v108 row_shr:1 row_mask:0xf bank_mask:0xf bound_ctrl:1\n" \
  "s_nop 1\n" \
  "v_add_u32_dpp v108, v108, v108 row_shr:2 row_mask:0xf bank_mask:0xf bound_ctrl:1\n" \
  "s_nop 1\n" \
  "v_add_u32_dpp v108, v108, v108 row_shr:4 row_mask:0xf bank_mask:0xf bound_ctrl:1\n" \
  "s_nop 1\n" \
  "v_add_u32_dpp v108, v108, v108 row_shr:8 row_mask:0xf bank_mask:0xf bound_ctrl:1\n" \
  "s_nop 1\n" \
  "v_add_u32_dpp v108, v108, v108 row_bcast:15 row_mask:0xa bank_mask:0xf\n" \
  "s_nop 1\n" \
  "v_add_u32_dpp v108, v108, v108 row_bcast:31 row_mask:0xc bank_mask:0xf\n" \
  "s_nop 0\n" \
  "v_readlane_b32 s65, v108, 63\n" \
  "s_waitcnt lgkmcnt(0)\n" \
  "s_cmp_lg_u32 s65, 0\n" \
  "s_cbranch_scc1 Lpr%=\n" \
  "Lpw%=:\n" \
  "s_barrier\n" \
  "ds_read_b64 v[106:107], v14\n" \
  "ds_read_b64 v[104:105], v15\n" \
  "s_setprio 1\n" \
  "v_mov_b64 v[32:33], 0\n" \
  "v_mov_b64 v[34:35], 0\n" \
  "v_mov_b64 v[36:37], 0\n" \
  "v_mov_b64 v[38:39], 0\n" \
  "v_mov_b64 v[40:41], 0\n" \
  "v_mov_b64 v[42:43], 0\n" \
  "v_mov_b64 v[44:45], 0\n" \
  "v_mov_b64 v[46:47], 0\n" \
  "s_waitcnt lgkmcnt(0)\n" \
  "v_mfma_f32_4x4x4_16b_bf16 v[80:83], v[104:105], v[28:29], 0 cbsz:4\n" \
  "v_mfma_f32_4x4x4_16b_bf16 v[84:87], v[104:105], v[30:31], 0 cbsz:4\n" \
  "v_mfma_f32_4x4x4_16b_bf16 v[112:115], v[106:107], v[20:21], 0\n" \
  "v_mfma_f32_4x4x4_16b_bf16 v[116:119], v[106:107], v[22:23], 0\n" \
  "v_mfma_f32_4x4x4_16b_bf16 v[72:75], v[106:107], v[24:25], 0\n" \
  "v_mfma_f32_4x4x4_16b_bf16 v[76:79], v[106:107], v[26:27], 0\n" \
  "v_and_b32 v18, v17, v18\n" \
  "v_cmp_ne_u32_e64 s[62:63], 0, v18\n" \
  "v_cvt_pkrtz_f16_f32 v97, v80, v81\n" \
  "v_and_b32 v96, v16, v97\n" \
  "v_and_b32_sdwa v97, v16, v97 dst_sel:DWORD dst_unused:UNUSED_PAD src0_sel:DWORD src1_sel:WORD_1\n" \
  "ds_read_b32 v96, v96 offset:%[tb]\n" \
  "ds_read_b32 v97, v97 offset:%[tb]\n" \
  "v_cvt_pkrtz_f16_f32 v99, v84, v85\n" \
  "v_and_b32 v98, v16, v99\n" \
  "v_and_b32_sdwa v99, v16, v99 dst_sel:DWORD dst_unused:UNUSED_PAD src0_sel:DWORD src1_sel:WORD_1\n" \
  "ds_read_b32 v98, v98 offset:%[tb]\n" \
  "ds_read_b32 v99, v99 offset:%[tb]\n" \
  "v_cvt_pkrtz_f16_f32 v101, v82, v83\n" \
  "v_and_b32 v100, v16, v101\n" \
  "v_and_b32_sdwa v101, v16, v101 dst_sel:DWORD dst_unused:UNUSED_PAD src0_sel:DWORD src1_sel:WORD_1\n" \
  "ds_read_b32 v100, v100 offset:%[tb]\n" \
  "ds_read_b32 v101, v101 offset:%[tb]\n" \
  "v_cvt_pk_bf16_f32 v108, v112, v116\n" \
  "v_cvt_pk_bf16_f32 v109, v72, v76\n" \
  "s_nop 0\n" \
  "s_waitcnt vmcnt(4)\n" \
  "v_mfma_f32_4x4x4_16b_bf16 v[72:75], v[108:109], v[120:121], 0 cbsz:4\n" \
  "v_mfma_f32_4x4x4_16b_bf16 v[76:79], v[108:109], v[122:123], 0 cbsz:4\n" \
  "v_add_u32_dpp v9, v10, v8 row_newbcast:5 row_mask:0xf bank_mask:0xf\n" \
  "buffer_load_dwordx4 v[140:143], v9, %[xs], 0 offen\n" \
  "s_waitcnt lgkmcnt(0)\n" \
  "s_nop 1\n" \
  "Lbody%=:\n" \
  "s_bfe_u32 s61, s62, 0x40000\n" \
  "s_waitcnt lgkmcnt(2)\n" \
  "v_pk_mul_f32 v[80:81], v[80:81], v[96:97]\n" \
  "v_pk_mul_f32 v[84:85], v[84:85], v[98:99]\n" \
  "s_set_gpr_idx_on s61, gpr_idx(SRC2,DST)\n" \
  "v_pk_fma_f32 v[32:33], v[80:81], v[72:73], v[32:33] op_sel:[0,0,0] op_sel_hi:[0,1,1]\n" \
  "v_pk_fma_f32 v[42:43], v[80:81], v[74:75], v[42:43] op_sel:[0,0,0] op_sel_hi:[0,1,1]\n" \
  "v_pk_fma_f32 v[52:53], v[84:85], v[76:77], v[52:53] op_sel:[0,0,0] op_sel_hi:[0,1,1]\n" \
  "v_pk_fma_f32 v[62:63], v[84:85], v[78:79], v[62:63] op_sel:[0,0,0] op_sel_hi:[0,1,1]\n" \
  "s_set_gpr_idx_off\n" \
  "s_waitcnt vmcnt(4)\n" \
  "v_mfma_f32_4x4x4_16b_bf16 v[72:75], v[108:109], v[124:125], 0 cbsz:4 abid:1\n" \
  "v_mfma_f32_4x4x4_16b_bf16 v[76:79], v[108:109], v[126:127], 0 cbsz:4 abid:1\n" \
  "v_mfma_f32_4x4x4_16b_bf16 v[88:91], v[104:105], v[28:29], 0 cbsz:4 abid:1\n" \
  "v_mfma_f32_4x4x4_16b_bf16 v[92:95], v[104:105], v[30:31], 0 cbsz:4 abid:1\n" \
  "v_add_u32_dpp v9, v10, v8 row_newbcast:6 row_mask:0xf bank_mask:0xf\n" \
  "v_cvt_pkrtz_f16_f32 v103, v86, v87\n" \
  "buffer_load_dwordx4 v[144:147], v9, %[xs], 0 offen\n" \
  "v_and_b32 v102, v16, v103\n" \
  "v_and_b32_sdwa v103, v16, v103 dst_sel:DWORD dst_unused:UNUSED_PAD src0_sel:DWORD src1_sel:WORD_1\n" \
  "ds_read_b32 v102, v102 offset:%[tb]\n" \
  "ds_read_b32 v103, v103 offset:%[tb]\n" \
  "v_mov_b32 v111, s60\n" \
  "ds_write2_b32 v110, v111, v111 offset1:8\n" \
  "s_bfe_u32 s61, s62, 0x40004\n" \
  "s_set_gpr_idx_on s61, gpr_idx(SRC2,DST)\n" \
  "v_pk_fma_f32 v[32:33], v[80:81], v[72:73], v[32:33] op_sel:[1,0,0] op_sel_hi:[1,1,1]\n" \
  "v_pk_fma_f32 v[42:43], v[80:81], v[74:75], v[42:43] op_sel:[1,0,0] op_sel_hi:[1,1,1]\n" \
  "v_pk_fma_f32 v[52:53], v[84:85], v[76:77], v[52:53] op_sel:[1,0,0] op_sel_hi:[1,1,1]\n" \
  "v_pk_fma_f32 v[62:63], v[84:85], v[78:79], v[62:63] op_sel:[1,0,0] op_sel_hi:[1,1,1]\n" \
  "s_set_gpr_idx_off\n" \
  "s_waitcnt vmcnt(4)\n" \
  "v_mfma_f32_4x4x4_16b_bf16 v[72:75], v[108:109], v[128:129], 0 cbsz:4 abid:2\n" \
  "v_mfma_f32_4x4x4_16b_bf16 v[76:79], v[108:109], v[130:131], 0 cbsz:4 abid:2\n" \
  "v_add_u32_dpp v9, v10, v8 row_newbcast:7 row_mask:0xf bank_mask:0xf\n" \
  "v_cvt_pkrtz_f16_f32 v97, v88, v89\n" \
  "buffer_load_dwordx4 v[148:151], v9, %[xs], 0 offen\n" \
  "v_and_b32 v96, v16, v97\n" \
  "v_and_b32_sdwa v97, v16, v97 dst_sel:DWORD dst_unused:UNUSED_PAD src0_sel:DWORD src1_sel:WORD_1\n" \
  "ds_read_b32 v96, v96 offset:%[tb]\n" \
  "ds_read_b32 v97, v97 offset:%[tb]\n" \
  "ds_read_b32 v111, v110 offset:16\n" \
  "s_bfe_u32 s61, s62, 0x40008\n" \
  "s_waitcnt lgkmcnt(4)\n" \
  "v_pk_mul_f32 v[82:83], v[82:83], v[100:101]\n" \
  "v_pk_mul_f32 v[86:87], v[86:87], v[102:103]\n" \
  "s_set_gpr_idx_on s61, gpr_idx(SRC2,DST)\n" \
  "v_pk_fma_f32 v[32:33], v[82:83], v[72:73], v[32:33] op_sel:[0,0,0] op_sel_hi:[0,1,1]\n" \
  "v_pk_fma_f32 v[42:43], v[82:83], v[74:75], v[42:43] op_sel:[0,0,0] op_sel_hi:[0,1,1]\n" \
  "v_pk_fma_f32 v[52:53], v[86:87], v[76:77], v[52:53] op_sel:[0,0,0] op_sel_hi:[0,1,1]\n" \
  "v_pk_fma_f32 v[62:63], v[86:87], v[78:79], v[62:63] op_sel:[0,0,0] op_sel_hi:[0,1,1]\n" \
  "s_set_gpr_idx_off\n" \
  "s_waitcnt vmcnt(4)\n" \
  "v_mfma_f32_4x4x4_16b_bf16 v[72:75], v[108:109], v[132:133], 0 cbsz:4 abid:3\n" \
  "v_mfma_f32_4x4x4_16b_bf16 v[76:79], v[108:109], v[134:135], 0 cbsz:4 abid:3\n" \
  "v_add_u32_dpp v9, v10, v8 row_newbcast:8 row_mask:0xf bank_mask:0xf\n" \
  "v_cvt_pkrtz_f16_f32 v99, v92, v93\n" \
  "buffer_load_dwordx4 v[120:123], v9, %[xs], 0 offen\n" \
  "v_and_b32 v98, v16, v99\n" \
  "v_and_b32_sdwa v99, v16, v99 dst_sel:DWORD dst_unused:UNUSED_PAD src0_sel:DWORD src1_sel:WORD_1\n" \
  "ds_read_b32 v98, v98 offset:%[tb]\n" \
  "ds_read_b32 v99, v99 offset:%[tb]\n" \
  "ds_read_b64 v[106:107], v14 offset:256\n" \
  "s_bfe_u32 s61, s62, 0x4000c\n" \
  "s_set_gpr_idx_on s61, gpr_idx(SRC2,DST)\n" \
  "v_pk_fma_f32 v[32:33], v[82:83], v[72:73], v[32:33] op_sel:[1,0,0] op_sel_hi:[1,1,1]\n" \
  "v_pk_fma_f32 v[42:43], v[82:83], v[74:75], v[42:43] op_sel:[1,0,0] op_sel_hi:[1,1,1]\n" \
  "v_pk_fma_f32 v[52:53], v[86:87], v[76:77], v[52:53] op_sel:[1,0,0] op_sel_hi:[1,1,1]\n" \
  "v_pk_fma_f32 v[62:63], v[86:87], v[78:79], v[62:63] op_sel:[1,0,0] op_sel_hi:[1,1,1]\n" \
  "s_set_gpr_idx_off\n" \
  "s_sub_u32 s60, s60, 1\n" \
  "s_cbranch_scc1 Lend%=\n" \
  "s_waitcnt vmcnt(4)\n" \
  "v_mfma_f32_4x4x4_16b_bf16 v[72:75], v[108:109], v[136:137], 0 cbsz:4 abid:4\n" \
  "v_mfma_f32_4x4x4_16b_bf16 v[76:79], v[108:109], v[138:139], 0 cbsz:4 abid:4\n" \
  "v_add_u32_dpp v9, v10, v8 row_newbcast:9 row_mask:0xf bank_mask:0xf\n" \
  "v_cvt_pkrtz_f16_f32 v101, v90, v91\n" \
  "buffer_load_dwordx4 v[124:127], v9, %[xs], 0 offen\n" \
  "v_and_b32 v100, v16, v101\n" \
  "v_and_b32_sdwa v101, v16, v101 dst_sel:DWORD dst_unused:UNUSED_PAD src0_sel:DWORD src1_sel:WORD_1\n" \
  "ds_read_b32 v100, v100 offset:%[tb]\n" \
  "ds_read_b32 v101, v101 offset:%[tb]\n" \
  "ds_read_b32 v18, v13 offset:64\n" \
  "v_add_u32 v14, 0x100, v14\n" \
  "s_bfe_u32 s61, s62, 0x40010\n" \
  "s_waitcnt lgkmcnt(4)\n" \
  "v_pk_mul_f32 v[88:89], v[88:89], v[96:97]\n" \
  "v_pk_mul_f32 v[92:93], v[92:93], v[98:99]\n" \
  "s_set_gpr_idx_on s61, gpr_idx(SRC2,DST)\n" \
  "v_pk_fma_f32 v[32:33], v[88:89], v[72:73], v[32:33] op_sel:[0,0,0] op_sel_hi:[0,1,1]\n" \
  "v_pk_fma_f32 v[42:43], v[88:89], v[74:75], v[42:43] op_sel:[0,0,0] op_sel_hi:[0,1,1]\n" \
  "v_pk_fma_f32 v[52:53], v[92:93], v[76:77], v[52:53] op_sel:[0,0,0] op_sel_hi:[0,1,1]\n" \
  "v_pk_fma_f32 v[62:63], v[92:93], v[78:79], v[62:63] op_sel:[0,0,0] op_sel_hi:[0,1,1]\n" \
  "s_set_gpr_idx_off\n" \
  "s_waitcnt vmcnt(4)\n" \
  "v_mfma_f32_4x4x4_16b_bf16 v[72:75], v[108:109], v[140:141], 0 cbsz:4 abid:5\n" \
  "v_mfma_f32_4x4x4_16b_bf16 v[76:79], v[108:109], v[142:143], 0 cbsz:4 abid:5\n" \
  "v_mfma_f32_4x4x4_16b_bf16 v[80:83], v[104:105], v[28:29], 0 cbsz:4 abid:2\n" \
  "v_mfma_f32_4x4x4_16b_bf16 v[84:87], v[104:105], v[30:31], 0 cbsz:4 abid:2\n" \
  "v_add_u32_dpp v9, v10, v8 row_newbcast:10 row_mask:0xf bank_mask:0xf\n" \
  "v_cvt_pkrtz_f16_f32 v103, v94, v95\n" \
  "buffer_load_dwordx4 v[128:131], v9, %[xs], 0 offen\n" \
  "v_and_b32 v102, v16, v103\n" \
  "v_and_b32_sdwa v103, v16, v103 dst_sel:DWORD dst_unused:UNUSED_PAD src0_sel:DWORD src1_sel:WORD_1\n" \
  "ds_read_b32 v102, v102 offset:%[tb]\n" \
  "ds_read_b32 v103, v103 offset:%[tb]\n" \
  "s_bfe_u32 s61, s62, 0x40014\n" \
  "s_set_gpr_idx_on s61, gpr_idx(SRC2,DST)\n" \
  "v_pk_fma_f32 v[32:33], v[88:89], v[72:73], v[32:33] op_sel:[1,0,0] op_sel_hi:[1,1,1]\n" \
  "v_pk_fma_f32 v[42:43], v[88:89], v[74:75], v[42:43] op_sel:[1,0,0] op_sel_hi:[1,1,1]\n" \
  "v_pk_fma_f32 v[52:53], v[92:93], v[76:77], v[52:53] op_sel:[1,0,0] op_sel_hi:[1,1,1]\n" \
  "v_pk_fma_f32 v[62:63], v[92:93], v[78:79], v[62:63] op_sel:[1,0,0] op_sel_hi:[1,1,1]\n" \
  "s_set_gpr_idx_off\n" \
  "s_waitcnt vmcnt(4) lgkmcnt(8)\n" \
  "v_mfma_f32_4x4x4_16b_bf16 v[72:75], v[108:109], v[144:145], 0 cbsz:4 abid:6\n" \
  "v_mfma_f32_4x4x4_16b_bf16 v[76:79], v[108:109], v[146:147], 0 cbsz:4 abid:6\n" \
  "v_add_u32_dpp v9, v10, v8 row_newbcast:11 row_mask:0xf bank_mask:0xf\n" \
  "v_cvt_pkrtz_f16_f32 v97, v80, v81\n" \
  "buffer_load_dwordx4 v[132:135], v9, %[xs], 0 offen\n" \
  "v_and_b32 v96, v16, v97\n" \
  "v_and_b32_sdwa v97, v16, v97 dst_sel:DWORD dst_unused:UNUSED_PAD src0_sel:DWORD src1_sel:WORD_1\n" \
  "ds_read_b32 v96, v96 offset:%[tb]\n" \
  "ds_read_b32 v97, v97 offset:%[tb]\n" \
  "v_add_u32 v13, 64, v13\n" \
  "v_readfirstlane_b32 s66, v111\n" \
  "s_setprio 1\n" \
  "s_cmp_ge_u32 s60, s66\n" \
  "s_cbranch_scc0 Lf5_%=\n" \
  "s_setprio 2\n" \
  "Lf5_%=:\n" \
  "s_bfe_u32 s61, s62, 0x40018\n" \
  "s_waitcnt lgkmcnt(2)\n" \
  "v_pk_mul_f32 v[90:91], v[90:91], v[100:101]\n" \
  "v_pk_mul_f32 v[94:95], v[94:95], v[102:103]\n" \
  "s_set_gpr_idx_on s61, gpr_idx(SRC2,DST)\n" \
  "v_pk_fma_f32 v[32:33], v[90:91], v[72:73], v[32:33] op_sel:[0,0,0] op_sel_hi:[0,1,1]\n" \
  "v_pk_fma_f32 v[42:43], v[90:91], v[74:75], v[42:43] op_sel:[0,0,0] op_sel_hi:[0,1,1]\n" \
  "v_pk_fma_f32 v[52:53], v[94:95], v[76:77], v[52:53] op_sel:[0,0,0] op_sel_hi:[0,1,1]\n" \
  "v_pk_fma_f32 v[62:63], v[94:95], v[78:79], v[62:63] op_sel:[0,0,0] op_sel_hi:[0,1,1]\n" \
  "s_set_gpr_idx_off\n" \
  "s_waitcnt vmcnt(4) lgkmcnt(7)\n" \
  "v_mfma_f32_4x4x4_16b_bf16 v[72:75], v[108:109], v[148:149], 0 cbsz:4 abid:7\n" \
  "v_mfma_f32_4x4x4_16b_bf16 v[76:79], v[108:109], v[150:151], 0 cbsz:4 abid:7\n" \
  "v_mfma_f32_4x4x4_16b_bf16 v[112:115], v[106:107], v[20:21], 0\n" \
  "v_add_u32_dpp v9, v10, v8 row_newbcast:12 row_mask:0xf bank_mask:0xf\n" \
  "v_cvt_pkrtz_f16_f32 v99, v84, v85\n" \
  "buffer_load_dwordx4 v[136:139], v9, %[xs], 0 offen\n" \
  "v_and_b32 v98, v16, v99\n" \
  "v_and_b32_sdwa v99, v16, v99 dst_sel:DWORD dst_unused:UNUSED_PAD src0_sel:DWORD src1_sel:WORD_1\n" \
  "ds_read_b32 v98, v98 offset:%[tb]\n" \
  "ds_read_b32 v99, v99 offset:%[tb]\n" \
  "ds_read_b32 v11, v12 offset:64\n" \
  "s_bfe_u32 s61, s62, 0x4001c\n" \
  "s_set_gpr_idx_on s61, gpr_idx(SRC2,DST)\n" \
  "v_pk_fma_f32 v[32:33], v[90:91], v[72:73], v[32:33] op_sel:[1,0,0] op_sel_hi:[1,1,1]\n" \
  "v_pk_fma_f32 v[42:43], v[90:91], v[74:75], v[42:43] op_sel:[1,0,0] op_sel_hi:[1,1,1]\n" \
  "v_pk_fma_f32 v[52:53], v[94:95], v[76:77], v[52:53] op_sel:[1,0,0] op_sel_hi:[1,1,1]\n" \
  "v_pk_fma_f32 v[62:63], v[94:95], v[78:79], v[62:63] op_sel:[1,0,0] op_sel_hi:[1,1,1]\n" \
  "s_set_gpr_idx_off\n" \
  "s_sub_u32 s60, s60, 1\n" \
  "s_cbranch_scc1 Lend%=\n" \
  "s_waitcnt vmcnt(4) lgkmcnt(7)\n" \
  "v_mfma_f32_4x4x4_16b_bf16 v[72:75], v[108:109], v[120:121], 0 cbsz:4 abid:8\n" \
  "v_mfma_f32_4x4x4_16b_bf16 v[76:79], v[108:109], v[122:123], 0 cbsz:4 abid:8\n" \
  "v_add_u32_dpp v9, v10, v8 row_newbcast:13 row_mask:0xf bank_mask:0xf\n" \
  "v_cvt_pkrtz_f16_f32 v101, v82, v83\n" \
  "buffer_load_dwordx4 v[140:143], v9, %[xs], 0 offen\n" \
  "v_and_b32 v100, v16, v101\n" \
  "v_and_b32_sdwa v101, v16, v101 dst_sel:DWORD dst_unused:UNUSED_PAD src0_sel:DWORD src1_sel:WORD_1\n" \
  "ds_read_b32 v100, v100 offset:%[tb]\n" \
  "ds_read_b32 v101, v101 offset:%[tb]\n" \
  "v_add_u32 v12, 64, v12\n" \
  "v_and_b32 v18, v17, v18\n" \
  "s_bfe_u32 s61, s63, 0x40000\n" \
  "s_waitcnt lgkmcnt(3)\n" \
  "v_pk_mul_f32 v[80:81], v[80:81], v[96:97]\n" \
  "v_pk_mul_f32 v[84:85], v[84:85], v[98:99]\n" \
  "s_set_gpr_idx_on s61, gpr_idx(SRC2,DST)\n" \
  "v_pk_fma_f32 v[32:33], v[80:81], v[72:73], v[32:33] op_sel:[0,0,0] op_sel_hi:[0,1,1]\n" \
  "v_pk_fma_f32 v[42:43], v[80:81], v[74:75], v[42:43] op_sel:[0,0,0] op_sel_hi:[0,1,1]\n" \
  "v_pk_fma_f32 v[52:53], v[84:85], v[76:77], v[52:53] op_sel:[0,0,0] op_sel_hi:[0,1,1]\n" \
  "v_pk_fma_f32 v[62:63], v[84:85], v[78:79], v[62:63] op_sel:[0,0,0] op_sel_hi:[0,1,1]\n" \
  "s_set_gpr_idx_off\n" \
  "s_waitcnt vmcnt(4)\n" \
  "v_mfma_f32_4x4x4_16b_bf16 v[72:75], v[108:109], v[124:125], 0 cbsz:4 abid:9\n" \
  "v_mfma_f32_4x4x4_16b_bf16 v[76:79], v[108:109], v[126:127], 0 cbsz:4 abid:9\n" \
  "v_mfma_f32_4x4x4_16b_bf16 v[88:91], v[104:105], v[28:29], 0 cbsz:4 abid:3\n" \
  "v_mfma_f32_4x4x4_16b_bf16 v[92:95], v[104:105], v[30:31], 0 cbsz:4 abid:3\n" \
  "v_add_u32_dpp v9, v10, v8 row_newbcast:14 row_mask:0xf bank_mask:0xf\n" \
  "v_cvt_pkrtz_f16_f32 v103, v86, v87\n" \
  "buffer_load_dwordx4 v[144:147], v9, %[xs], 0 offen\n" \
  "v_and_b32 v102, v16, v103\n" \
  "v_and_b32_sdwa v103, v16, v103 dst_sel:DWORD dst_unused:UNUSED_PAD src0_sel:DWORD src1_sel:WORD_1\n" \
  "ds_read_b32 v102, v102 offset:%[tb]\n" \
  "ds_read_b32 v103, v103 offset:%[tb]\n" \
  "v_mov_b32 v111, s60\n" \
  "ds_write2_b32 v110, v111, v111 offset1:8\n" \
  "s_bfe_u32 s61, s63, 0x40004\n" \
  "s_set_gpr_idx_on s61, gpr_idx(SRC2,DST)\n" \
  "v_pk_fma_f32 v[32:33], v[80:81], v[72:73], v[32:33] op_sel:[1,0,0] op_sel_hi:[1,1,1]\n" \
  "v_pk_fma_f32 v[42:43], v[80:81], v[74:75], v[42:43] op_sel:[1,0,0] op_sel_hi:[1,1,1]\n" \
  "v_pk_fma_f32 v[52:53], v[84:85], v[76:77], v[52:53] op_sel:[1,0,0] op_sel_hi:[1,1,1]\n" \
  "v_pk_fma_f32 v[62:63], v[84:85], v[78:79], v[62:63] op_sel:[1,0,0] op_sel_hi:[1,1,1]\n" \
  "s_set_gpr_idx_off\n" \
  "s_waitcnt vmcnt(4)\n" \
  "v_mfma_f32_4x4x4_16b_bf16 v[72:75], v[108:109], v[128:129], 0 cbsz:4 abid:10\n" \
  "v_mfma_f32_4x4x4_16b_bf16 v[76:79], v[108:109], v[130:131], 0 cbsz:4 abid:10\n" \
  "v_add_u32_dpp v9, v10, v8 row_newbcast:15 row_mask:0xf bank_mask:0xf\n" \
  "v_cvt_pkrtz_f16_f32 v97, v88, v89\n" \
  "buffer_load_dwordx4 v[148:151], v9, %[xs], 0 offen\n" \
  "v_and_b32 v96, v16, v97\n" \
  "v_and_b32_sdwa v97, v16, v97 dst_sel:DWORD dst_unused:UNUSED_PAD src0_sel:DWORD src1_sel:WORD_1\n" \
  "ds_read_b32 v96, v96 offset:%[tb]\n" \
  "ds_read_b32 v97, v97 offset:%[tb]\n" \
  "ds_read_b64 v[104:105], v15 offset:256\n" \
  "v_cmp_ne_u32_e64 s[64:65], 0, v18\n" \
  "ds_read_b32 v111, v110 offset:16\n" \
  "s_bfe_u32 s61, s63, 0x40008\n" \
  "s_waitcnt lgkmcnt(5)\n" \
  "v_pk_mul_f32 v[82:83], v[82:83], v[100:101]\n" \
  "v_pk_mul_f32 v[86:87], v[86:87], v[102:103]\n" \
  "s_set_gpr_idx_on s61, gpr_idx(SRC2,DST)\n" \
  "v_pk_fma_f32 v[32:33], v[82:83], v[72:73], v[32:33] op_sel:[0,0,0] op_sel_hi:[0,1,1]\n" \
  "v_pk_fma_f32 v[42:43], v[82:83], v[74:75], v[42:43] op_sel:[0,0,0] op_sel_hi:[0,1,1]\n" \
  "v_pk_fma_f32 v[52:53], v[86:87], v[76:77], v[52:53] op_sel:[0,0,0] op_sel_hi:[0,1,1]\n" \
  "v_pk_fma_f32 v[62:63], v[86:87], v[78:79], v[62:63] op_sel:[0,0,0] op_sel_hi:[0,1,1]\n" \
  "s_set_gpr_idx_off\n" \
  "s_waitcnt vmcnt(4) lgkmcnt(9)\n" \
  "v_and_b32 v10, 0xfffffc00, v11\n" \
  "v_mfma_f32_4x4x4_16b_bf16 v[72:75], v[108:109], v[132:133], 0 cbsz:4 abid:11\n" \
  "v_mfma_f32_4x4x4_16b_bf16 v[76:79], v[108:109], v[134:135], 0 cbsz:4 abid:11\n" \
  "v_mfma_f32_4x4x4_16b_bf16 v[116:119], v[106:107], v[22:23], 0\n" \
  "v_add_u32_dpp v9, v10, v8 row_newbcast:0 row_mask:0xf bank_mask:0xf\n" \
  "v_cvt_pkrtz_f16_f32 v99, v92, v93\n" \
  "buffer_load_dwordx4 v[120:123], v9, %[xs], 0 offen\n" \
  "v_and_b32 v98, v16, v99\n" \
  "v_and_b32_sdwa v99, v16, v99 dst_sel:DWORD dst_unused:UNUSED_PAD src0_sel:DWORD src1_sel:WORD_1\n" \
  "ds_read_b32 v98, v98 offset:%[tb]\n" \
  "ds_read_b32 v99, v99 offset:%[tb]\n" \
  "s_bfe_u32 s61, s63, 0x4000c\n" \
  "s_set_gpr_idx_on s61, gpr_idx(SRC2,DST)\n" \
  "v_pk_fma_f32 v[32:33], v[82:83], v[72:73], v[32:33] op_sel:[1,0,0] op_sel_hi:[1,1,1]\n" \
  "v_pk_fma_f32 v[42:43], v[82:83], v[74:75], v[42:43] op_sel:[1,0,0] op_sel_hi:[1,1,1]\n" \
  "v_pk_fma_f32 v[52:53], v[86:87], v[76:77], v[52:53] op_sel:[1,0,0] op_sel_hi:[1,1,1]\n" \
  "v_pk_fma_f32 v[62:63], v[86:87], v[78:79], v[62:63] op_sel:[1,0,0] op_sel_hi:[1,1,1]\n" \
  "s_set_gpr_idx_off\n" \
  "s_sub_u32 s60, s60, 1\n" \
  "s_cbranch_scc1 Lend%=\n" \
  "s_waitcnt vmcnt(4)\n" \
  "v_mfma_f32_4x4x4_16b_bf16 v[72:75], v[108:109], v[136:137], 0 cbsz:4 abid:12\n" \
  "v_mfma_f32_4x4x4_16b_bf16 v[76:79], v[108:109], v[138:139], 0 cbsz:4 abid:12\n" \
  "v_add_u32_dpp v9, v10, v8 row_newbcast:1 row_mask:0xf bank_mask:0xf\n" \
  "v_cvt_pkrtz_f16_f32 v101, v90, v91\n" \
  "buffer_load_dwordx4 v[124:127], v9, %[xs], 0 offen\n" \
  "v_and_b32 v100, v16, v101\n" \
  "v_and_b32_sdwa v101, v16, v101 dst_sel:DWORD dst_unused:UNUSED_PAD src0_sel:DWORD src1_sel:WORD_1\n" \
  "ds_read_b32 v100, v100 offset:%[tb]\n" \
  "ds_read_b32 v101, v101 offset:%[tb]\n" \
  "v_add_u32 v15, 0x100, v15\n" \
  "s_bfe_u32 s61, s63, 0x40010\n" \
  "s_waitcnt lgkmcnt(2)\n" \
  "v_pk_mul_f32 v[88:89], v[88:89], v[96:97]\n" \
  "v_pk_mul_f32 v[92:93], v[92:93], v[98:99]\n" \
  "s_set_gpr_idx_on s61, gpr_idx(SRC2,DST)\n" \
  "v_pk_fma_f32 v[32:33], v[88:89], v[72:73], v[32:33] op_sel:[0,0,0] op_sel_hi:[0,1,1]\n" \
  "v_pk_fma_f32 v[42:43], v[88:89], v[74:75], v[42:43] op_sel:[0,0,0] op_sel_hi:[0,1,1]\n" \
  "v_pk_fma_f32 v[52:53], v[92:93], v[76:77], v[52:53] op_sel:[0,0,0] op_sel_hi:[0,1,1]\n" \
  "v_pk_fma_f32 v[62:63], v[92:93], v[78:79], v[62:63] op_sel:[0,0,0] op_sel_hi:[0,1,1]\n" \
  "s_set_gpr_idx_off\n" \
  "s_waitcnt vmcnt(4) lgkmcnt(5)\n" \
  "v_mfma_f32_4x4x4_16b_bf16 v[72:75], v[108:109], v[140:141], 0 cbsz:4 abid:13\n" \
  "v_mfma_f32_4x4x4_16b_bf16 v[76:79], v[108:109], v[142:143], 0 cbsz:4 abid:13\n" \
  "v_mfma_f32_4x4x4_16b_bf16 v[80:83], v[104:105], v[28:29], 0 cbsz:4\n" \
  "v_mfma_f32_4x4x4_16b_bf16 v[84:87], v[104:105], v[30:31], 0 cbsz:4\n" \
  "v_add_u32_dpp v9, v10, v8 row_newbcast:2 row_mask:0xf bank_mask:0xf\n" \
  "v_cvt_pkrtz_f16_f32 v103, v94, v95\n" \
  "buffer_load_dwordx4 v[128:131], v9, %[xs], 0 offen\n" \
  "v_and_b32 v102, v16, v103\n" \
  "v_and_b32_sdwa v103, v16, v103 dst_sel:DWORD dst_unused:UNUSED_PAD src0_sel:DWORD src1_sel:WORD_1\n" \
  "ds_read_b32 v102, v102 offset:%[tb]\n" \
  "ds_read_b32 v103, v103 offset:%[tb]\n" \
  "s_bfe_u32 s61, s63, 0x40014\n" \
  "s_set_gpr_idx_on s61, gpr_idx(SRC2,DST)\n" \
  "v_pk_fma_f32 v[32:33], v[88:89], v[72:73], v[32:33] op_sel:[1,0,0] op_sel_hi:[1,1,1]\n" \
  "v_pk_fma_f32 v[42:43], v[88:89], v[74:75], v[42:43] op_sel:[1,0,0] op_sel_hi:[1,1,1]\n" \
  "v_pk_fma_f32 v[52:53], v[92:93], v[76:77], v[52:53] op_sel:[1,0,0] op_sel_hi:[1,1,1]\n" \
  "v_pk_fma_f32 v[62:63], v[92:93], v[78:79], v[62:63] op_sel:[1,0,0] op_sel_hi:[1,1,1]\n" \
  "s_set_gpr_idx_off\n" \
  "s_waitcnt vmcnt(4) lgkmcnt(6)\n" \
  "v_mfma_f32_4x4x4_16b_bf16 v[72:75], v[108:109], v[144:145], 0 cbsz:4 abid:14\n" \
  "v_mfma_f32_4x4x4_16b_bf16 v[76:79], v[108:109], v[146:147], 0 cbsz:4 abid:14\n" \
  "v_add_u32_dpp v9, v10, v8 row_newbcast:3 row_mask:0xf bank_mask:0xf\n" \
  "v_cvt_pkrtz_f16_f32 v97, v80, v81\n" \
  "buffer_load_dwordx4 v[132:135], v9, %[xs], 0 offen\n" \
  "v_and_b32 v96, v16, v97\n" \
  "v_and_b32_sdwa v97, v16, v97 dst_sel:DWORD dst_unused:UNUSED_PAD src0_sel:DWORD src1_sel:WORD_1\n" \
  "ds_read_b32 v96, v96 offset:%[tb]\n" \
  "ds_read_b32 v97, v97 offset:%[tb]\n" \
  "v_readfirstlane_b32 s66, v111\n" \
  "s_setprio 1\n" \
  "s_cmp_ge_u32 s60, s66\n" \
  "s_cbranch_scc0 Lf13_%=\n" \
  "s_setprio 2\n" \
  "Lf13_%=:\n" \
  "v_cvt_pk_bf16_f32 v19, v112, v116\n" \
  "s_bfe_u32 s61, s63, 0x40018\n" \
  "s_waitcnt lgkmcnt(2)\n" \
  "v_pk_mul_f32 v[90:91], v[90:91], v[100:101]\n" \
  "v_pk_mul_f32 v[94:95], v[94:95], v[102:103]\n" \
  "s_set_gpr_idx_on s61, gpr_idx(SRC2,DST)\n" \
  "v_pk_fma_f32 v[32:33], v[90:91], v[72:73], v[32:33] op_sel:[0,0,0] op_sel_hi:[0,1,1]\n" \
  "v_pk_fma_f32 v[42:43], v[90:91], v[74:75], v[42:43] op_sel:[0,0,0] op_sel_hi:[0,1,1]\n" \
  "v_pk_fma_f32 v[52:53], v[94:95], v[76:77], v[52:53] op_sel:[0,0,0] op_sel_hi:[0,1,1]\n" \
  "v_pk_fma_f32 v[62:63], v[94:95], v[78:79], v[62:63] op_sel:[0,0,0] op_sel_hi:[0,1,1]\n" \
  "s_set_gpr_idx_off\n" \
  "s_waitcnt vmcnt(4)\n" \
  "v_mfma_f32_4x4x4_16b_bf16 v[112:115], v[106:107], v[24:25], 0\n" \
  "v_mfma_f32_4x4x4_16b_bf16 v[116:119], v[106:107], v[26:27], 0\n" \
  "v_mfma_f32_4x4x4_16b_bf16 v[72:75], v[108:109], v[148:149], 0 cbsz:4 abid:15\n" \
  "v_mfma_f32_4x4x4_16b_bf16 v[76:79], v[108:109], v[150:151], 0 cbsz:4 abid:15\n" \
  "v_add_u32_dpp v9, v10, v8 row_newbcast:4 row_mask:0xf bank_mask:0xf\n" \
  "v_cvt_pkrtz_f16_f32 v99, v84, v85\n" \
  "buffer_load_dwordx4 v[136:139], v9, %[xs], 0 offen\n" \
  "v_and_b32 v98, v16, v99\n" \
  "v_and_b32_sdwa v99, v16, v99 dst_sel:DWORD dst_unused:UNUSED_PAD src0_sel:DWORD src1_sel:WORD_1\n" \
  "ds_read_b32 v98, v98 offset:%[tb]\n" \
  "ds_read_b32 v99, v99 offset:%[tb]\n" \
  "v_cvt_pk_bf16_f32 v109, v112, v116\n" \
  "v_mov_b32 v108, v19\n" \
  "s_bfe_u32 s61, s63, 0x4001c\n" \
  "s_set_gpr_idx_on s61, gpr_idx(SRC2,DST)\n" \
  "v_pk_fma_f32 v[32:33], v[90:91], v[72:73], v[32:33] op_sel:[1,0,0] op_sel_hi:[1,1,1]\n" \
  "v_pk_fma_f32 v[42:43], v[90:91], v[74:75], v[42:43] op_sel:[1,0,0] op_sel_hi:[1,1,1]\n" \
  "v_pk_fma_f32 v[52:53], v[94:95], v[76:77], v[52:53] op_sel:[1,0,0] op_sel_hi:[1,1,1]\n" \
  "v_pk_fma_f32 v[62:63], v[94:95], v[78:79], v[62:63] op_sel:[1,0,0] op_sel_hi:[1,1,1]\n" \
  "s_set_gpr_idx_off\n" \
  "s_sub_u32 s60, s60, 1\n" \
  "s_cbranch_scc1 Lend%=\n" \
  "s_waitcnt vmcnt(4)\n" \
  "v_mfma_f32_4x4x4_16b_bf16 v[72:75], v[108:109], v[120:121], 0 cbsz:4\n" \
  "v_mfma_f32_4x4x4_16b_bf16 v[76:79], v[108:109], v[122:123], 0 cbsz:4\n" \
  "v_add_u32_dpp v9, v10, v8 row_newbcast:5 row_mask:0xf bank_mask:0xf\n" \
  "v_cvt_pkrtz_f16_f32 v101, v82, v83\n" \
  "buffer_load_dwordx4 v[140:143], v9, %[xs], 0 offen\n" \
  "v_and_b32 v100, v16, v101\n" \
  "v_and_b32_sdwa v101, v16, v101 dst_sel:DWORD dst_unused:UNUSED_PAD src0_sel:DWORD src1_sel:WORD_1\n" \
  "ds_read_b32 v100, v100 offset:%[tb]\n" \
  "ds_read_b32 v101, v101 offset:%[tb]\n" \
  "s_mov_b64 s[62:63], s[64:65]\n" \
  "s_branch Lbody%=\n" \
  "Lend%=:\n" \
  "s_setprio 0\n" \
  "v_mov_b32 v20, 0\n" \
  "v_mov_b32 v21, %[bit]\n" \
  "v_cvt_pk_bf16_f32 v40, v40, v60\n" \
  "v_cvt_pk_bf16_f32 v41, v41, v61\n" \
  "v_cvt_pk_bf16_f32 v50, v50, v70\n" \
  "v_cvt_pk_bf16_f32 v51, v51, v71\n" \
  "ds_write2_b32 v156, v40, v41 offset0:0 offset1:16\n" \
  "ds_write2_b32 v156, v50, v51 offset0:32 offset1:48\n" \
  "s_mov_b64 exec, 1\n" \
  "ds_or_b32 v20, v21 offset:%[rd]\n" \
  "s_mov_b64 exec, -1\n" \
  "ds_read_b32 v20, v20 offset:%[wr]\n" \
  "v_cvt_pk_bf16_f32 v32, v32, v52\n" \
  "v_cvt_pk_bf16_f32 v33, v33, v53\n" \
  "v_cvt_pk_bf16_f32 v42, v42, v62\n" \
  "v_cvt_pk_bf16_f32 v43, v43, v63\n" \
  "ds_read_b128 v[72:75], v158 offset:0\n" \
  "ds_read_b128 v[76:79], v158 offset:1024\n" \
  "ds_read_b128 v[80:83], v158 offset:8192\n" \
  "ds_read_b128 v[84:87], v158 offset:9216\n" \
  "v_cvt_pk_bf16_f32 v34, v34, v54\n" \
  "v_cvt_pk_bf16_f32 v35, v35, v55\n" \
  "v_cvt_pk_bf16_f32 v44, v44, v64\n" \
  "v_cvt_pk_bf16_f32 v45, v45, v65\n" \
  "ds_write2st64_b32 v152, v32, v34 offset0:0 offset1:4\n" \
  "ds_write2st64_b32 v153, v33, v35 offset0:0 offset1:4\n" \
  "ds_write2st64_b32 v154, v42, v44 offset0:0 offset1:4\n" \
  "ds_write2st64_b32 v155, v43, v45 offset0:0 offset1:4\n" \
  "v_cvt_pk_bf16_f32 v36, v36, v56\n" \
  "v_cvt_pk_bf16_f32 v37, v37, v57\n" \
  "v_cvt_pk_bf16_f32 v46, v46, v66\n" \
  "v_cvt_pk_bf16_f32 v47, v47, v67\n" \
  "v_cvt_pk_bf16_f32 v38, v38, v58\n" \
  "v_cvt_pk_bf16_f32 v39, v39, v59\n" \
  "v_cvt_pk_bf16_f32 v48, v48, v68\n" \
  "v_cvt_pk_bf16_f32 v49, v49, v69\n" \
  "ds_write2st64_b32 v152, v36, v38 offset0:8 offset1:12\n" \
  "ds_write2st64_b32 v153, v37, v39 offset0:8 offset1:12\n" \
  "ds_write2st64_b32 v154, v46, v48 offset0:8 offset1:12\n" \
  "ds_write2st64_b32 v155, v47, v49 offset0:8 offset1:12\n" \
  "ds_read_b128 v[32:35], v157 offset:0\n" \
  "ds_read_b128 v[36:39], v157 offset:256\n" \
  "ds_read_b128 v[40:43], v157 offset:512\n" \
  "ds_read_b128 v[44:47], v157 offset:768\n" \
  "ds_read_b128 v[88:91], v158 offset:16384\n" \
  "ds_read_b128 v[92:95], v158 offset:17408\n" \
  "ds_read_b128 v[96:99], v158 offset:24576\n" \
  "ds_read_b128 v[100:103], v158 offset:25600\n" \
  "ds_read_b128 v[104:107], v158 offset:2048\n" \
  "ds_read_b128 v[108:111], v158 offset:3072\n" \
  "ds_read_b128 v[112:115], v158 offset:10240\n" \
  "ds_read_b128 v[116:119], v158 offset:11264\n" \
  "s_waitcnt lgkmcnt(11)\n" \
  "v_readfirstlane_b32 s66, v20\n" \
  "s_cmp_lg_u32 s66, 4\n" \
  "s_cbranch_scc1 Lredo%=\n" \
  "Lmix%=:\n" \
  "s_waitcnt lgkmcnt(11)\n" \
  "v_mfma_f32_16x16x32_bf16 v[48:51], v[32:35], v[72:75], 0\n" \
  "ds_read_b128 v[72:75], v158 offset:18432\n" \
  "v_mfma_f32_16x16x32_bf16 v[52:55], v[32:35], v[76:79], 0\n" \
  "ds_read_b128 v[76:79], v158 offset:19456\n" \
  "s_waitcnt lgkmcnt(12)\n" \
  "v_mfma_f32_16x16x32_bf16 v[48:51], v[36:39], v[80:83], v[48:51]\n" \
  "ds_read_b128 v[80:83], v158 offset:26624\n" \
  "v_mfma_f32_16x16x32_bf16 v[52:55], v[36:39], v[84:87], v[52:55]\n" \
  "ds_read_b128 v[84:87], v158 offset:27648\n" \
  "s_waitcnt lgkmcnt(11)\n" \
  "v_mfma_f32_16x16x32_bf16 v[48:51], v[40:43], v[88:91], v[48:51]\n" \
  "ds_read_b128 v[88:91], v158 offset:4096\n" \
  "s_waitcnt lgkmcnt(11)\n" \
  "v_mfma_f32_16x16x32_bf16 v[52:55], v[40:43], v[92:95], v[52:55]\n" \
  "ds_read_b128 v[92:95], v158 offset:5120\n" \
  "s_waitcnt lgkmcnt(11)\n" \
  "v_mfma_f32_16x16x32_bf16 v[48:51], v[44:47], v[96:99], v[48:51]\n" \
  "ds_read_b128 v[96:99], v158 offset:12288\n" \
  "s_waitcnt lgkmcnt(11)\n" \
  "v_mfma_f32_16x16x32_bf16 v[52:55], v[44:47], v[100:103], v[52:55]\n" \
  "ds_read_b128 v[100:103], v158 offset:13312\n" \
  "s_waitcnt lgkmcnt(11)\n" \
  "v_mfma_f32_16x16x32_bf16 v[56:59], v[32:35], v[104:107], 0\n" \
  "ds_read_b128 v[104:107], v158 offset:20480\n" \
  "s_waitcnt lgkmcnt(11)\n" \
  "v_mfma_f32_16x16x32_bf16 v[60:63], v[32:35], v[108:111], 0\n" \
  "ds_read_b128 v[108:111], v158 offset:21504\n" \
  "s_waitcnt lgkmcnt(11)\n" \
  "v_mfma_f32_16x16x32_bf16 v[56:59], v[36:39], v[112:115], v[56:59]\n" \
  "ds_read_b128 v[112:115], v158 offset:28672\n" \
  "s_waitcnt lgkmcnt(11)\n" \
  "v_mfma_f32_16x16x32_bf16 v[60:63], v[36:39], v[116:119], v[60:63]\n" \
  "ds_read_b128 v[116:119], v158 offset:29696\n" \
  "s_waitcnt lgkmcnt(11)\n" \
  "v_mfma_f32_16x16x32_bf16 v[56:59], v[40:43], v[72:75], v[56:59]\n" \
  "ds_read_b128 v[72:75], v158 offset:6144\n" \
  "v_cvt_pk_bf16_f32 v64, v48, v49\n" \
  "v_cvt_pk_bf16_f32 v65, v50, v51\n" \
  "v_cvt_pk_bf16_f32 v66, v52, v53\n" \
  "v_cvt_pk_bf16_f32 v67, v54, v55\n" \
  "buffer_store_dwordx4 v[64:67], v159, %[os], 0 offen offset:0 sc1\n" \
  "s_waitcnt lgkmcnt(11)\n" \
  "v_mfma_f32_16x16x32_bf16 v[60:63], v[40:43], v[76:79], v[60:63]\n" \
  "ds_read_b128 v[76:79], v158 offset:7168\n" \
  "s_waitcnt lgkmcnt(11)\n" \
  "v_mfma_f32_16x16x32_bf16 v[56:59], v[44:47], v[80:83], v[56:59]\n" \
  "ds_read_b128 v[80:83], v158 offset:14336\n" \
  "s_waitcnt lgkmcnt(11)\n" \
  "v_mfma_f32_16x16x32_bf16 v[60:63], v[44:47], v[84:87], v[60:63]\n" \
  "ds_read_b128 v[84:87], v158 offset:15360\n" \
  "s_waitcnt lgkmcnt(11)\n" \
  "v_mfma_f32_16x16x32_bf16 v[48:51], v[32:35], v[88:91], 0\n" \
  "ds_read_b128 v[88:91], v158 offset:22528\n" \
  "s_waitcnt lgkmcnt(11)\n" \
  "v_mfma_f32_16x16x32_bf16 v[52:55], v[32:35], v[92:95], 0\n" \
  "ds_read_b128 v[92:95], v158 offset:23552\n" \
  "s_waitcnt lgkmcnt(11)\n" \
  "v_mfma_f32_16x16x32_bf16 v[48:51], v[36:39], v[96:99], v[48:51]\n" \
  "ds_read_b128 v[96:99], v158 offset:30720\n" \
  "s_waitcnt lgkmcnt(11)\n" \
  "v_mfma_f32_16x16x32_bf16 v[52:55], v[36:39], v[100:103], v[52:55]\n" \
  "ds_read_b128 v[100:103], v158 offset:31744\n" \
  "s_waitcnt lgkmcnt(11)\n" \
  "v_mfma_f32_16x16x32_bf16 v[48:51], v[40:43], v[104:107], v[48:51]\n" \
  "v_cvt_pk_bf16_f32 v68, v56, v57\n" \
  "v_cvt_pk_bf16_f32 v69, v58, v59\n" \
  "v_cvt_pk_bf16_f32 v70, v60, v61\n" \
  "v_cvt_pk_bf16_f32 v71, v62, v63\n" \
  "buffer_store_dwordx4 v[68:71], v159, %[os], 0 offen offset:256 sc1\n" \
  "s_waitcnt lgkmcnt(10)\n" \
  "v_mfma_f32_16x16x32_bf16 v[52:55], v[40:43], v[108:111], v[52:55]\n" \
  "s_waitcnt lgkmcnt(9)\n" \
  "v_mfma_f32_16x16x32_bf16 v[48:51], v[44:47], v[112:115], v[48:51]\n" \
  "s_waitcnt lgkmcnt(8)\n" \
  "v_mfma_f32_16x16x32_bf16 v[52:55], v[44:47], v[116:119], v[52:55]\n" \
  "s_waitcnt lgkmcnt(7)\n" \
  "v_mfma_f32_16x16x32_bf16 v[56:59], v[32:35], v[72:75], 0\n" \
  "s_waitcnt lgkmcnt(6)\n" \
  "v_mfma_f32_16x16x32_bf16 v[60:63], v[32:35], v[76:79], 0\n" \
  "s_waitcnt lgkmcnt(5)\n" \
  "v_mfma_f32_16x16x32_bf16 v[56:59], v[36:39], v[80:83], v[56:59]\n" \
  "s_waitcnt lgkmcnt(4)\n" \
  "v_mfma_f32_16x16x32_bf16 v[60:63], v[36:39], v[84:87], v[60:63]\n" \
  "s_waitcnt lgkmcnt(3)\n" \
  "v_mfma_f32_16x16x32_bf16 v[56:59], v[40:43], v[88:91], v[56:59]\n" \
  "v_cvt_pk_bf16_f32 v64, v48, v49\n" \
  "v_cvt_pk_bf16_f32 v65, v50, v51\n" \
  "v_cvt_pk_bf16_f32 v66, v52, v53\n" \
  "v_cvt_pk_bf16_f32 v67, v54, v55\n" \
  "buffer_store_dwordx4 v[64:67], v159, %[os], 0 offen offset:512 sc1\n" \
  "s_waitcnt lgkmcnt(2)\n" \
  "v_mfma_f32_16x16x32_bf16 v[60:63], v[40:43], v[92:95], v[60:63]\n" \
  "s_waitcnt lgkmcnt(1)\n" \
  "v_mfma_f32_16x16x32_bf16 v[56:59], v[44:47], v[96:99], v[56:59]\n" \
  "s_waitcnt lgkmcnt(0)\n" \
  "v_mfma_f32_16x16x32_bf16 v[60:63], v[44:47], v[100:103], v[60:63]\n" \
  "s_nop 7\n" \
  "v_cvt_pk_bf16_f32 v68, v56, v57\n" \
  "v_cvt_pk_bf16_f32 v69, v58, v59\n" \
  "v_cvt_pk_bf16_f32 v70, v60, v61\n" \
  "v_cvt_pk_bf16_f32 v71, v62, v63\n" \
  "buffer_store_dwordx4 v[68:71], v159, %[os], 0 offen offset:768 sc1\n" \
  "s_branch Lout%=\n" \
  "Lredo%=:\n" \
  "v_mov_b32 v20, 0\n" \
  "s_sleep 1\n" \
  "ds_read_b32 v20, v20 offset:%[wr]\n" \
  "ds_read_b128 v[72:75], v158 offset:0\n" \
  "ds_read_b128 v[76:79], v158 offset:1024\n" \
  "ds_read_b128 v[80:83], v158 offset:8192\n" \
  "ds_read_b128 v[84:87], v158 offset:9216\n" \
  "ds_read_b128 v[88:91], v158 offset:16384\n" \
  "ds_read_b128 v[92:95], v158 offset:17408\n" \
  "ds_read_b128 v[96:99], v158 offset:24576\n" \
  "ds_read_b128 v[100:103], v158 offset:25600\n" \
  "ds_read_b128 v[104:107], v158 offset:2048\n" \
  "ds_read_b128 v[108:111], v158 offset:3072\n" \
  "ds_read_b128 v[112:115], v158 offset:10240\n" \
  "ds_read_b128 v[116:119], v158 offset:11264\n" \
  "s_waitcnt lgkmcnt(0)\n" \
  "v_readfirstlane_b32 s66, v20\n" \
  "s_cmp_lg_u32 s66, 4\n" \
  "s_cbranch_scc1 Lredo%=\n" \
  "s_branch Lmix%=\n" \
  "Lout%=:\n" \
  "s_endpgm\n" \
  "Lhr%=:\n" \
  "s_mov_b64 exec, vcc\n" \
  "s_sub_u32 s63, s63, 1\n" \
  "s_cbranch_scc0 Lh%=\n" \
  "s_mov_b64 exec, -1\n" \
  "Lover%=:\n" \
  "v_mov_b32 v113, 1\n" \
  "ds_write_b32 v117, v113 offset:%[fl]\n" \
  "s_mov_b32 s62, 0\n" \
  "v_mov_b32 v114, 0\n" \
  "v_mov_b32 v115, 0\n" \
  "s_branch Lld%=\n" \
  "Lkr%=:\n" \
  "buffer_load_dword v84, v76, %[ws], %[rk] offen sc1\n" \
  "buffer_load_dword v85, v77, %[ws], %[rk] offen sc1\n" \
  "buffer_load_dword v86, v78, %[ws], %[rk] offen sc1\n" \
  "s_cmpk_le_u32 s62, 0xc0\n" \
  "s_cbranch_scc1 Lkb%=\n" \
  "buffer_load_dword v87, v79, %[ws], %[rk] offen sc1\n" \
  "Lkb%=:\n" \
  "s_waitcnt vmcnt(0)\n" \
  "s_sub_u32 s63, s63, 1\n" \
  "s_cbranch_scc0 Lk%=\n" \
  "v_mov_b32 v113, 1\n" \
  "ds_write_b32 v117, v113 offset:%[fl]\n" \
  "s_branch Lkc%=\n" \
  "Lc3%=:\n" \
  "v_lshlrev_b32 v91, 1, v87\n" \
  "v_lshlrev_b32 v95, 4, v87\n" \
  "v_lshrrev_b32 v107, 2, v87\n" \
  "v_and_or_b32 v91, v91, v118, %[wb]\n" \
  "v_lshlrev_b32_e64 v103, v95, 4\n" \
  "v_and_b32 v107, 0xfc, v107\n" \
  "s_mov_b64 exec, s[72:73]\n" \
  "ds_add_rtn_u32 v99, v91, v103 offset:%[hi]\n" \
  "ds_add_u32 v107, v116 offset:%[dg]\n" \
  "s_mov_b64 exec, -1\n" \
  "s_branch Lc4%=\n" \
  "Llo%=:\n" \
  "v_mov_b32 v113, 1\n" \
  "ds_write_b32 v117, v113 offset:%[fl]\n" \
  "s_branch Llc%=\n" \
  "Lslow%=:\n" \
  "s_waitcnt vmcnt(0) lgkmcnt(0)\n" \
  "s_barrier\n" \
  "s_barrier\n" \
  "s_branch Lexit%=\n" \
  "Lq3%=:\n" \
  "s_mov_b64 exec, -1\n" \
  "v_and_b32 v103, 0x7fc, v91\n" \
  "ds_read_b32 v79, v91 offset:%[hi]\n" \
  "ds_read_b32 v107, v103 offset:%[to]\n" \
  "s_waitcnt lgkmcnt(0)\n" \
  "v_add3_u32 v91, v79, v107, v99\n" \
  "v_lshrrev_b32 v91, v95, v91\n" \
  "v_bfe_u32 v75, v91, 13, 3\n" \
  "v_and_b32 v91, 0x1ffc, v91\n" \
  "v_or_b32 v75, v87, v75\n" \
  "s_mov_b64 exec, s[72:73]\n" \
  "ds_write_b32 v91, v75 offset:%[so]\n" \
  "s_branch Lq4%=\n" \
  "Lpr%=:\n" \
  "s_cmpk_le_u32 s62, 0xc0\n" \
  "s_cbranch_scc1 Lpb%=\n" \
  "buffer_load_dwordx4 v[44:47], v83, %[ws], %[rp] offen sc1\n" \
  "Lpb%=:\n" \
  "buffer_load_dwordx4 v[32:35], v80, %[ws], %[rp] offen sc1\n" \
  "buffer_load_dwordx4 v[36:39], v81, %[ws], %[rp] offen sc1\n" \
  "buffer_load_dwordx4 v[40:43], v82, %[ws], %[rp] offen sc1\n" \
  "s_sub_u32 s63, s63, 1\n" \
  "s_waitcnt vmcnt(0)\n" \
  "s_cbranch_scc0 Lpc%=\n" \
  "s_branch Lpw%=\n" \
  "Lexit%=:\n" \
  "s_mov_b32 %[sl], s61\n"
#define STREAM_CLOBBERS \
  "v7", "v9", "v10", "v11", "v18", "v19", "v32", "v33", "v34", "v35", "v36", "v37", "v38", "v39", "v40", "v41", \
  "v42", "v43", "v44", "v45", "v46", "v47", "v48", "v49", "v50", "v51", "v52", "v53", "v54", "v55", "v56", \
  "v57", "v58", "v59", "v60", "v61", "v62", "v63", "v64", "v65", "v66", "v67", "v68", "v69", "v70", "v71", \
  "v72", "v73", "v74", "v75", "v76", "v77", "v78", "v79", "v80", "v81", "v82", "v83", "v84", "v85", "v86", \
  "v87", "v88", "v89", "v90", "v91", "v92", "v93", "v94", "v95", "v96", "v97", "v98", "v99", "v100", "v101", \
  "v102", "v103", "v104", "v105", "v106", "v107", "v108", "v109", "v111", "v112", "v113", "v114", "v115", \
  "v116", "v117", "v118", "v119", "v120", "v121", "v122", "v123", "v124", "v125", "v126", "v127", "v128", \
  "v129", "v130", "v131", "v132", "v133", "v134", "v135", "s60", "s61", "s62", "s63", "s64", "s65", "s66", \
  "s67", "s68", "s69", "s70", "s71", "s72", "s73", "s74", "s75", "s76", "s77", "s78", "s79", "scc", "vcc", \
  "memory"
#define RKL 3

namespace {

constexpr int C_ = 128;
constexpr int MW = 12;
constexpr int MT = MW * 64;
constexpr int SW = 8;
constexpr int NS = 5;
constexpr int NPB = SW * NS;
constexpr int NPASS = 16;
constexpr int HS = 64 * NPASS / 2;
constexpr int NBMAX = 256;
constexpr int PW = 5;
constexpr int PT = PW * 64;
constexpr int PK = 4;
constexpr int CH = PT * PK;
constexpr int MK = 4;
constexpr int CAPW = 256;
constexpr unsigned OOB = 0x80000000u;
#define OUTAUX 16
#define PFN 5
#define WSL 64
#define TB1 1
#define SUMP 1
#define PFS 2
#define PFP 2
#define HWV 10
#define HSL 24
constexpr unsigned WS_EPOCH = 0;
constexpr unsigned WS_OFF = 4096;
constexpr unsigned WS_RECK = WS_OFF + NBMAX * NBMAX * 16;
constexpr unsigned WS_RECP = WS_RECK + NBMAX * CH * 4;
constexpr unsigned WS_END = WS_RECP + NBMAX * CH * 16;

__device__ __forceinline__ float bf2f(unsigned short v) { return __uint_as_float(((unsigned)v) << 16); }
__device__ __forceinline__ unsigned short f2bf(float f) {
  __hip_bfloat16 b = __float2bfloat16(f);
  return *reinterpret_cast<unsigned short*>(&b);
}
__device__ __forceinline__ unsigned pk2bf(float lo, float hi) {
  return __builtin_bit_cast(unsigned, __builtin_convertvector(f32x2{lo, hi}, bf16x2));
}
__device__ __forceinline__ unsigned wave_incl_scan(unsigned x) {
  x += __builtin_amdgcn_update_dpp(0u, x, 0x111, 0xf, 0xf, false);
  x += __builtin_amdgcn_update_dpp(0u, x, 0x112, 0xf, 0xf, false);
  x += __builtin_amdgcn_update_dpp(0u, x, 0x114, 0xf, 0xf, false);
  x += __builtin_amdgcn_update_dpp(0u, x, 0x118, 0xf, 0xf, false);
  x += __builtin_amdgcn_update_dpp(0u, x, 0x142, 0xa, 0xf, false);
  x += __builtin_amdgcn_update_dpp(0u, x, 0x143, 0xc, 0xf, false);
  return x;
}

struct Lds {
  u32x4 sink[16];
  unsigned flag, wrdy, rdone, go;
  unsigned long long tag, pad2_;
  unsigned prog[16];
  int deg[64];
  unsigned byrank[64];
  unsigned nl[64];
  u32x4 ct[72];
  __attribute__((aligned(16))) unsigned so[SW * CAPW];
  union {
    __attribute__((aligned(16))) unsigned hist[(SW + 1) * HS];
    u32x4 row5[SW * 64];
  };
  union {
    struct {
      __attribute__((aligned(16))) unsigned seg[2][NBMAX];
      unsigned phist[PW][NBMAX / 2];
      unsigned pbase[PW][NBMAX / 2];
      u32x4 ri[SW][MK * 16];
    };
    u32x4 pay[SW * CAPW];
  };
  f32x4 sg[4096];
  union {
    struct {
      __attribute__((aligned(16))) unsigned sk[CH];
      u32x4 sp[CH];
    };
    u32x4 wf[C_ * C_ / 8];
  };
};
static_assert(sizeof(Lds) <= 163840, "LDS budget");
static_assert(offsetof(Lds, sg) < 65536, "table offset");

__device__ __forceinline__ unsigned lds_addr(const void* p) {
  return (unsigned)(uintptr_t)(__attribute__((address_space(3))) const void*)p;
}
__device__ __forceinline__ u32x4 make_srd(const void* p, unsigned num) {
  unsigned long long a = (unsigned long long)p;
  return u32x4{(unsigned)a, (unsigned)(a >> 32), num, 0x00020000u};
}

}

extern "C" __global__ __launch_bounds__(MT) void tfn_main_kernel(
    const int* dst_, int E_, int c0_, const int* src_, int CE_, int c1_, const u32x2* Y_, int N_, int c2_, const u32x2* ef_,
    unsigned M2_, int c3_, const unsigned short* x_, char* ws_, u32x4* out_, const unsigned short* wrad_,
    const unsigned short* cg_, const unsigned short* wmix_) {
  __shared__ Lds L;
  const int tid = threadIdx.x, lane = tid & 63;
  TDECL;
  STAMP(0);
  u32x2 d0, d1, s0, s1;
  u32x4 y0, y1, f0, f1;
  u32x16 ka;
  u32x8 kb, kc;
  u32x4 kd;
  unsigned long long ev;
  asm volatile(
      "s_load_dwordx16 s[8:23], %[kp], 0\n s_load_dwordx8 s[24:31], %[kp], 0x40\n s_load_dwordx4 s[32:35], %[kp], 0x60\n"
      "v_readfirstlane_b32 s36, %[tid]\n v_lshlrev_b32 v90, 3, %[tid]\n v_mov_b64 v[92:93], 0\n"
      "s_and_b32 s37, %[bid], 7\n s_lshr_b32 s38, %[bid], 3\n s_lshr_b32 s36, s36, 6\n"
      "ds_write_b64 v90, v[92:93] offset:%[zo]\n"
      "s_cmp_lg_u32 s36, %[pw]\n s_cbranch_scc1 1f\n"
      "s_getpc_b64 s[44:45]\n .Ltfn_p0:\n s_max_i32 s46, 0, .Ltfn_p1-.Ltfn_p0\n s_mov_b32 m0, %[sk]\n s_mov_b32 s47, 0x20000\n"
      "v_lshlrev_b32 v91, 12, %[tid]\n s_lshl_b32 s39, s38, 7\n v_and_b32 v91, 0x3f000, v91\n"
      "buffer_load_dword v91, s[44:47], s39 offen lds\n"
      "1:\n s_lshl_b32 s37, s37, 5\n s_add_u32 s37, s37, s38\n v_lshlrev_b32 v91, 4, %[tid]\n"
      "s_waitcnt lgkmcnt(0)\n"
      "s_mul_i32 s38, s37, s14\n s_mov_b32 s40, s10\n s_add_i32 s39, s38, s14\n s_lshl_b32 s44, s38, 2\n"
      "s_min_i32 s39, s39, s10\n s_add_i32 s45, s44, %[h4]\n s_lshl_b32 s10, s39, 2\n s_lshl_b32 s14, s39, 2\n"
      "s_cmp_ge_u32 s36, %[pw]\n s_cbranch_scc1 2f\n"
      "buffer_load_dwordx2 %0, v90, s[8:11], s44 offen\n buffer_load_dwordx2 %1, v90, s[8:11], s45 offen\n"
      "buffer_load_dwordx2 %2, v90, s[12:15], s44 offen\n buffer_load_dwordx2 %3, v90, s[12:15], s45 offen\n"
      "2:\n s_mov_b32 s41, s18\n s_mov_b32 s42, s22\n s_lshl_b32 s18, s39, 3\n s_lshl_b32 s22, s39, 3\n"
      "s_lshl_b32 s44, s44, 1\n s_lshl_b32 s45, s45, 1\n s_cmp_ge_u32 s36, %[pw]\n"
      "s_barrier\n s_cbranch_scc1 3f\n s_setprio 3\n"
      "buffer_load_dwordx4 v[100:103], v91, s[16:19], s44 offen\n buffer_load_dwordx4 v[104:107], v91, s[16:19], s45 offen\n"
      "buffer_load_dwordx4 v[108:111], v91, s[20:23], s44 offen\n buffer_load_dwordx4 v[112:115], v91, s[20:23], s45 offen\n"
      "3:\n s_cmp_lg_u32 s36, %[hw]\n s_cbranch_scc1 4f\n s_load_dwordx2 s[46:47], s[26:27], 0\n s_setprio 3\n"
      "s_waitcnt lgkmcnt(0)\n 4:"
      : "=&v"(d0), "=&v"(d1), "=&v"(s0), "=&v"(s1), "=&{v[100:103]}"(y0), "=&{v[104:107]}"(y1), "=&{v[108:111]}"(f0),
        "=&{v[112:115]}"(f1), "=&{s[8:23]}"(ka), "=&{s[24:31]}"(kb), "=&{s[32:35]}"(kd), "=&{s[36:43]}"(kc),
        "=&{s[46:47]}"(ev)
      : [kp] "s"(__builtin_amdgcn_kernarg_segment_ptr()), [tid] "v"(tid), [bid] "s"(blockIdx.x), [pw] "n"(PW), [hw] "n"(HWV),
        [zo] "n"(offsetof(Lds, seg)), [sk] "n"(offsetof(Lds, sink)), [h4] "n"(CH * 2)
      : "v90", "v91", "v92", "v93", "s44", "s45", "scc", "memory");
  typedef __attribute__((address_space(1))) char gchar;
  auto ptr = [](unsigned lo, unsigned hi) { return (char*)(gchar*)(lo | (unsigned long long)hi << 32); };
  const int* __restrict__ dst = (const int*)ptr(ka[0], ka[1]);
  const int* __restrict__ src = (const int*)ptr(ka[4], ka[5]);
  const u32x2* __restrict__ Y = (const u32x2*)ptr(ka[8], ka[9]);
  const u32x2* __restrict__ ef = (const u32x2*)ptr(ka[12], ka[13]);
  const unsigned short* __restrict__ x = (const unsigned short*)ptr(kb[0], kb[1]);
  char* ws = ptr(kb[2], kb[3]);
  u32x4* __restrict__ out = (u32x4*)ptr(kb[4], kb[5]);
  const unsigned short* __restrict__ wrad = (const unsigned short*)ptr(kb[6], kb[7]);
  const unsigned short* __restrict__ cg = (const unsigned short*)ptr(kd[0], kd[1]);
  const unsigned short* __restrict__ wmix = (const unsigned short*)ptr(kd[2], kd[3]);
  const int w = kc[0], bucket = kc[1], e0 = kc[2] + tid * 2, e1 = kc[3], E = kc[4], N = kc[5];
  const unsigned M2 = kc[6];
  __builtin_assume(w >= 0 && w < MW && bucket >= 0 && bucket < NBMAX);
  const int xcd = blockIdx.x & 7;
  const u32x4 wsd = make_srd(ws, WS_END);
  STAMP(1);
  auto table = [&](int q) {
    f32x4 p, n;
#pragma unroll
    for (int i = 0; i < 4; i++) {
      const unsigned short c = min(q * 16 + i * 4 + 2, 0x7bff);
      const float e = __builtin_amdgcn_exp2f(-1.4426950408889634f * (float)__builtin_bit_cast(_Float16, c));
      p[i] = __builtin_amdgcn_rcpf(1.f + e);
      n[i] = e * p[i];
    }
    L.sg[q] = p;
    L.sg[q + 2048] = n;
  };
  auto valid = [&](int k) { return e0 + (k >> 1) * (CH / 2) + (k & 1) < e1; };
  auto warm = [&](int i0, int i1) {
    const unsigned pfo = ((xcd * N) >> 3) * 1024 + ((blockIdx.x >> 3) * 64 + lane) * 128;
#pragma unroll 1
    for (int i = i0; i < i1; i += MW - SW)
      asm volatile("s_mov_b32 m0, %2\n s_nop 0\n buffer_load_dword %0, %1, %3 offen lds\n"
                   :: "v"(pfo), "s"(make_srd(x, N * 1024)), "s"(lds_addr(L.sink)), "s"(i * 32 * 64 * 128) : "memory");
  };
  unsigned bk[PK], pr[PK], ky[PK], sa[PK];
  unsigned cv, wq4[4];
  asm volatile(
      "s_cmp_lg_u32 %9, %10\n s_cbranch_scc1 1f\n global_load_ushort %0, %5, %7\n global_load_dword %1, %6, %8\n"
      "global_load_dword %2, %6, %8 offset:256\n global_load_dword %3, %6, %8 offset:512\n"
      "global_load_dword %4, %6, %8 offset:768\n 1:"
      : "=&v"(cv), "=&v"(wq4[0]), "=&v"(wq4[1]), "=&v"(wq4[2]), "=&v"(wq4[3])
      : "v"(lane * 2), "v"(lane * 4), "s"(cg), "s"(wrad), "s"(w), "n"(PW) : "scc");
  const int zt = tid - PT - (w > HWV ? 64 : 0);
  unsigned so1 = (tid - PT) * 16 + WS_RECP + bucket * (CH * 16);
  unsigned so0 = w < PW ? tid * 16 + WS_RECK + bucket * (CH * 4) : so1;
  so1 += MT * 16;
  unsigned ho0 = w < SW ? ((lane >> 1) * SW + w) * (NBMAX * 16) : lane * 16 + WS_OFF + bucket * (NBMAX * 16);
  int node0 = __builtin_amdgcn_readfirstlane(((unsigned)(bucket << 22) + M2 - 1) / M2);
  int nn = min(N, __builtin_amdgcn_readfirstlane(((unsigned)((bucket + 1) << 22) + M2 - 1) / M2)) - node0;
  asm volatile("" : "+v"(so0), "+v"(so1), "+v"(ho0), "+s"(node0), "+s"(nn));
  if (w < PW) {
    unsigned one[PK], far[PK];
#pragma unroll
    for (int k = 0; k < PK; k++) {
      one[k] = valid(k) ? 1u : 0u;
      far[k] = valid(k) ? 0u : CH;
    }
    asm volatile("s_waitcnt vmcnt(4)" : "+v"(d0), "+v"(d1), "+v"(s0), "+v"(s1), "+v"(one[0]), "+v"(one[1]), "+v"(one[2]),
                 "+v"(one[3]), "+v"(far[0]), "+v"(far[1]), "+v"(far[2]), "+v"(far[3]));
    STAMP(3);
    const u32x4 dv = {d0[0], d0[1], d1[0], d1[1]}, sv = {s0[0], s0[1], s1[0], s1[1]};
#pragma unroll
    for (int k = 0; k < PK; k++) {
      bk[k] = (unsigned)__umul24(dv[k], M2) >> 22;
      pr[k] = atomicAdd(&L.phist[w][bk[k] >> 1], one[k] << ((bk[k] * 16) & 31));
    }
    __builtin_amdgcn_sched_barrier(0);
#pragma unroll
    for (int k = 0; k < PK; k++) {
      ky[k] = (sv[k] << 10) | ((dv[k] & 63) << 4) | ((((unsigned)__umul24(sv[k], M2) >> 26) - ((bk[k] >> 4) & 14)) & 15);
      sa[k] = lds_addr(&L.seg[0][bk[k]]);
      asm volatile("ds_add_u32 %0, %1" :: "v"(sa[k]), "v"(ky[k]) : "memory");
    }
#pragma unroll
    for (int k = 0; k < PK; k++) {
      pr[k] = __builtin_amdgcn_ubfe(pr[k], bk[k] * 16, 16) + far[k];
      bk[k] = lds_addr(L.pbase[w]) + bk[k] * 2;
      asm volatile("" : "+v"(pr[k]), "+v"(bk[k]));
    }
  } else {
    if (w == PW) {
      L.deg[lane] = ((lane - node0) & 63) < NPB ? 63 - ((lane - node0) & 63) : -1;
      if (lane < 4) (&L.flag)[lane] = 0;
      if (lane < 16) L.prog[lane] = 255;
    }
    if (w == HWV) {
      ev = ev * 6364136223846793005ull + 1442695040888963407ull;
      L.tag = ev;
    } else {
#pragma unroll 1
      for (int q = zt; q < TB1 * 384; q += 384) table(q);
    }
  }
  STAMP(4);
  asm volatile("s_waitcnt lgkmcnt(0)\n s_barrier" ::: "memory");
  STAMP(5);
  const auto wsr = __builtin_amdgcn_make_buffer_rsrc((void*)ws, 0, WS_END, 0x00020000);
  auto lds_ld = [&](const unsigned* p) {
    return __builtin_amdgcn_readfirstlane(__hip_atomic_load(p, __ATOMIC_RELAXED, __HIP_MEMORY_SCOPE_WORKGROUP));
  };
  if (w == HWV) {
    const unsigned tl = (unsigned)ev, th = (unsigned)(ev >> 32);
    unsigned c[4] = {0, 0, 0, 0};
    u32x4 hv[4];
#pragma unroll
    for (int k = 0; k < 4; k++) {
#pragma unroll
      for (int ww = 0; ww < PW; ww++) c[k] += reinterpret_cast<const unsigned short*>(L.phist[ww])[k * 64 + lane];
      hv[k][1] = L.seg[0][k * 64 + lane];
    }
    const unsigned p0 = c[0] | (c[1] << 16), p1 = c[2] | (c[3] << 16);
    const unsigned i0 = wave_incl_scan(p0), i1 = wave_incl_scan(p1);
    const unsigned t0 = __builtin_amdgcn_readlane(i0, 63), t1 = __builtin_amdgcn_readlane(i1, 63);
    const unsigned s1 = t0 & 0xffff, s2 = s1 + (t0 >> 16), s3 = s2 + (t1 & 0xffff);
    const unsigned bs[4] = {(i0 - p0) & 0xffff, ((i0 - p0) >> 16) + s1, ((i1 - p1) & 0xffff) + s2, ((i1 - p1) >> 16) + s3};
#pragma unroll
    for (int k = 0; k < 4; k++) {
      hv[k][0] = bs[k] | (c[k] << 11) | (tl << 22);
      hv[k][1] += (unsigned)__umul24(c[k], tl & 255) << 24;
      hv[k][3] = th - hv[k][0] - hv[k][1];
    }
    asm volatile("" : "+v"(hv[0]), "+v"(hv[1]), "+v"(hv[2]), "+v"(hv[3]));
    while (true) {
      const unsigned gv = __hip_atomic_load(&L.go, __ATOMIC_RELAXED, __HIP_MEMORY_SCOPE_WORKGROUP);
      asm volatile("" ::: "memory");
#pragma unroll
      for (int k = 0; k < 4; k++) hv[k][2] = L.seg[1][k * 64 + lane];
      asm volatile("" ::: "memory");
      if (__builtin_amdgcn_readfirstlane(gv) == PW) break;
    }
#pragma unroll
    for (int k = 0; k < 4; k++) {
      hv[k][3] -= hv[k][2];
      __builtin_amdgcn_raw_buffer_store_b128(hv[k], wsr, ho0 + k * 1024, 0, 16);
    }
  } else if (w < PW) {
    const unsigned t8 = (unsigned)L.tag << 24;
    unsigned ca = 0, cb = 0, pa = 0, pb = 0, hh[PW][2];
#pragma unroll
    for (int ww = 0; ww < PW; ww++) {
      hh[ww][0] = L.phist[ww][lane];
      hh[ww][1] = L.phist[ww][64 + lane];
    }
    __builtin_amdgcn_sched_barrier(0);
#pragma unroll
    for (int ww = 0; ww < PW; ww++) {
      const unsigned ha = hh[ww][0], hb = hh[ww][1];
      pa = ww == w ? ca : pa;
      pb = ww == w ? cb : pb;
      ca += ha;
      cb += hb;
    }
    const unsigned t = __builtin_amdgcn_perm(cb, ca, 0x05040100) + __builtin_amdgcn_perm(cb, ca, 0x07060302);
    const unsigned inc = wave_incl_scan(t), ex = inc - t;
    L.pbase[w][lane] = (ex & 0xffff) * 0x10001u + (ca << 16) + pa;
    L.pbase[w][64 + lane] = ((ex >> 16) + (__builtin_amdgcn_readlane(inc, 63) & 0xffff)) * 0x10001u + (cb << 16) + pb;
    STAMP(6);
    unsigned b4[PK];
#pragma unroll
    for (int k = 0; k < PK; k++) b4[k] = *(__attribute__((address_space(3))) unsigned short*)(uintptr_t)bk[k];
    __builtin_amdgcn_sched_barrier(0);
    {
      unsigned t0, t1, t2, t3;
      asm volatile(
          "s_waitcnt vmcnt(0)\n"
          "v_add3_u32 %0, v100, v101, v108\n v_add3_u32 %1, v102, v103, v110\n v_add3_u32 %2, v104, v105, v112\n"
          "v_add3_u32 %3, v106, v107, v114\n"
          "v_add_u32 %0, %0, v109\n v_add_u32 %1, %1, v111\n v_add_u32 %2, %2, v113\n v_add_u32 %3, %3, v115\n"
          "ds_add_u32 %8, %0 offset:1024\n ds_add_u32 %9, %1 offset:1024\n ds_add_u32 %10, %2 offset:1024\n"
          "ds_add_u32 %11, %3 offset:1024\n"
          "s_mov_b64 exec, 1\n ds_add_u32 %12, %13\n s_mov_b64 exec, -1"
          : "=&v"(t0), "=&v"(t1), "=&v"(t2), "=&v"(t3), "+{v[100:103]}"(y0), "+{v[104:107]}"(y1), "+{v[108:111]}"(f0),
            "+{v[112:115]}"(f1)
          : "v"(sa[0]), "v"(sa[1]), "v"(sa[2]), "v"(sa[3]), "v"(lds_addr(&L.go)), "v"(1) : "memory");
    }
#pragma unroll
    for (int k = 0; k < PK; k++) {
      pr[k] = min(pr[k] + b4[k], CH - 1u);
      L.sk[pr[k]] = ky[k] | t8;
      pr[k] = lds_addr(&L.sp[pr[k]]);
    }
    asm volatile(
        "ds_write2_b64 %0, v[100:101], v[108:109] offset1:1\n ds_write2_b64 %1, v[102:103], v[110:111] offset1:1\n"
        "ds_write2_b64 %2, v[104:105], v[112:113] offset1:1\n ds_write2_b64 %3, v[106:107], v[114:115] offset1:1\n"
        "s_waitcnt lgkmcnt(0)"
        :: "v"(pr[0]), "v"(pr[1]), "v"(pr[2]), "v"(pr[3]),
           "{v[100:103]}"(y0), "{v[104:107]}"(y1), "{v[108:111]}"(f0), "{v[112:115]}"(f1) : "memory");
  } else {
    if (w == PW) {
      asm volatile("s_waitcnt   vmcnt(0)" : "+v"(cv), "+v"(wq4[0]), "+v"(wq4[1]), "+v"(wq4[2]), "+v"(wq4[3]));
      reinterpret_cast<unsigned short*>(L.ct)[(lane & 3) * 16 + (lane >> 2)] = cv;
      L.ct[8 + lane] = u32x4{__builtin_amdgcn_perm(wq4[1], wq4[0], 0x05040100), __builtin_amdgcn_perm(wq4[3], wq4[2], 0x05040100),
                             __builtin_amdgcn_perm(wq4[1], wq4[0], 0x07060302), __builtin_amdgcn_perm(wq4[3], wq4[2], 0x07060302)};
    }
#pragma unroll
    for (int i = 0; i < 3; i++)
      if (i * 384 + zt < SW * HS / 4) reinterpret_cast<u32x4*>(L.hist)[i * 384 + zt] = u32x4{0, 0, 0, 0};
    __builtin_amdgcn_s_setprio(3);
  }
  STAMP(7);
  __syncthreads();
  STAMP(27);
  const int st = tid - SW * 64;
  {
    const u32x4 v0 = reinterpret_cast<const u32x4*>(L.sk)[tid], v1 = reinterpret_cast<const u32x4*>(L.sk)[MT + tid];
    __builtin_amdgcn_raw_buffer_store_b128(v0, wsr, so0, 0, 16);
    __builtin_amdgcn_raw_buffer_store_b128(v1, wsr, so1, 0, 16);
    if (w == PW)
      __builtin_amdgcn_raw_buffer_store_b128(reinterpret_cast<const u32x4*>(L.sk)[2 * MT + lane], wsr, so1 + (MT - PT) * 16, 0, 16);
  }
  STAMP(8);
  __builtin_amdgcn_s_barrier();
  STAMP(9);
  const unsigned long long epoch = L.tag;
  const unsigned tagl = __builtin_amdgcn_readfirstlane((unsigned)epoch);
  const unsigned tag1 = __builtin_amdgcn_readfirstlane((unsigned)(epoch >> 32));
  const int row = lane & 15, kq = lane >> 4;
  if (w >= SW) {
    __builtin_amdgcn_s_setprio(0);
#pragma unroll 1
    for (int q = st + TB1 * 384; q < 2048; q += MT - SW * 64) table(q);
    __builtin_amdgcn_s_setprio(3);
    constexpr int HR = (NPB - RKL * SW) / (MW - SW);
    const int id = (node0 + RKL * SW + (w - SW) * HR + lane) & 63;
    __builtin_amdgcn_s_barrier();
    __builtin_amdgcn_s_sleep(PFS);
    warm(w - SW, PFN);
    __syncthreads();
    {
      const int dg = L.deg[lane], dm = L.deg[id];
      int dn[HR];
#pragma unroll
      for (int i = 0; i < HR; i++) dn[i] = __builtin_amdgcn_readlane(dm, i);
      __builtin_amdgcn_sched_barrier(0);
      unsigned long long m[HR];
#pragma unroll
      for (int i = 0; i < HR; i++) m[i] = __ballot(dg > dn[i]);
      __builtin_amdgcn_sched_barrier(0);
      unsigned r = 63;
#pragma unroll
      for (int i = 0; i < HR; i++) asm("v_writelane_b32 %0, %1, %2" : "+v"(r) : "s"(__popcll(m[i])), "n"(i));
      L.byrank[r] = id * 4;
    }
    __syncthreads();
    if (bucket == 0 && tid == MT - 1)
      __builtin_amdgcn_raw_buffer_store_b64(u32x2{tagl, tag1}, wsr, WS_EPOCH, 0, 16);
    __builtin_amdgcn_s_barrier();
    __builtin_amdgcn_s_barrier();
    __builtin_amdgcn_s_barrier();
    __builtin_amdgcn_s_setprio(0);
    __builtin_amdgcn_s_sleep(WSL);
    const u32x4* wp = reinterpret_cast<const u32x4*>(wmix) + ((w - SW) * 32 + (lane >> 4) * 8) * 16 + (lane & 15);
    u32x4 q[8];
#pragma unroll
    for (int j = 0; j < 8; j++) q[j] = wp[j * 16];
    u32x4* fp = &L.wf[((w - SW) * 8 + ((lane >> 2) & 3) * 2) * 64 + (lane & 48) + (lane & 3) * 4];
#pragma unroll
    for (int i = 0; i < 4; i++) {
      fp[i] = u32x4{__builtin_amdgcn_perm(q[1][i], q[0][i], 0x05040100), __builtin_amdgcn_perm(q[3][i], q[2][i], 0x05040100),
                    __builtin_amdgcn_perm(q[5][i], q[4][i], 0x05040100), __builtin_amdgcn_perm(q[7][i], q[6][i], 0x05040100)};
      fp[64 + i] = u32x4{__builtin_amdgcn_perm(q[1][i], q[0][i], 0x07060302), __builtin_amdgcn_perm(q[3][i], q[2][i], 0x07060302),
                         __builtin_amdgcn_perm(q[5][i], q[4][i], 0x07060302), __builtin_amdgcn_perm(q[7][i], q[6][i], 0x07060302)};
    }
    asm volatile("" ::: "memory");
    if (lane == 0) atomicAdd(&L.wrdy, 1u);
    while (lds_ld(&L.wrdy) != MW - SW) __builtin_amdgcn_s_sleep(8);
  } else {
    L.ri[w][lane] = u32x4{0x20000000u, 0x20000000u, 0x20000000u, 0x20000000u};
    const unsigned long long t0 = w & 1 ? 0x26796281267d7281ull : 0x277dc200272c8180ull, t1 = w & 1 ? 0x246942c324755245ull : 0x25755242217984c2ull;
    const unsigned long long t2 = w & 1 ? 0x22613345226d6483ull : 0x236d130423694307ull, t3 = w & 1 ? 0x206503c725710384ull : 0x215d2386206513cdull;
    const unsigned long long t = w & 4 ? (w & 2 ? t3 : t2) : (w & 2 ? t1 : t0);
    const unsigned rks = nn == NPB ? t >> 32 : t;
    const int start = w * CAPW;
    unsigned sop = lds_addr(&L.so[start + (lane & 15)]);
    unsigned s4p = lds_addr(&L.so[start + (lane >> 2)]);
    unsigned yp = lds_addr(&L.pay[start + (lane >> 2)]);
    unsigned ep = lds_addr(&L.pay[start + (lane & 15)]) + 8;
    u32x4 cg0 = L.ct[(lane & 3) * 2];
    const u32x4 cg1 = L.ct[(lane & 3) * 2 + 1], wq = L.ct[8 + lane];
    const unsigned rp = lds_addr(&L.pay[start]) + kq * 256 + row * 4;
    unsigned pg = lds_addr(&L.prog[w]), oo = node0 * 1024 + row * 16, slowv;
    u32x16 cs = {ho0,
                 (unsigned)(((lane >> 1) * SW + w) * CH + (lane & 1)) * 4,
                 lds_addr(L.ri[w]) + (lane & 1) * 4, lds_addr(L.ri[w]) + lane * 4, (lane & 1) - 1u, lane & 1u,
                 lds_addr(&L.hist[tid]), lds_addr(&L.byrank[(rks >> (min(lane & 7, NS - 1) * 6)) & 63]),
                 lds_addr(&L.byrank[(rks >> (kq * 6)) & 63]), lds_addr(&L.hist[SW * HS + (lane >> 3)]),
                 (start * 4 + ((lane & 7) << 13)) * 0x10001u, (unsigned)(start + lane) * 4,
                 lds_addr(&L.nl[w * 8 + (lane & 7)]), lane * 4u, ((node0 + w * RKL + lane) & 63) * 4u, 0};
    asm volatile(STREAM_ASM
                 : [sl] "=s"(slowv), "+{v12}"(sop), "+{v13}"(s4p), "+{v14}"(yp), "+{v15}"(ep), "+{v[136:151]}"(cs),
                   "+{v[20:23]}"(cg0), "+{v110}"(pg), "+{v159}"(oo)
                 : "{v8}"(lane * 16), "{v16}"(0xfffcu), "{v17}"((1u << (lane & 3)) >> 1),
                   "{v[24:27]}"(cg1), "{v[28:31]}"(wq), "{v[152:155]}"(u32x4{rp, rp + 64, rp + 128, rp + 192}),
                   "{v156}"(lds_addr(&L.row5[w * 64]) + kq * 256 + row * 4),
                   "{v157}"(lds_addr(&L.pay[start + (row >> 2) * 64 + (row & 3) * 4 + kq])), "{v158}"(lds_addr(&L.wf[lane])),
                   [xs] "s"(make_srd(x, N * 1024)), [os] "s"(make_srd(out, (node0 + nn) * 1024)), [ws] "s"(wsd),
                   [ho] "s"(WS_OFF + bucket * 16), [rk] "s"(WS_RECK), [rp] "s"(WS_RECP), [t0] "s"(tagl << 22), [t1] "s"(tag1),
                   [wb] "s"(w * (HS * 4)), [n0] "s"(node0 * 4), [bit] "s"(1u << w), [tb] "n"(offsetof(Lds, sg)),
                   [rd] "n"(offsetof(Lds, rdone)), [wr] "n"(offsetof(Lds, wrdy)), [fl] "n"(offsetof(Lds, flag)),
                   [hi] "n"(offsetof(Lds, hist)), [to] "n"(offsetof(Lds, hist) + SW * HS * 4), [dg] "n"(offsetof(Lds, deg)),
                   [br] "n"(offsetof(Lds, byrank)), [so] "n"(offsetof(Lds, so)), [pa] "n"(offsetof(Lds, pay))
                 : STREAM_CLOBBERS);
  }
  {
  int lane;
  asm volatile("v_mbcnt_lo_u32_b32 %0, -1, 0\n v_mbcnt_hi_u32_b32 %0, -1, %0" : "=v"(lane));
  __builtin_assume(lane >= 0 && lane < 64);
  const int row = lane & 15, kq = lane >> 4;
  const int ht = (w - SW) >> 1;
  u32x4* rows = w < SW ? &L.pay[w * CAPW] : &L.row5[ht * 256];
  const u32x4* wfp = &L.wf[(w < SW ? 0 : (w & 1) * 256) + lane];
  auto wfrag = [&](int i) {
    return __builtin_bit_cast(s16x8, wfp[(((i >> 1) & 3) * 8 + (i >> 3) * 2 + (i & 1)) * 64]);
  };
  unsigned oo = (node0 + ((L.nl[w < SW ? w * 8 + kq : (ht * 4 + kq) * 8 + 4] / 4 - node0) & 63)) * 1024 + row * 16 + (w < SW ? 0 : (w & 1) * 512);
  s16x8 wv[16];
  if (w >= SW) {
#pragma unroll
    for (int i = 0; i < 16; i++) wv[i] = wfrag(i);
    const unsigned need = 15u << (ht * 4);
    while (lds_ld(&L.prog[ht * 4]) > 4 && lds_ld(&L.rdone) == 0) __builtin_amdgcn_s_sleep(HSL);
    while ((lds_ld(&L.rdone) & need) != need) __builtin_amdgcn_s_sleep(2);
  } else {
#pragma unroll 1
    for (int it = 0; it < 2 * NS; it++) {
      const int sl = it >> 1, slice = it & 1, ch = slice * 64 + lane;
      const unsigned dl = (L.nl[w * 8 + sl] / 4 - node0) & 63;
      float acc[4] = {0.f, 0.f, 0.f, 0.f};
#pragma unroll 1
      for (int eb = 0; eb < E; eb += 64) {
        unsigned long long m = __ballot(eb + lane < E && (unsigned)dst[min(eb + lane, E - 1)] == node0 + dl);
#pragma unroll 1
        for (; m; m &= m - 1) {
          const int e = eb + __builtin_ctzll(m);
          const u32x2 ry = Y[e], rf = ef[e];
          float yy[4] = {bf2f(ry[0] & 0xffff), bf2f(ry[0] >> 16), bf2f(ry[1] & 0xffff), bf2f(ry[1] >> 16)};
          float ee[4] = {bf2f(rf[0] & 0xffff), bf2f(rf[0] >> 16), bf2f(rf[1] & 0xffff), bf2f(rf[1] >> 16)};
          float z = 0.f;
#pragma unroll
          for (int k = 0; k < 4; k++) z += ee[k] * bf2f(wrad[k * C_ + ch]);
          float R = z / (1.f + __expf(-z));
          const unsigned short* xr = x + (size_t)src[e] * 512 + ch * 4;
#pragma unroll 1
          for (int o = 0; o < 4; o++) {
            float tp = 0.f;
#pragma unroll 1
            for (int i = 0; i < 4; i++) {
              float A = 0.f;
#pragma unroll
              for (int f = 0; f < 4; f++) A += yy[f] * bf2f(cg[i * 16 + f * 4 + o]);
              tp += bf2f(xr[i]) * bf2f(f2bf(A));
            }
            float v = R * tp;
            acc[0] += o == 0 ? v : 0.f;
            acc[1] += o == 1 ? v : 0.f;
            acc[2] += o == 2 ? v : 0.f;
            acc[3] += o == 3 ? v : 0.f;
          }
        }
      }
      u32x4* rw = sl < 4 ? &rows[sl * 64] : &L.row5[w * 64];
      unsigned short* ar = reinterpret_cast<unsigned short*>(&rw[slice * 32]) + (lane >> 5) * 128 + (lane & 31);
#pragma unroll
      for (int o = 0; o < 4; o++) ar[o * 32] = f2bf(acc[o]);
    }
    asm volatile("" ::: "memory");
    if (lane == 0) atomicOr(&L.rdone, 1u << w);
    while (lds_ld(&L.wrdy) != MW - SW) __builtin_amdgcn_s_sleep(1);
    asm volatile("" ::: "memory");
#pragma unroll
    for (int i = 0; i < 16; i++) wv[i] = wfrag(i);
  }
  asm volatile("" ::: "memory");
  const u32x4* op = &rows[(row >> 2) * 64 + (row & 3) * 4 + kq];
  s16x8 f[4];
#pragma unroll
  for (int k = 0; k < 4; k++) f[k] = __builtin_bit_cast(s16x8, op[k * 16]);
  const auto ors = __builtin_amdgcn_make_buffer_rsrc((void*)out, 0, (node0 + nn) * 1024, 0x00020000);
  auto pair = [&](int m, bool more) {
    f32x4 a = {0.f, 0.f, 0.f, 0.f}, b = {0.f, 0.f, 0.f, 0.f};
#pragma unroll
    for (int k = 0; k < 4; k++) {
      const int i = (m & 1) * 8 + 2 * k;
      a = __builtin_amdgcn_mfma_f32_16x16x32_bf16(f[k], wv[i], a, 0, 0, 0);
      b = __builtin_amdgcn_mfma_f32_16x16x32_bf16(f[k], wv[i + 1], b, 0, 0, 0);
      if (more) {
        wv[i] = wfrag(i + 16);
        wv[i + 1] = wfrag(i + 17);
      }
    }
    __builtin_amdgcn_raw_buffer_store_b128(
        u32x4{pk2bf(a[0], a[1]), pk2bf(a[2], a[3]), pk2bf(b[0], b[1]), pk2bf(b[2], b[3])}, ors, oo + m * 256, 0, OUTAUX);
  };
  if (w < SW) {
    pair(0, true);
    pair(1, true);
    oo += 512;
  }
  pair(0, false);
  pair(1, false);
  }
  asm volatile(".Ltfn_p1:");
  STAMP(26);
  TFLUSH;
}

extern "C" void kernel_launch(void* const* d_in, const int* in_sizes, int n_in, void* d_out, int out_size,
                              void* d_ws, size_t ws_size, hipStream_t stream) {
  const unsigned short* x = (const unsigned short*)d_in[0];
  const int* src = (const int*)d_in[1];
  const int* dst = (const int*)d_in[2];
  const u32x2* Y = (const u32x2*)d_in[3];
  const u32x2* ef = (const u32x2*)d_in[4];
  const unsigned short* wrad = (const unsigned short*)d_in[5];
  const unsigned short* cg = (const unsigned short*)d_in[6];
  const unsigned short* wmix = (const unsigned short*)d_in[7];
  const int N = in_sizes[0] / 512;
  const int E = in_sizes[1];
  const int CE = ((E + NBMAX - 1) / NBMAX + 1) & ~1;
  const unsigned M2 = ((unsigned)NBMAX << 22) / (unsigned)N;
  tfn_main_kernel<<<256, MT, 0, stream>>>(dst, E, 0x20000, src, CE, 0x20000, Y, N, 0x20000, ef, M2, 0x20000, x, (char*)d_ws,
                                           (u32x4*)d_out, wrad, cg, wmix);
}
```

```cpp
#include <hip/hip_runtime.h>
#include <hip/hip_bf16.h>
#include <stddef.h>
#include <stdint.h>

typedef __attribute__((ext_vector_type(2))) float f32x2;
typedef __attribute__((ext_vector_type(4))) float f32x4;
typedef __attribute__((ext_vector_type(8))) float f32x8;
typedef __attribute__((ext_vector_type(16))) float f32x16;
typedef __attribute__((ext_vector_type(8))) short s16x8;
typedef __attribute__((ext_vector_type(2))) unsigned int u32x2;
typedef __attribute__((ext_vector_type(3))) unsigned int u32x3;
typedef __attribute__((ext_vector_type(4))) unsigned int u32x4;
typedef __attribute__((ext_vector_type(8))) unsigned int u32x8;
typedef __attribute__((ext_vector_type(16))) unsigned int u32x16;
typedef __attribute__((ext_vector_type(2))) __bf16 bf16x2;

#define TDECL
#define STAMP(i)
#define CSTAMP(i)
#define TFLUSH

#define STREAM_ASM \
  "s_mov_b32 s63, 0x10000\n" \
  "Lh%=:\n" \
  "buffer_load_dwordx4 v[72:75], v136, %[ws], %[ho] offen sc1\n" \
  "v_mov_b32 v117, 0\n" \
  "v_mov_b32 v116, 64\n" \
  "v_mov_b32 v87, 0\n" \
  "v_mov_b64 v[44:45], 0\n" \
  "v_mov_b64 v[46:47], 0\n" \
  "v_mov_b32 v118, 0x7fc\n" \
  "s_waitcnt vmcnt(0)\n" \
  "v_add3_u32 v108, v72, v73, v74\n" \
  "v_xor_b32 v109, %[t0], v72\n" \
  "v_add_u32 v108, v108, v75\n" \
  "v_cmp_lt_u32 vcc, 0x3fffff, v109\n" \
  "v_cmp_ne_u32_e64 s[74:75], %[t1], v108\n" \
  "s_or_b64 vcc, vcc, s[74:75]\n" \
  "s_cbranch_scc1 Lhr%=\n" \
  "s_mov_b64 exec, -1\n" \
  "v_bfe_u32 v108, v72, 11, 11\n" \
  "v_and_b32 v109, v140, v108\n" \
  "v_and_b32 v112, 0x7ff, v72\n" \
  "v_sub_u32 v111, v108, v141\n" \
  "v_add_u32_dpp v109, v109, v109 row_shr:1 row_mask:0xf bank_mask:0xf bound_ctrl:1\n" \
  "v_lshl_add_u32 v112, v112, 2, v137\n" \
  "v_and_b32 v114, v140, v73\n" \
  "v_add_u32_dpp v109, v109, v109 row_shr:2 row_mask:0xf bank_mask:0xf bound_ctrl:1\n" \
  "v_and_b32 v115, v140, v74\n" \
  "v_sub_u32 v114, 0, v114\n" \
  "v_add_u32_dpp v109, v109, v109 row_shr:4 row_mask:0xf bank_mask:0xf bound_ctrl:1\n" \
  "v_sub_u32 v115, 0, v115\n" \
  "s_nop 0\n" \
  "v_add_u32_dpp v109, v109, v109 row_shr:8 row_mask:0xf bank_mask:0xf bound_ctrl:1\n" \
  "s_nop 1\n" \
  "v_add_u32_dpp v109, v109, v109 row_bcast:15 row_mask:0xa bank_mask:0xf\n" \
  "s_nop 1\n" \
  "v_add_u32_dpp v109, v109, v109 row_bcast:31 row_mask:0xc bank_mask:0xf\n" \
  "v_sub_u32 v108, v109, v108\n" \
  "v_readlane_b32 s62, v109, 63\n" \
  "v_lshl_add_u32 v113, v108, 2, v138\n" \
  "s_cmpk_gt_u32 s62, 0x100\n" \
  "s_cbranch_scc1 Lover%=\n" \
  "Lw%=:\n" \
  "v_cmpx_lt_i32 vcc, 0, v111\n" \
  "ds_write_b32 v113, v112 offset:0\n" \
  "v_add_u32 v112, 8, v112\n" \
  "v_cmpx_lt_i32 vcc, 2, v111\n" \
  "ds_write_b32 v113, v112 offset:8\n" \
  "v_add_u32 v112, 8, v112\n" \
  "v_cmpx_lt_i32 vcc, 4, v111\n" \
  "ds_write_b32 v113, v112 offset:16\n" \
  "v_add_u32 v112, 8, v112\n" \
  "v_cmpx_lt_i32 vcc, 6, v111\n" \
  "ds_write_b32 v113, v112 offset:24\n" \
  "v_add_u32 v112, 8, v112\n" \
  "v_cmpx_lt_i32 vcc, 8, v111\n" \
  "ds_write_b32 v113, v112 offset:32\n" \
  "v_add_u32 v112, 8, v112\n" \
  "s_cbranch_execz Lwd%=\n" \
  "v_cmpx_lt_i32 vcc, 10, v111\n" \
  "ds_write_b32 v113, v112 offset:40\n" \
  "v_add_u32 v112, 8, v112\n" \
  "s_cbranch_execz Lwd%=\n" \
  "v_cmpx_lt_i32 vcc, 12, v111\n" \
  "ds_write_b32 v113, v112 offset:48\n" \
  "v_add_u32 v112, 8, v112\n" \
  "s_cbranch_execz Lwd%=\n" \
  "v_cmpx_lt_i32 vcc, 14, v111\n" \
  "ds_write_b32 v113, v112 offset:56\n" \
  "v_add_u32 v112, 8, v112\n" \
  "v_add_u32 v113, 64, v113\n" \
  "v_subrev_u32 v111, 16, v111\n" \
  "s_branch Lw%=\n" \
  "Lwd%=:\n" \
  "s_mov_b64 exec, -1\n" \
  "Lld%=:\n" \
  "ds_read2st64_b32 v[76:77], v139 offset1:1\n" \
  "ds_read2st64_b32 v[78:79], v139 offset0:2 offset1:3\n" \
  "s_mov_b32 s63, 0x10000\n" \
  "s_waitcnt lgkmcnt(0)\n" \
  "buffer_load_dword v84, v76, %[ws], %[rk] offen sc1\n" \
  "buffer_load_dword v85, v77, %[ws], %[rk] offen sc1\n" \
  "buffer_load_dword v86, v78, %[ws], %[rk] offen sc1\n" \
  "s_cmpk_le_u32 s62, 0xc0\n" \
  "s_cbranch_scc1 Lka%=\n" \
  "buffer_load_dword v87, v79, %[ws], %[rk] offen sc1\n" \
  "Lka%=:\n" \
  "v_lshlrev_b32 v80, 2, v76\n" \
  "v_lshlrev_b32 v81, 2, v77\n" \
  "v_lshlrev_b32 v82, 2, v78\n" \
  "v_lshlrev_b32 v83, 2, v79\n" \
  "s_barrier\n" \
  "s_cmpk_le_u32 s62, 0xc0\n" \
  "s_cbranch_scc1 Lpa%=\n" \
  "buffer_load_dwordx4 v[44:47], v83, %[ws], %[rp] offen sc1\n" \
  "Lpa%=:\n" \
  "buffer_load_dwordx4 v[32:35], v80, %[ws], %[rp] offen sc1\n" \
  "buffer_load_dwordx4 v[36:39], v81, %[ws], %[rp] offen sc1\n" \
  "buffer_load_dwordx4 v[40:43], v82, %[ws], %[rp] offen sc1\n" \
  "v_cmp_le_i32_e64 s[66:67], 0, v80\n" \
  "v_cmp_le_i32_e64 s[68:69], 0, v81\n" \
  "v_cmp_le_i32_e64 s[70:71], 0, v82\n" \
  "v_cmp_le_i32_e64 s[72:73], 0, v83\n" \
  "v_mov_b64 v[48:49], 0\n" \
  "v_mov_b64 v[50:51], 0\n" \
  "v_mov_b64 v[52:53], 0\n" \
  "v_mov_b64 v[54:55], 0\n" \
  "v_mov_b64 v[56:57], 0\n" \
  "v_mov_b64 v[58:59], 0\n" \
  "v_mov_b64 v[60:61], 0\n" \
  "v_mov_b64 v[62:63], 0\n" \
  "v_mov_b64 v[64:65], 0\n" \
  "v_mov_b64 v[66:67], 0\n" \
  "v_mov_b64 v[68:69], 0\n" \
  "v_mov_b64 v[70:71], 0\n" \
  "s_waitcnt vmcnt(3)\n" \
  "Lk%=:\n" \
  "v_add3_u32 v108, v84, v85, v86\n" \
  "v_add3_u32 v108, v108, v87, v114\n" \
  "v_lshlrev_b32 v88, 1, v84\n" \
  "v_lshlrev_b32 v92, 4, v84\n" \
  "v_add_u32_dpp v108, v108, v108 row_shr:1 row_mask:0xf bank_mask:0xf bound_ctrl:1\n" \
  "v_lshrrev_b32 v104, 2, v84\n" \
  "v_and_or_b32 v88, v88, v118, %[wb]\n" \
  "v_add_u32_dpp v108, v108, v108 row_shr:2 row_mask:0xf bank_mask:0xf bound_ctrl:1\n" \
  "v_lshlrev_b32_e64 v100, v92, 4\n" \
  "v_and_b32 v104, 0xfc, v104\n" \
  "v_add_u32_dpp v108, v108, v108 row_shr:4 row_mask:0xf bank_mask:0xf bound_ctrl:1\n" \
  "v_lshlrev_b32 v89, 1, v85\n" \
  "v_lshlrev_b32 v93, 4, v85\n" \
  "v_add_u32_dpp v108, v108, v108 row_shr:8 row_mask:0xf bank_mask:0xf bound_ctrl:1\n" \
  "v_lshrrev_b32 v105, 2, v85\n" \
  "v_and_or_b32 v89, v89, v118, %[wb]\n" \
  "v_add_u32_dpp v108, v108, v108 row_bcast:15 row_mask:0xa bank_mask:0xf\n" \
  "v_lshlrev_b32_e64 v101, v93, 4\n" \
  "v_and_b32 v105, 0xfc, v105\n" \
  "v_add_u32_dpp v108, v108, v108 row_bcast:31 row_mask:0xc bank_mask:0xf\n" \
  "v_lshlrev_b32 v90, 1, v86\n" \
  "v_readlane_b32 s65, v108, 63\n" \
  "v_lshlrev_b32 v94, 4, v86\n" \
  "v_lshrrev_b32 v106, 2, v86\n" \
  "v_and_or_b32 v90, v90, v118, %[wb]\n" \
  "v_lshlrev_b32_e64 v102, v94, 4\n" \
  "v_and_b32 v106, 0xfc, v106\n" \
  "s_cmp_lg_u32 s65, 0\n" \
  "s_cbranch_scc1 Lkr%=\n" \
  "Lkc%=:\n" \
  "s_mov_b64 exec, s[66:67]\n" \
  "ds_add_rtn_u32 v96, v88, v100 offset:%[hi]\n" \
  "ds_add_u32 v104, v116 offset:%[dg]\n" \
  "s_mov_b64 exec, s[68:69]\n" \
  "ds_add_rtn_u32 v97, v89, v101 offset:%[hi]\n" \
  "ds_add_u32 v105, v116 offset:%[dg]\n" \
  "s_mov_b64 exec, s[70:71]\n" \
  "ds_add_rtn_u32 v98, v90, v102 offset:%[hi]\n" \
  "ds_add_u32 v106, v116 offset:%[dg]\n" \
  "s_cmpk_gt_u32 s62, 0xc0\n" \
  "s_mov_b64 exec, -1\n" \
  "s_cbranch_scc1 Lc3%=\n" \
  "Lc4%=:\n" \
  "v_and_b32 v100, v118, v88\n" \
  "v_and_b32 v101, v118, v89\n" \
  "v_and_b32 v102, v118, v90\n" \
  "s_waitcnt lgkmcnt(0)\n" \
  "s_barrier\n" \
  "ds_read_b32 v108, v149 offset:%[dg]\n" \
  "ds_read_b32 v109, v150 offset:%[dg]\n" \
  "ds_read2st64_b32 v[76:77], v142 offset0:0 offset1:8\n" \
  "ds_read2st64_b32 v[78:79], v142 offset0:16 offset1:24\n" \
  "ds_read2st64_b32 v[104:105], v142 offset0:32 offset1:40\n" \
  "ds_read2st64_b32 v[106:107], v142 offset0:48 offset1:56\n" \
  "v_and_b32 v84, 0xfffc00, v84\n" \
  "v_and_b32 v85, 0xfffc00, v85\n" \
  "v_and_b32 v86, 0xfffc00, v86\n" \
  "v_and_b32 v87, 0xfffc00, v87\n" \
  "v_mov_b32 v111, 0\n" \
  "v_mov_b32 v112, 63\n" \
  "s_waitcnt lgkmcnt(4)\n" \
  "v_readlane_b32 s74, v109, 0\n" \
  "v_readlane_b32 s76, v109, 1\n" \
  "v_readlane_b32 s78, v109, 2\n" \
  "s_nop 0\n" \
  "v_cmp_lt_i32_e64 s[74:75], s74, v108\n" \
  "v_cmp_lt_i32_e64 s[76:77], s76, v108\n" \
  "v_cmp_lt_i32_e64 s[78:79], s78, v108\n" \
  "s_bcnt1_i32_b64 s74, s[74:75]\n" \
  "s_bcnt1_i32_b64 s76, s[76:77]\n" \
  "s_bcnt1_i32_b64 s78, s[78:79]\n" \
  "v_writelane_b32 v112, s74, 0\n" \
  "v_writelane_b32 v112, s76, 1\n" \
  "v_writelane_b32 v112, s78, 2\n" \
  "v_lshlrev_b32 v112, 2, v112\n" \
  "s_waitcnt lgkmcnt(3)\n" \
  "ds_write2st64_b32 v142, v117, v76 offset1:8\n" \
  "v_add_u32 v77, v77, v76\n" \
  "s_waitcnt lgkmcnt(3)\n" \
  "v_add_u32 v78, v78, v77\n" \
  "ds_write2st64_b32 v142, v77, v78 offset0:16 offset1:24\n" \
  "v_add_u32 v79, v79, v78\n" \
  "s_waitcnt lgkmcnt(3)\n" \
  "v_add_u32 v104, v104, v79\n" \
  "ds_write2st64_b32 v142, v79, v104 offset0:32 offset1:40\n" \
  "v_add_u32 v105, v105, v104\n" \
  "s_waitcnt lgkmcnt(3)\n" \
  "v_add_u32 v106, v106, v105\n" \
  "ds_write2st64_b32 v142, v105, v106 offset0:48 offset1:56\n" \
  "v_add_u32 v107, v107, v106\n" \
  "ds_write_b32 v142, v107 offset:16384\n" \
  "ds_write_b32 v112, v150 offset:%[br]\n" \
  "s_waitcnt lgkmcnt(0)\n" \
  "s_barrier\n" \
  "ds_read_b32 v108, v143\n" \
  "ds_read_b32 v109, v144\n" \
  "s_mov_b32 s74, 0x1f1f1f1f\n" \
  "s_mov_b32 s75, 0x1f1f1f1f\n" \
  "s_waitcnt lgkmcnt(1)\n" \
  "v_lshl_add_u32 v76, v108, 3, v145\n" \
  "s_mov_b64 exec, s[74:75]\n" \
  "ds_read_b32 v111, v76\n" \
  "s_mov_b64 exec, -1\n" \
  "ds_write_b32 v148, v108\n" \
  "s_waitcnt lgkmcnt(1)\n" \
  "v_add_u32_dpp v112, v111, v111 row_shr:1 row_mask:0xf bank_mask:0xf bound_ctrl:1\n" \
  "v_subrev_u32 v109, %[n0], v109\n" \
  "v_and_b32 v109, 0xfc, v109\n" \
  "v_add_u32_dpp v112, v112, v112 row_shr:2 row_mask:0xf bank_mask:0xf bound_ctrl:1\n" \
  "v_lshl_add_u32 v159, v109, 8, v159\n" \
  "v_sub_u32 v77, v146, v111\n" \
  "v_add_u32_dpp v112, v112, v112 row_shr:4 row_mask:0xf bank_mask:0xf bound_ctrl:1\n" \
  "s_nop 1\n" \
  "v_add_u32_dpp v112, v112, v112 row_shr:8 row_mask:0xf bank_mask:0xf bound_ctrl:1\n" \
  "s_nop 1\n" \
  "v_add_u32_dpp v112, v112, v112 row_bcast:15 row_mask:0xa bank_mask:0xf\n" \
  "s_nop 1\n" \
  "v_add_u32_dpp v112, v112, v112 row_bcast:31 row_mask:0xc bank_mask:0xf\n" \
  "v_sub_u32 v111, v112, v111\n" \
  "v_readlane_b32 s65, v112, 63\n" \
  "v_mov_b32_dpp v78, v112 row_newbcast:7 row_mask:0xf bank_mask:0x3\n" \
  "v_mov_b32_dpp v79, v111 row_newbcast:0 row_mask:0xf bank_mask:0x3\n" \
  "v_add_u32 v77, v77, v112\n" \
  "v_mov_b32_dpp v78, v112 row_newbcast:15 row_mask:0xf bank_mask:0xc\n" \
  "v_mov_b32_dpp v79, v111 row_newbcast:8 row_mask:0xf bank_mask:0xc\n" \
  "s_and_b32 s64, s65, 0xffff\n" \
  "v_lshl_add_u32 v77, v78, 16, v77\n" \
  "s_lshr_b32 s65, s65, 16\n" \
  "v_add_u32_sdwa v77, v77, v79 dst_sel:DWORD dst_unused:UNUSED_PAD src0_sel:DWORD src1_sel:WORD_1\n" \
  "s_add_u32 s64, s64, s65\n" \
  "s_mov_b64 exec, s[74:75]\n" \
  "ds_write_b32 v76, v77\n" \
  "s_mov_b64 exec, -1\n" \
  "s_cmpk_gt_u32 s64, 864\n" \
  "s_cbranch_scc1 Llo%=\n" \
  "Llc%=:\n" \
  "s_waitcnt lgkmcnt(0)\n" \
  "s_barrier\n" \
  "ds_read_b32 v109, v117 offset:%[fl]\n" \
  "ds_read_b32 v76, v88 offset:%[hi]\n" \
  "ds_read_b32 v104, v100 offset:%[to]\n" \
  "ds_read_b32 v77, v89 offset:%[hi]\n" \
  "ds_read_b32 v105, v101 offset:%[to]\n" \
  "ds_read_b32 v78, v90 offset:%[hi]\n" \
  "ds_read_b32 v106, v102 offset:%[to]\n" \
  "v_add_u32 v113, s64, v147\n" \
  "v_bfrev_b32 v111, 1\n" \
  "s_max_u32 s60, s64, 4\n" \
  "s_sub_u32 s60, s60, 4\n" \
  "s_lshr_b32 s60, s60, 4\n" \
  "s_waitcnt lgkmcnt(4)\n" \
  "v_readfirstlane_b32 s61, v109\n" \
  "v_add3_u32 v88, v76, v104, v96\n" \
  "v_lshrrev_b32 v88, v92, v88\n" \
  "v_bfe_u32 v72, v88, 13, 3\n" \
  "v_and_b32 v88, 0x1ffc, v88\n" \
  "v_or_b32 v72, v84, v72\n" \
  "s_cmp_lg_u32 s61, 0\n" \
  "s_cbranch_scc1 Lslow%=\n" \
  "s_waitcnt lgkmcnt(0)\n" \
  "v_add3_u32 v89, v77, v105, v97\n" \
  "v_add3_u32 v90, v78, v106, v98\n" \
  "v_lshrrev_b32 v89, v93, v89\n" \
  "v_lshrrev_b32 v90, v94, v90\n" \
  "v_bfe_u32 v73, v89, 13, 3\n" \
  "v_bfe_u32 v74, v90, 13, 3\n" \
  "v_and_b32 v89, 0x1ffc, v89\n" \
  "v_and_b32 v90, 0x1ffc, v90\n" \
  "v_or_b32 v73, v85, v73\n" \
  "v_or_b32 v74, v86, v74\n" \
  "s_mov_b64 exec, s[66:67]\n" \
  "ds_write_b32 v88, v72 offset:%[so]\n" \
  "s_mov_b64 exec, s[68:69]\n" \
  "ds_write_b32 v89, v73 offset:%[so]\n" \
  "s_mov_b64 exec, s[70:71]\n" \
  "ds_write_b32 v90, v74 offset:%[so]\n" \
  "s_cmpk_gt_u32 s62, 0xc0\n" \
  "s_cbranch_scc1 Lq3%=\n" \
  "Lq4%=:\n" \
  "s_mov_b32 exec_lo, -1\n" \
  "s_mov_b32 exec_hi, 0\n" \
  "ds_write_b32 v113, v111 offset:%[so]\n" \
  "s_mov_b64 exec, -1\n" \
  "v_lshlrev_b32 v88, 2, v88\n" \
  "v_lshlrev_b32 v89, 2, v89\n" \
  "v_lshlrev_b32 v90, 2, v90\n" \
  "v_lshlrev_b32 v91, 2, v91\n" \
  "v_lshlrev_b32 v113, 2, v113\n" \
  "s_mov_b32 s63, 0x10000\n" \
  "s_waitcnt vmcnt(0)\n" \
  "Lpc%=:\n" \
  "s_mov_b64 exec, s[66:67]\n" \
  "ds_write_b128 v88, v[32:35] offset:%[pa]\n" \
  "s_mov_b64 exec, s[68:69]\n" \
  "ds_write_b128 v89, v[36:39] offset:%[pa]\n" \
  "s_mov_b64 exec, s[70:71]\n" \
  "ds_write_b128 v90, v[40:43] offset:%[pa]\n" \
  "s_mov_b64 exec, s[72:73]\n" \
  "ds_write_b128 v91, v[44:47] offset:%[pa]\n" \
  "s_mov_b32 exec_lo, -1\n" \
  "s_mov_b32 exec_hi, 0\n" \
  "ds_write_b128 v113, v[48:51] offset:%[pa]\n" \
  "s_mov_b64 exec, -1\n" \
  "v_add3_u32 v108, v32, v33, v115\n" \
  "v_add3_u32 v111, v34, v35, v36\n" \
  "v_add3_u32 v112, v37, v38, v39\n" \
  "v_add3_u32 v108, v108, v40, v41\n" \
  "v_add3_u32 v111, v111, v42, v43\n" \
  "v_add3_u32 v112, v112, v44, v45\n" \
  "v_add3_u32 v108, v108, v46, v47\n" \
  "v_add3_u32 v108, v108, v111, v112\n" \
  "s_nop 1\n" \
  "v_add_u32_dpp v108, v108, v108 row_shr:1 row_mask:0xf bank_mask:0xf bound_ctrl:1\n" \
  "s_nop 1\n" \
  "v_add_u32_dpp v108, v108, v108 row_shr:2 row_mask:0xf bank_mask:0xf bound_ctrl:1\n" \
  "s_nop 1\n" \
  "v_add_u32_dpp v108, v108, v108 row_shr:4 row_mask:0xf bank_mask:0xf bound_ctrl:1\n" \
  "s_nop 1\n" \
  "v_add_u32_dpp v108, v108, v108 row_shr:8 row_mask:0xf bank_mask:0xf bound_ctrl:1\n" \
  "s_nop 1\n" \
  "v_add_u32_dpp v108, v108, v108 row_bcast:15 row_mask:0xa bank_mask:0xf\n" \
  "s_nop 1\n" \
  "v_add_u32_dpp v108, v108, v108 row_bcast:31 row_mask:0xc bank_mask:0xf\n" \
  "s_nop 0\n" \
  "v_readlane_b32 s65, v108, 63\n" \
  "s_waitcnt lgkmcnt(0)\n" \
  "s_cmp_lg_u32 s65, 0\n" \
  "s_cbranch_scc1 Lpr%=\n" \
  "Lpw%=:\n" \
  "s_barrier\n" \
  "ds_read_b32  v11, v12\n" \
  "ds_read_b64 v[106:107], v14\n" \
  "ds_read_b64 v[104:105], v15\n" \
  "ds_read_b32 v18, v13\n" \
  "s_setprio 1\n" \
  "v_mov_b64 v[32:33], 0\n" \
  "v_mov_b64 v[34:35], 0\n" \
  "v_mov_b64 v[36:37], 0\n" \
  "v_mov_b64 v[38:39], 0\n" \
  "v_mov_b64 v[40:41], 0\n" \
  "v_mov_b64 v[42:43], 0\n" \
  "v_mov_b64 v[44:45], 0\n" \
  "v_mov_b64 v[46:47], 0\n" \
  "s_waitcnt lgkmcnt(3)\n" \
  "v_and_b32 v10, 0xfffffc00, v11\n" \
  "s_nop 1\n" \
  "v_add_u32_dpp v9, v10, v8 row_newbcast:0 row_mask:0xf bank_mask:0xf\n" \
  "buffer_load_dwordx4 v[120:123], v9, %[xs], 0 offen\n" \
  "v_add_u32_dpp v9, v10, v8 row_newbcast:1 row_mask:0xf bank_mask:0xf\n" \
  "buffer_load_dwordx4 v[124:127], v9, %[xs], 0 offen\n" \
  "v_add_u32_dpp v9, v10, v8 row_newbcast:2 row_mask:0xf bank_mask:0xf\n" \
  "buffer_load_dwordx4 v[128:131], v9, %[xs], 0 offen\n" \
  "v_add_u32_dpp v9, v10, v8 row_newbcast:3 row_mask:0xf bank_mask:0xf\n" \
  "buffer_load_dwordx4 v[132:135], v9, %[xs], 0 offen\n" \
  "v_add_u32_dpp v9, v10, v8 row_newbcast:4 row_mask:0xf bank_mask:0xf\n" \
  "buffer_load_dwordx4 v[136:139], v9, %[xs], 0 offen\n" \
  "s_waitcnt lgkmcnt(0)\n" \
  "v_mfma_f32_4x4x4_16b_bf16 v[80:83], v[104:105], v[28:29], 0 cbsz:4\n" \
  "v_mfma_f32_4x4x4_16b_bf16 v[84:87], v[104:105], v[30:31], 0 cbsz:4\n" \
  "v_mfma_f32_4x4x4_16b_bf16 v[112:115], v[106:107], v[20:21], 0\n" \
  "v_mfma_f32_4x4x4_16b_bf16 v[116:119], v[106:107], v[22:23], 0\n" \
  "v_mfma_f32_4x4x4_16b_bf16 v[72:75], v[106:107], v[24:25], 0\n" \
  "v_mfma_f32_4x4x4_16b_bf16 v[76:79], v[106:107], v[26:27], 0\n" \
  "v_and_b32 v18, v17, v18\n" \
  "v_cmp_ne_u32_e64 s[62:63], 0, v18\n" \
  "v_cvt_pkrtz_f16_f32 v97, v80, v81\n" \
  "v_and_b32 v96, v16, v97\n" \
  "v_and_b32_sdwa v97, v16, v97 dst_sel:DWORD dst_unused:UNUSED_PAD src0_sel:DWORD src1_sel:WORD_1\n" \
  "ds_read_b32 v96, v96 offset:%[tb]\n" \
  "ds_read_b32 v97, v97 offset:%[tb]\n" \
  "v_cvt_pkrtz_f16_f32 v99, v84, v85\n" \
  "v_and_b32 v98, v16, v99\n" \
  "v_and_b32_sdwa v99, v16, v99 dst_sel:DWORD dst_unused:UNUSED_PAD src0_sel:DWORD src1_sel:WORD_1\n" \
  "ds_read_b32 v98, v98 offset:%[tb]\n" \
  "ds_read_b32 v99, v99 offset:%[tb]\n" \
  "v_cvt_pkrtz_f16_f32 v101, v82, v83\n" \
  "v_and_b32 v100, v16, v101\n" \
  "v_and_b32_sdwa v101, v16, v101 dst_sel:DWORD dst_unused:UNUSED_PAD src0_sel:DWORD src1_sel:WORD_1\n" \
  "ds_read_b32 v100, v100 offset:%[tb]\n" \
  "ds_read_b32 v101, v101 offset:%[tb]\n" \
  "v_cvt_pk_bf16_f32 v108, v112, v116\n" \
  "v_cvt_pk_bf16_f32 v109, v72, v76\n" \
  "s_nop 0\n" \
  "s_waitcnt vmcnt(4)\n" \
  "v_mfma_f32_4x4x4_16b_bf16 v[72:75], v[108:109], v[120:121], 0 cbsz:4\n" \
  "v_mfma_f32_4x4x4_16b_bf16 v[76:79], v[108:109], v[122:123], 0 cbsz:4\n" \
  "v_add_u32_dpp v9, v10, v8 row_newbcast:5 row_mask:0xf bank_mask:0xf\n" \
  "buffer_load_dwordx4 v[140:143], v9, %[xs], 0 offen\n" \
  "s_waitcnt lgkmcnt(0)\n" \
  "s_nop 1\n" \
  "Lbody%=:\n" \
  "s_bfe_u32 s61, s62, 0x40000\n" \
  "s_waitcnt lgkmcnt(2)\n" \
  "v_pk_mul_f32 v[80:81], v[80:81], v[96:97]\n" \
  "v_pk_mul_f32 v[84:85], v[84:85], v[98:99]\n" \
  "s_set_gpr_idx_on s61, gpr_idx(SRC2,DST)\n" \
  "v_pk_fma_f32 v[32:33], v[80:81], v[72:73], v[32:33] op_sel:[0,0,0] op_sel_hi:[0,1,1]\n" \
  "v_pk_fma_f32 v[42:43], v[80:81], v[74:75], v[42:43] op_sel:[0,0,0] op_sel_hi:[0,1,1]\n" \
  "v_pk_fma_f32 v[52:53], v[84:85], v[76:77], v[52:53] op_sel:[0,0,0] op_sel_hi:[0,1,1]\n" \
  "v_pk_fma_f32 v[62:63], v[84:85], v[78:79], v[62:63] op_sel:[0,0,0] op_sel_hi:[0,1,1]\n" \
  "s_set_gpr_idx_off\n" \
  "s_waitcnt vmcnt(4)\n" \
  "v_mfma_f32_4x4x4_16b_bf16 v[72:75], v[108:109], v[124:125], 0 cbsz:4 abid:1\n" \
  "v_mfma_f32_4x4x4_16b_bf16 v[76:79], v[108:109], v[126:127], 0 cbsz:4 abid:1\n" \
  "v_mfma_f32_4x4x4_16b_bf16 v[88:91], v[104:105], v[28:29], 0 cbsz:4 abid:1\n" \
  "v_mfma_f32_4x4x4_16b_bf16 v[92:95], v[104:105], v[30:31], 0 cbsz:4 abid:1\n" \
  "v_add_u32_dpp v9, v10, v8 row_newbcast:6 row_mask:0xf bank_mask:0xf\n" \
  "v_cvt_pkrtz_f16_f32 v103, v86, v87\n" \
  "buffer_load_dwordx4 v[144:147], v9, %[xs], 0 offen\n" \
  "v_and_b32 v102, v16, v103\n" \
  "v_and_b32_sdwa v103, v16, v103 dst_sel:DWORD dst_unused:UNUSED_PAD src0_sel:DWORD src1_sel:WORD_1\n" \
  "ds_read_b32 v102, v102 offset:%[tb]\n" \
  "ds_read_b32 v103, v103 offset:%[tb]\n" \
  "v_mov_b32 v111, s60\n" \
  "ds_write2_b32 v110, v111, v111 offset1:8\n" \
  "s_bfe_u32 s61, s62, 0x40004\n" \
  "s_set_gpr_idx_on s61, gpr_idx(SRC2,DST)\n" \
  "v_pk_fma_f32 v[32:33], v[80:81], v[72:73], v[32:33] op_sel:[1,0,0] op_sel_hi:[1,1,1]\n" \
  "v_pk_fma_f32 v[42:43], v[80:81], v[74:75], v[42:43] op_sel:[1,0,0] op_sel_hi:[1,1,1]\n" \
  "v_pk_fma_f32 v[52:53], v[84:85], v[76:77], v[52:53] op_sel:[1,0,0] op_sel_hi:[1,1,1]\n" \
  "v_pk_fma_f32 v[62:63], v[84:85], v[78:79], v[62:63] op_sel:[1,0,0] op_sel_hi:[1,1,1]\n" \
  "s_set_gpr_idx_off\n" \
  "s_waitcnt vmcnt(4)\n" \
  "v_mfma_f32_4x4x4_16b_bf16 v[72:75], v[108:109], v[128:129], 0 cbsz:4 abid:2\n" \
  "v_mfma_f32_4x4x4_16b_bf16 v[76:79], v[108:109], v[130:131], 0 cbsz:4 abid:2\n" \
  "v_add_u32_dpp v9, v10, v8 row_newbcast:7 row_mask:0xf bank_mask:0xf\n" \
  "v_cvt_pkrtz_f16_f32 v97, v88, v89\n" \
  "buffer_load_dwordx4 v[148:151], v9, %[xs], 0 offen\n" \
  "v_and_b32 v96, v16, v97\n" \
  "v_and_b32_sdwa v97, v16, v97 dst_sel:DWORD dst_unused:UNUSED_PAD src0_sel:DWORD src1_sel:WORD_1\n" \
  "ds_read_b32 v96, v96 offset:%[tb]\n" \
  "ds_read_b32 v97, v97 offset:%[tb]\n" \
  "ds_read_b32 v111, v110 offset:16\n" \
  "s_bfe_u32 s61, s62, 0x40008\n" \
  "s_waitcnt lgkmcnt(4)\n" \
  "v_pk_mul_f32 v[82:83], v[82:83], v[100:101]\n" \
  "v_pk_mul_f32 v[86:87], v[86:87], v[102:103]\n" \
  "s_set_gpr_idx_on s61, gpr_idx(SRC2,DST)\n" \
  "v_pk_fma_f32 v[32:33], v[82:83], v[72:73], v[32:33] op_sel:[0,0,0] op_sel_hi:[0,1,1]\n" \
  "v_pk_fma_f32 v[42:43], v[82:83], v[74:75], v[42:43] op_sel:[0,0,0] op_sel_hi:[0,1,1]\n" \
  "v_pk_fma_f32 v[52:53], v[86:87], v[76:77], v[52:53] op_sel:[0,0,0] op_sel_hi:[0,1,1]\n" \
  "v_pk_fma_f32 v[62:63], v[86:87], v[78:79], v[62:63] op_sel:[0,0,0] op_sel_hi:[0,1,1]\n" \
  "s_set_gpr_idx_off\n" \
  "s_waitcnt vmcnt(4)\n" \
  "v_mfma_f32_4x4x4_16b_bf16 v[72:75], v[108:109], v[132:133], 0 cbsz:4 abid:3\n" \
  "v_mfma_f32_4x4x4_16b_bf16 v[76:79], v[108:109], v[134:135], 0 cbsz:4 abid:3\n" \
  "v_add_u32_dpp v9, v10, v8 row_newbcast:8 row_mask:0xf bank_mask:0xf\n" \
  "v_cvt_pkrtz_f16_f32 v99, v92, v93\n" \
  "buffer_load_dwordx4 v[120:123], v9, %[xs], 0 offen\n" \
  "v_and_b32 v98, v16, v99\n" \
  "v_and_b32_sdwa v99, v16, v99 dst_sel:DWORD dst_unused:UNUSED_PAD src0_sel:DWORD src1_sel:WORD_1\n" \
  "ds_read_b32 v98, v98 offset:%[tb]\n" \
  "ds_read_b32 v99, v99 offset:%[tb]\n" \
  "ds_read_b64 v[106:107], v14 offset:256\n" \
  "s_bfe_u32 s61, s62, 0x4000c\n" \
  "s_set_gpr_idx_on s61, gpr_idx(SRC2,DST)\n" \
  "v_pk_fma_f32 v[32:33], v[82:83], v[72:73], v[32:33] op_sel:[1,0,0] op_sel_hi:[1,1,1]\n" \
  "v_pk_fma_f32 v[42:43], v[82:83], v[74:75], v[42:43] op_sel:[1,0,0] op_sel_hi:[1,1,1]\n" \
  "v_pk_fma_f32 v[52:53], v[86:87], v[76:77], v[52:53] op_sel:[1,0,0] op_sel_hi:[1,1,1]\n" \
  "v_pk_fma_f32 v[62:63], v[86:87], v[78:79], v[62:63] op_sel:[1,0,0] op_sel_hi:[1,1,1]\n" \
  "s_set_gpr_idx_off\n" \
  "s_sub_u32 s60, s60, 1\n" \
  "s_cbranch_scc1 Lend%=\n" \
  "s_waitcnt vmcnt(4)\n" \
  "v_mfma_f32_4x4x4_16b_bf16 v[72:75], v[108:109], v[136:137], 0 cbsz:4 abid:4\n" \
  "v_mfma_f32_4x4x4_16b_bf16 v[76:79], v[108:109], v[138:139], 0 cbsz:4 abid:4\n" \
  "v_add_u32_dpp v9, v10, v8 row_newbcast:9 row_mask:0xf bank_mask:0xf\n" \
  "v_cvt_pkrtz_f16_f32 v101, v90, v91\n" \
  "buffer_load_dwordx4 v[124:127], v9, %[xs], 0 offen\n" \
  "v_and_b32 v100, v16, v101\n" \
  "v_and_b32_sdwa v101, v16, v101 dst_sel:DWORD dst_unused:UNUSED_PAD src0_sel:DWORD src1_sel:WORD_1\n" \
  "ds_read_b32 v100, v100 offset:%[tb]\n" \
  "ds_read_b32 v101, v101 offset:%[tb]\n" \
  "ds_read_b32 v18, v13 offset:64\n" \
  "v_add_u32 v14, 0x100, v14\n" \
  "s_bfe_u32 s61, s62, 0x40010\n" \
  "s_waitcnt lgkmcnt(4)\n" \
  "v_pk_mul_f32 v[88:89], v[88:89], v[96:97]\n" \
  "v_pk_mul_f32 v[92:93], v[92:93], v[98:99]\n" \
  "s_set_gpr_idx_on s61, gpr_idx(SRC2,DST)\n" \
  "v_pk_fma_f32 v[32:33], v[88:89], v[72:73], v[32:33] op_sel:[0,0,0] op_sel_hi:[0,1,1]\n" \
  "v_pk_fma_f32 v[42:43], v[88:89], v[74:75], v[42:43] op_sel:[0,0,0] op_sel_hi:[0,1,1]\n" \
  "v_pk_fma_f32 v[52:53], v[92:93], v[76:77], v[52:53] op_sel:[0,0,0] op_sel_hi:[0,1,1]\n" \
  "v_pk_fma_f32 v[62:63], v[92:93], v[78:79], v[62:63] op_sel:[0,0,0] op_sel_hi:[0,1,1]\n" \
  "s_set_gpr_idx_off\n" \
  "s_waitcnt vmcnt(4)\n" \
  "v_mfma_f32_4x4x4_16b_bf16 v[72:75], v[108:109], v[140:141], 0 cbsz:4 abid:5\n" \
  "v_mfma_f32_4x4x4_16b_bf16 v[76:79], v[108:109], v[142:143], 0 cbsz:4 abid:5\n" \
  "v_mfma_f32_4x4x4_16b_bf16 v[80:83], v[104:105], v[28:29], 0 cbsz:4 abid:2\n" \
  "v_mfma_f32_4x4x4_16b_bf16 v[84:87], v[104:105], v[30:31], 0 cbsz:4 abid:2\n" \
  "v_add_u32_dpp v9, v10, v8 row_newbcast:10 row_mask:0xf bank_mask:0xf\n" \
  "v_cvt_pkrtz_f16_f32 v103, v94, v95\n" \
  "buffer_load_dwordx4 v[128:131], v9, %[xs], 0 offen\n" \
  "v_and_b32 v102, v16, v103\n" \
  "v_and_b32_sdwa v103, v16, v103 dst_sel:DWORD dst_unused:UNUSED_PAD src0_sel:DWORD src1_sel:WORD_1\n" \
  "ds_read_b32 v102, v102 offset:%[tb]\n" \
  "ds_read_b32 v103, v103 offset:%[tb]\n" \
  "s_bfe_u32 s61, s62, 0x40014\n" \
  "s_set_gpr_idx_on s61, gpr_idx(SRC2,DST)\n" \
  "v_pk_fma_f32 v[32:33], v[88:89], v[72:73], v[32:33] op_sel:[1,0,0] op_sel_hi:[1,1,1]\n" \
  "v_pk_fma_f32 v[42:43], v[88:89], v[74:75], v[42:43] op_sel:[1,0,0] op_sel_hi:[1,1,1]\n" \
  "v_pk_fma_f32 v[52:53], v[92:93], v[76:77], v[52:53] op_sel:[1,0,0] op_sel_hi:[1,1,1]\n" \
  "v_pk_fma_f32 v[62:63], v[92:93], v[78:79], v[62:63] op_sel:[1,0,0] op_sel_hi:[1,1,1]\n" \
  "s_set_gpr_idx_off\n" \
  "s_waitcnt vmcnt(4) lgkmcnt(8)\n" \
  "v_mfma_f32_4x4x4_16b_bf16 v[72:75], v[108:109], v[144:145], 0 cbsz:4 abid:6\n" \
  "v_mfma_f32_4x4x4_16b_bf16 v[76:79], v[108:109], v[146:147], 0 cbsz:4 abid:6\n" \
  "v_add_u32_dpp v9, v10, v8 row_newbcast:11 row_mask:0xf bank_mask:0xf\n" \
  "v_cvt_pkrtz_f16_f32 v97, v80, v81\n" \
  "buffer_load_dwordx4 v[132:135], v9, %[xs], 0 offen\n" \
  "v_and_b32 v96, v16, v97\n" \
  "v_and_b32_sdwa v97, v16, v97 dst_sel:DWORD dst_unused:UNUSED_PAD src0_sel:DWORD src1_sel:WORD_1\n" \
  "ds_read_b32 v96, v96 offset:%[tb]\n" \
  "ds_read_b32 v97, v97 offset:%[tb]\n" \
  "v_add_u32 v13, 64, v13\n" \
  "v_readfirstlane_b32 s66, v111\n" \
  "s_setprio 1\n" \
  "s_cmp_ge_u32 s60, s66\n" \
  "s_cbranch_scc0 Lf5_%=\n" \
  "s_setprio 2\n" \
  "Lf5_%=:\n" \
  "s_bfe_u32 s61, s62, 0x40018\n" \
  "s_waitcnt lgkmcnt(2)\n" \
  "v_pk_mul_f32 v[90:91], v[90:91], v[100:101]\n" \
  "v_pk_mul_f32 v[94:95], v[94:95], v[102:103]\n" \
  "s_set_gpr_idx_on s61, gpr_idx(SRC2,DST)\n" \
  "v_pk_fma_f32 v[32:33], v[90:91], v[72:73], v[32:33] op_sel:[0,0,0] op_sel_hi:[0,1,1]\n" \
  "v_pk_fma_f32 v[42:43], v[90:91], v[74:75], v[42:43] op_sel:[0,0,0] op_sel_hi:[0,1,1]\n" \
  "v_pk_fma_f32 v[52:53], v[94:95], v[76:77], v[52:53] op_sel:[0,0,0] op_sel_hi:[0,1,1]\n" \
  "v_pk_fma_f32 v[62:63], v[94:95], v[78:79], v[62:63] op_sel:[0,0,0] op_sel_hi:[0,1,1]\n" \
  "s_set_gpr_idx_off\n" \
  "s_waitcnt vmcnt(4) lgkmcnt(7)\n" \
  "v_mfma_f32_4x4x4_16b_bf16 v[72:75], v[108:109], v[148:149], 0 cbsz:4 abid:7\n" \
  "v_mfma_f32_4x4x4_16b_bf16 v[76:79], v[108:109], v[150:151], 0 cbsz:4 abid:7\n" \
  "v_mfma_f32_4x4x4_16b_bf16 v[112:115], v[106:107], v[20:21], 0\n" \
  "v_add_u32_dpp v9, v10, v8 row_newbcast:12 row_mask:0xf bank_mask:0xf\n" \
  "v_cvt_pkrtz_f16_f32 v99, v84, v85\n" \
  "buffer_load_dwordx4 v[136:139], v9, %[xs], 0 offen\n" \
  "v_and_b32 v98, v16, v99\n" \
  "v_and_b32_sdwa v99, v16, v99 dst_sel:DWORD dst_unused:UNUSED_PAD src0_sel:DWORD src1_sel:WORD_1\n" \
  "ds_read_b32 v98, v98 offset:%[tb]\n" \
  "ds_read_b32 v99, v99 offset:%[tb]\n" \
  "ds_read_b32 v11, v12 offset:64\n" \
  "s_bfe_u32 s61, s62, 0x4001c\n" \
  "s_set_gpr_idx_on s61, gpr_idx(SRC2,DST)\n" \
  "v_pk_fma_f32 v[32:33], v[90:91], v[72:73], v[32:33] op_sel:[1,0,0] op_sel_hi:[1,1,1]\n" \
  "v_pk_fma_f32 v[42:43], v[90:91], v[74:75], v[42:43] op_sel:[1,0,0] op_sel_hi:[1,1,1]\n" \
  "v_pk_fma_f32 v[52:53], v[94:95], v[76:77], v[52:53] op_sel:[1,0,0] op_sel_hi:[1,1,1]\n" \
  "v_pk_fma_f32 v[62:63], v[94:95], v[78:79], v[62:63] op_sel:[1,0,0] op_sel_hi:[1,1,1]\n" \
  "s_set_gpr_idx_off\n" \
  "s_sub_u32 s60, s60, 1\n" \
  "s_cbranch_scc1 Lend%=\n" \
  "s_waitcnt vmcnt(4) lgkmcnt(7)\n" \
  "v_mfma_f32_4x4x4_16b_bf16 v[72:75], v[108:109], v[120:121], 0 cbsz:4 abid:8\n" \
  "v_mfma_f32_4x4x4_16b_bf16 v[76:79], v[108:109], v[122:123], 0 cbsz:4 abid:8\n" \
  "v_add_u32_dpp v9, v10, v8 row_newbcast:13 row_mask:0xf bank_mask:0xf\n" \
  "v_cvt_pkrtz_f16_f32 v101, v82, v83\n" \
  "buffer_load_dwordx4 v[140:143], v9, %[xs], 0 offen\n" \
  "v_and_b32 v100, v16, v101\n" \
  "v_and_b32_sdwa v101, v16, v101 dst_sel:DWORD dst_unused:UNUSED_PAD src0_sel:DWORD src1_sel:WORD_1\n" \
  "ds_read_b32 v100, v100 offset:%[tb]\n" \
  "ds_read_b32 v101, v101 offset:%[tb]\n" \
  "v_add_u32 v12, 64, v12\n" \
  "v_and_b32 v18, v17, v18\n" \
  "s_bfe_u32 s61, s63, 0x40000\n" \
  "s_waitcnt lgkmcnt(3)\n" \
  "v_pk_mul_f32 v[80:81], v[80:81], v[96:97]\n" \
  "v_pk_mul_f32 v[84:85], v[84:85], v[98:99]\n" \
  "s_set_gpr_idx_on s61, gpr_idx(SRC2,DST)\n" \
  "v_pk_fma_f32 v[32:33], v[80:81], v[72:73], v[32:33] op_sel:[0,0,0] op_sel_hi:[0,1,1]\n" \
  "v_pk_fma_f32 v[42:43], v[80:81], v[74:75], v[42:43] op_sel:[0,0,0] op_sel_hi:[0,1,1]\n" \
  "v_pk_fma_f32 v[52:53], v[84:85], v[76:77], v[52:53] op_sel:[0,0,0] op_sel_hi:[0,1,1]\n" \
  "v_pk_fma_f32 v[62:63], v[84:85], v[78:79], v[62:63] op_sel:[0,0,0] op_sel_hi:[0,1,1]\n" \
  "s_set_gpr_idx_off\n" \
  "s_waitcnt vmcnt(4)\n" \
  "v_mfma_f32_4x4x4_16b_bf16 v[72:75], v[108:109], v[124:125], 0 cbsz:4 abid:9\n" \
  "v_mfma_f32_4x4x4_16b_bf16 v[76:79], v[108:109], v[126:127], 0 cbsz:4 abid:9\n" \
  "v_mfma_f32_4x4x4_16b_bf16 v[88:91], v[104:105], v[28:29], 0 cbsz:4 abid:3\n" \
  "v_mfma_f32_4x4x4_16b_bf16 v[92:95], v[104:105], v[30:31], 0 cbsz:4 abid:3\n" \
  "v_add_u32_dpp v9, v10, v8 row_newbcast:14 row_mask:0xf bank_mask:0xf\n" \
  "v_cvt_pkrtz_f16_f32 v103, v86, v87\n" \
  "buffer_load_dwordx4 v[144:147], v9, %[xs], 0 offen\n" \
  "v_and_b32 v102, v16, v103\n" \
  "v_and_b32_sdwa v103, v16, v103 dst_sel:DWORD dst_unused:UNUSED_PAD src0_sel:DWORD src1_sel:WORD_1\n" \
  "ds_read_b32 v102, v102 offset:%[tb]\n" \
  "ds_read_b32 v103, v103 offset:%[tb]\n" \
  "v_mov_b32 v111, s60\n" \
  "ds_write2_b32 v110, v111, v111 offset1:8\n" \
  "s_bfe_u32 s61, s63, 0x40004\n" \
  "s_set_gpr_idx_on s61, gpr_idx(SRC2,DST)\n" \
  "v_pk_fma_f32 v[32:33], v[80:81], v[72:73], v[32:33] op_sel:[1,0,0] op_sel_hi:[1,1,1]\n" \
  "v_pk_fma_f32 v[42:43], v[80:81], v[74:75], v[42:43] op_sel:[1,0,0] op_sel_hi:[1,1,1]\n" \
  "v_pk_fma_f32 v[52:53], v[84:85], v[76:77], v[52:53] op_sel:[1,0,0] op_sel_hi:[1,1,1]\n" \
  "v_pk_fma_f32 v[62:63], v[84:85], v[78:79], v[62:63] op_sel:[1,0,0] op_sel_hi:[1,1,1]\n" \
  "s_set_gpr_idx_off\n" \
  "s_waitcnt vmcnt(4)\n" \
  "v_mfma_f32_4x4x4_16b_bf16 v[72:75], v[108:109], v[128:129], 0 cbsz:4 abid:10\n" \
  "v_mfma_f32_4x4x4_16b_bf16 v[76:79], v[108:109], v[130:131], 0 cbsz:4 abid:10\n" \
  "v_add_u32_dpp v9, v10, v8 row_newbcast:15 row_mask:0xf bank_mask:0xf\n" \
  "v_cvt_pkrtz_f16_f32 v97, v88, v89\n" \
  "buffer_load_dwordx4 v[148:151], v9, %[xs], 0 offen\n" \
  "v_and_b32 v96, v16, v97\n" \
  "v_and_b32_sdwa v97, v16, v97 dst_sel:DWORD dst_unused:UNUSED_PAD src0_sel:DWORD src1_sel:WORD_1\n" \
  "ds_read_b32 v96, v96 offset:%[tb]\n" \
  "ds_read_b32 v97, v97 offset:%[tb]\n" \
  "ds_read_b64 v[104:105], v15 offset:256\n" \
  "v_cmp_ne_u32_e64 s[64:65], 0, v18\n" \
  "ds_read_b32 v111, v110 offset:16\n" \
  "s_bfe_u32 s61, s63, 0x40008\n" \
  "s_waitcnt lgkmcnt(5)\n" \
  "v_pk_mul_f32 v[82:83], v[82:83], v[100:101]\n" \
  "v_pk_mul_f32 v[86:87], v[86:87], v[102:103]\n" \
  "s_set_gpr_idx_on s61, gpr_idx(SRC2,DST)\n" \
  "v_pk_fma_f32 v[32:33], v[82:83], v[72:73], v[32:33] op_sel:[0,0,0] op_sel_hi:[0,1,1]\n" \
  "v_pk_fma_f32 v[42:43], v[82:83], v[74:75], v[42:43] op_sel:[0,0,0] op_sel_hi:[0,1,1]\n" \
  "v_pk_fma_f32 v[52:53], v[86:87], v[76:77], v[52:53] op_sel:[0,0,0] op_sel_hi:[0,1,1]\n" \
  "v_pk_fma_f32 v[62:63], v[86:87], v[78:79], v[62:63] op_sel:[0,0,0] op_sel_hi:[0,1,1]\n" \
  "s_set_gpr_idx_off\n" \
  "s_waitcnt vmcnt(4) lgkmcnt(9)\n" \
  "v_and_b32 v10, 0xfffffc00, v11\n" \
  "v_mfma_f32_4x4x4_16b_bf16 v[72:75], v[108:109], v[132:133], 0 cbsz:4 abid:11\n" \
  "v_mfma_f32_4x4x4_16b_bf16 v[76:79], v[108:109], v[134:135], 0 cbsz:4 abid:11\n" \
  "v_mfma_f32_4x4x4_16b_bf16 v[116:119], v[106:107], v[22:23], 0\n" \
  "v_add_u32_dpp v9, v10, v8 row_newbcast:0 row_mask:0xf bank_mask:0xf\n" \
  "v_cvt_pkrtz_f16_f32 v99, v92, v93\n" \
  "buffer_load_dwordx4 v[120:123], v9, %[xs], 0 offen\n" \
  "v_and_b32 v98, v16, v99\n" \
  "v_and_b32_sdwa v99, v16, v99 dst_sel:DWORD dst_unused:UNUSED_PAD src0_sel:DWORD src1_sel:WORD_1\n" \
  "ds_read_b32 v98, v98 offset:%[tb]\n" \
  "ds_read_b32 v99, v99 offset:%[tb]\n" \
  "s_bfe_u32 s61, s63, 0x4000c\n" \
  "s_set_gpr_idx_on s61, gpr_idx(SRC2,DST)\n" \
  "v_pk_fma_f32 v[32:33], v[82:83], v[72:73], v[32:33] op_sel:[1,0,0] op_sel_hi:[1,1,1]\n" \
  "v_pk_fma_f32 v[42:43], v[82:83], v[74:75], v[42:43] op_sel:[1,0,0] op_sel_hi:[1,1,1]\n" \
  "v_pk_fma_f32 v[52:53], v[86:87], v[76:77], v[52:53] op_sel:[1,0,0] op_sel_hi:[1,1,1]\n" \
  "v_pk_fma_f32 v[62:63], v[86:87], v[78:79], v[62:63] op_sel:[1,0,0] op_sel_hi:[1,1,1]\n" \
  "s_set_gpr_idx_off\n" \
  "s_sub_u32 s60, s60, 1\n" \
  "s_cbranch_scc1 Lend%=\n" \
  "s_waitcnt vmcnt(4)\n" \
  "v_mfma_f32_4x4x4_16b_bf16 v[72:75], v[108:109], v[136:137], 0 cbsz:4 abid:12\n" \
  "v_mfma_f32_4x4x4_16b_bf16 v[76:79], v[108:109], v[138:139], 0 cbsz:4 abid:12\n" \
  "v_add_u32_dpp v9, v10, v8 row_newbcast:1 row_mask:0xf bank_mask:0xf\n" \
  "v_cvt_pkrtz_f16_f32 v101, v90, v91\n" \
  "buffer_load_dwordx4 v[124:127], v9, %[xs], 0 offen\n" \
  "v_and_b32 v100, v16, v101\n" \
  "v_and_b32_sdwa v101, v16, v101 dst_sel:DWORD dst_unused:UNUSED_PAD src0_sel:DWORD src1_sel:WORD_1\n" \
  "ds_read_b32 v100, v100 offset:%[tb]\n" \
  "ds_read_b32 v101, v101 offset:%[tb]\n" \
  "v_add_u32 v15, 0x100, v15\n" \
  "s_bfe_u32 s61, s63, 0x40010\n" \
  "s_waitcnt lgkmcnt(2)\n" \
  "v_pk_mul_f32 v[88:89], v[88:89], v[96:97]\n" \
  "v_pk_mul_f32 v[92:93], v[92:93], v[98:99]\n" \
  "s_set_gpr_idx_on s61, gpr_idx(SRC2,DST)\n" \
  "v_pk_fma_f32 v[32:33], v[88:89], v[72:73], v[32:33] op_sel:[0,0,0] op_sel_hi:[0,1,1]\n" \
  "v_pk_fma_f32 v[42:43], v[88:89], v[74:75], v[42:43] op_sel:[0,0,0] op_sel_hi:[0,1,1]\n" \
  "v_pk_fma_f32 v[52:53], v[92:93], v[76:77], v[52:53] op_sel:[0,0,0] op_sel_hi:[0,1,1]\n" \
  "v_pk_fma_f32 v[62:63], v[92:93], v[78:79], v[62:63] op_sel:[0,0,0] op_sel_hi:[0,1,1]\n" \
  "s_set_gpr_idx_off\n" \
  "s_waitcnt vmcnt(4) lgkmcnt(5)\n" \
  "v_mfma_f32_4x4x4_16b_bf16 v[72:75], v[108:109], v[140:141], 0 cbsz:4 abid:13\n" \
  "v_mfma_f32_4x4x4_16b_bf16 v[76:79], v[108:109], v[142:143], 0 cbsz:4 abid:13\n" \
  "v_mfma_f32_4x4x4_16b_bf16 v[80:83], v[104:105], v[28:29], 0 cbsz:4\n" \
  "v_mfma_f32_4x4x4_16b_bf16 v[84:87], v[104:105], v[30:31], 0 cbsz:4\n" \
  "v_add_u32_dpp v9, v10, v8 row_newbcast:2 row_mask:0xf bank_mask:0xf\n" \
  "v_cvt_pkrtz_f16_f32 v103, v94, v95\n" \
  "buffer_load_dwordx4 v[128:131], v9, %[xs], 0 offen\n" \
  "v_and_b32 v102, v16, v103\n" \
  "v_and_b32_sdwa v103, v16, v103 dst_sel:DWORD dst_unused:UNUSED_PAD src0_sel:DWORD src1_sel:WORD_1\n" \
  "ds_read_b32 v102, v102 offset:%[tb]\n" \
  "ds_read_b32 v103, v103 offset:%[tb]\n" \
  "s_bfe_u32 s61, s63, 0x40014\n" \
  "s_set_gpr_idx_on s61, gpr_idx(SRC2,DST)\n" \
  "v_pk_fma_f32 v[32:33], v[88:89], v[72:73], v[32:33] op_sel:[1,0,0] op_sel_hi:[1,1,1]\n" \
  "v_pk_fma_f32 v[42:43], v[88:89], v[74:75], v[42:43] op_sel:[1,0,0] op_sel_hi:[1,1,1]\n" \
  "v_pk_fma_f32 v[52:53], v[92:93], v[76:77], v[52:53] op_sel:[1,0,0] op_sel_hi:[1,1,1]\n" \
  "v_pk_fma_f32 v[62:63], v[92:93], v[78:79], v[62:63] op_sel:[1,0,0] op_sel_hi:[1,1,1]\n" \
  "s_set_gpr_idx_off\n" \
  "s_waitcnt vmcnt(4) lgkmcnt(6)\n" \
  "v_mfma_f32_4x4x4_16b_bf16 v[72:75], v[108:109], v[144:145], 0 cbsz:4 abid:14\n" \
  "v_mfma_f32_4x4x4_16b_bf16 v[76:79], v[108:109], v[146:147], 0 cbsz:4 abid:14\n" \
  "v_add_u32_dpp v9, v10, v8 row_newbcast:3 row_mask:0xf bank_mask:0xf\n" \
  "v_cvt_pkrtz_f16_f32 v97, v80, v81\n" \
  "buffer_load_dwordx4 v[132:135], v9, %[xs], 0 offen\n" \
  "v_and_b32 v96, v16, v97\n" \
  "v_and_b32_sdwa v97, v16, v97 dst_sel:DWORD dst_unused:UNUSED_PAD src0_sel:DWORD src1_sel:WORD_1\n" \
  "ds_read_b32 v96, v96 offset:%[tb]\n" \
  "ds_read_b32 v97, v97 offset:%[tb]\n" \
  "v_readfirstlane_b32 s66, v111\n" \
  "s_setprio 1\n" \
  "s_cmp_ge_u32 s60, s66\n" \
  "s_cbranch_scc0 Lf13_%=\n" \
  "s_setprio 2\n" \
  "Lf13_%=:\n" \
  "v_cvt_pk_bf16_f32 v19, v112, v116\n" \
  "s_bfe_u32 s61, s63, 0x40018\n" \
  "s_waitcnt lgkmcnt(2)\n" \
  "v_pk_mul_f32 v[90:91], v[90:91], v[100:101]\n" \
  "v_pk_mul_f32 v[94:95], v[94:95], v[102:103]\n" \
  "s_set_gpr_idx_on s61, gpr_idx(SRC2,DST)\n" \
  "v_pk_fma_f32 v[32:33], v[90:91], v[72:73], v[32:33] op_sel:[0,0,0] op_sel_hi:[0,1,1]\n" \
  "v_pk_fma_f32 v[42:43], v[90:91], v[74:75], v[42:43] op_sel:[0,0,0] op_sel_hi:[0,1,1]\n" \
  "v_pk_fma_f32 v[52:53], v[94:95], v[76:77], v[52:53] op_sel:[0,0,0] op_sel_hi:[0,1,1]\n" \
  "v_pk_fma_f32 v[62:63], v[94:95], v[78:79], v[62:63] op_sel:[0,0,0] op_sel_hi:[0,1,1]\n" \
  "s_set_gpr_idx_off\n" \
  "s_waitcnt vmcnt(4)\n" \
  "v_mfma_f32_4x4x4_16b_bf16 v[112:115], v[106:107], v[24:25], 0\n" \
  "v_mfma_f32_4x4x4_16b_bf16 v[116:119], v[106:107], v[26:27], 0\n" \
  "v_mfma_f32_4x4x4_16b_bf16 v[72:75], v[108:109], v[148:149], 0 cbsz:4 abid:15\n" \
  "v_mfma_f32_4x4x4_16b_bf16 v[76:79], v[108:109], v[150:151], 0 cbsz:4 abid:15\n" \
  "v_add_u32_dpp v9, v10, v8 row_newbcast:4 row_mask:0xf bank_mask:0xf\n" \
  "v_cvt_pkrtz_f16_f32 v99, v84, v85\n" \
  "buffer_load_dwordx4 v[136:139], v9, %[xs], 0 offen\n" \
  "v_and_b32 v98, v16, v99\n" \
  "v_and_b32_sdwa v99, v16, v99 dst_sel:DWORD dst_unused:UNUSED_PAD src0_sel:DWORD src1_sel:WORD_1\n" \
  "ds_read_b32 v98, v98 offset:%[tb]\n" \
  "ds_read_b32 v99, v99 offset:%[tb]\n" \
  "v_cvt_pk_bf16_f32 v109, v112, v116\n" \
  "v_mov_b32 v108, v19\n" \
  "s_bfe_u32 s61, s63, 0x4001c\n" \
  "s_set_gpr_idx_on s61, gpr_idx(SRC2,DST)\n" \
  "v_pk_fma_f32 v[32:33], v[90:91], v[72:73], v[32:33] op_sel:[1,0,0] op_sel_hi:[1,1,1]\n" \
  "v_pk_fma_f32 v[42:43], v[90:91], v[74:75], v[42:43] op_sel:[1,0,0] op_sel_hi:[1,1,1]\n" \
  "v_pk_fma_f32 v[52:53], v[94:95], v[76:77], v[52:53] op_sel:[1,0,0] op_sel_hi:[1,1,1]\n" \
  "v_pk_fma_f32 v[62:63], v[94:95], v[78:79], v[62:63] op_sel:[1,0,0] op_sel_hi:[1,1,1]\n" \
  "s_set_gpr_idx_off\n" \
  "s_sub_u32 s60, s60, 1\n" \
  "s_cbranch_scc1 Lend%=\n" \
  "s_waitcnt vmcnt(4)\n" \
  "v_mfma_f32_4x4x4_16b_bf16 v[72:75], v[108:109], v[120:121], 0 cbsz:4\n" \
  "v_mfma_f32_4x4x4_16b_bf16 v[76:79], v[108:109], v[122:123], 0 cbsz:4\n" \
  "v_add_u32_dpp v9, v10, v8 row_newbcast:5 row_mask:0xf bank_mask:0xf\n" \
  "v_cvt_pkrtz_f16_f32 v101, v82, v83\n" \
  "buffer_load_dwordx4 v[140:143], v9, %[xs], 0 offen\n" \
  "v_and_b32 v100, v16, v101\n" \
  "v_and_b32_sdwa v101, v16, v101 dst_sel:DWORD dst_unused:UNUSED_PAD src0_sel:DWORD src1_sel:WORD_1\n" \
  "ds_read_b32 v100, v100 offset:%[tb]\n" \
  "ds_read_b32 v101, v101 offset:%[tb]\n" \
  "s_mov_b64 s[62:63], s[64:65]\n" \
  "s_branch Lbody%=\n" \
  "Lend%=:\n" \
  "s_setprio 0\n" \
  "v_mov_b32 v20, 0\n" \
  "v_mov_b32 v21, %[bit]\n" \
  "v_cvt_pk_bf16_f32 v40, v40, v60\n" \
  "v_cvt_pk_bf16_f32 v41, v41, v61\n" \
  "v_cvt_pk_bf16_f32 v50, v50, v70\n" \
  "v_cvt_pk_bf16_f32 v51, v51, v71\n" \
  "ds_write2_b32 v156, v40, v41 offset0:0 offset1:16\n" \
  "ds_write2_b32 v156, v50, v51 offset0:32 offset1:48\n" \
  "s_mov_b64 exec, 1\n" \
  "ds_or_b32 v20, v21 offset:%[rd]\n" \
  "s_mov_b64 exec, -1\n" \
  "v_cvt_pk_bf16_f32 v32, v32, v52\n" \
  "v_cvt_pk_bf16_f32 v33, v33, v53\n" \
  "v_cvt_pk_bf16_f32 v42, v42, v62\n" \
  "v_cvt_pk_bf16_f32 v43, v43, v63\n" \
  "ds_read_b128 v[72:75], v158 offset:0\n" \
  "ds_read_b128 v[76:79], v158 offset:1024\n" \
  "ds_read_b128 v[80:83], v158 offset:8192\n" \
  "ds_read_b128 v[84:87], v158 offset:9216\n" \
  "v_cvt_pk_bf16_f32 v34, v34, v54\n" \
  "v_cvt_pk_bf16_f32 v35, v35, v55\n" \
  "v_cvt_pk_bf16_f32 v44, v44, v64\n" \
  "v_cvt_pk_bf16_f32 v45, v45, v65\n" \
  "ds_write2st64_b32 v152, v32, v34 offset0:0 offset1:4\n" \
  "ds_write2st64_b32 v153, v33, v35 offset0:0 offset1:4\n" \
  "ds_write2st64_b32 v154, v42, v44 offset0:0 offset1:4\n" \
  "ds_write2st64_b32 v155, v43, v45 offset0:0 offset1:4\n" \
  "v_cvt_pk_bf16_f32 v36, v36, v56\n" \
  "v_cvt_pk_bf16_f32 v37, v37, v57\n" \
  "v_cvt_pk_bf16_f32 v46, v46, v66\n" \
  "v_cvt_pk_bf16_f32 v47, v47, v67\n" \
  "v_cvt_pk_bf16_f32 v38, v38, v58\n" \
  "v_cvt_pk_bf16_f32 v39, v39, v59\n" \
  "v_cvt_pk_bf16_f32 v48, v48, v68\n" \
  "v_cvt_pk_bf16_f32 v49, v49, v69\n" \
  "ds_write2st64_b32 v152, v36, v38 offset0:8 offset1:12\n" \
  "ds_write2st64_b32 v153, v37, v39 offset0:8 offset1:12\n" \
  "ds_write2st64_b32 v154, v46, v48 offset0:8 offset1:12\n" \
  "ds_write2st64_b32 v155, v47, v49 offset0:8 offset1:12\n" \
  "ds_read_b128 v[32:35], v157 offset:0\n" \
  "ds_read_b128 v[36:39], v157 offset:256\n" \
  "ds_read_b128 v[40:43], v157 offset:512\n" \
  "ds_read_b128 v[44:47], v157 offset:768\n" \
  "ds_read_b128 v[88:91], v158 offset:16384\n" \
  "ds_read_b128 v[92:95], v158 offset:17408\n" \
  "ds_read_b128 v[96:99], v158 offset:24576\n" \
  "ds_read_b128 v[100:103], v158 offset:25600\n" \
  "ds_read_b128 v[104:107], v158 offset:2048\n" \
  "ds_read_b128 v[108:111], v158 offset:3072\n" \
  "ds_read_b128 v[112:115], v158 offset:10240\n" \
  "ds_read_b128 v[116:119], v158 offset:11264\n" \
  "s_waitcnt lgkmcnt(11)\n" \
  "v_mfma_f32_16x16x32_bf16 v[48:51], v[32:35], v[72:75], 0\n" \
  "ds_read_b128 v[72:75], v158 offset:18432\n" \
  "v_mfma_f32_16x16x32_bf16 v[52:55], v[32:35], v[76:79], 0\n" \
  "ds_read_b128 v[76:79], v158 offset:19456\n" \
  "s_waitcnt lgkmcnt(12)\n" \
  "v_mfma_f32_16x16x32_bf16 v[48:51], v[36:39], v[80:83], v[48:51]\n" \
  "ds_read_b128 v[80:83], v158 offset:26624\n" \
  "v_mfma_f32_16x16x32_bf16 v[52:55], v[36:39], v[84:87], v[52:55]\n" \
  "ds_read_b128 v[84:87], v158 offset:27648\n" \
  "s_waitcnt lgkmcnt(11)\n" \
  "v_mfma_f32_16x16x32_bf16 v[48:51], v[40:43], v[88:91], v[48:51]\n" \
  "ds_read_b128 v[88:91], v158 offset:4096\n" \
  "s_waitcnt lgkmcnt(11)\n" \
  "v_mfma_f32_16x16x32_bf16 v[52:55], v[40:43], v[92:95], v[52:55]\n" \
  "ds_read_b128 v[92:95], v158 offset:5120\n" \
  "s_waitcnt lgkmcnt(11)\n" \
  "v_mfma_f32_16x16x32_bf16 v[48:51], v[44:47], v[96:99], v[48:51]\n" \
  "ds_read_b128 v[96:99], v158 offset:12288\n" \
  "s_waitcnt lgkmcnt(11)\n" \
  "v_mfma_f32_16x16x32_bf16 v[52:55], v[44:47], v[100:103], v[52:55]\n" \
  "ds_read_b128 v[100:103], v158 offset:13312\n" \
  "s_waitcnt lgkmcnt(11)\n" \
  "v_mfma_f32_16x16x32_bf16 v[56:59], v[32:35], v[104:107], 0\n" \
  "ds_read_b128 v[104:107], v158 offset:20480\n" \
  "s_waitcnt lgkmcnt(11)\n" \
  "v_mfma_f32_16x16x32_bf16 v[60:63], v[32:35], v[108:111], 0\n" \
  "ds_read_b128 v[108:111], v158 offset:21504\n" \
  "s_waitcnt lgkmcnt(11)\n" \
  "v_mfma_f32_16x16x32_bf16 v[56:59], v[36:39], v[112:115], v[56:59]\n" \
  "ds_read_b128 v[112:115], v158 offset:28672\n" \
  "s_waitcnt lgkmcnt(11)\n" \
  "v_mfma_f32_16x16x32_bf16 v[60:63], v[36:39], v[116:119], v[60:63]\n" \
  "ds_read_b128 v[116:119], v158 offset:29696\n" \
  "s_waitcnt lgkmcnt(11)\n" \
  "v_mfma_f32_16x16x32_bf16 v[56:59], v[40:43], v[72:75], v[56:59]\n" \
  "ds_read_b128 v[72:75], v158 offset:6144\n" \
  "v_cvt_pk_bf16_f32 v64, v48, v49\n" \
  "v_cvt_pk_bf16_f32 v65, v50, v51\n" \
  "v_cvt_pk_bf16_f32 v66, v52, v53\n" \
  "v_cvt_pk_bf16_f32 v67, v54, v55\n" \
  "buffer_store_dwordx4 v[64:67], v159, %[os], 0 offen offset:0 sc1\n" \
  "s_waitcnt lgkmcnt(11)\n" \
  "v_mfma_f32_16x16x32_bf16 v[60:63], v[40:43], v[76:79], v[60:63]\n" \
  "ds_read_b128 v[76:79], v158 offset:7168\n" \
  "s_waitcnt lgkmcnt(11)\n" \
  "v_mfma_f32_16x16x32_bf16 v[56:59], v[44:47], v[80:83], v[56:59]\n" \
  "ds_read_b128 v[80:83], v158 offset:14336\n" \
  "s_waitcnt lgkmcnt(11)\n" \
  "v_mfma_f32_16x16x32_bf16 v[60:63], v[44:47], v[84:87], v[60:63]\n" \
  "ds_read_b128 v[84:87], v158 offset:15360\n" \
  "s_waitcnt lgkmcnt(11)\n" \
  "v_mfma_f32_16x16x32_bf16 v[48:51], v[32:35], v[88:91], 0\n" \
  "ds_read_b128 v[88:91], v158 offset:22528\n" \
  "s_waitcnt lgkmcnt(11)\n" \
  "v_mfma_f32_16x16x32_bf16 v[52:55], v[32:35], v[92:95], 0\n" \
  "ds_read_b128 v[92:95], v158 offset:23552\n" \
  "s_waitcnt lgkmcnt(11)\n" \
  "v_mfma_f32_16x16x32_bf16 v[48:51], v[36:39], v[96:99], v[48:51]\n" \
  "ds_read_b128 v[96:99], v158 offset:30720\n" \
  "s_waitcnt lgkmcnt(11)\n" \
  "v_mfma_f32_16x16x32_bf16 v[52:55], v[36:39], v[100:103], v[52:55]\n" \
  "ds_read_b128 v[100:103], v158 offset:31744\n" \
  "s_waitcnt lgkmcnt(11)\n" \
  "v_mfma_f32_16x16x32_bf16 v[48:51], v[40:43], v[104:107], v[48:51]\n" \
  "v_cvt_pk_bf16_f32 v68, v56, v57\n" \
  "v_cvt_pk_bf16_f32 v69, v58, v59\n" \
  "v_cvt_pk_bf16_f32 v70, v60, v61\n" \
  "v_cvt_pk_bf16_f32 v71, v62, v63\n" \
  "buffer_store_dwordx4 v[68:71], v159, %[os], 0 offen offset:256 sc1\n" \
  "s_waitcnt lgkmcnt(10)\n" \
  "v_mfma_f32_16x16x32_bf16 v[52:55], v[40:43], v[108:111], v[52:55]\n" \
  "s_waitcnt lgkmcnt(9)\n" \
  "v_mfma_f32_16x16x32_bf16 v[48:51], v[44:47], v[112:115], v[48:51]\n" \
  "s_waitcnt lgkmcnt(8)\n" \
  "v_mfma_f32_16x16x32_bf16 v[52:55], v[44:47], v[116:119], v[52:55]\n" \
  "s_waitcnt lgkmcnt(7)\n" \
  "v_mfma_f32_16x16x32_bf16 v[56:59], v[32:35], v[72:75], 0\n" \
  "s_waitcnt lgkmcnt(6)\n" \
  "v_mfma_f32_16x16x32_bf16 v[60:63], v[32:35], v[76:79], 0\n" \
  "s_waitcnt lgkmcnt(5)\n" \
  "v_mfma_f32_16x16x32_bf16 v[56:59], v[36:39], v[80:83], v[56:59]\n" \
  "s_waitcnt lgkmcnt(4)\n" \
  "v_mfma_f32_16x16x32_bf16 v[60:63], v[36:39], v[84:87], v[60:63]\n" \
  "s_waitcnt lgkmcnt(3)\n" \
  "v_mfma_f32_16x16x32_bf16 v[56:59], v[40:43], v[88:91], v[56:59]\n" \
  "v_cvt_pk_bf16_f32 v64, v48, v49\n" \
  "v_cvt_pk_bf16_f32 v65, v50, v51\n" \
  "v_cvt_pk_bf16_f32 v66, v52, v53\n" \
  "v_cvt_pk_bf16_f32 v67, v54, v55\n" \
  "buffer_store_dwordx4 v[64:67], v159, %[os], 0 offen offset:512 sc1\n" \
  "s_waitcnt lgkmcnt(2)\n" \
  "v_mfma_f32_16x16x32_bf16 v[60:63], v[40:43], v[92:95], v[60:63]\n" \
  "s_waitcnt lgkmcnt(1)\n" \
  "v_mfma_f32_16x16x32_bf16 v[56:59], v[44:47], v[96:99], v[56:59]\n" \
  "s_waitcnt lgkmcnt(0)\n" \
  "v_mfma_f32_16x16x32_bf16 v[60:63], v[44:47], v[100:103], v[60:63]\n" \
  "s_nop 7\n" \
  "v_cvt_pk_bf16_f32 v68, v56, v57\n" \
  "v_cvt_pk_bf16_f32 v69, v58, v59\n" \
  "v_cvt_pk_bf16_f32 v70, v60, v61\n" \
  "v_cvt_pk_bf16_f32 v71, v62, v63\n" \
  "buffer_store_dwordx4 v[68:71], v159, %[os], 0 offen offset:768 sc1\n" \
  "s_endpgm\n" \
  "Lhr%=:\n" \
  "s_mov_b64 exec, vcc\n" \
  "s_sub_u32 s63, s63, 1\n" \
  "s_cbranch_scc0 Lh%=\n" \
  "s_mov_b64 exec, -1\n" \
  "Lover%=:\n" \
  "v_mov_b32 v113, 1\n" \
  "ds_write_b32 v117, v113 offset:%[fl]\n" \
  "s_mov_b32 s62, 0\n" \
  "v_mov_b32 v114, 0\n" \
  "v_mov_b32 v115, 0\n" \
  "s_branch Lld%=\n" \
  "Lkr%=:\n" \
  "buffer_load_dword v84, v76, %[ws], %[rk] offen sc1\n" \
  "buffer_load_dword v85, v77, %[ws], %[rk] offen sc1\n" \
  "buffer_load_dword v86, v78, %[ws], %[rk] offen sc1\n" \
  "s_cmpk_le_u32 s62, 0xc0\n" \
  "s_cbranch_scc1 Lkb%=\n" \
  "buffer_load_dword v87, v79, %[ws], %[rk] offen sc1\n" \
  "Lkb%=:\n" \
  "s_waitcnt vmcnt(0)\n" \
  "s_sub_u32 s63, s63, 1\n" \
  "s_cbranch_scc0 Lk%=\n" \
  "v_mov_b32 v113, 1\n" \
  "ds_write_b32 v117, v113 offset:%[fl]\n" \
  "s_branch Lkc%=\n" \
  "Lc3%=:\n" \
  "v_lshlrev_b32 v91, 1, v87\n" \
  "v_lshlrev_b32 v95, 4, v87\n" \
  "v_lshrrev_b32 v107, 2, v87\n" \
  "v_and_or_b32 v91, v91, v118, %[wb]\n" \
  "v_lshlrev_b32_e64 v103, v95, 4\n" \
  "v_and_b32 v107, 0xfc, v107\n" \
  "s_mov_b64 exec, s[72:73]\n" \
  "ds_add_rtn_u32 v99, v91, v103 offset:%[hi]\n" \
  "ds_add_u32 v107, v116 offset:%[dg]\n" \
  "s_mov_b64 exec, -1\n" \
  "s_branch Lc4%=\n" \
  "Llo%=:\n" \
  "v_mov_b32 v113, 1\n" \
  "ds_write_b32 v117, v113 offset:%[fl]\n" \
  "s_branch Llc%=\n" \
  "Lslow%=:\n" \
  "s_waitcnt vmcnt(0) lgkmcnt(0)\n" \
  "s_barrier\n" \
  "s_branch Lexit%=\n" \
  "Lq3%=:\n" \
  "s_mov_b64 exec, -1\n" \
  "v_and_b32 v103, 0x7fc, v91\n" \
  "ds_read_b32 v79, v91 offset:%[hi]\n" \
  "ds_read_b32 v107, v103 offset:%[to]\n" \
  "s_waitcnt lgkmcnt(0)\n" \
  "v_add3_u32 v91, v79, v107, v99\n" \
  "v_lshrrev_b32 v91, v95, v91\n" \
  "v_bfe_u32 v75, v91, 13, 3\n" \
  "v_and_b32 v91, 0x1ffc, v91\n" \
  "v_or_b32 v75, v87, v75\n" \
  "s_mov_b64 exec, s[72:73]\n" \
  "ds_write_b32 v91, v75 offset:%[so]\n" \
  "s_branch Lq4%=\n" \
  "Lpr%=:\n" \
  "s_cmpk_le_u32 s62, 0xc0\n" \
  "s_cbranch_scc1 Lpb%=\n" \
  "buffer_load_dwordx4 v[44:47], v83, %[ws], %[rp] offen sc1\n" \
  "Lpb%=:\n" \
  "buffer_load_dwordx4 v[32:35], v80, %[ws], %[rp] offen sc1\n" \
  "buffer_load_dwordx4 v[36:39], v81, %[ws], %[rp] offen sc1\n" \
  "buffer_load_dwordx4 v[40:43], v82, %[ws], %[rp] offen sc1\n" \
  "s_sub_u32 s63, s63, 1\n" \
  "s_waitcnt vmcnt(0)\n" \
  "s_cbranch_scc0 Lpc%=\n" \
  "s_branch Lpw%=\n" \
  "Lexit%=:\n" \
  "s_mov_b32 %[sl], s61\n"
#define STREAM_CLOBBERS \
  "v7", "v9", "v10", "v11", "v18", "v19", "v32", "v33", "v34", "v35", "v36", "v37", "v38", "v39", "v40", "v41", \
  "v42", "v43", "v44", "v45", "v46", "v47", "v48", "v49", "v50", "v51", "v52", "v53", "v54", "v55", "v56", \
  "v57", "v58", "v59", "v60", "v61", "v62", "v63", "v64", "v65", "v66", "v67", "v68", "v69", "v70", "v71", \
  "v72", "v73", "v74", "v75", "v76", "v77", "v78", "v79", "v80", "v81", "v82", "v83", "v84", "v85", "v86", \
  "v87", "v88", "v89", "v90", "v91", "v92", "v93", "v94", "v95", "v96", "v97", "v98", "v99", "v100", "v101", \
  "v102", "v103", "v104", "v105", "v106", "v107", "v108", "v109", "v111", "v112", "v113", "v114", "v115", \
  "v116", "v117", "v118", "v119", "v120", "v121", "v122", "v123", "v124", "v125", "v126", "v127", "v128", \
  "v129", "v130", "v131", "v132", "v133", "v134", "v135", "s60", "s61", "s62", "s63", "s64", "s65", "s66", \
  "s67", "s68", "s69", "s70", "s71", "s72", "s73", "s74", "s75", "s76", "s77", "s78", "s79", "scc", "vcc", \
  "memory"
#define RKL 3
#define MERGE 1

namespace {

constexpr int C_ = 128;
constexpr int MW = 12;
constexpr int MT = MW * 64;
constexpr int SW = 8;
constexpr int NS = 5;
constexpr int NPB = SW * NS;
constexpr int NPASS = 16;
constexpr int HS = 64 * NPASS / 2;
constexpr int NBMAX = 256;
constexpr int PW = 5;
constexpr int PT = PW * 64;
constexpr int PK = 4;
constexpr int CH = PT * PK;
constexpr int MK = 4;
constexpr int CAPW = 256;
constexpr unsigned OOB = 0x80000000u;
#define OUTAUX 16
#define PFN 5
#define TB1 1
#define SUMP 1
#define PFS 2
#define WA 3
#define WB 6
#define WC 8
#define TB2 2
#define HS0 0
#define PFP 2
#define HWV 10
#define HSL 24
constexpr unsigned WS_EPOCH = 0;
constexpr unsigned WS_OFF = 4096;
constexpr unsigned WS_RECK = WS_OFF + NBMAX * NBMAX * 16;
constexpr unsigned WS_RECP = WS_RECK + NBMAX * CH * 4;
constexpr unsigned WS_END = WS_RECP + NBMAX * CH * 16;

__device__ __forceinline__ float bf2f(unsigned short v) { return __uint_as_float(((unsigned)v) << 16); }
__device__ __forceinline__ unsigned short f2bf(float f) {
  __hip_bfloat16 b = __float2bfloat16(f);
  return *reinterpret_cast<unsigned short*>(&b);
}
__device__ __forceinline__ unsigned pk2bf(float lo, float hi) {
  return __builtin_bit_cast(unsigned, __builtin_convertvector(f32x2{lo, hi}, bf16x2));
}
__device__ __forceinline__ unsigned wave_incl_scan(unsigned x) {
  x += __builtin_amdgcn_update_dpp(0u, x, 0x111, 0xf, 0xf, false);
  x += __builtin_amdgcn_update_dpp(0u, x, 0x112, 0xf, 0xf, false);
  x += __builtin_amdgcn_update_dpp(0u, x, 0x114, 0xf, 0xf, false);
  x += __builtin_amdgcn_update_dpp(0u, x, 0x118, 0xf, 0xf, false);
  x += __builtin_amdgcn_update_dpp(0u, x, 0x142, 0xa, 0xf, false);
  x += __builtin_amdgcn_update_dpp(0u, x, 0x143, 0xc, 0xf, false);
  return x;
}

struct Lds {
  u32x4 sink[16];
  unsigned flag, rdone, go, pad_;
  unsigned long long tag, pad2_;
  unsigned prog[16];
  int deg[64];
  unsigned byrank[64];
  unsigned nl[64];
  u32x4 ct[72];
  union {
    struct {
      __attribute__((aligned(16))) unsigned so[SW * CAPW];
      union {
        __attribute__((aligned(16))) unsigned hist[(SW + 1) * HS];
        u32x4 row5[SW * 64];
      };
    };
    struct {
      __attribute__((aligned(16))) unsigned sk[CH];
      u32x4 sp[CH];
    };
  };
  union {
    struct {
      __attribute__((aligned(16))) unsigned seg[2][NBMAX];
      unsigned phist[PW][NBMAX / 2];
      unsigned pbase[PW][NBMAX / 2];
      u32x4 ri[SW][MK * 16];
    };
    u32x4 pay[SW * CAPW];
  };
  f32x4 sg[4096];
  u32x4 wf[C_ * C_ / 8];
};
static_assert(sizeof(Lds) <= 163840, "LDS budget");
static_assert(offsetof(Lds, sg) < 65536, "table offset");

__device__ __forceinline__ unsigned lds_addr(const void* p) {
  return (unsigned)(uintptr_t)(__attribute__((address_space(3))) const void*)p;
}
__device__ __forceinline__ u32x4 make_srd(const void* p, unsigned num) {
  unsigned long long a = (unsigned long long)p;
  return u32x4{(unsigned)a, (unsigned)(a >> 32), num, 0x00020000u};
}

}

extern "C" __global__ __launch_bounds__(MT) void tfn_main_kernel(
    const int* dst_, int E_, int c0_, const int* src_, int CE_, int c1_, const u32x2* Y_, int N_, int c2_, const u32x2* ef_,
    unsigned M2_, int c3_, const unsigned short* x_, char* ws_, u32x4* out_, const unsigned short* wrad_,
    const unsigned short* cg_, const unsigned short* wmix_) {
  __shared__ Lds L;
  const int tid = threadIdx.x, lane = tid & 63;
  TDECL;
  STAMP(0);
  u32x2 d0, d1, s0, s1;
  u32x4 y0, y1, f0, f1;
  u32x16 ka;
  u32x8 kb, kc;
  u32x4 kd;
  unsigned long long ev;
  asm volatile(
      "s_load_dwordx16 s[8:23], %[kp], 0\n s_load_dwordx8 s[24:31], %[kp], 0x40\n s_load_dwordx4 s[32:35], %[kp], 0x60\n"
      "v_readfirstlane_b32 s36, %[tid]\n v_lshlrev_b32 v90, 3, %[tid]\n v_mov_b64 v[92:93], 0\n"
      "s_and_b32 s37, %[bid], 7\n s_lshr_b32 s38, %[bid], 3\n s_lshr_b32 s36, s36, 6\n"
      "ds_write_b64 v90, v[92:93] offset:%[zo]\n"
      "s_cmp_lg_u32 s36, %[pw]\n s_cbranch_scc1 1f\n"
      "s_getpc_b64 s[44:45]\n .Ltfn_p0:\n s_max_i32 s46, 0, .Ltfn_p1-.Ltfn_p0\n s_mov_b32 m0, %[sk]\n s_mov_b32 s47, 0x20000\n"
      "v_lshlrev_b32 v91, 12, %[tid]\n s_lshl_b32 s39, s38, 7\n v_and_b32 v91, 0x3f000, v91\n"
      "buffer_load_dword v91, s[44:47], s39 offen lds\n"
      "1:\n s_lshl_b32 s37, s37, 5\n s_add_u32 s37, s37, s38\n v_lshlrev_b32 v91, 4, %[tid]\n"
      "s_waitcnt lgkmcnt(0)\n"
      "s_mul_i32 s38, s37, s14\n s_mov_b32 s40, s10\n s_add_i32 s39, s38, s14\n s_lshl_b32 s44, s38, 2\n"
      "s_min_i32 s39, s39, s10\n s_add_i32 s45, s44, %[h4]\n s_lshl_b32 s10, s39, 2\n s_lshl_b32 s14, s39, 2\n"
      "s_cmp_ge_u32 s36, %[pw]\n s_cbranch_scc1 2f\n"
      "buffer_load_dwordx2 %0, v90, s[8:11], s44 offen\n buffer_load_dwordx2 %1, v90, s[8:11], s45 offen\n"
      "buffer_load_dwordx2 %2, v90, s[12:15], s44 offen\n buffer_load_dwordx2 %3, v90, s[12:15], s45 offen\n"
      "2:\n s_mov_b32 s41, s18\n s_mov_b32 s42, s22\n s_lshl_b32 s18, s39, 3\n s_lshl_b32 s22, s39, 3\n"
      "s_lshl_b32 s44, s44, 1\n s_lshl_b32 s45, s45, 1\n s_cmp_ge_u32 s36, %[pw]\n"
      "s_barrier\n s_cbranch_scc1 3f\n s_setprio 3\n"
      "buffer_load_dwordx4 v[100:103], v91, s[16:19], s44 offen\n buffer_load_dwordx4 v[104:107], v91, s[16:19], s45 offen\n"
      "buffer_load_dwordx4 v[108:111], v91, s[20:23], s44 offen\n buffer_load_dwordx4 v[112:115], v91, s[20:23], s45 offen\n"
      "3:\n s_cmp_lg_u32 s36, %[hw]\n s_cbranch_scc1 4f\n s_load_dwordx2 s[46:47], s[26:27], 0\n s_setprio 3\n"
      "s_waitcnt lgkmcnt(0)\n 4:"
      : "=&v"(d0), "=&v"(d1), "=&v"(s0), "=&v"(s1), "=&{v[100:103]}"(y0), "=&{v[104:107]}"(y1), "=&{v[108:111]}"(f0),
        "=&{v[112:115]}"(f1), "=&{s[8:23]}"(ka), "=&{s[24:31]}"(kb), "=&{s[32:35]}"(kd), "=&{s[36:43]}"(kc),
        "=&{s[46:47]}"(ev)
      : [kp] "s"(__builtin_amdgcn_kernarg_segment_ptr()), [tid] "v"(tid), [bid] "s"(blockIdx.x), [pw] "n"(PW), [hw] "n"(HWV),
        [zo] "n"(offsetof(Lds, seg)), [sk] "n"(offsetof(Lds, sink)), [h4] "n"(CH * 2)
      : "v90", "v91", "v92", "v93", "s44", "s45", "scc", "memory");
  typedef __attribute__((address_space(1))) char gchar;
  auto ptr = [](unsigned lo, unsigned hi) { return (char*)(gchar*)(lo | (unsigned long long)hi << 32); };
  const int* __restrict__ dst = (const int*)ptr(ka[0], ka[1]);
  const int* __restrict__ src = (const int*)ptr(ka[4], ka[5]);
  const u32x2* __restrict__ Y = (const u32x2*)ptr(ka[8], ka[9]);
  const u32x2* __restrict__ ef = (const u32x2*)ptr(ka[12], ka[13]);
  const unsigned short* __restrict__ x = (const unsigned short*)ptr(kb[0], kb[1]);
  char* ws = ptr(kb[2], kb[3]);
  u32x4* __restrict__ out = (u32x4*)ptr(kb[4], kb[5]);
  const unsigned short* __restrict__ wrad = (const unsigned short*)ptr(kb[6], kb[7]);
  const unsigned short* __restrict__ cg = (const unsigned short*)ptr(kd[0], kd[1]);
  const unsigned short* __restrict__ wmix = (const unsigned short*)ptr(kd[2], kd[3]);
  const int w = kc[0], bucket = kc[1], e0 = kc[2] + tid * 2, e1 = kc[3], E = kc[4], N = kc[5];
  const unsigned M2 = kc[6];
  __builtin_assume(w >= 0 && w < MW && bucket >= 0 && bucket < NBMAX);
  const int xcd = blockIdx.x & 7;
  const u32x4 wsd = make_srd(ws, WS_END);
  STAMP(1);
  auto table = [&](int q) {
    f32x4 p, n;
#pragma unroll
    for (int i = 0; i < 4; i++) {
      const unsigned short c = min(q * 16 + i * 4 + 2, 0x7bff);
      const float e = __builtin_amdgcn_exp2f(-1.4426950408889634f * (float)__builtin_bit_cast(_Float16, c));
      p[i] = __builtin_amdgcn_rcpf(1.f + e);
      n[i] = e * p[i];
    }
    L.sg[q] = p;
    L.sg[q + 2048] = n;
  };
  auto valid = [&](int k) { return e0 + (k >> 1) * (CH / 2) + (k & 1) < e1; };
  auto warm = [&](int i0, int i1) {
    const unsigned pfo = ((xcd * N) >> 3) * 1024 + ((blockIdx.x >> 3) * 64 + lane) * 128;
#pragma unroll 1
    for (int i = i0; i < i1; i += MW - SW)
      asm volatile("s_mov_b32 m0, %2\n s_nop 0\n buffer_load_dword %0, %1, %3 offen lds\n"
                   :: "v"(pfo), "s"(make_srd(x, N * 1024)), "s"(lds_addr(L.sink)), "s"(i * 32 * 64 * 128) : "memory");
  };
  unsigned bk[PK], pr[PK], ky[PK], sa[PK];
  unsigned cv, wq4[4];
  asm volatile(
      "s_cmp_lg_u32 %9, %10\n s_cbranch_scc1 1f\n global_load_ushort %0, %5, %7\n global_load_dword %1, %6, %8\n"
      "global_load_dword %2, %6, %8 offset:256\n global_load_dword %3, %6, %8 offset:512\n"
      "global_load_dword %4, %6, %8 offset:768\n 1:"
      : "=&v"(cv), "=&v"(wq4[0]), "=&v"(wq4[1]), "=&v"(wq4[2]), "=&v"(wq4[3])
      : "v"(lane * 2), "v"(lane * 4), "s"(cg), "s"(wrad), "s"(w), "n"(PW) : "scc");
  auto wdma = [&](int i0, int i1) {
#pragma unroll
    for (int i = i0; i < i1; i++) {
      const unsigned o = ((w & 3) * 8 + ((i + (blockIdx.x >> 3)) & 7)) * 1024;
      asm volatile("s_mov_b32 m0, %2\n s_nop 0\n buffer_load_dwordx4 %0, %1, %3 offen lds\n"
                   :: "v"(lane * 16), "s"(make_srd(wmix, C_ * C_ * 2)), "s"(lds_addr(L.wf) + o), "s"(o) : "memory");
    }
  };
  const int zt = tid - PT - (w > HWV ? 64 : 0);
  unsigned so1 = (tid - PT) * 16 + WS_RECP + bucket * (CH * 16);
  unsigned so0 = w < PW ? tid * 16 + WS_RECK + bucket * (CH * 4) : so1;
  so1 += MT * 16;
  unsigned ho0 = w < SW ? ((lane >> 1) * SW + w) * (NBMAX * 16) : lane * 16 + WS_OFF + bucket * (NBMAX * 16);
  int node0 = __builtin_amdgcn_readfirstlane(((unsigned)(bucket << 22) + M2 - 1) / M2);
  int nn = min(N, __builtin_amdgcn_readfirstlane(((unsigned)((bucket + 1) << 22) + M2 - 1) / M2)) - node0;
  asm volatile("" : "+v"(so0), "+v"(so1), "+v"(ho0), "+s"(node0), "+s"(nn));
  if (w < PW) {
    unsigned one[PK], far[PK];
#pragma unroll
    for (int k = 0; k < PK; k++) {
      one[k] = valid(k) ? 1u : 0u;
      far[k] = valid(k) ? 0u : CH;
    }
    asm volatile("s_waitcnt vmcnt(4)" : "+v"(d0), "+v"(d1), "+v"(s0), "+v"(s1), "+v"(one[0]), "+v"(one[1]), "+v"(one[2]),
                 "+v"(one[3]), "+v"(far[0]), "+v"(far[1]), "+v"(far[2]), "+v"(far[3]));
    STAMP(3);
    const u32x4 dv = {d0[0], d0[1], d1[0], d1[1]}, sv = {s0[0], s0[1], s1[0], s1[1]};
#pragma unroll
    for (int k = 0; k < PK; k++) {
      bk[k] = (unsigned)__umul24(dv[k], M2) >> 22;
      pr[k] = atomicAdd(&L.phist[w][bk[k] >> 1], one[k] << ((bk[k] * 16) & 31));
    }
    __builtin_amdgcn_sched_barrier(0);
#pragma unroll
    for (int k = 0; k < PK; k++) {
      ky[k] = (sv[k] << 10) | ((dv[k] & 63) << 4) | ((((unsigned)__umul24(sv[k], M2) >> 26) - ((bk[k] >> 4) & 14)) & 15);
      sa[k] = lds_addr(&L.seg[0][bk[k]]);
      asm volatile("ds_add_u32 %0, %1" :: "v"(sa[k]), "v"(ky[k]) : "memory");
    }
#pragma unroll
    for (int k = 0; k < PK; k++) {
      pr[k] = __builtin_amdgcn_ubfe(pr[k], bk[k] * 16, 16) + far[k];
      bk[k] = lds_addr(L.pbase[w]) + bk[k] * 2;
      asm volatile("" : "+v"(pr[k]), "+v"(bk[k]));
    }
  } else {
    if (w == PW) {
      L.deg[lane] = ((lane - node0) & 63) < NPB ? 63 - ((lane - node0) & 63) : -1;
      if (lane < 4) (&L.flag)[lane] = 0;
      if (lane < 16) L.prog[lane] = 255;
    }
    if (w == HWV) {
      ev = ev * 6364136223846793005ull + 1442695040888963407ull;
      L.tag = ev;
    } else {
#pragma unroll 1
      for (int q = zt; q < TB1 * 384; q += 384) table(q);
    }
  }
  STAMP(4);
  asm volatile("s_waitcnt lgkmcnt(0)\n s_barrier" ::: "memory");
  STAMP(5);
  const auto wsr = __builtin_amdgcn_make_buffer_rsrc((void*)ws, 0, WS_END, 0x00020000);
  auto lds_ld = [&](const unsigned* p) {
    return __builtin_amdgcn_readfirstlane(__hip_atomic_load(p, __ATOMIC_RELAXED, __HIP_MEMORY_SCOPE_WORKGROUP));
  };
  if (w == HWV) {
    const unsigned tl = (unsigned)ev, th = (unsigned)(ev >> 32);
    unsigned c[4] = {0, 0, 0, 0};
    u32x4 hv[4];
#pragma unroll
    for (int k = 0; k < 4; k++) {
#pragma unroll
      for (int ww = 0; ww < PW; ww++) c[k] += reinterpret_cast<const unsigned short*>(L.phist[ww])[k * 64 + lane];
      hv[k][1] = L.seg[0][k * 64 + lane];
    }
    const unsigned p0 = c[0] | (c[1] << 16), p1 = c[2] | (c[3] << 16);
    const unsigned i0 = wave_incl_scan(p0), i1 = wave_incl_scan(p1);
    const unsigned t0 = __builtin_amdgcn_readlane(i0, 63), t1 = __builtin_amdgcn_readlane(i1, 63);
    const unsigned s1 = t0 & 0xffff, s2 = s1 + (t0 >> 16), s3 = s2 + (t1 & 0xffff);
    const unsigned bs[4] = {(i0 - p0) & 0xffff, ((i0 - p0) >> 16) + s1, ((i1 - p1) & 0xffff) + s2, ((i1 - p1) >> 16) + s3};
#pragma unroll
    for (int k = 0; k < 4; k++) {
      hv[k][0] = bs[k] | (c[k] << 11) | (tl << 22);
      hv[k][1] += (unsigned)__umul24(c[k], tl & 255) << 24;
      hv[k][3] = th - hv[k][0] - hv[k][1];
    }
    asm volatile("" : "+v"(hv[0]), "+v"(hv[1]), "+v"(hv[2]), "+v"(hv[3]));
    while (true) {
      const unsigned gv = __hip_atomic_load(&L.go, __ATOMIC_RELAXED, __HIP_MEMORY_SCOPE_WORKGROUP);
      asm volatile("" ::: "memory");
#pragma unroll
      for (int k = 0; k < 4; k++) hv[k][2] = L.seg[1][k * 64 + lane];
      asm volatile("" ::: "memory");
      if (__builtin_amdgcn_readfirstlane(gv) == PW) break;
    }
#pragma unroll
    for (int k = 0; k < 4; k++) {
      hv[k][3] -= hv[k][2];
      __builtin_amdgcn_raw_buffer_store_b128(hv[k], wsr, ho0 + k * 1024, 0, 16);
    }
  } else if (w < PW) {
    const unsigned t8 = (unsigned)L.tag << 24;
    unsigned ca = 0, cb = 0, pa = 0, pb = 0, hh[PW][2];
#pragma unroll
    for (int ww = 0; ww < PW; ww++) {
      hh[ww][0] = L.phist[ww][lane];
      hh[ww][1] = L.phist[ww][64 + lane];
    }
    __builtin_amdgcn_sched_barrier(0);
#pragma unroll
    for (int ww = 0; ww < PW; ww++) {
      const unsigned ha = hh[ww][0], hb = hh[ww][1];
      pa = ww == w ? ca : pa;
      pb = ww == w ? cb : pb;
      ca += ha;
      cb += hb;
    }
    const unsigned t = __builtin_amdgcn_perm(cb, ca, 0x05040100) + __builtin_amdgcn_perm(cb, ca, 0x07060302);
    const unsigned inc = wave_incl_scan(t), ex = inc - t;
    L.pbase[w][lane] = (ex & 0xffff) * 0x10001u + (ca << 16) + pa;
    L.pbase[w][64 + lane] = ((ex >> 16) + (__builtin_amdgcn_readlane(inc, 63) & 0xffff)) * 0x10001u + (cb << 16) + pb;
    STAMP(6);
    unsigned b4[PK];
#pragma unroll
    for (int k = 0; k < PK; k++) b4[k] = *(__attribute__((address_space(3))) unsigned short*)(uintptr_t)bk[k];
    __builtin_amdgcn_sched_barrier(0);
    {
      unsigned t0, t1, t2, t3;
      asm volatile(
          "s_waitcnt vmcnt(0)\n"
          "v_add3_u32 %0, v100, v101, v108\n v_add3_u32 %1, v102, v103, v110\n v_add3_u32 %2, v104, v105, v112\n"
          "v_add3_u32 %3, v106, v107, v114\n"
          "v_add_u32 %0, %0, v109\n v_add_u32 %1, %1, v111\n v_add_u32 %2, %2, v113\n v_add_u32 %3, %3, v115\n"
          "ds_add_u32 %8, %0 offset:1024\n ds_add_u32 %9, %1 offset:1024\n ds_add_u32 %10, %2 offset:1024\n"
          "ds_add_u32 %11, %3 offset:1024\n"
          "s_mov_b64 exec, 1\n ds_add_u32 %12, %13\n s_mov_b64 exec, -1"
          : "=&v"(t0), "=&v"(t1), "=&v"(t2), "=&v"(t3), "+{v[100:103]}"(y0), "+{v[104:107]}"(y1), "+{v[108:111]}"(f0),
            "+{v[112:115]}"(f1)
          : "v"(sa[0]), "v"(sa[1]), "v"(sa[2]), "v"(sa[3]), "v"(lds_addr(&L.go)), "v"(1) : "memory");
    }
#pragma unroll
    for (int k = 0; k < PK; k++) {
      pr[k] = min(pr[k] + b4[k], CH - 1u);
      L.sk[pr[k]] = ky[k] | t8;
      pr[k] = lds_addr(&L.sp[pr[k]]);
    }
    asm volatile(
        "ds_write2_b64 %0, v[100:101], v[108:109] offset1:1\n ds_write2_b64 %1, v[102:103], v[110:111] offset1:1\n"
        "ds_write2_b64 %2, v[104:105], v[112:113] offset1:1\n ds_write2_b64 %3, v[106:107], v[114:115] offset1:1\n"
        "s_waitcnt lgkmcnt(0)"
        :: "v"(pr[0]), "v"(pr[1]), "v"(pr[2]), "v"(pr[3]),
           "{v[100:103]}"(y0), "{v[104:107]}"(y1), "{v[108:111]}"(f0), "{v[112:115]}"(f1) : "memory");
  } else {
    if (w == PW) {
      asm volatile("s_waitcnt   vmcnt(0)" : "+v"(cv), "+v"(wq4[0]), "+v"(wq4[1]), "+v"(wq4[2]), "+v"(wq4[3]));
      reinterpret_cast<unsigned short*>(L.ct)[(lane & 3) * 16 + (lane >> 2)] = cv;
      L.ct[8 + lane] = u32x4{__builtin_amdgcn_perm(wq4[1], wq4[0], 0x05040100), __builtin_amdgcn_perm(wq4[3], wq4[2], 0x05040100),
                             __builtin_amdgcn_perm(wq4[1], wq4[0], 0x07060302), __builtin_amdgcn_perm(wq4[3], wq4[2], 0x07060302)};
    }
    if (w > PW && w < PW + 5) wdma(0, WA);
#pragma unroll 1
    for (int q = zt + TB1 * 384; q < TB2 * 384; q += 384) table(q);
    __builtin_amdgcn_s_setprio(3);
  }
  STAMP(7);
  __syncthreads();
  STAMP(27);
  const int st = tid - SW * 64;
  {
    const u32x4 v0 = reinterpret_cast<const u32x4*>(L.sk)[tid], v1 = reinterpret_cast<const u32x4*>(L.sk)[MT + tid];
    __builtin_amdgcn_raw_buffer_store_b128(v0, wsr, so0, 0, 16);
    __builtin_amdgcn_raw_buffer_store_b128(v1, wsr, so1, 0, 16);
    if (w == PW)
      __builtin_amdgcn_raw_buffer_store_b128(reinterpret_cast<const u32x4*>(L.sk)[2 * MT + lane], wsr, so1 + (MT - PT) * 16, 0, 16);
  }
  if (w >= SW) wdma(WA, WB);
  STAMP(8);
  __syncthreads();
  STAMP(9);
  const unsigned long long epoch = L.tag;
  const unsigned tagl = __builtin_amdgcn_readfirstlane((unsigned)epoch);
  const unsigned tag1 = __builtin_amdgcn_readfirstlane((unsigned)(epoch >> 32));
  const int row = lane & 15, kq = lane >> 4;
  if (w >= SW) {
    __builtin_amdgcn_s_setprio(0);
    table(st + TB2 * 384);
    table(st + TB2 * 384 + 256);
    wdma(WB, WC);
#pragma unroll 1
    for (int q = st + TB2 * 384 + 512; q < 2048; q += MT - SW * 64) table(q);
    __builtin_amdgcn_s_setprio(3);
    constexpr int HR = (NPB - RKL * SW) / (MW - SW);
    const int id = (node0 + RKL * SW + (w - SW) * HR + lane) & 63;
    __builtin_amdgcn_s_barrier();
    wdma(WC, 8);
    if (WC == 8) asm volatile("s_waitcnt     vmcnt(0)" ::: "memory");
    __builtin_amdgcn_s_sleep(PFS);
    warm(w - SW, PFN);
    __syncthreads();
    {
      const int dg = L.deg[lane], dm = L.deg[id];
      int dn[HR];
#pragma unroll
      for (int i = 0; i < HR; i++) dn[i] = __builtin_amdgcn_readlane(dm, i);
      __builtin_amdgcn_sched_barrier(0);
      unsigned long long m[HR];
#pragma unroll
      for (int i = 0; i < HR; i++) m[i] = __ballot(dg > dn[i]);
      __builtin_amdgcn_sched_barrier(0);
      unsigned r = 63;
#pragma unroll
      for (int i = 0; i < HR; i++) asm("v_writelane_b32 %0, %1, %2" : "+v"(r) : "s"(__popcll(m[i])), "n"(i));
      L.byrank[r] = id * 4;
    }
    __syncthreads();
    if (bucket == 0 && tid == MT - 1)
      __builtin_amdgcn_raw_buffer_store_b64(u32x2{tagl, tag1}, wsr, WS_EPOCH, 0, 16);
    __builtin_amdgcn_s_barrier();
    if (!MERGE) __builtin_amdgcn_s_barrier();
    __builtin_amdgcn_s_setprio(0);
    {
      if (WC < 8) asm volatile("s_waitcnt     vmcnt(1)" ::: "memory");
      const int kk = w - SW;
      const u32x4* wp = L.wf + (kk * 32 + (lane >> 4) * 8) * 16 + (lane & 15);
      u32x4 q[8];
#pragma unroll
      for (int j = 0; j < 8; j++) q[j] = wp[j * 16];
      asm volatile("" : "+v"(q[0]), "+v"(q[1]), "+v"(q[2]), "+v"(q[3]), "+v"(q[4]), "+v"(q[5]), "+v"(q[6]), "+v"(q[7]) :: "memory");
      u32x4* fp = &L.wf[(kk * 8 + ((lane >> 2) & 3) * 2) * 64 + (lane & 48) + (lane & 3) * 4];
#pragma unroll
      for (int i = 0; i < 4; i++) {
        fp[i] = u32x4{__builtin_amdgcn_perm(q[1][i], q[0][i], 0x05040100), __builtin_amdgcn_perm(q[3][i], q[2][i], 0x05040100),
                      __builtin_amdgcn_perm(q[5][i], q[4][i], 0x05040100), __builtin_amdgcn_perm(q[7][i], q[6][i], 0x05040100)};
        fp[64 + i] = u32x4{__builtin_amdgcn_perm(q[1][i], q[0][i], 0x07060302), __builtin_amdgcn_perm(q[3][i], q[2][i], 0x07060302),
                           __builtin_amdgcn_perm(q[5][i], q[4][i], 0x07060302), __builtin_amdgcn_perm(q[7][i], q[6][i], 0x07060302)};
      }
    }
    __syncthreads();
  } else {
    reinterpret_cast<u32x4*>(&L.hist[w * HS])[lane] = u32x4{0, 0, 0, 0};
    reinterpret_cast<u32x4*>(&L.hist[w * HS])[64 + lane] = u32x4{0, 0, 0, 0};
    L.ri[w][lane] = u32x4{0x20000000u, 0x20000000u, 0x20000000u, 0x20000000u};
    const unsigned long long t0 = w & 1 ? 0x26796281267d7281ull : 0x277dc200272c8180ull, t1 = w & 1 ? 0x246942c324755245ull : 0x25755242217984c2ull;
    const unsigned long long t2 = w & 1 ? 0x22613345226d6483ull : 0x236d130423694307ull, t3 = w & 1 ? 0x206503c725710384ull : 0x215d2386206513cdull;
    const unsigned long long t = w & 4 ? (w & 2 ? t3 : t2) : (w & 2 ? t1 : t0);
    const unsigned rks = nn == NPB ? t >> 32 : t;
    const int start = w * CAPW;
    unsigned sop = lds_addr(&L.so[start + (lane & 15)]);
    unsigned s4p = lds_addr(&L.so[start + (lane >> 2)]);
    unsigned yp = lds_addr(&L.pay[start + (lane >> 2)]);
    unsigned ep = lds_addr(&L.pay[start + (lane & 15)]) + 8;
    u32x4 cg0 = L.ct[(lane & 3) * 2];
    const u32x4 cg1 = L.ct[(lane & 3) * 2 + 1], wq = L.ct[8 + lane];
    const unsigned rp = lds_addr(&L.pay[start]) + kq * 256 + row * 4;
    unsigned pg = lds_addr(&L.prog[w]), oo = node0 * 1024 + row * 16, slowv;
    u32x16 cs = {ho0,
                 (unsigned)(((lane >> 1) * SW + w) * CH + (lane & 1)) * 4,
                 lds_addr(L.ri[w]) + (lane & 1) * 4, lds_addr(L.ri[w]) + lane * 4, (lane & 1) - 1u, lane & 1u,
                 lds_addr(&L.hist[tid]), lds_addr(&L.byrank[(rks >> (min(lane & 7, NS - 1) * 6)) & 63]),
                 lds_addr(&L.byrank[(rks >> (kq * 6)) & 63]), lds_addr(&L.hist[SW * HS + (lane >> 3)]),
                 (start * 4 + ((lane & 7) << 13)) * 0x10001u, (unsigned)(start + lane) * 4,
                 lds_addr(&L.nl[w * 8 + (lane & 7)]), lane * 4u, ((node0 + w * RKL + lane) & 63) * 4u, 0};
    asm volatile(STREAM_ASM
                 : [sl] "=s"(slowv), "+{v12}"(sop), "+{v13}"(s4p), "+{v14}"(yp), "+{v15}"(ep), "+{v[136:151]}"(cs),
                   "+{v[20:23]}"(cg0), "+{v110}"(pg), "+{v159}"(oo)
                 : "{v8}"(lane * 16), "{v16}"(0xfffcu), "{v17}"((1u << (lane & 3)) >> 1),
                   "{v[24:27]}"(cg1), "{v[28:31]}"(wq), "{v[152:155]}"(u32x4{rp, rp + 64, rp + 128, rp + 192}),
                   "{v156}"(lds_addr(&L.row5[w * 64]) + kq * 256 + row * 4),
                   "{v157}"(lds_addr(&L.pay[start + (row >> 2) * 64 + (row & 3) * 4 + kq])), "{v158}"(lds_addr(&L.wf[lane])),
                   [xs] "s"(make_srd(x, N * 1024)), [os] "s"(make_srd(out, (node0 + nn) * 1024)), [ws] "s"(wsd),
                   [ho] "s"(WS_OFF + bucket * 16), [rk] "s"(WS_RECK), [rp] "s"(WS_RECP), [t0] "s"(tagl << 22), [t1] "s"(tag1),
                   [wb] "s"(w * (HS * 4)), [n0] "s"(node0 * 4), [bit] "s"(1u << w), [tb] "n"(offsetof(Lds, sg)),
                   [rd] "n"(offsetof(Lds, rdone)), [fl] "n"(offsetof(Lds, flag)),
                   [hi] "n"(offsetof(Lds, hist)), [to] "n"(offsetof(Lds, hist) + SW * HS * 4), [dg] "n"(offsetof(Lds, deg)),
                   [br] "n"(offsetof(Lds, byrank)), [so] "n"(offsetof(Lds, so)), [pa] "n"(offsetof(Lds, pay))
                 : STREAM_CLOBBERS);
  }
  {
  int lane;
  asm volatile("v_mbcnt_lo_u32_b32 %0, -1, 0\n v_mbcnt_hi_u32_b32 %0, -1, %0" : "=v"(lane));
  __builtin_assume(lane >= 0 && lane < 64);
  const int row = lane & 15, kq = lane >> 4;
  const int ht = (w - SW) >> 1;
  u32x4* rows = w < SW ? &L.pay[w * CAPW] : &L.row5[ht * 256];
  const u32x4* wfp = &L.wf[(w < SW ? 0 : (w & 1) * 256) + lane];
  auto wfrag = [&](int i) {
    return __builtin_bit_cast(s16x8, wfp[(((i >> 1) & 3) * 8 + (i >> 3) * 2 + (i & 1)) * 64]);
  };
  unsigned oo = (node0 + ((L.nl[w < SW ? w * 8 + kq : (ht * 4 + kq) * 8 + 4] / 4 - node0) & 63)) * 1024 + row * 16 + (w < SW ? 0 : (w & 1) * 512);
  s16x8 wv[16];
  if (w >= SW) {
#pragma unroll
    for (int i = 0; i < 16; i++) wv[i] = wfrag(i);
    const unsigned need = 15u << (ht * 4);
    __builtin_amdgcn_s_sleep(HS0);
    while (lds_ld(&L.prog[ht * 4]) > 4 && lds_ld(&L.rdone) == 0) __builtin_amdgcn_s_sleep(HSL);
    while ((lds_ld(&L.rdone) & need) != need) __builtin_amdgcn_s_sleep(2);
  } else {
#pragma unroll 1
    for (int it = 0; it < 2 * NS; it++) {
      const int sl = it >> 1, slice = it & 1, ch = slice * 64 + lane;
      const unsigned dl = (L.nl[w * 8 + sl] / 4 - node0) & 63;
      float acc[4] = {0.f, 0.f, 0.f, 0.f};
#pragma unroll 1
      for (int eb = 0; eb < E; eb += 64) {
        unsigned long long m = __ballot(eb + lane < E && (unsigned)dst[min(eb + lane, E - 1)] == node0 + dl);
#pragma unroll 1
        for (; m; m &= m - 1) {
          const int e = eb + __builtin_ctzll(m);
          const u32x2 ry = Y[e], rf = ef[e];
          float yy[4] = {bf2f(ry[0] & 0xffff), bf2f(ry[0] >> 16), bf2f(ry[1] & 0xffff), bf2f(ry[1] >> 16)};
          float ee[4] = {bf2f(rf[0] & 0xffff), bf2f(rf[0] >> 16), bf2f(rf[1] & 0xffff), bf2f(rf[1] >> 16)};
          float z = 0.f;
#pragma unroll
          for (int k = 0; k < 4; k++) z += ee[k] * bf2f(wrad[k * C_ + ch]);
          float R = z / (1.f + __expf(-z));
          const unsigned short* xr = x + (size_t)src[e] * 512 + ch * 4;
#pragma unroll 1
          for (int o = 0; o < 4; o++) {
            float tp = 0.f;
#pragma unroll 1
            for (int i = 0; i < 4; i++) {
              float A = 0.f;
#pragma unroll
              for (int f = 0; f < 4; f++) A += yy[f] * bf2f(cg[i * 16 + f * 4 + o]);
              tp += bf2f(xr[i]) * bf2f(f2bf(A));
            }
            float v = R * tp;
            acc[0] += o == 0 ? v : 0.f;
            acc[1] += o == 1 ? v : 0.f;
            acc[2] += o == 2 ? v : 0.f;
            acc[3] += o == 3 ? v : 0.f;
          }
        }
      }
      u32x4* rw = sl < 4 ? &rows[sl * 64] : &L.row5[w * 64];
      unsigned short* ar = reinterpret_cast<unsigned short*>(&rw[slice * 32]) + (lane >> 5) * 128 + (lane & 31);
#pragma unroll
      for (int o = 0; o < 4; o++) ar[o * 32] = f2bf(acc[o]);
    }
    asm volatile("" ::: "memory");
    if (lane == 0) atomicOr(&L.rdone, 1u << w);
#pragma unroll
    for (int i = 0; i < 16; i++) wv[i] = wfrag(i);
  }
  asm volatile("" ::: "memory");
  const u32x4* op = &rows[(row >> 2) * 64 + (row & 3) * 4 + kq];
  s16x8 f[4];
#pragma unroll
  for (int k = 0; k < 4; k++) f[k] = __builtin_bit_cast(s16x8, op[k * 16]);
  const auto ors = __builtin_amdgcn_make_buffer_rsrc((void*)out, 0, (node0 + nn) * 1024, 0x00020000);
  auto pair = [&](int m, bool more) {
    f32x4 a = {0.f, 0.f, 0.f, 0.f}, b = {0.f, 0.f, 0.f, 0.f};
#pragma unroll
    for (int k = 0; k < 4; k++) {
      const int i = (m & 1) * 8 + 2 * k;
      a = __builtin_amdgcn_mfma_f32_16x16x32_bf16(f[k], wv[i], a, 0, 0, 0);
      b = __builtin_amdgcn_mfma_f32_16x16x32_bf16(f[k], wv[i + 1], b, 0, 0, 0);
      if (more) {
        wv[i] = wfrag(i + 16);
        wv[i + 1] = wfrag(i + 17);
      }
    }
    __builtin_amdgcn_raw_buffer_store_b128(
        u32x4{pk2bf(a[0], a[1]), pk2bf(a[2], a[3]), pk2bf(b[0], b[1]), pk2bf(b[2], b[3])}, ors, oo + m * 256, 0, OUTAUX);
  };
  if (w < SW) {
    pair(0, true);
    pair(1, true);
    oo += 512;
  }
  pair(0, false);
  pair(1, false);
  }
  asm volatile(".Ltfn_p1:");
  STAMP(26);
  TFLUSH;
}

extern "C" void kernel_launch(void* const* d_in, const int* in_sizes, int n_in, void* d_out, int out_size,
                              void* d_ws, size_t ws_size, hipStream_t stream) {
  const unsigned short* x = (const unsigned short*)d_in[0];
  const int* src = (const int*)d_in[1];
  const int* dst = (const int*)d_in[2];
  const u32x2* Y = (const u32x2*)d_in[3];
  const u32x2* ef = (const u32x2*)d_in[4];
  const unsigned short* wrad = (const unsigned short*)d_in[5];
  const unsigned short* cg = (const unsigned short*)d_in[6];
  const unsigned short* wmix = (const unsigned short*)d_in[7];
  const int N = in_sizes[0] / 512;
  const int E = in_sizes[1];
  const int CE = ((E + NBMAX - 1) / NBMAX + 1) & ~1;
  const unsigned M2 = ((unsigned)NBMAX << 22) / (unsigned)N;
  tfn_main_kernel<<<256, MT, 0, stream>>>(dst, E, 0x20000, src, CE, 0x20000, Y, N, 0x20000, ef, M2, 0x20000, x, (char*)d_ws,
                                           (u32x4*)d_out, wrad, cg, wmix);
}
```

```cpp
#include <hip/hip_runtime.h>
#include <hip/hip_bf16.h>
#include <stddef.h>
#include <stdint.h>

typedef __attribute__((ext_vector_type(2))) float f32x2;
typedef __attribute__((ext_vector_type(4))) float f32x4;
typedef __attribute__((ext_vector_type(8))) float f32x8;
typedef __attribute__((ext_vector_type(16))) float f32x16;
typedef __attribute__((ext_vector_type(8))) short s16x8;
typedef __attribute__((ext_vector_type(2))) unsigned int u32x2;
typedef __attribute__((ext_vector_type(3))) unsigned int u32x3;
typedef __attribute__((ext_vector_type(4))) unsigned int u32x4;
typedef __attribute__((ext_vector_type(8))) unsigned int u32x8;
typedef __attribute__((ext_vector_type(16))) unsigned int u32x16;
typedef __attribute__((ext_vector_type(2))) __bf16 bf16x2;

#define STREAM_ASM \
  "s_mov_b32 s63, 0x10000\n" \
  "Lh%=:\n" \
  "buffer_load_dwordx4 v[72:75], v136, %[ws], %[ho] offen sc1\n" \
  "v_mov_b32 v117, 0\n" \
  "v_mov_b32 v116, 64\n" \
  "v_mov_b32 v87, 0\n" \
  "v_mov_b64 v[44:45], 0\n" \
  "v_mov_b64 v[46:47], 0\n" \
  "v_mov_b32 v118, 0x7fc\n" \
  "s_waitcnt vmcnt(0)\n" \
  "v_add3_u32 v108, v72, v73, v74\n" \
  "v_xor_b32 v109, %[t0], v72\n" \
  "v_add_u32 v108, v108, v75\n" \
  "v_cmp_lt_u32 vcc, 0x3fffff, v109\n" \
  "v_cmp_ne_u32_e64 s[74:75], %[t1], v108\n" \
  "s_or_b64 vcc, vcc, s[74:75]\n" \
  "s_cbranch_scc1 Lhr%=\n" \
  "s_mov_b64 exec, -1\n" \
  "v_bfe_u32 v108, v72, 11, 11\n" \
  "v_and_b32 v109, v140, v108\n" \
  "v_and_b32 v112, 0x7ff, v72\n" \
  "v_sub_u32 v111, v108, v141\n" \
  "v_add_u32_dpp v109, v109, v109 row_shr:1 row_mask:0xf bank_mask:0xf bound_ctrl:1\n" \
  "v_lshl_add_u32 v112, v112, 2, v137\n" \
  "v_and_b32 v114, v140, v73\n" \
  "v_add_u32_dpp v109, v109, v109 row_shr:2 row_mask:0xf bank_mask:0xf bound_ctrl:1\n" \
  "v_and_b32 v115, v140, v74\n" \
  "v_sub_u32 v114, 0, v114\n" \
  "v_add_u32_dpp v109, v109, v109 row_shr:4 row_mask:0xf bank_mask:0xf bound_ctrl:1\n" \
  "v_sub_u32 v115, 0, v115\n" \
  "s_nop 0\n" \
  "v_add_u32_dpp v109, v109, v109 row_shr:8 row_mask:0xf bank_mask:0xf bound_ctrl:1\n" \
  "s_nop 1\n" \
  "v_add_u32_dpp v109, v109, v109 row_bcast:15 row_mask:0xa bank_mask:0xf\n" \
  "s_nop 1\n" \
  "v_add_u32_dpp v109, v109, v109 row_bcast:31 row_mask:0xc bank_mask:0xf\n" \
  "v_sub_u32 v108, v109, v108\n" \
  "v_readlane_b32 s62, v109, 63\n" \
  "v_lshl_add_u32 v113, v108, 2, v138\n" \
  "s_cmpk_gt_u32 s62, 0x100\n" \
  "s_cbranch_scc1 Lover%=\n" \
  "Lw%=:\n" \
  "v_cmpx_lt_i32 vcc, 0, v111\n" \
  "ds_write_b32 v113, v112 offset:0\n" \
  "v_add_u32 v112, 8, v112\n" \
  "v_cmpx_lt_i32 vcc, 2, v111\n" \
  "ds_write_b32 v113, v112 offset:8\n" \
  "v_add_u32 v112, 8, v112\n" \
  "v_cmpx_lt_i32 vcc, 4, v111\n" \
  "ds_write_b32 v113, v112 offset:16\n" \
  "v_add_u32 v112, 8, v112\n" \
  "v_cmpx_lt_i32 vcc, 6, v111\n" \
  "ds_write_b32 v113, v112 offset:24\n" \
  "v_add_u32 v112, 8, v112\n" \
  "v_cmpx_lt_i32 vcc, 8, v111\n" \
  "ds_write_b32 v113, v112 offset:32\n" \
  "v_add_u32 v112, 8, v112\n" \
  "s_cbranch_execz Lwd%=\n" \
  "v_cmpx_lt_i32 vcc, 10, v111\n" \
  "ds_write_b32 v113, v112 offset:40\n" \
  "v_add_u32 v112, 8, v112\n" \
  "s_cbranch_execz Lwd%=\n" \
  "v_cmpx_lt_i32 vcc, 12, v111\n" \
  "ds_write_b32 v113, v112 offset:48\n" \
  "v_add_u32 v112, 8, v112\n" \
  "s_cbranch_execz Lwd%=\n" \
  "v_cmpx_lt_i32 vcc, 14, v111\n" \
  "ds_write_b32 v113, v112 offset:56\n" \
  "v_add_u32 v112, 8, v112\n" \
  "v_add_u32 v113, 64, v113\n" \
  "v_subrev_u32 v111, 16, v111\n" \
  "s_branch Lw%=\n" \
  "Lwd%=:\n" \
  "s_mov_b64 exec, -1\n" \
  "Lld%=:\n" \
  "ds_read2st64_b32 v[76:77], v139 offset1:1\n" \
  "ds_read2st64_b32 v[78:79], v139 offset0:2 offset1:3\n" \
  "s_mov_b32 s63, 0x10000\n" \
  "s_waitcnt lgkmcnt(0)\n" \
  "buffer_load_dword v84, v76, %[ws], %[rk] offen sc1\n" \
  "buffer_load_dword v85, v77, %[ws], %[rk] offen sc1\n" \
  "buffer_load_dword v86, v78, %[ws], %[rk] offen sc1\n" \
  "s_cmpk_le_u32 s62, 0xc0\n" \
  "s_cbranch_scc1 Lka%=\n" \
  "buffer_load_dword v87, v79, %[ws], %[rk] offen sc1\n" \
  "Lka%=:\n" \
  "v_lshlrev_b32 v80, 2, v76\n" \
  "v_lshlrev_b32 v81, 2, v77\n" \
  "v_lshlrev_b32 v82, 2, v78\n" \
  "v_lshlrev_b32 v83, 2, v79\n" \
  "s_barrier\n" \
  "s_cmpk_le_u32 s62, 0xc0\n" \
  "s_cbranch_scc1 Lpa%=\n" \
  "buffer_load_dwordx4 v[44:47], v83, %[ws], %[rp] offen sc1\n" \
  "Lpa%=:\n" \
  "buffer_load_dwordx4 v[32:35], v80, %[ws], %[rp] offen sc1\n" \
  "buffer_load_dwordx4 v[36:39], v81, %[ws], %[rp] offen sc1\n" \
  "buffer_load_dwordx4 v[40:43], v82, %[ws], %[rp] offen sc1\n" \
  "v_cmp_le_i32_e64 s[66:67], 0, v80\n" \
  "v_cmp_le_i32_e64 s[68:69], 0, v81\n" \
  "v_cmp_le_i32_e64 s[70:71], 0, v82\n" \
  "v_cmp_le_i32_e64 s[72:73], 0, v83\n" \
  "v_mov_b64 v[48:49], 0\n" \
  "v_mov_b64 v[50:51], 0\n" \
  "v_mov_b64 v[52:53], 0\n" \
  "v_mov_b64 v[54:55], 0\n" \
  "v_mov_b64 v[56:57], 0\n" \
  "v_mov_b64 v[58:59], 0\n" \
  "v_mov_b64 v[60:61], 0\n" \
  "v_mov_b64 v[62:63], 0\n" \
  "v_mov_b64 v[64:65], 0\n" \
  "v_mov_b64 v[66:67], 0\n" \
  "v_mov_b64 v[68:69], 0\n" \
  "v_mov_b64 v[70:71], 0\n" \
  "s_waitcnt vmcnt(3)\n" \
  "Lk%=:\n" \
  "v_add3_u32 v108, v84, v85, v86\n" \
  "v_add3_u32 v108, v108, v87, v114\n" \
  "v_lshlrev_b32 v88, 1, v84\n" \
  "v_lshlrev_b32 v92, 4, v84\n" \
  "v_add_u32_dpp v108, v108, v108 row_shr:1 row_mask:0xf bank_mask:0xf bound_ctrl:1\n" \
  "v_lshrrev_b32 v104, 2, v84\n" \
  "v_and_or_b32 v88, v88, v118, %[wb]\n" \
  "v_add_u32_dpp v108, v108, v108 row_shr:2 row_mask:0xf bank_mask:0xf bound_ctrl:1\n" \
  "v_lshlrev_b32_e64 v100, v92, 4\n" \
  "v_and_b32 v104, 0xfc, v104\n" \
  "v_add_u32_dpp v108, v108, v108 row_shr:4 row_mask:0xf bank_mask:0xf bound_ctrl:1\n" \
  "v_lshlrev_b32 v89, 1, v85\n" \
  "v_lshlrev_b32 v93, 4, v85\n" \
  "v_add_u32_dpp v108, v108, v108 row_shr:8 row_mask:0xf bank_mask:0xf bound_ctrl:1\n" \
  "v_lshrrev_b32 v105, 2, v85\n" \
  "v_and_or_b32 v89, v89, v118, %[wb]\n" \
  "v_add_u32_dpp v108, v108, v108 row_bcast:15 row_mask:0xa bank_mask:0xf\n" \
  "v_lshlrev_b32_e64 v101, v93, 4\n" \
  "v_and_b32 v105, 0xfc, v105\n" \
  "v_add_u32_dpp v108, v108, v108 row_bcast:31 row_mask:0xc bank_mask:0xf\n" \
  "v_lshlrev_b32 v90, 1, v86\n" \
  "v_readlane_b32 s65, v108, 63\n" \
  "v_lshlrev_b32 v94, 4, v86\n" \
  "v_lshrrev_b32 v106, 2, v86\n" \
  "v_and_or_b32 v90, v90, v118, %[wb]\n" \
  "v_lshlrev_b32_e64 v102, v94, 4\n" \
  "v_and_b32 v106, 0xfc, v106\n" \
  "s_cmp_lg_u32 s65, 0\n" \
  "s_cbranch_scc1 Lkr%=\n" \
  "Lkc%=:\n" \
  "s_mov_b64 exec, s[66:67]\n" \
  "ds_add_rtn_u32 v96, v88, v100 offset:%[hi]\n" \
  "ds_add_u32 v104, v116 offset:%[dg]\n" \
  "s_mov_b64 exec, s[68:69]\n" \
  "ds_add_rtn_u32 v97, v89, v101 offset:%[hi]\n" \
  "ds_add_u32 v105, v116 offset:%[dg]\n" \
  "s_mov_b64 exec, s[70:71]\n" \
  "ds_add_rtn_u32 v98, v90, v102 offset:%[hi]\n" \
  "ds_add_u32 v106, v116 offset:%[dg]\n" \
  "s_cmpk_gt_u32 s62, 0xc0\n" \
  "s_mov_b64 exec, -1\n" \
  "s_cbranch_scc1 Lc3%=\n" \
  "Lc4%=:\n" \
  "v_and_b32 v100, v118, v88\n" \
  "v_and_b32 v101, v118, v89\n" \
  "v_and_b32 v102, v118, v90\n" \
  "s_waitcnt lgkmcnt(0)\n" \
  "s_barrier\n" \
  "ds_read_b32 v108, v149 offset:%[dg]\n" \
  "ds_read_b32 v109, v150 offset:%[dg]\n" \
  "ds_read2st64_b32 v[76:77], v142 offset0:0 offset1:8\n" \
  "ds_read2st64_b32 v[78:79], v142 offset0:16 offset1:24\n" \
  "ds_read2st64_b32 v[104:105], v142 offset0:32 offset1:40\n" \
  "ds_read2st64_b32 v[106:107], v142 offset0:48 offset1:56\n" \
  "v_and_b32 v84, 0xfffc00, v84\n" \
  "v_and_b32 v85, 0xfffc00, v85\n" \
  "v_and_b32 v86, 0xfffc00, v86\n" \
  "v_and_b32 v87, 0xfffc00, v87\n" \
  "v_mov_b32 v111, 0\n" \
  "v_mov_b32 v112, 63\n" \
  "s_waitcnt lgkmcnt(4)\n" \
  "v_readlane_b32 s74, v109, 0\n" \
  "v_readlane_b32 s76, v109, 1\n" \
  "v_readlane_b32 s78, v109, 2\n" \
  "s_nop 0\n" \
  "v_cmp_lt_i32_e64 s[74:75], s74, v108\n" \
  "v_cmp_lt_i32_e64 s[76:77], s76, v108\n" \
  "v_cmp_lt_i32_e64 s[78:79], s78, v108\n" \
  "s_bcnt1_i32_b64 s74, s[74:75]\n" \
  "s_bcnt1_i32_b64 s76, s[76:77]\n" \
  "s_bcnt1_i32_b64 s78, s[78:79]\n" \
  "v_writelane_b32 v112, s74, 0\n" \
  "v_writelane_b32 v112, s76, 1\n" \
  "v_writelane_b32 v112, s78, 2\n" \
  "v_lshlrev_b32 v112, 2, v112\n" \
  "s_waitcnt lgkmcnt(3)\n" \
  "ds_write2st64_b32 v142, v117, v76 offset1:8\n" \
  "v_add_u32 v77, v77, v76\n" \
  "s_waitcnt lgkmcnt(3)\n" \
  "v_add_u32 v78, v78, v77\n" \
  "ds_write2st64_b32 v142, v77, v78 offset0:16 offset1:24\n" \
  "v_add_u32 v79, v79, v78\n" \
  "s_waitcnt lgkmcnt(3)\n" \
  "v_add_u32 v104, v104, v79\n" \
  "ds_write2st64_b32 v142, v79, v104 offset0:32 offset1:40\n" \
  "v_add_u32 v105, v105, v104\n" \
  "s_waitcnt lgkmcnt(3)\n" \
  "v_add_u32 v106, v106, v105\n" \
  "ds_write2st64_b32 v142, v105, v106 offset0:48 offset1:56\n" \
  "v_add_u32 v107, v107, v106\n" \
  "ds_write_b32 v142, v107 offset:16384\n" \
  "ds_write_b32 v112, v150 offset:%[br]\n" \
  "s_waitcnt lgkmcnt(0)\n" \
  "s_barrier\n" \
  "ds_read_b32 v108, v143\n" \
  "ds_read_b32 v109, v144\n" \
  "s_mov_b32 s74, 0x1f1f1f1f\n" \
  "s_mov_b32 s75, 0x1f1f1f1f\n" \
  "s_waitcnt lgkmcnt(1)\n" \
  "v_lshl_add_u32 v76, v108, 3, v145\n" \
  "s_mov_b64 exec, s[74:75]\n" \
  "ds_read_b32 v111, v76\n" \
  "s_mov_b64 exec, -1\n" \
  "ds_write_b32 v148, v108\n" \
  "s_waitcnt lgkmcnt(1)\n" \
  "v_add_u32_dpp v112, v111, v111 row_shr:1 row_mask:0xf bank_mask:0xf bound_ctrl:1\n" \
  "v_subrev_u32 v109, %[n0], v109\n" \
  "v_and_b32 v109, 0xfc, v109\n" \
  "v_add_u32_dpp v112, v112, v112 row_shr:2 row_mask:0xf bank_mask:0xf bound_ctrl:1\n" \
  "v_lshl_add_u32 v159, v109, 8, v159\n" \
  "v_sub_u32 v77, v146, v111\n" \
  "v_add_u32_dpp v112, v112, v112 row_shr:4 row_mask:0xf bank_mask:0xf bound_ctrl:1\n" \
  "s_nop 1\n" \
  "v_add_u32_dpp v112, v112, v112 row_shr:8 row_mask:0xf bank_mask:0xf bound_ctrl:1\n" \
  "s_nop 1\n" \
  "v_add_u32_dpp v112, v112, v112 row_bcast:15 row_mask:0xa bank_mask:0xf\n" \
  "s_nop 1\n" \
  "v_add_u32_dpp v112, v112, v112 row_bcast:31 row_mask:0xc bank_mask:0xf\n" \
  "v_sub_u32 v111, v112, v111\n" \
  "v_readlane_b32 s65, v112, 63\n" \
  "v_mov_b32_dpp v78, v112 row_newbcast:7 row_mask:0xf bank_mask:0x3\n" \
  "v_mov_b32_dpp v79, v111 row_newbcast:0 row_mask:0xf bank_mask:0x3\n" \
  "v_add_u32 v77, v77, v112\n" \
  "v_mov_b32_dpp v78, v112 row_newbcast:15 row_mask:0xf bank_mask:0xc\n" \
  "v_mov_b32_dpp v79, v111 row_newbcast:8 row_mask:0xf bank_mask:0xc\n" \
  "s_and_b32 s64, s65, 0xffff\n" \
  "v_lshl_add_u32 v77, v78, 16, v77\n" \
  "s_lshr_b32 s65, s65, 16\n" \
  "v_add_u32_sdwa v77, v77, v79 dst_sel:DWORD dst_unused:UNUSED_PAD src0_sel:DWORD src1_sel:WORD_1\n" \
  "s_add_u32 s64, s64, s65\n" \
  "s_mov_b64 exec, s[74:75]\n" \
  "ds_write_b32 v76, v77\n" \
  "s_mov_b64 exec, -1\n" \
  "s_cmpk_gt_u32 s64, 864\n" \
  "s_cbranch_scc1 Llo%=\n" \
  "Llc%=:\n" \
  "s_waitcnt lgkmcnt(0)\n" \
  "s_barrier\n" \
  "ds_read_b32 v109, v117 offset:%[fl]\n" \
  "ds_read_b32 v76, v88 offset:%[hi]\n" \
  "ds_read_b32 v104, v100 offset:%[to]\n" \
  "ds_read_b32 v77, v89 offset:%[hi]\n" \
  "ds_read_b32 v105, v101 offset:%[to]\n" \
  "ds_read_b32 v78, v90 offset:%[hi]\n" \
  "ds_read_b32 v106, v102 offset:%[to]\n" \
  "v_add_u32 v113, s64, v147\n" \
  "v_bfrev_b32 v111, 1\n" \
  "s_max_u32 s60, s64, 4\n" \
  "s_sub_u32 s60, s60, 4\n" \
  "s_lshr_b32 s60, s60, 4\n" \
  "s_waitcnt lgkmcnt(4)\n" \
  "v_readfirstlane_b32 s61, v109\n" \
  "v_add3_u32 v88, v76, v104, v96\n" \
  "v_lshrrev_b32 v88, v92, v88\n" \
  "v_bfe_u32 v72, v88, 13, 3\n" \
  "v_and_b32 v88, 0x1ffc, v88\n" \
  "v_or_b32 v72, v84, v72\n" \
  "s_cmp_lg_u32 s61, 0\n" \
  "s_cbranch_scc1 Lslow%=\n" \
  "s_waitcnt lgkmcnt(0)\n" \
  "v_add3_u32 v89, v77, v105, v97\n" \
  "v_add3_u32 v90, v78, v106, v98\n" \
  "v_lshrrev_b32 v89, v93, v89\n" \
  "v_lshrrev_b32 v90, v94, v90\n" \
  "v_bfe_u32 v73, v89, 13, 3\n" \
  "v_bfe_u32 v74, v90, 13, 3\n" \
  "v_and_b32 v89, 0x1ffc, v89\n" \
  "v_and_b32 v90, 0x1ffc, v90\n" \
  "v_or_b32 v73, v85, v73\n" \
  "v_or_b32 v74, v86, v74\n" \
  "s_mov_b64 exec, s[66:67]\n" \
  "ds_write_b32 v88, v72 offset:%[so]\n" \
  "s_mov_b64 exec, s[68:69]\n" \
  "ds_write_b32 v89, v73 offset:%[so]\n" \
  "s_mov_b64 exec, s[70:71]\n" \
  "ds_write_b32 v90, v74 offset:%[so]\n" \
  "s_cmpk_gt_u32 s62, 0xc0\n" \
  "s_cbranch_scc1 Lq3%=\n" \
  "Lq4%=:\n" \
  "s_mov_b32 exec_lo, -1\n" \
  "s_mov_b32 exec_hi, 0\n" \
  "ds_write_b32 v113, v111 offset:%[so]\n" \
  "s_mov_b64 exec, -1\n" \
  "v_lshlrev_b32 v88, 2, v88\n" \
  "v_lshlrev_b32 v89, 2, v89\n" \
  "v_lshlrev_b32 v90, 2, v90\n" \
  "v_lshlrev_b32 v91, 2, v91\n" \
  "v_lshlrev_b32 v113, 2, v113\n" \
  "s_mov_b32 s63, 0x10000\n" \
  "s_waitcnt vmcnt(0)\n" \
  "Lpc%=:\n" \
  "s_mov_b64 exec, s[66:67]\n" \
  "ds_write_b128 v88, v[32:35] offset:%[pa]\n" \
  "s_mov_b64 exec, s[68:69]\n" \
  "ds_write_b128 v89, v[36:39] offset:%[pa]\n" \
  "s_mov_b64 exec, s[70:71]\n" \
  "ds_write_b128 v90, v[40:43] offset:%[pa]\n" \
  "s_mov_b64 exec, s[72:73]\n" \
  "ds_write_b128 v91, v[44:47] offset:%[pa]\n" \
  "s_mov_b32 exec_lo, -1\n" \
  "s_mov_b32 exec_hi, 0\n" \
  "ds_write_b128 v113, v[48:51] offset:%[pa]\n" \
  "s_mov_b64 exec, -1\n" \
  "v_add3_u32 v108, v32, v33, v115\n" \
  "v_add3_u32 v111, v34, v35, v36\n" \
  "v_add3_u32 v112, v37, v38, v39\n" \
  "v_add3_u32 v108, v108, v40, v41\n" \
  "v_add3_u32 v111, v111, v42, v43\n" \
  "v_add3_u32 v112, v112, v44, v45\n" \
  "v_add3_u32 v108, v108, v46, v47\n" \
  "v_add3_u32 v108, v108, v111, v112\n" \
  "s_nop 1\n" \
  "v_add_u32_dpp v108, v108, v108 row_shr:1 row_mask:0xf bank_mask:0xf bound_ctrl:1\n" \
  "s_nop 1\n" \
  "v_add_u32_dpp v108, v108, v108 row_shr:2 row_mask:0xf bank_mask:0xf bound_ctrl:1\n" \
  "s_nop 1\n" \
  "v_add_u32_dpp v108, v108, v108 row_shr:4 row_mask:0xf bank_mask:0xf bound_ctrl:1\n" \
  "s_nop 1\n" \
  "v_add_u32_dpp v108, v108, v108 row_shr:8 row_mask:0xf bank_mask:0xf bound_ctrl:1\n" \
  "s_nop 1\n" \
  "v_add_u32_dpp v108, v108, v108 row_bcast:15 row_mask:0xa bank_mask:0xf\n" \
  "s_nop 1\n" \
  "v_add_u32_dpp v108, v108, v108 row_bcast:31 row_mask:0xc bank_mask:0xf\n" \
  "s_nop 0\n" \
  "v_readlane_b32 s65, v108, 63\n" \
  "s_waitcnt lgkmcnt(0)\n" \
  "s_cmp_lg_u32 s65, 0\n" \
  "s_cbranch_scc1 Lpr%=\n" \
  "Lpw%=:\n" \
  "s_barrier\n" \
  "ds_read_b32  v11, v12\n" \
  "ds_read_b64 v[106:107], v14\n" \
  "ds_read_b64 v[104:105], v15\n" \
  "ds_read_b32 v18, v13\n" \
  "s_setprio 1\n" \
  "v_mov_b64 v[32:33], 0\n" \
  "v_mov_b64 v[34:35], 0\n" \
  "v_mov_b64 v[36:37], 0\n" \
  "v_mov_b64 v[38:39], 0\n" \
  "v_mov_b64 v[40:41], 0\n" \
  "v_mov_b64 v[42:43], 0\n" \
  "v_mov_b64 v[44:45], 0\n" \
  "v_mov_b64 v[46:47], 0\n" \
  "s_waitcnt lgkmcnt(3)\n" \
  "v_and_b32 v10, 0xfffffc00, v11\n" \
  "s_nop 1\n" \
  "v_add_u32_dpp v9, v10, v8 row_newbcast:0 row_mask:0xf bank_mask:0xf\n" \
  "buffer_load_dwordx4 v[120:123], v9, %[xs], 0 offen\n" \
  "v_add_u32_dpp v9, v10, v8 row_newbcast:1 row_mask:0xf bank_mask:0xf\n" \
  "buffer_load_dwordx4 v[124:127], v9, %[xs], 0 offen\n" \
  "v_add_u32_dpp v9, v10, v8 row_newbcast:2 row_mask:0xf bank_mask:0xf\n" \
  "buffer_load_dwordx4 v[128:131], v9, %[xs], 0 offen\n" \
  "v_add_u32_dpp v9, v10, v8 row_newbcast:3 row_mask:0xf bank_mask:0xf\n" \
  "buffer_load_dwordx4 v[132:135], v9, %[xs], 0 offen\n" \
  "v_add_u32_dpp v9, v10, v8 row_newbcast:4 row_mask:0xf bank_mask:0xf\n" \
  "buffer_load_dwordx4 v[136:139], v9, %[xs], 0 offen\n" \
  "s_waitcnt lgkmcnt(0)\n" \
  "v_mfma_f32_4x4x4_16b_bf16 v[80:83], v[104:105], v[28:29], 0 cbsz:4\n" \
  "v_mfma_f32_4x4x4_16b_bf16 v[84:87], v[104:105], v[30:31], 0 cbsz:4\n" \
  "v_mfma_f32_4x4x4_16b_bf16 v[112:115], v[106:107], v[20:21], 0\n" \
  "v_mfma_f32_4x4x4_16b_bf16 v[116:119], v[106:107], v[22:23], 0\n" \
  "v_mfma_f32_4x4x4_16b_bf16 v[72:75], v[106:107], v[24:25], 0\n" \
  "v_mfma_f32_4x4x4_16b_bf16 v[76:79], v[106:107], v[26:27], 0\n" \
  "v_and_b32 v18, v17, v18\n" \
  "v_cmp_ne_u32_e64 s[62:63], 0, v18\n" \
  "v_cvt_pkrtz_f16_f32 v97, v80, v81\n" \
  "v_and_b32 v96, v16, v97\n" \
  "v_and_b32_sdwa v97, v16, v97 dst_sel:DWORD dst_unused:UNUSED_PAD src0_sel:DWORD src1_sel:WORD_1\n" \
  "ds_read_b32 v96, v96 offset:%[tb]\n" \
  "ds_read_b32 v97, v97 offset:%[tb]\n" \
  "v_cvt_pkrtz_f16_f32 v99, v84, v85\n" \
  "v_and_b32 v98, v16, v99\n" \
  "v_and_b32_sdwa v99, v16, v99 dst_sel:DWORD dst_unused:UNUSED_PAD src0_sel:DWORD src1_sel:WORD_1\n" \
  "ds_read_b32 v98, v98 offset:%[tb]\n" \
  "ds_read_b32 v99, v99 offset:%[tb]\n" \
  "v_cvt_pkrtz_f16_f32 v101, v82, v83\n" \
  "v_and_b32 v100, v16, v101\n" \
  "v_and_b32_sdwa v101, v16, v101 dst_sel:DWORD dst_unused:UNUSED_PAD src0_sel:DWORD src1_sel:WORD_1\n" \
  "ds_read_b32 v100, v100 offset:%[tb]\n" \
  "ds_read_b32 v101, v101 offset:%[tb]\n" \
  "v_cvt_pk_bf16_f32 v108, v112, v116\n" \
  "v_cvt_pk_bf16_f32 v109, v72, v76\n" \
  "s_nop 0\n" \
  "s_waitcnt vmcnt(4)\n" \
  "v_mfma_f32_4x4x4_16b_bf16 v[72:75], v[108:109], v[120:121], 0 cbsz:4\n" \
  "v_mfma_f32_4x4x4_16b_bf16 v[76:79], v[108:109], v[122:123], 0 cbsz:4\n" \
  "v_add_u32_dpp v9, v10, v8 row_newbcast:5 row_mask:0xf bank_mask:0xf\n" \
  "buffer_load_dwordx4 v[140:143], v9, %[xs], 0 offen\n" \
  "s_waitcnt lgkmcnt(0)\n" \
  "s_nop 1\n" \
  "Lbody%=:\n" \
  "s_bfe_u32 s61, s62, 0x40000\n" \
  "s_waitcnt lgkmcnt(2)\n" \
  "v_pk_mul_f32 v[80:81], v[80:81], v[96:97]\n" \
  "v_pk_mul_f32 v[84:85], v[84:85], v[98:99]\n" \
  "s_set_gpr_idx_on s61, gpr_idx(SRC2,DST)\n" \
  "v_pk_fma_f32 v[32:33], v[80:81], v[72:73], v[32:33] op_sel:[0,0,0] op_sel_hi:[0,1,1]\n" \
  "v_pk_fma_f32 v[42:43], v[80:81], v[74:75], v[42:43] op_sel:[0,0,0] op_sel_hi:[0,1,1]\n" \
  "v_pk_fma_f32 v[52:53], v[84:85], v[76:77], v[52:53] op_sel:[0,0,0] op_sel_hi:[0,1,1]\n" \
  "v_pk_fma_f32 v[62:63], v[84:85], v[78:79], v[62:63] op_sel:[0,0,0] op_sel_hi:[0,1,1]\n" \
  "s_set_gpr_idx_off\n" \
  "s_waitcnt vmcnt(4)\n" \
  "v_mfma_f32_4x4x4_16b_bf16 v[72:75], v[108:109], v[124:125], 0 cbsz:4 abid:1\n" \
  "v_mfma_f32_4x4x4_16b_bf16 v[76:79], v[108:109], v[126:127], 0 cbsz:4 abid:1\n" \
  "v_mfma_f32_4x4x4_16b_bf16 v[88:91], v[104:105], v[28:29], 0 cbsz:4 abid:1\n" \
  "v_mfma_f32_4x4x4_16b_bf16 v[92:95], v[104:105], v[30:31], 0 cbsz:4 abid:1\n" \
  "v_add_u32_dpp v9, v10, v8 row_newbcast:6 row_mask:0xf bank_mask:0xf\n" \
  "v_cvt_pkrtz_f16_f32 v103, v86, v87\n" \
  "buffer_load_dwordx4 v[144:147], v9, %[xs], 0 offen\n" \
  "v_and_b32 v102, v16, v103\n" \
  "v_and_b32_sdwa v103, v16, v103 dst_sel:DWORD dst_unused:UNUSED_PAD src0_sel:DWORD src1_sel:WORD_1\n" \
  "ds_read_b32 v102, v102 offset:%[tb]\n" \
  "ds_read_b32 v103, v103 offset:%[tb]\n" \
  "v_mov_b32 v111, s60\n" \
  "ds_write2_b32 v110, v111, v111 offset1:8\n" \
  "s_bfe_u32 s61, s62, 0x40004\n" \
  "s_set_gpr_idx_on s61, gpr_idx(SRC2,DST)\n" \
  "v_pk_fma_f32 v[32:33], v[80:81], v[72:73], v[32:33] op_sel:[1,0,0] op_sel_hi:[1,1,1]\n" \
  "v_pk_fma_f32 v[42:43], v[80:81], v[74:75], v[42:43] op_sel:[1,0,0] op_sel_hi:[1,1,1]\n" \
  "v_pk_fma_f32 v[52:53], v[84:85], v[76:77], v[52:53] op_sel:[1,0,0] op_sel_hi:[1,1,1]\n" \
  "v_pk_fma_f32 v[62:63], v[84:85], v[78:79], v[62:63] op_sel:[1,0,0] op_sel_hi:[1,1,1]\n" \
  "s_set_gpr_idx_off\n" \
  "s_waitcnt vmcnt(4)\n" \
  "v_mfma_f32_4x4x4_16b_bf16 v[72:75], v[108:109], v[128:129], 0 cbsz:4 abid:2\n" \
  "v_mfma_f32_4x4x4_16b_bf16 v[76:79], v[108:109], v[130:131], 0 cbsz:4 abid:2\n" \
  "v_add_u32_dpp v9, v10, v8 row_newbcast:7 row_mask:0xf bank_mask:0xf\n" \
  "v_cvt_pkrtz_f16_f32 v97, v88, v89\n" \
  "buffer_load_dwordx4 v[148:151], v9, %[xs], 0 offen\n" \
  "v_and_b32 v96, v16, v97\n" \
  "v_and_b32_sdwa v97, v16, v97 dst_sel:DWORD dst_unused:UNUSED_PAD src0_sel:DWORD src1_sel:WORD_1\n" \
  "ds_read_b32 v96, v96 offset:%[tb]\n" \
  "ds_read_b32 v97, v97 offset:%[tb]\n" \
  "ds_read_b32 v111, v110 offset:16\n" \
  "s_bfe_u32 s61, s62, 0x40008\n" \
  "s_waitcnt lgkmcnt(4)\n" \
  "v_pk_mul_f32 v[82:83], v[82:83], v[100:101]\n" \
  "v_pk_mul_f32 v[86:87], v[86:87], v[102:103]\n" \
  "s_set_gpr_idx_on s61, gpr_idx(SRC2,DST)\n" \
  "v_pk_fma_f32 v[32:33], v[82:83], v[72:73], v[32:33] op_sel:[0,0,0] op_sel_hi:[0,1,1]\n" \
  "v_pk_fma_f32 v[42:43], v[82:83], v[74:75], v[42:43] op_sel:[0,0,0] op_sel_hi:[0,1,1]\n" \
  "v_pk_fma_f32 v[52:53], v[86:87], v[76:77], v[52:53] op_sel:[0,0,0] op_sel_hi:[0,1,1]\n" \
  "v_pk_fma_f32 v[62:63], v[86:87], v[78:79], v[62:63] op_sel:[0,0,0] op_sel_hi:[0,1,1]\n" \
  "s_set_gpr_idx_off\n" \
  "s_waitcnt vmcnt(4)\n" \
  "v_mfma_f32_4x4x4_16b_bf16 v[72:75], v[108:109], v[132:133], 0 cbsz:4 abid:3\n" \
  "v_mfma_f32_4x4x4_16b_bf16 v[76:79], v[108:109], v[134:135], 0 cbsz:4 abid:3\n" \
  "v_add_u32_dpp v9, v10, v8 row_newbcast:8 row_mask:0xf bank_mask:0xf\n" \
  "v_cvt_pkrtz_f16_f32 v99, v92, v93\n" \
  "buffer_load_dwordx4 v[120:123], v9, %[xs], 0 offen\n" \
  "v_and_b32 v98, v16, v99\n" \
  "v_and_b32_sdwa v99, v16, v99 dst_sel:DWORD dst_unused:UNUSED_PAD src0_sel:DWORD src1_sel:WORD_1\n" \
  "ds_read_b32 v98, v98 offset:%[tb]\n" \
  "ds_read_b32 v99, v99 offset:%[tb]\n" \
  "ds_read_b64 v[106:107], v14 offset:256\n" \
  "s_bfe_u32 s61, s62, 0x4000c\n" \
  "s_set_gpr_idx_on s61, gpr_idx(SRC2,DST)\n" \
  "v_pk_fma_f32 v[32:33], v[82:83], v[72:73], v[32:33] op_sel:[1,0,0] op_sel_hi:[1,1,1]\n" \
  "v_pk_fma_f32 v[42:43], v[82:83], v[74:75], v[42:43] op_sel:[1,0,0] op_sel_hi:[1,1,1]\n" \
  "v_pk_fma_f32 v[52:53], v[86:87], v[76:77], v[52:53] op_sel:[1,0,0] op_sel_hi:[1,1,1]\n" \
  "v_pk_fma_f32 v[62:63], v[86:87], v[78:79], v[62:63] op_sel:[1,0,0] op_sel_hi:[1,1,1]\n" \
  "s_set_gpr_idx_off\n" \
  "s_sub_u32 s60, s60, 1\n" \
  "s_cbranch_scc1 Lend%=\n" \
  "s_waitcnt vmcnt(4)\n" \
  "v_mfma_f32_4x4x4_16b_bf16 v[72:75], v[108:109], v[136:137], 0 cbsz:4 abid:4\n" \
  "v_mfma_f32_4x4x4_16b_bf16 v[76:79], v[108:109], v[138:139], 0 cbsz:4 abid:4\n" \
  "v_add_u32_dpp v9, v10, v8 row_newbcast:9 row_mask:0xf bank_mask:0xf\n" \
  "v_cvt_pkrtz_f16_f32 v101, v90, v91\n" \
  "buffer_load_dwordx4 v[124:127], v9, %[xs], 0 offen\n" \
  "v_and_b32 v100, v16, v101\n" \
  "v_and_b32_sdwa v101, v16, v101 dst_sel:DWORD dst_unused:UNUSED_PAD src0_sel:DWORD src1_sel:WORD_1\n" \
  "ds_read_b32 v100, v100 offset:%[tb]\n" \
  "ds_read_b32 v101, v101 offset:%[tb]\n" \
  "ds_read_b32 v18, v13 offset:64\n" \
  "v_add_u32 v14, 0x100, v14\n" \
  "s_bfe_u32 s61, s62, 0x40010\n" \
  "s_waitcnt lgkmcnt(4)\n" \
  "v_pk_mul_f32 v[88:89], v[88:89], v[96:97]\n" \
  "v_pk_mul_f32 v[92:93], v[92:93], v[98:99]\n" \
  "s_set_gpr_idx_on s61, gpr_idx(SRC2,DST)\n" \
  "v_pk_fma_f32 v[32:33], v[88:89], v[72:73], v[32:33] op_sel:[0,0,0] op_sel_hi:[0,1,1]\n" \
  "v_pk_fma_f32 v[42:43], v[88:89], v[74:75], v[42:43] op_sel:[0,0,0] op_sel_hi:[0,1,1]\n" \
  "v_pk_fma_f32 v[52:53], v[92:93], v[76:77], v[52:53] op_sel:[0,0,0] op_sel_hi:[0,1,1]\n" \
  "v_pk_fma_f32 v[62:63], v[92:93], v[78:79], v[62:63] op_sel:[0,0,0] op_sel_hi:[0,1,1]\n" \
  "s_set_gpr_idx_off\n" \
  "s_waitcnt vmcnt(4)\n" \
  "v_mfma_f32_4x4x4_16b_bf16 v[72:75], v[108:109], v[140:141], 0 cbsz:4 abid:5\n" \
  "v_mfma_f32_4x4x4_16b_bf16 v[76:79], v[108:109], v[142:143], 0 cbsz:4 abid:5\n" \
  "v_mfma_f32_4x4x4_16b_bf16 v[80:83], v[104:105], v[28:29], 0 cbsz:4 abid:2\n" \
  "v_mfma_f32_4x4x4_16b_bf16 v[84:87], v[104:105], v[30:31], 0 cbsz:4 abid:2\n" \
  "v_add_u32_dpp v9, v10, v8 row_newbcast:10 row_mask:0xf bank_mask:0xf\n" \
  "v_cvt_pkrtz_f16_f32 v103, v94, v95\n" \
  "buffer_load_dwordx4 v[128:131], v9, %[xs], 0 offen\n" \
  "v_and_b32 v102, v16, v103\n" \
  "v_and_b32_sdwa v103, v16, v103 dst_sel:DWORD dst_unused:UNUSED_PAD src0_sel:DWORD src1_sel:WORD_1\n" \
  "ds_read_b32 v102, v102 offset:%[tb]\n" \
  "ds_read_b32 v103, v103 offset:%[tb]\n" \
  "s_bfe_u32 s61, s62, 0x40014\n" \
  "s_set_gpr_idx_on s61, gpr_idx(SRC2,DST)\n" \
  "v_pk_fma_f32 v[32:33], v[88:89], v[72:73], v[32:33] op_sel:[1,0,0] op_sel_hi:[1,1,1]\n" \
  "v_pk_fma_f32 v[42:43], v[88:89], v[74:75], v[42:43] op_sel:[1,0,0] op_sel_hi:[1,1,1]\n" \
  "v_pk_fma_f32 v[52:53], v[92:93], v[76:77], v[52:53] op_sel:[1,0,0] op_sel_hi:[1,1,1]\n" \
  "v_pk_fma_f32 v[62:63], v[92:93], v[78:79], v[62:63] op_sel:[1,0,0] op_sel_hi:[1,1,1]\n" \
  "s_set_gpr_idx_off\n" \
  "s_waitcnt vmcnt(4) lgkmcnt(8)\n" \
  "v_mfma_f32_4x4x4_16b_bf16 v[72:75], v[108:109], v[144:145], 0 cbsz:4 abid:6\n" \
  "v_mfma_f32_4x4x4_16b_bf16 v[76:79], v[108:109], v[146:147], 0 cbsz:4 abid:6\n" \
  "v_add_u32_dpp v9, v10, v8 row_newbcast:11 row_mask:0xf bank_mask:0xf\n" \
  "v_cvt_pkrtz_f16_f32 v97, v80, v81\n" \
  "buffer_load_dwordx4 v[132:135], v9, %[xs], 0 offen\n" \
  "v_and_b32 v96, v16, v97\n" \
  "v_and_b32_sdwa v97, v16, v97 dst_sel:DWORD dst_unused:UNUSED_PAD src0_sel:DWORD src1_sel:WORD_1\n" \
  "ds_read_b32 v96, v96 offset:%[tb]\n" \
  "ds_read_b32 v97, v97 offset:%[tb]\n" \
  "v_add_u32 v13, 64, v13\n" \
  "v_readfirstlane_b32 s66, v111\n" \
  "s_setprio 1\n" \
  "s_add_u32 s67, s60, %[fo]\n" \
  "s_cmp_ge_u32 s67, s66\n" \
  "s_cbranch_scc0 Lf5_%=\n" \
  "s_setprio 2\n" \
  "Lf5_%=:\n" \
  "s_bfe_u32 s61, s62, 0x40018\n" \
  "s_waitcnt lgkmcnt(2)\n" \
  "v_pk_mul_f32 v[90:91], v[90:91], v[100:101]\n" \
  "v_pk_mul_f32 v[94:95], v[94:95], v[102:103]\n" \
  "s_set_gpr_idx_on s61, gpr_idx(SRC2,DST)\n" \
  "v_pk_fma_f32 v[32:33], v[90:91], v[72:73], v[32:33] op_sel:[0,0,0] op_sel_hi:[0,1,1]\n" \
  "v_pk_fma_f32 v[42:43], v[90:91], v[74:75], v[42:43] op_sel:[0,0,0] op_sel_hi:[0,1,1]\n" \
  "v_pk_fma_f32 v[52:53], v[94:95], v[76:77], v[52:53] op_sel:[0,0,0] op_sel_hi:[0,1,1]\n" \
  "v_pk_fma_f32 v[62:63], v[94:95], v[78:79], v[62:63] op_sel:[0,0,0] op_sel_hi:[0,1,1]\n" \
  "s_set_gpr_idx_off\n" \
  "s_waitcnt vmcnt(4) lgkmcnt(7)\n" \
  "v_mfma_f32_4x4x4_16b_bf16 v[72:75], v[108:109], v[148:149], 0 cbsz:4 abid:7\n" \
  "v_mfma_f32_4x4x4_16b_bf16 v[76:79], v[108:109], v[150:151], 0 cbsz:4 abid:7\n" \
  "v_mfma_f32_4x4x4_16b_bf16 v[112:115], v[106:107], v[20:21], 0\n" \
  "v_add_u32_dpp v9, v10, v8 row_newbcast:12 row_mask:0xf bank_mask:0xf\n" \
  "v_cvt_pkrtz_f16_f32 v99, v84, v85\n" \
  "buffer_load_dwordx4 v[136:139], v9, %[xs], 0 offen\n" \
  "v_and_b32 v98, v16, v99\n" \
  "v_and_b32_sdwa v99, v16, v99 dst_sel:DWORD dst_unused:UNUSED_PAD src0_sel:DWORD src1_sel:WORD_1\n" \
  "ds_read_b32 v98, v98 offset:%[tb]\n" \
  "ds_read_b32 v99, v99 offset:%[tb]\n" \
  "ds_read_b32 v11, v12 offset:64\n" \
  "s_bfe_u32 s61, s62, 0x4001c\n" \
  "s_set_gpr_idx_on s61, gpr_idx(SRC2,DST)\n" \
  "v_pk_fma_f32 v[32:33], v[90:91], v[72:73], v[32:33] op_sel:[1,0,0] op_sel_hi:[1,1,1]\n" \
  "v_pk_fma_f32 v[42:43], v[90:91], v[74:75], v[42:43] op_sel:[1,0,0] op_sel_hi:[1,1,1]\n" \
  "v_pk_fma_f32 v[52:53], v[94:95], v[76:77], v[52:53] op_sel:[1,0,0] op_sel_hi:[1,1,1]\n" \
  "v_pk_fma_f32 v[62:63], v[94:95], v[78:79], v[62:63] op_sel:[1,0,0] op_sel_hi:[1,1,1]\n" \
  "s_set_gpr_idx_off\n" \
  "s_sub_u32 s60, s60, 1\n" \
  "s_cbranch_scc1 Lend%=\n" \
  "s_waitcnt vmcnt(4) lgkmcnt(7)\n" \
  "v_mfma_f32_4x4x4_16b_bf16 v[72:75], v[108:109], v[120:121], 0 cbsz:4 abid:8\n" \
  "v_mfma_f32_4x4x4_16b_bf16 v[76:79], v[108:109], v[122:123], 0 cbsz:4 abid:8\n" \
  "v_add_u32_dpp v9, v10, v8 row_newbcast:13 row_mask:0xf bank_mask:0xf\n" \
  "v_cvt_pkrtz_f16_f32 v101, v82, v83\n" \
  "buffer_load_dwordx4 v[140:143], v9, %[xs], 0 offen\n" \
  "v_and_b32 v100, v16, v101\n" \
  "v_and_b32_sdwa v101, v16, v101 dst_sel:DWORD dst_unused:UNUSED_PAD src0_sel:DWORD src1_sel:WORD_1\n" \
  "ds_read_b32 v100, v100 offset:%[tb]\n" \
  "ds_read_b32 v101, v101 offset:%[tb]\n" \
  "v_add_u32 v12, 64, v12\n" \
  "v_and_b32 v18, v17, v18\n" \
  "s_bfe_u32 s61, s63, 0x40000\n" \
  "s_waitcnt lgkmcnt(3)\n" \
  "v_pk_mul_f32 v[80:81], v[80:81], v[96:97]\n" \
  "v_pk_mul_f32 v[84:85], v[84:85], v[98:99]\n" \
  "s_set_gpr_idx_on s61, gpr_idx(SRC2,DST)\n" \
  "v_pk_fma_f32 v[32:33], v[80:81], v[72:73], v[32:33] op_sel:[0,0,0] op_sel_hi:[0,1,1]\n" \
  "v_pk_fma_f32 v[42:43], v[80:81], v[74:75], v[42:43] op_sel:[0,0,0] op_sel_hi:[0,1,1]\n" \
  "v_pk_fma_f32 v[52:53], v[84:85], v[76:77], v[52:53] op_sel:[0,0,0] op_sel_hi:[0,1,1]\n" \
  "v_pk_fma_f32 v[62:63], v[84:85], v[78:79], v[62:63] op_sel:[0,0,0] op_sel_hi:[0,1,1]\n" \
  "s_set_gpr_idx_off\n" \
  "s_waitcnt vmcnt(4)\n" \
  "v_mfma_f32_4x4x4_16b_bf16 v[72:75], v[108:109], v[124:125], 0 cbsz:4 abid:9\n" \
  "v_mfma_f32_4x4x4_16b_bf16 v[76:79], v[108:109], v[126:127], 0 cbsz:4 abid:9\n" \
  "v_mfma_f32_4x4x4_16b_bf16 v[88:91], v[104:105], v[28:29], 0 cbsz:4 abid:3\n" \
  "v_mfma_f32_4x4x4_16b_bf16 v[92:95], v[104:105], v[30:31], 0 cbsz:4 abid:3\n" \
  "v_add_u32_dpp v9, v10, v8 row_newbcast:14 row_mask:0xf bank_mask:0xf\n" \
  "v_cvt_pkrtz_f16_f32 v103, v86, v87\n" \
  "buffer_load_dwordx4 v[144:147], v9, %[xs], 0 offen\n" \
  "v_and_b32 v102, v16, v103\n" \
  "v_and_b32_sdwa v103, v16, v103 dst_sel:DWORD dst_unused:UNUSED_PAD src0_sel:DWORD src1_sel:WORD_1\n" \
  "ds_read_b32 v102, v102 offset:%[tb]\n" \
  "ds_read_b32 v103, v103 offset:%[tb]\n" \
  "v_mov_b32 v111, s60\n" \
  "ds_write2_b32 v110, v111, v111 offset1:8\n" \
  "s_bfe_u32 s61, s63, 0x40004\n" \
  "s_set_gpr_idx_on s61, gpr_idx(SRC2,DST)\n" \
  "v_pk_fma_f32 v[32:33], v[80:81], v[72:73], v[32:33] op_sel:[1,0,0] op_sel_hi:[1,1,1]\n" \
  "v_pk_fma_f32 v[42:43], v[80:81], v[74:75], v[42:43] op_sel:[1,0,0] op_sel_hi:[1,1,1]\n" \
  "v_pk_fma_f32 v[52:53], v[84:85], v[76:77], v[52:53] op_sel:[1,0,0] op_sel_hi:[1,1,1]\n" \
  "v_pk_fma_f32 v[62:63], v[84:85], v[78:79], v[62:63] op_sel:[1,0,0] op_sel_hi:[1,1,1]\n" \
  "s_set_gpr_idx_off\n" \
  "s_waitcnt vmcnt(4)\n" \
  "v_mfma_f32_4x4x4_16b_bf16 v[72:75], v[108:109], v[128:129], 0 cbsz:4 abid:10\n" \
  "v_mfma_f32_4x4x4_16b_bf16 v[76:79], v[108:109], v[130:131], 0 cbsz:4 abid:10\n" \
  "v_add_u32_dpp v9, v10, v8 row_newbcast:15 row_mask:0xf bank_mask:0xf\n" \
  "v_cvt_pkrtz_f16_f32 v97, v88, v89\n" \
  "buffer_load_dwordx4 v[148:151], v9, %[xs], 0 offen\n" \
  "v_and_b32 v96, v16, v97\n" \
  "v_and_b32_sdwa v97, v16, v97 dst_sel:DWORD dst_unused:UNUSED_PAD src0_sel:DWORD src1_sel:WORD_1\n" \
  "ds_read_b32 v96, v96 offset:%[tb]\n" \
  "ds_read_b32 v97, v97 offset:%[tb]\n" \
  "ds_read_b64 v[104:105], v15 offset:256\n" \
  "v_cmp_ne_u32_e64 s[64:65], 0, v18\n" \
  "ds_read_b32 v111, v110 offset:16\n" \
  "s_bfe_u32 s61, s63, 0x40008\n" \
  "s_waitcnt lgkmcnt(5)\n" \
  "v_pk_mul_f32 v[82:83], v[82:83], v[100:101]\n" \
  "v_pk_mul_f32 v[86:87], v[86:87], v[102:103]\n" \
  "s_set_gpr_idx_on s61, gpr_idx(SRC2,DST)\n" \
  "v_pk_fma_f32 v[32:33], v[82:83], v[72:73], v[32:33] op_sel:[0,0,0] op_sel_hi:[0,1,1]\n" \
  "v_pk_fma_f32 v[42:43], v[82:83], v[74:75], v[42:43] op_sel:[0,0,0] op_sel_hi:[0,1,1]\n" \
  "v_pk_fma_f32 v[52:53], v[86:87], v[76:77], v[52:53] op_sel:[0,0,0] op_sel_hi:[0,1,1]\n" \
  "v_pk_fma_f32 v[62:63], v[86:87], v[78:79], v[62:63] op_sel:[0,0,0] op_sel_hi:[0,1,1]\n" \
  "s_set_gpr_idx_off\n" \
  "s_waitcnt vmcnt(4) lgkmcnt(9)\n" \
  "v_and_b32 v10, 0xfffffc00, v11\n" \
  "v_mfma_f32_4x4x4_16b_bf16 v[72:75], v[108:109], v[132:133], 0 cbsz:4 abid:11\n" \
  "v_mfma_f32_4x4x4_16b_bf16 v[76:79], v[108:109], v[134:135], 0 cbsz:4 abid:11\n" \
  "v_mfma_f32_4x4x4_16b_bf16 v[116:119], v[106:107], v[22:23], 0\n" \
  "v_add_u32_dpp v9, v10, v8 row_newbcast:0 row_mask:0xf bank_mask:0xf\n" \
  "v_cvt_pkrtz_f16_f32 v99, v92, v93\n" \
  "buffer_load_dwordx4 v[120:123], v9, %[xs], 0 offen\n" \
  "v_and_b32 v98, v16, v99\n" \
  "v_and_b32_sdwa v99, v16, v99 dst_sel:DWORD dst_unused:UNUSED_PAD src0_sel:DWORD src1_sel:WORD_1\n" \
  "ds_read_b32 v98, v98 offset:%[tb]\n" \
  "ds_read_b32 v99, v99 offset:%[tb]\n" \
  "s_bfe_u32 s61, s63, 0x4000c\n" \
  "s_set_gpr_idx_on s61, gpr_idx(SRC2,DST)\n" \
  "v_pk_fma_f32 v[32:33], v[82:83], v[72:73], v[32:33] op_sel:[1,0,0] op_sel_hi:[1,1,1]\n" \
  "v_pk_fma_f32 v[42:43], v[82:83], v[74:75], v[42:43] op_sel:[1,0,0] op_sel_hi:[1,1,1]\n" \
  "v_pk_fma_f32 v[52:53], v[86:87], v[76:77], v[52:53] op_sel:[1,0,0] op_sel_hi:[1,1,1]\n" \
  "v_pk_fma_f32 v[62:63], v[86:87], v[78:79], v[62:63] op_sel:[1,0,0] op_sel_hi:[1,1,1]\n" \
  "s_set_gpr_idx_off\n" \
  "s_sub_u32 s60, s60, 1\n" \
  "s_cbranch_scc1 Lend%=\n" \
  "s_waitcnt vmcnt(4)\n" \
  "v_mfma_f32_4x4x4_16b_bf16 v[72:75], v[108:109], v[136:137], 0 cbsz:4 abid:12\n" \
  "v_mfma_f32_4x4x4_16b_bf16 v[76:79], v[108:109], v[138:139], 0 cbsz:4 abid:12\n" \
  "v_add_u32_dpp v9, v10, v8 row_newbcast:1 row_mask:0xf bank_mask:0xf\n" \
  "v_cvt_pkrtz_f16_f32 v101, v90, v91\n" \
  "buffer_load_dwordx4 v[124:127], v9, %[xs], 0 offen\n" \
  "v_and_b32 v100, v16, v101\n" \
  "v_and_b32_sdwa v101, v16, v101 dst_sel:DWORD dst_unused:UNUSED_PAD src0_sel:DWORD src1_sel:WORD_1\n" \
  "ds_read_b32 v100, v100 offset:%[tb]\n" \
  "ds_read_b32 v101, v101 offset:%[tb]\n" \
  "v_add_u32 v15, 0x100, v15\n" \
  "s_bfe_u32 s61, s63, 0x40010\n" \
  "s_waitcnt lgkmcnt(2)\n" \
  "v_pk_mul_f32 v[88:89], v[88:89], v[96:97]\n" \
  "v_pk_mul_f32 v[92:93], v[92:93], v[98:99]\n" \
  "s_set_gpr_idx_on s61, gpr_idx(SRC2,DST)\n" \
  "v_pk_fma_f32 v[32:33], v[88:89], v[72:73], v[32:33] op_sel:[0,0,0] op_sel_hi:[0,1,1]\n" \
  "v_pk_fma_f32 v[42:43], v[88:89], v[74:75], v[42:43] op_sel:[0,0,0] op_sel_hi:[0,1,1]\n" \
  "v_pk_fma_f32 v[52:53], v[92:93], v[76:77], v[52:53] op_sel:[0,0,0] op_sel_hi:[0,1,1]\n" \
  "v_pk_fma_f32 v[62:63], v[92:93], v[78:79], v[62:63] op_sel:[0,0,0] op_sel_hi:[0,1,1]\n" \
  "s_set_gpr_idx_off\n" \
  "s_waitcnt vmcnt(4) lgkmcnt(5)\n" \
  "v_mfma_f32_4x4x4_16b_bf16 v[72:75], v[108:109], v[140:141], 0 cbsz:4 abid:13\n" \
  "v_mfma_f32_4x4x4_16b_bf16 v[76:79], v[108:109], v[142:143], 0 cbsz:4 abid:13\n" \
  "v_mfma_f32_4x4x4_16b_bf16 v[80:83], v[104:105], v[28:29], 0 cbsz:4\n" \
  "v_mfma_f32_4x4x4_16b_bf16 v[84:87], v[104:105], v[30:31], 0 cbsz:4\n" \
  "v_add_u32_dpp v9, v10, v8 row_newbcast:2 row_mask:0xf bank_mask:0xf\n" \
  "v_cvt_pkrtz_f16_f32 v103, v94, v95\n" \
  "buffer_load_dwordx4 v[128:131], v9, %[xs], 0 offen\n" \
  "v_and_b32 v102, v16, v103\n" \
  "v_and_b32_sdwa v103, v16, v103 dst_sel:DWORD dst_unused:UNUSED_PAD src0_sel:DWORD src1_sel:WORD_1\n" \
  "ds_read_b32 v102, v102 offset:%[tb]\n" \
  "ds_read_b32 v103, v103 offset:%[tb]\n" \
  "s_bfe_u32 s61, s63, 0x40014\n" \
  "s_set_gpr_idx_on s61, gpr_idx(SRC2,DST)\n" \
  "v_pk_fma_f32 v[32:33], v[88:89], v[72:73], v[32:33] op_sel:[1,0,0] op_sel_hi:[1,1,1]\n" \
  "v_pk_fma_f32 v[42:43], v[88:89], v[74:75], v[42:43] op_sel:[1,0,0] op_sel_hi:[1,1,1]\n" \
  "v_pk_fma_f32 v[52:53], v[92:93], v[76:77], v[52:53] op_sel:[1,0,0] op_sel_hi:[1,1,1]\n" \
  "v_pk_fma_f32 v[62:63], v[92:93], v[78:79], v[62:63] op_sel:[1,0,0] op_sel_hi:[1,1,1]\n" \
  "s_set_gpr_idx_off\n" \
  "s_waitcnt vmcnt(4) lgkmcnt(6)\n" \
  "v_mfma_f32_4x4x4_16b_bf16 v[72:75], v[108:109], v[144:145], 0 cbsz:4 abid:14\n" \
  "v_mfma_f32_4x4x4_16b_bf16 v[76:79], v[108:109], v[146:147], 0 cbsz:4 abid:14\n" \
  "v_add_u32_dpp v9, v10, v8 row_newbcast:3 row_mask:0xf bank_mask:0xf\n" \
  "v_cvt_pkrtz_f16_f32 v97, v80, v81\n" \
  "buffer_load_dwordx4 v[132:135], v9, %[xs], 0 offen\n" \
  "v_and_b32 v96, v16, v97\n" \
  "v_and_b32_sdwa v97, v16, v97 dst_sel:DWORD dst_unused:UNUSED_PAD src0_sel:DWORD src1_sel:WORD_1\n" \
  "ds_read_b32 v96, v96 offset:%[tb]\n" \
  "ds_read_b32 v97, v97 offset:%[tb]\n" \
  "v_readfirstlane_b32 s66, v111\n" \
  "s_setprio 1\n" \
  "s_add_u32 s67, s60, %[fo]\n" \
  "s_cmp_ge_u32 s67, s66\n" \
  "s_cbranch_scc0 Lf13_%=\n" \
  "s_setprio 2\n" \
  "Lf13_%=:\n" \
  "v_cvt_pk_bf16_f32 v19, v112, v116\n" \
  "s_bfe_u32 s61, s63, 0x40018\n" \
  "s_waitcnt lgkmcnt(2)\n" \
  "v_pk_mul_f32 v[90:91], v[90:91], v[100:101]\n" \
  "v_pk_mul_f32 v[94:95], v[94:95], v[102:103]\n" \
  "s_set_gpr_idx_on s61, gpr_idx(SRC2,DST)\n" \
  "v_pk_fma_f32 v[32:33], v[90:91], v[72:73], v[32:33] op_sel:[0,0,0] op_sel_hi:[0,1,1]\n" \
  "v_pk_fma_f32 v[42:43], v[90:91], v[74:75], v[42:43] op_sel:[0,0,0] op_sel_hi:[0,1,1]\n" \
  "v_pk_fma_f32 v[52:53], v[94:95], v[76:77], v[52:53] op_sel:[0,0,0] op_sel_hi:[0,1,1]\n" \
  "v_pk_fma_f32 v[62:63], v[94:95], v[78:79], v[62:63] op_sel:[0,0,0] op_sel_hi:[0,1,1]\n" \
  "s_set_gpr_idx_off\n" \
  "s_waitcnt vmcnt(4)\n" \
  "v_mfma_f32_4x4x4_16b_bf16 v[112:115], v[106:107], v[24:25], 0\n" \
  "v_mfma_f32_4x4x4_16b_bf16 v[116:119], v[106:107], v[26:27], 0\n" \
  "v_mfma_f32_4x4x4_16b_bf16 v[72:75], v[108:109], v[148:149], 0 cbsz:4 abid:15\n" \
  "v_mfma_f32_4x4x4_16b_bf16 v[76:79], v[108:109], v[150:151], 0 cbsz:4 abid:15\n" \
  "v_add_u32_dpp v9, v10, v8 row_newbcast:4 row_mask:0xf bank_mask:0xf\n" \
  "v_cvt_pkrtz_f16_f32 v99, v84, v85\n" \
  "buffer_load_dwordx4 v[136:139], v9, %[xs], 0 offen\n" \
  "v_and_b32 v98, v16, v99\n" \
  "v_and_b32_sdwa v99, v16, v99 dst_sel:DWORD dst_unused:UNUSED_PAD src0_sel:DWORD src1_sel:WORD_1\n" \
  "ds_read_b32 v98, v98 offset:%[tb]\n" \
  "ds_read_b32 v99, v99 offset:%[tb]\n" \
  "v_cvt_pk_bf16_f32 v109, v112, v116\n" \
  "v_mov_b32 v108, v19\n" \
  "s_bfe_u32 s61, s63, 0x4001c\n" \
  "s_set_gpr_idx_on s61, gpr_idx(SRC2,DST)\n" \
  "v_pk_fma_f32 v[32:33], v[90:91], v[72:73], v[32:33] op_sel:[1,0,0] op_sel_hi:[1,1,1]\n" \
  "v_pk_fma_f32 v[42:43], v[90:91], v[74:75], v[42:43] op_sel:[1,0,0] op_sel_hi:[1,1,1]\n" \
  "v_pk_fma_f32 v[52:53], v[94:95], v[76:77], v[52:53] op_sel:[1,0,0] op_sel_hi:[1,1,1]\n" \
  "v_pk_fma_f32 v[62:63], v[94:95], v[78:79], v[62:63] op_sel:[1,0,0] op_sel_hi:[1,1,1]\n" \
  "s_set_gpr_idx_off\n" \
  "s_sub_u32 s60, s60, 1\n" \
  "s_cbranch_scc1 Lend%=\n" \
  "s_waitcnt vmcnt(4)\n" \
  "v_mfma_f32_4x4x4_16b_bf16 v[72:75], v[108:109], v[120:121], 0 cbsz:4\n" \
  "v_mfma_f32_4x4x4_16b_bf16 v[76:79], v[108:109], v[122:123], 0 cbsz:4\n" \
  "v_add_u32_dpp v9, v10, v8 row_newbcast:5 row_mask:0xf bank_mask:0xf\n" \
  "v_cvt_pkrtz_f16_f32 v101, v82, v83\n" \
  "buffer_load_dwordx4 v[140:143], v9, %[xs], 0 offen\n" \
  "v_and_b32 v100, v16, v101\n" \
  "v_and_b32_sdwa v101, v16, v101 dst_sel:DWORD dst_unused:UNUSED_PAD src0_sel:DWORD src1_sel:WORD_1\n" \
  "ds_read_b32 v100, v100 offset:%[tb]\n" \
  "ds_read_b32 v101, v101 offset:%[tb]\n" \
  "s_mov_b64 s[62:63], s[64:65]\n" \
  "s_branch Lbody%=\n" \
  "Lend%=:\n" \
  "s_setprio 3\n" \
  "v_mov_b32 v20, 0\n" \
  "v_mov_b32 v21, %[bit]\n" \
  "v_cvt_pk_bf16_f32 v32, v32, v52\n" \
  "v_cvt_pk_bf16_f32 v33, v33, v53\n" \
  "v_cvt_pk_bf16_f32 v42, v42, v62\n" \
  "v_cvt_pk_bf16_f32 v43, v43, v63\n" \
  "ds_read_b128 v[72:75], v158 offset:0\n" \
  "ds_read_b128 v[76:79], v158 offset:1024\n" \
  "ds_read_b128 v[80:83], v158 offset:8192\n" \
  "ds_read_b128 v[84:87], v158 offset:9216\n" \
  "v_cvt_pk_bf16_f32 v34, v34, v54\n" \
  "v_cvt_pk_bf16_f32 v35, v35, v55\n" \
  "v_cvt_pk_bf16_f32 v44, v44, v64\n" \
  "v_cvt_pk_bf16_f32 v45, v45, v65\n" \
  "ds_write2st64_b32 v152, v32, v34 offset0:0 offset1:4\n" \
  "ds_write2st64_b32 v153, v33, v35 offset0:0 offset1:4\n" \
  "ds_write2st64_b32 v154, v42, v44 offset0:0 offset1:4\n" \
  "ds_write2st64_b32 v155, v43, v45 offset0:0 offset1:4\n" \
  "v_cvt_pk_bf16_f32 v36, v36, v56\n" \
  "v_cvt_pk_bf16_f32 v37, v37, v57\n" \
  "v_cvt_pk_bf16_f32 v46, v46, v66\n" \
  "v_cvt_pk_bf16_f32 v47, v47, v67\n" \
  "v_cvt_pk_bf16_f32 v38, v38, v58\n" \
  "v_cvt_pk_bf16_f32 v39, v39, v59\n" \
  "v_cvt_pk_bf16_f32 v48, v48, v68\n" \
  "v_cvt_pk_bf16_f32 v49, v49, v69\n" \
  "ds_write2st64_b32 v152, v36, v38 offset0:8 offset1:12\n" \
  "ds_write2st64_b32 v153, v37, v39 offset0:8 offset1:12\n" \
  "ds_write2st64_b32 v154, v46, v48 offset0:8 offset1:12\n" \
  "ds_write2st64_b32 v155, v47, v49 offset0:8 offset1:12\n" \
  "v_cvt_pk_bf16_f32 v40, v40, v60\n" \
  "v_cvt_pk_bf16_f32 v41, v41, v61\n" \
  "v_cvt_pk_bf16_f32 v50, v50, v70\n" \
  "v_cvt_pk_bf16_f32 v51, v51, v71\n" \
  "ds_write2_b32 v156, v40, v41 offset0:0 offset1:16\n" \
  "ds_write2_b32 v156, v50, v51 offset0:32 offset1:48\n" \
  "s_mov_b64 exec, 1\n" \
  "ds_or_b32 v20, v21 offset:%[rd]\n" \
  "s_mov_b64 exec, -1\n" \
  "ds_read_b128 v[32:35], v157 offset:0\n" \
  "ds_read_b128 v[36:39], v157 offset:256\n" \
  "ds_read_b128 v[40:43], v157 offset:512\n" \
  "ds_read_b128 v[44:47], v157 offset:768\n" \
  "ds_read_b128 v[88:91], v158 offset:16384\n" \
  "ds_read_b128 v[92:95], v158 offset:17408\n" \
  "ds_read_b128 v[96:99], v158 offset:24576\n" \
  "ds_read_b128 v[100:103], v158 offset:25600\n" \
  "ds_read_b128 v[104:107], v158 offset:2048\n" \
  "ds_read_b128 v[108:111], v158 offset:3072\n" \
  "ds_read_b128 v[112:115], v158 offset:10240\n" \
  "ds_read_b128 v[116:119], v158 offset:11264\n" \
  "s_setprio 0\n" \
  "s_waitcnt lgkmcnt(11)\n" \
  "v_mfma_f32_16x16x32_bf16 v[48:51], v[32:35], v[72:75], 0\n" \
  "ds_read_b128 v[72:75], v158 offset:18432\n" \
  "v_mfma_f32_16x16x32_bf16 v[52:55], v[32:35], v[76:79], 0\n" \
  "ds_read_b128 v[76:79], v158 offset:19456\n" \
  "s_waitcnt lgkmcnt(12)\n" \
  "v_mfma_f32_16x16x32_bf16 v[48:51], v[36:39], v[80:83], v[48:51]\n" \
  "ds_read_b128 v[80:83], v158 offset:26624\n" \
  "v_mfma_f32_16x16x32_bf16 v[52:55], v[36:39], v[84:87], v[52:55]\n" \
  "ds_read_b128 v[84:87], v158 offset:27648\n" \
  "s_waitcnt lgkmcnt(11)\n" \
  "v_mfma_f32_16x16x32_bf16 v[48:51], v[40:43], v[88:91], v[48:51]\n" \
  "s_waitcnt lgkmcnt(10)\n" \
  "v_mfma_f32_16x16x32_bf16 v[52:55], v[40:43], v[92:95], v[52:55]\n" \
  "s_waitcnt lgkmcnt(9)\n" \
  "v_mfma_f32_16x16x32_bf16 v[48:51], v[44:47], v[96:99], v[48:51]\n" \
  "s_waitcnt lgkmcnt(8)\n" \
  "v_mfma_f32_16x16x32_bf16 v[52:55], v[44:47], v[100:103], v[52:55]\n" \
  "s_waitcnt lgkmcnt(7)\n" \
  "v_mfma_f32_16x16x32_bf16 v[56:59], v[32:35], v[104:107], 0\n" \
  "s_waitcnt lgkmcnt(6)\n" \
  "v_mfma_f32_16x16x32_bf16 v[60:63], v[32:35], v[108:111], 0\n" \
  "s_waitcnt lgkmcnt(5)\n" \
  "v_mfma_f32_16x16x32_bf16 v[56:59], v[36:39], v[112:115], v[56:59]\n" \
  "s_waitcnt lgkmcnt(4)\n" \
  "v_mfma_f32_16x16x32_bf16 v[60:63], v[36:39], v[116:119], v[60:63]\n" \
  "s_waitcnt lgkmcnt(3)\n" \
  "v_mfma_f32_16x16x32_bf16 v[56:59], v[40:43], v[72:75], v[56:59]\n" \
  "v_cvt_pk_bf16_f32 v64, v48, v49\n" \
  "v_cvt_pk_bf16_f32 v65, v50, v51\n" \
  "v_cvt_pk_bf16_f32 v66, v52, v53\n" \
  "v_cvt_pk_bf16_f32 v67, v54, v55\n" \
  "buffer_store_dwordx4 v[64:67], v159, %[os], 0 offen offset:0 sc1\n" \
  "s_waitcnt lgkmcnt(2)\n" \
  "v_mfma_f32_16x16x32_bf16 v[60:63], v[40:43], v[76:79], v[60:63]\n" \
  "s_waitcnt lgkmcnt(1)\n" \
  "v_mfma_f32_16x16x32_bf16 v[56:59], v[44:47], v[80:83], v[56:59]\n" \
  "s_waitcnt lgkmcnt(0)\n" \
  "v_mfma_f32_16x16x32_bf16 v[60:63], v[44:47], v[84:87], v[60:63]\n" \
  "s_nop 7\n" \
  "v_cvt_pk_bf16_f32 v68, v56, v57\n" \
  "v_cvt_pk_bf16_f32 v69, v58, v59\n" \
  "v_cvt_pk_bf16_f32 v70, v60, v61\n" \
  "v_cvt_pk_bf16_f32 v71, v62, v63\n" \
  "buffer_store_dwordx4 v[68:71], v159, %[os], 0 offen offset:256 sc1\n" \
  "s_endpgm\n" \
  "Lhr%=:\n" \
  "s_mov_b64 exec, vcc\n" \
  "s_sub_u32 s63, s63, 1\n" \
  "s_cbranch_scc0 Lh%=\n" \
  "s_mov_b64 exec, -1\n" \
  "Lover%=:\n" \
  "v_mov_b32 v113, 1\n" \
  "ds_write_b32 v117, v113 offset:%[fl]\n" \
  "s_mov_b32 s62, 0\n" \
  "v_mov_b32 v114, 0\n" \
  "v_mov_b32 v115, 0\n" \
  "s_branch Lld%=\n" \
  "Lkr%=:\n" \
  "buffer_load_dword v84, v76, %[ws], %[rk] offen sc1\n" \
  "buffer_load_dword v85, v77, %[ws], %[rk] offen sc1\n" \
  "buffer_load_dword v86, v78, %[ws], %[rk] offen sc1\n" \
  "s_cmpk_le_u32 s62, 0xc0\n" \
  "s_cbranch_scc1 Lkb%=\n" \
  "buffer_load_dword v87, v79, %[ws], %[rk] offen sc1\n" \
  "Lkb%=:\n" \
  "s_waitcnt vmcnt(0)\n" \
  "s_sub_u32 s63, s63, 1\n" \
  "s_cbranch_scc0 Lk%=\n" \
  "v_mov_b32 v113, 1\n" \
  "ds_write_b32 v117, v113 offset:%[fl]\n" \
  "s_branch Lkc%=\n" \
  "Lc3%=:\n" \
  "v_lshlrev_b32 v91, 1, v87\n" \
  "v_lshlrev_b32 v95, 4, v87\n" \
  "v_lshrrev_b32 v107, 2, v87\n" \
  "v_and_or_b32 v91, v91, v118, %[wb]\n" \
  "v_lshlrev_b32_e64 v103, v95, 4\n" \
  "v_and_b32 v107, 0xfc, v107\n" \
  "s_mov_b64 exec, s[72:73]\n" \
  "ds_add_rtn_u32 v99, v91, v103 offset:%[hi]\n" \
  "ds_add_u32 v107, v116 offset:%[dg]\n" \
  "s_mov_b64 exec, -1\n" \
  "s_branch Lc4%=\n" \
  "Llo%=:\n" \
  "v_mov_b32 v113, 1\n" \
  "ds_write_b32 v117, v113 offset:%[fl]\n" \
  "s_branch Llc%=\n" \
  "Lslow%=:\n" \
  "s_waitcnt vmcnt(0) lgkmcnt(0)\n" \
  "s_barrier\n" \
  "s_branch Lexit%=\n" \
  "Lq3%=:\n" \
  "s_mov_b64 exec, -1\n" \
  "v_and_b32 v103, 0x7fc, v91\n" \
  "ds_read_b32 v79, v91 offset:%[hi]\n" \
  "ds_read_b32 v107, v103 offset:%[to]\n" \
  "s_waitcnt lgkmcnt(0)\n" \
  "v_add3_u32 v91, v79, v107, v99\n" \
  "v_lshrrev_b32 v91, v95, v91\n" \
  "v_bfe_u32 v75, v91, 13, 3\n" \
  "v_and_b32 v91, 0x1ffc, v91\n" \
  "v_or_b32 v75, v87, v75\n" \
  "s_mov_b64 exec, s[72:73]\n" \
  "ds_write_b32 v91, v75 offset:%[so]\n" \
  "s_branch Lq4%=\n" \
  "Lpr%=:\n" \
  "s_cmpk_le_u32 s62, 0xc0\n" \
  "s_cbranch_scc1 Lpb%=\n" \
  "buffer_load_dwordx4 v[44:47], v83, %[ws], %[rp] offen sc1\n" \
  "Lpb%=:\n" \
  "buffer_load_dwordx4 v[32:35], v80, %[ws], %[rp] offen sc1\n" \
  "buffer_load_dwordx4 v[36:39], v81, %[ws], %[rp] offen sc1\n" \
  "buffer_load_dwordx4 v[40:43], v82, %[ws], %[rp] offen sc1\n" \
  "s_sub_u32 s63, s63, 1\n" \
  "s_waitcnt vmcnt(0)\n" \
  "s_cbranch_scc0 Lpc%=\n" \
  "s_branch Lpw%=\n" \
  "Lexit%=:\n" \
  "s_mov_b32 %[sl], s61\n"
#define STREAM_CLOBBERS \
  "v7", "v9", "v10", "v11", "v18", "v19", "v32", "v33", "v34", "v35", "v36", "v37", "v38", "v39", "v40", "v41", \
  "v42", "v43", "v44", "v45", "v46", "v47", "v48", "v49", "v50", "v51", "v52", "v53", "v54", "v55", "v56", \
  "v57", "v58", "v59", "v60", "v61", "v62", "v63", "v64", "v65", "v66", "v67", "v68", "v69", "v70", "v71", \
  "v72", "v73", "v74", "v75", "v76", "v77", "v78", "v79", "v80", "v81", "v82", "v83", "v84", "v85", "v86", \
  "v87", "v88", "v89", "v90", "v91", "v92", "v93", "v94", "v95", "v96", "v97", "v98", "v99", "v100", "v101", \
  "v102", "v103", "v104", "v105", "v106", "v107", "v108", "v109", "v111", "v112", "v113", "v114", "v115", \
  "v116", "v117", "v118", "v119", "v120", "v121", "v122", "v123", "v124", "v125", "v126", "v127", "v128", \
  "v129", "v130", "v131", "v132", "v133", "v134", "v135", "s60", "s61", "s62", "s63", "s64", "s65", "s66", \
  "s67", "s68", "s69", "s70", "s71", "s72", "s73", "s74", "s75", "s76", "s77", "s78", "s79", "scc", "vcc", \
  "memory"
#define RKL 3
#define MIX_ASM \
  "ds_read_b128 v[64:67], v7 offset:0\n" \
  "ds_read_b128 v[68:71], v7 offset:1024\n" \
  "ds_read_b128 v[72:75], v7 offset:8192\n" \
  "ds_read_b128 v[76:79], v7 offset:9216\n" \
  "ds_read_b128 v[80:83], v7 offset:16384\n" \
  "ds_read_b128 v[84:87], v7 offset:17408\n" \
  "ds_read_b128 v[88:91], v7 offset:24576\n" \
  "ds_read_b128 v[92:95], v7 offset:25600\n" \
  "ds_read_b128 v[96:99], v7 offset:2048\n" \
  "ds_read_b128 v[100:103], v7 offset:3072\n" \
  "ds_read_b128 v[104:107], v7 offset:10240\n" \
  "ds_read_b128 v[108:111], v7 offset:11264\n" \
  "ds_read_b128 v[112:115], v7 offset:18432\n" \
  "ds_read_b128 v[116:119], v7 offset:19456\n" \
  "ds_read_b128 v[120:123], v7 offset:26624\n" \
  "ds_read_b128 v[124:127], v7 offset:27648\n" \
  "v_mov_b32 v8, 0\n" \
  "s_mov_b32 s60, 7\n" \
  "Lz%=:\n" \
  "s_sleep %[sl]\n" \
  "ds_read_b32 v9, v6\n" \
  "ds_read_b32 v10, v8 offset:%[rd]\n" \
  "s_waitcnt lgkmcnt(0)\n" \
  "v_readfirstlane_b32 s61, v9\n" \
  "v_readfirstlane_b32 s62, v10\n" \
  "s_cmp_gt_u32 s61, 4\n" \
  "s_cselect_b32 s61, s62, 1\n" \
  "s_cmp_eq_u32 s61, 0\n" \
  "s_cbranch_scc1 Lz%=\n" \
  "s_setprio 1\n" \
  "Lp%=:\n" \
  "ds_read_b32 v9, v8 offset:%[rd]\n" \
  "s_waitcnt lgkmcnt(0)\n" \
  "v_readfirstlane_b32 s62, v9\n" \
  "s_lshr_b32 s63, s62, %[h]\n" \
  "s_and_b32 s61, s63, 1\n" \
  "s_bfe_u32 s63, s63, 0x10004\n" \
  "s_lshl1_add_u32 s61, s63, s61\n" \
  "s_andn2_b32 s63, %[nd], s62\n" \
  "s_cselect_b32 s63, 0, 4\n" \
  "s_or_b32 s61, s61, s63\n" \
  "s_and_b32 s61, s61, s60\n" \
  "s_cbranch_scc1 Lgo%=\n" \
  "s_sleep 1\n" \
  "s_branch Lp%=\n" \
  "Lgo%=:\n" \
  "s_andn2_b32 s60, s60, s61\n" \
  "s_bitcmp1_b32 s61, 0\n" \
  "s_cbranch_scc0 Lr0_%=\n" \
  "ds_read_b128 v[16:19], v0 offset:0\n" \
  "ds_read_b128 v[20:23], v0 offset:256\n" \
  "ds_read_b128 v[24:27], v0 offset:512\n" \
  "ds_read_b128 v[28:31], v0 offset:768\n" \
  "Lr0_%=:\n" \
  "s_bitcmp1_b32 s61, 1\n" \
  "s_cbranch_scc0 Lr1_%=\n" \
  "ds_read_b128 v[32:35], v1 offset:0\n" \
  "ds_read_b128 v[36:39], v1 offset:256\n" \
  "ds_read_b128 v[40:43], v1 offset:512\n" \
  "ds_read_b128 v[44:47], v1 offset:768\n" \
  "Lr1_%=:\n" \
  "s_bitcmp1_b32 s61, 2\n" \
  "s_cbranch_scc0 Lr2_%=\n" \
  "ds_read_b128 v[48:51], v2 offset:0\n" \
  "ds_read_b128 v[52:55], v2 offset:256\n" \
  "ds_read_b128 v[56:59], v2 offset:512\n" \
  "ds_read_b128 v[60:63], v2 offset:768\n" \
  "Lr2_%=:\n" \
  "s_waitcnt lgkmcnt(0)\n" \
  "s_bitcmp1_b32 s61, 0\n" \
  "s_cbranch_scc0 Lj0_%=\n" \
  "v_mfma_f32_16x16x32_bf16 v[128:131], v[16:19], v[64:67], 0\n" \
  "v_mfma_f32_16x16x32_bf16 v[132:135], v[16:19], v[68:71], 0\n" \
  "v_mfma_f32_16x16x32_bf16 v[128:131], v[20:23], v[72:75], v[128:131]\n" \
  "v_mfma_f32_16x16x32_bf16 v[132:135], v[20:23], v[76:79], v[132:135]\n" \
  "v_mfma_f32_16x16x32_bf16 v[128:131], v[24:27], v[80:83], v[128:131]\n" \
  "v_mfma_f32_16x16x32_bf16 v[132:135], v[24:27], v[84:87], v[132:135]\n" \
  "v_mfma_f32_16x16x32_bf16 v[128:131], v[28:31], v[88:91], v[128:131]\n" \
  "v_mfma_f32_16x16x32_bf16 v[132:135], v[28:31], v[92:95], v[132:135]\n" \
  "v_mfma_f32_16x16x32_bf16 v[136:139], v[16:19], v[96:99], 0\n" \
  "v_mfma_f32_16x16x32_bf16 v[140:143], v[16:19], v[100:103], 0\n" \
  "v_mfma_f32_16x16x32_bf16 v[136:139], v[20:23], v[104:107], v[136:139]\n" \
  "v_mfma_f32_16x16x32_bf16 v[140:143], v[20:23], v[108:111], v[140:143]\n" \
  "v_mfma_f32_16x16x32_bf16 v[136:139], v[24:27], v[112:115], v[136:139]\n" \
  "v_mfma_f32_16x16x32_bf16 v[140:143], v[24:27], v[116:119], v[140:143]\n" \
  "v_mfma_f32_16x16x32_bf16 v[136:139], v[28:31], v[120:123], v[136:139]\n" \
  "v_mfma_f32_16x16x32_bf16 v[140:143], v[28:31], v[124:127], v[140:143]\n" \
  "v_cvt_pk_bf16_f32 v128, v128, v129\n" \
  "v_cvt_pk_bf16_f32 v129, v130, v131\n" \
  "v_cvt_pk_bf16_f32 v130, v132, v133\n" \
  "v_cvt_pk_bf16_f32 v131, v134, v135\n" \
  "buffer_store_dwordx4 v[128:131], v3, %[os], 0 offen offset:0 sc1\n" \
  "s_nop 2\n" \
  "v_cvt_pk_bf16_f32 v136, v136, v137\n" \
  "v_cvt_pk_bf16_f32 v137, v138, v139\n" \
  "v_cvt_pk_bf16_f32 v138, v140, v141\n" \
  "v_cvt_pk_bf16_f32 v139, v142, v143\n" \
  "buffer_store_dwordx4 v[136:139], v3, %[os], 0 offen offset:256 sc1\n" \
  "Lj0_%=:\n" \
  "s_bitcmp1_b32 s61, 1\n" \
  "s_cbranch_scc0 Lj1_%=\n" \
  "v_mfma_f32_16x16x32_bf16 v[144:147], v[32:35], v[64:67], 0\n" \
  "v_mfma_f32_16x16x32_bf16 v[148:151], v[32:35], v[68:71], 0\n" \
  "v_mfma_f32_16x16x32_bf16 v[144:147], v[36:39], v[72:75], v[144:147]\n" \
  "v_mfma_f32_16x16x32_bf16 v[148:151], v[36:39], v[76:79], v[148:151]\n" \
  "v_mfma_f32_16x16x32_bf16 v[144:147], v[40:43], v[80:83], v[144:147]\n" \
  "v_mfma_f32_16x16x32_bf16 v[148:151], v[40:43], v[84:87], v[148:151]\n" \
  "v_mfma_f32_16x16x32_bf16 v[144:147], v[44:47], v[88:91], v[144:147]\n" \
  "v_mfma_f32_16x16x32_bf16 v[148:151], v[44:47], v[92:95], v[148:151]\n" \
  "v_mfma_f32_16x16x32_bf16 v[152:155], v[32:35], v[96:99], 0\n" \
  "v_mfma_f32_16x16x32_bf16 v[156:159], v[32:35], v[100:103], 0\n" \
  "v_mfma_f32_16x16x32_bf16 v[152:155], v[36:39], v[104:107], v[152:155]\n" \
  "v_mfma_f32_16x16x32_bf16 v[156:159], v[36:39], v[108:111], v[156:159]\n" \
  "v_mfma_f32_16x16x32_bf16 v[152:155], v[40:43], v[112:115], v[152:155]\n" \
  "v_mfma_f32_16x16x32_bf16 v[156:159], v[40:43], v[116:119], v[156:159]\n" \
  "v_mfma_f32_16x16x32_bf16 v[152:155], v[44:47], v[120:123], v[152:155]\n" \
  "v_mfma_f32_16x16x32_bf16 v[156:159], v[44:47], v[124:127], v[156:159]\n" \
  "v_cvt_pk_bf16_f32 v144, v144, v145\n" \
  "v_cvt_pk_bf16_f32 v145, v146, v147\n" \
  "v_cvt_pk_bf16_f32 v146, v148, v149\n" \
  "v_cvt_pk_bf16_f32 v147, v150, v151\n" \
  "buffer_store_dwordx4 v[144:147], v4, %[os], 0 offen offset:0 sc1\n" \
  "s_nop 2\n" \
  "v_cvt_pk_bf16_f32 v152, v152, v153\n" \
  "v_cvt_pk_bf16_f32 v153, v154, v155\n" \
  "v_cvt_pk_bf16_f32 v154, v156, v157\n" \
  "v_cvt_pk_bf16_f32 v155, v158, v159\n" \
  "buffer_store_dwordx4 v[152:155], v4, %[os], 0 offen offset:256 sc1\n" \
  "Lj1_%=:\n" \
  "s_bitcmp1_b32 s61, 2\n" \
  "s_cbranch_scc0 Lj2_%=\n" \
  "v_mfma_f32_16x16x32_bf16 v[128:131], v[48:51], v[64:67], 0\n" \
  "v_mfma_f32_16x16x32_bf16 v[132:135], v[48:51], v[68:71], 0\n" \
  "v_mfma_f32_16x16x32_bf16 v[128:131], v[52:55], v[72:75], v[128:131]\n" \
  "v_mfma_f32_16x16x32_bf16 v[132:135], v[52:55], v[76:79], v[132:135]\n" \
  "v_mfma_f32_16x16x32_bf16 v[128:131], v[56:59], v[80:83], v[128:131]\n" \
  "v_mfma_f32_16x16x32_bf16 v[132:135], v[56:59], v[84:87], v[132:135]\n" \
  "v_mfma_f32_16x16x32_bf16 v[128:131], v[60:63], v[88:91], v[128:131]\n" \
  "v_mfma_f32_16x16x32_bf16 v[132:135], v[60:63], v[92:95], v[132:135]\n" \
  "v_mfma_f32_16x16x32_bf16 v[136:139], v[48:51], v[96:99], 0\n" \
  "v_mfma_f32_16x16x32_bf16 v[140:143], v[48:51], v[100:103], 0\n" \
  "v_mfma_f32_16x16x32_bf16 v[136:139], v[52:55], v[104:107], v[136:139]\n" \
  "v_mfma_f32_16x16x32_bf16 v[140:143], v[52:55], v[108:111], v[140:143]\n" \
  "v_mfma_f32_16x16x32_bf16 v[136:139], v[56:59], v[112:115], v[136:139]\n" \
  "v_mfma_f32_16x16x32_bf16 v[140:143], v[56:59], v[116:119], v[140:143]\n" \
  "v_mfma_f32_16x16x32_bf16 v[136:139], v[60:63], v[120:123], v[136:139]\n" \
  "v_mfma_f32_16x16x32_bf16 v[140:143], v[60:63], v[124:127], v[140:143]\n" \
  "v_cvt_pk_bf16_f32 v128, v128, v129\n" \
  "v_cvt_pk_bf16_f32 v129, v130, v131\n" \
  "v_cvt_pk_bf16_f32 v130, v132, v133\n" \
  "v_cvt_pk_bf16_f32 v131, v134, v135\n" \
  "buffer_store_dwordx4 v[128:131], v5, %[os], 0 offen offset:0 sc1\n" \
  "s_nop 2\n" \
  "v_cvt_pk_bf16_f32 v136, v136, v137\n" \
  "v_cvt_pk_bf16_f32 v137, v138, v139\n" \
  "v_cvt_pk_bf16_f32 v138, v140, v141\n" \
  "v_cvt_pk_bf16_f32 v139, v142, v143\n" \
  "buffer_store_dwordx4 v[136:139], v5, %[os], 0 offen offset:256 sc1\n" \
  "Lj2_%=:\n" \
  "s_cmp_lg_u32 s60, 0\n" \
  "s_cbranch_scc1 Lp%=\n"
#define MIX_CLOBBERS \
  "v8", "v9", "v10", "v11", "v12", "v13", "v14", "v15", "v16", "v17", "v18", "v19", "v20", "v21", "v22", "v23", \
  "v24", "v25", "v26", "v27", "v28", "v29", "v30", "v31", "v32", "v33", "v34", "v35", "v36", "v37", "v38", \
  "v39", "v40", "v41", "v42", "v43", "v44", "v45", "v46", "v47", "v48", "v49", "v50", "v51", "v52", "v53", \
  "v54", "v55", "v56", "v57", "v58", "v59", "v60", "v61", "v62", "v63", "v64", "v65", "v66", "v67", "v68", \
  "v69", "v70", "v71", "v72", "v73", "v74", "v75", "v76", "v77", "v78", "v79", "v80", "v81", "v82", "v83", \
  "v84", "v85", "v86", "v87", "v88", "v89", "v90", "v91", "v92", "v93", "v94", "v95", "v96", "v97", "v98", \
  "v99", "v100", "v101", "v102", "v103", "v104", "v105", "v106", "v107", "v108", "v109", "v110", "v111", "v112", \
  "v113", "v114", "v115", "v116", "v117", "v118", "v119", "v120", "v121", "v122", "v123", "v124", "v125", \
  "v126", "v127", "v128", "v129", "v130", "v131", "v132", "v133", "v134", "v135", "v136", "v137", "v138", \
  "v139", "v140", "v141", "v142", "v143", "v144", "v145", "v146", "v147", "v148", "v149", "v150", "v151", \
  "v152", "v153", "v154", "v155", "v156", "v157", "v158", "v159", "s60", "s61", "s62", "s63", "scc", "vcc", \
  "memory"


namespace {

constexpr int C_ = 128;
constexpr int MW = 12;
constexpr int MT = MW * 64;
constexpr int SW = 8;
constexpr int NS = 5;
constexpr int NPB = SW * NS;
constexpr int NPASS = 16;
constexpr int HS = 64 * NPASS / 2;
constexpr int NBMAX = 256;
constexpr int PW = 5;
constexpr int PT = PW * 64;
constexpr int PK = 4;
constexpr int CH = PT * PK;
constexpr int MK = 4;
constexpr int CAPW = 256;
constexpr int HWV = 10;
constexpr int PFN = 5;
constexpr int TB1 = 1;
constexpr int TB2 = 2;
constexpr int WA = 3;
constexpr int WB = 6;
constexpr int FO1 = 2;
constexpr int HSL = 24;
constexpr unsigned WS_EPOCH = 0;
constexpr unsigned WS_OFF = 4096;
constexpr unsigned WS_RECK = WS_OFF + NBMAX * NBMAX * 16;
constexpr unsigned WS_RECP = WS_RECK + NBMAX * CH * 4;
constexpr unsigned WS_END = WS_RECP + NBMAX * CH * 16;

__device__ __forceinline__ float bf2f(unsigned short v) { return __uint_as_float(((unsigned)v) << 16); }
__device__ __forceinline__ unsigned short f2bf(float f) {
  __hip_bfloat16 b = __float2bfloat16(f);
  return *reinterpret_cast<unsigned short*>(&b);
}
__device__ __forceinline__ unsigned pk2bf(float lo, float hi) {
  return __builtin_bit_cast(unsigned, __builtin_convertvector(f32x2{lo, hi}, bf16x2));
}
__device__ __forceinline__ unsigned wave_incl_scan(unsigned x) {
  x += __builtin_amdgcn_update_dpp(0u, x, 0x111, 0xf, 0xf, false);
  x += __builtin_amdgcn_update_dpp(0u, x, 0x112, 0xf, 0xf, false);
  x += __builtin_amdgcn_update_dpp(0u, x, 0x114, 0xf, 0xf, false);
  x += __builtin_amdgcn_update_dpp(0u, x, 0x118, 0xf, 0xf, false);
  x += __builtin_amdgcn_update_dpp(0u, x, 0x142, 0xa, 0xf, false);
  x += __builtin_amdgcn_update_dpp(0u, x, 0x143, 0xc, 0xf, false);
  return x;
}

struct Lds {
  u32x4 sink[16];
  unsigned flag, rdone, go, pad_;
  unsigned long long tag, pad2_;
  unsigned prog[16];
  int deg[64];
  unsigned byrank[64];
  unsigned nl[64];
  u32x4 ct[72];
  union {
    struct {
      __attribute__((aligned(16))) unsigned so[SW * CAPW];
      union {
        __attribute__((aligned(16))) unsigned hist[(SW + 1) * HS];
        u32x4 row5[SW * 64];
      };
    };
    struct {
      __attribute__((aligned(16))) unsigned sk[CH];
      u32x4 sp[CH];
    };
  };
  union {
    struct {
      __attribute__((aligned(16))) unsigned seg[2][NBMAX];
      unsigned phist[PW][NBMAX / 2];
      unsigned pbase[PW][NBMAX / 2];
      u32x4 ri[SW][MK * 16];
    };
    u32x4 pay[SW * CAPW];
  };
  f32x4 sg[4096];
  u32x4 wf[C_ * C_ / 8];
};
static_assert(sizeof(Lds) <= 163840, "LDS budget");
static_assert(offsetof(Lds, sg) < 65536, "table offset");

__device__ __forceinline__ unsigned lds_addr(const void* p) {
  return (unsigned)(uintptr_t)(__attribute__((address_space(3))) const void*)p;
}
__device__ __forceinline__ u32x4 make_srd(const void* p, unsigned num) {
  unsigned long long a = (unsigned long long)p;
  return u32x4{(unsigned)a, (unsigned)(a >> 32), num, 0x00020000u};
}

}

extern "C" __global__ __launch_bounds__(MT, 3) void tfn_main_kernel(
    const int* dst_, int E_, int c0_, const int* src_, int CE_, int c1_, const u32x2* Y_, int N_, int c2_, const u32x2* ef_,
    unsigned M2_, int c3_, const unsigned short* x_, char* ws_, u32x4* out_, const unsigned short* wrad_,
    const unsigned short* cg_, const unsigned short* wmix_) {
  __shared__ Lds L;
  const int tid = threadIdx.x, lane = tid & 63;
  u32x2 d0, d1, s0, s1;
  u32x4 y0, y1, f0, f1;
  u32x16 ka;
  u32x8 kb, kc;
  u32x4 kd;
  unsigned long long ev;
  asm volatile(
      "s_load_dwordx16 s[8:23], %[kp], 0\n s_load_dwordx8 s[24:31], %[kp], 0x40\n s_load_dwordx4 s[32:35], %[kp], 0x60\n"
      "v_readfirstlane_b32 s36, %[tid]\n v_lshlrev_b32 v90, 3, %[tid]\n v_mov_b64 v[92:93], 0\n"
      "s_and_b32 s37, %[bid], 7\n s_lshr_b32 s38, %[bid], 3\n s_lshr_b32 s36, s36, 6\n"
      "ds_write_b64 v90, v[92:93] offset:%[zo]\n"
      "s_cmp_lg_u32 s36, %[pw]\n s_cbranch_scc1 1f\n"
      "s_getpc_b64 s[44:45]\n .Ltfn_p0:\n s_max_i32 s46, 0, .Ltfn_p1-.Ltfn_p0\n s_mov_b32 m0, %[sk]\n s_mov_b32 s47, 0x20000\n"
      "v_lshlrev_b32 v91, 12, %[tid]\n s_lshl_b32 s39, s38, 7\n v_and_b32 v91, 0x3f000, v91\n"
      "buffer_load_dword v91, s[44:47], s39 offen lds\n"
      "1:\n s_lshl_b32 s37, s37, 5\n s_add_u32 s37, s37, s38\n v_lshlrev_b32 v91, 4, %[tid]\n"
      "s_waitcnt lgkmcnt(0)\n"
      "s_mul_i32 s38, s37, s14\n s_mov_b32 s40, s10\n s_add_i32 s39, s38, s14\n s_lshl_b32 s44, s38, 2\n"
      "s_min_i32 s39, s39, s10\n s_add_i32 s45, s44, %[h4]\n s_lshl_b32 s10, s39, 2\n s_lshl_b32 s14, s39, 2\n"
      "s_cmp_ge_u32 s36, %[pw]\n s_cbranch_scc1 2f\n"
      "buffer_load_dwordx2 %0, v90, s[8:11], s44 offen\n buffer_load_dwordx2 %1, v90, s[8:11], s45 offen\n"
      "buffer_load_dwordx2 %2, v90, s[12:15], s44 offen\n buffer_load_dwordx2 %3, v90, s[12:15], s45 offen\n"
      "2:\n s_mov_b32 s41, s18\n s_mov_b32 s42, s22\n s_lshl_b32 s18, s39, 3\n s_lshl_b32 s22, s39, 3\n"
      "s_lshl_b32 s44, s44, 1\n s_lshl_b32 s45, s45, 1\n s_cmp_ge_u32 s36, %[pw]\n"
      "s_barrier\n s_cbranch_scc1 3f\n s_setprio 3\n"
      "buffer_load_dwordx4 v[100:103], v91, s[16:19], s44 offen\n buffer_load_dwordx4 v[104:107], v91, s[16:19], s45 offen\n"
      "buffer_load_dwordx4 v[108:111], v91, s[20:23], s44 offen\n buffer_load_dwordx4 v[112:115], v91, s[20:23], s45 offen\n"
      "3:\n s_cmp_lg_u32 s36, %[hw]\n s_cbranch_scc1 4f\n s_load_dwordx2 s[46:47], s[26:27], 0\n s_setprio 3\n"
      "s_waitcnt lgkmcnt(0)\n 4:"
      : "=&v"(d0), "=&v"(d1), "=&v"(s0), "=&v"(s1), "=&{v[100:103]}"(y0), "=&{v[104:107]}"(y1), "=&{v[108:111]}"(f0),
        "=&{v[112:115]}"(f1), "=&{s[8:23]}"(ka), "=&{s[24:31]}"(kb), "=&{s[32:35]}"(kd), "=&{s[36:43]}"(kc),
        "=&{s[46:47]}"(ev)
      : [kp] "s"(__builtin_amdgcn_kernarg_segment_ptr()), [tid] "v"(tid), [bid] "s"(blockIdx.x), [pw] "n"(PW), [hw] "n"(HWV),
        [zo] "n"(offsetof(Lds, seg)), [sk] "n"(offsetof(Lds, sink)), [h4] "n"(CH * 2)
      : "v90", "v91", "v92", "v93", "s44", "s45", "scc", "memory");
  typedef __attribute__((address_space(1))) char gchar;
  auto ptr = [](unsigned lo, unsigned hi) { return (char*)(gchar*)(lo | (unsigned long long)hi << 32); };
  const int* __restrict__ dst = (const int*)ptr(ka[0], ka[1]);
  const int* __restrict__ src = (const int*)ptr(ka[4], ka[5]);
  const u32x2* __restrict__ Y = (const u32x2*)ptr(ka[8], ka[9]);
  const u32x2* __restrict__ ef = (const u32x2*)ptr(ka[12], ka[13]);
  const unsigned short* __restrict__ x = (const unsigned short*)ptr(kb[0], kb[1]);
  char* ws = ptr(kb[2], kb[3]);
  u32x4* __restrict__ out = (u32x4*)ptr(kb[4], kb[5]);
  const unsigned short* __restrict__ wrad = (const unsigned short*)ptr(kb[6], kb[7]);
  const unsigned short* __restrict__ cg = (const unsigned short*)ptr(kd[0], kd[1]);
  const unsigned short* __restrict__ wmix = (const unsigned short*)ptr(kd[2], kd[3]);
  const int w = kc[0], bucket = kc[1], e0 = kc[2] + tid * 2, e1 = kc[3], E = kc[4], N = kc[5];
  const unsigned M2 = kc[6];
  __builtin_assume(w >= 0 && w < MW && bucket >= 0 && bucket < NBMAX);
  const int xcd = blockIdx.x & 7;
  const u32x4 wsd = make_srd(ws, WS_END);
  auto table = [&](int q) {
    f32x4 p, n;
#pragma unroll
    for (int i = 0; i < 4; i++) {
      const unsigned short c = min(q * 16 + i * 4 + 2, 0x7bff);
      const float e = __builtin_amdgcn_exp2f(-1.4426950408889634f * (float)__builtin_bit_cast(_Float16, c));
      p[i] = __builtin_amdgcn_rcpf(1.f + e);
      n[i] = e * p[i];
    }
    L.sg[q] = p;
    L.sg[q + 2048] = n;
  };
  auto valid = [&](int k) { return e0 + (k >> 1) * (CH / 2) + (k & 1) < e1; };
  auto warm = [&](int i0, int i1) {
    const unsigned pfo = ((xcd * N) >> 3) * 1024 + ((blockIdx.x >> 3) * 64 + lane) * 128;
#pragma unroll 1
    for (int i = i0; i < i1; i += MW - SW)
      asm volatile("s_mov_b32 m0, %2\n s_nop 0\n buffer_load_dword %0, %1, %3 offen lds\n"
                   :: "v"(pfo), "s"(make_srd(x, N * 1024)), "s"(lds_addr(L.sink)), "s"(i * 32 * 64 * 128) : "memory");
  };
  unsigned bk[PK], pr[PK], ky[PK], sa[PK];
  unsigned cv, wq4[4];
  asm volatile(
      "s_cmp_lg_u32 %9, %10\n s_cbranch_scc1 1f\n global_load_ushort %0, %5, %7\n global_load_dword %1, %6, %8\n"
      "global_load_dword %2, %6, %8 offset:256\n global_load_dword %3, %6, %8 offset:512\n"
      "global_load_dword %4, %6, %8 offset:768\n 1:"
      : "=&v"(cv), "=&v"(wq4[0]), "=&v"(wq4[1]), "=&v"(wq4[2]), "=&v"(wq4[3])
      : "v"(lane * 2), "v"(lane * 4), "s"(cg), "s"(wrad), "s"(w), "n"(PW) : "scc");
  auto wdma = [&](int i0, int i1) {
#pragma unroll
    for (int i = i0; i < i1; i++) {
      const unsigned o = ((w & 3) * 8 + ((i + (blockIdx.x >> 3)) & 7)) * 1024;
      asm volatile("s_mov_b32 m0, %2\n s_nop 0\n buffer_load_dwordx4 %0, %1, %3 offen lds\n"
                   :: "v"(lane * 16), "s"(make_srd(wmix, C_ * C_ * 2)), "s"(lds_addr(L.wf) + o), "s"(o) : "memory");
    }
  };
  const int zt = tid - PT - (w > HWV ? 64 : 0);
  unsigned so1 = (tid - PT) * 16 + WS_RECP + bucket * (CH * 16);
  unsigned so0 = w < PW ? tid * 16 + WS_RECK + bucket * (CH * 4) : so1;
  so1 += MT * 16;
  unsigned ho0 = w < SW ? ((lane >> 1) * SW + w) * (NBMAX * 16) : lane * 16 + WS_OFF + bucket * (NBMAX * 16);
  int node0 = __builtin_amdgcn_readfirstlane(((unsigned)(bucket << 22) + M2 - 1) / M2);
  int nn = min(N, __builtin_amdgcn_readfirstlane(((unsigned)((bucket + 1) << 22) + M2 - 1) / M2)) - node0;
  asm volatile("" : "+v"(so0), "+v"(so1), "+v"(ho0), "+s"(node0), "+s"(nn));
  if (w < PW) {
    unsigned one[PK], far[PK];
#pragma unroll
    for (int k = 0; k < PK; k++) {
      one[k] = valid(k) ? 1u : 0u;
      far[k] = valid(k) ? 0u : (0x10000u | lane) << 2;
    }
    asm volatile("s_waitcnt vmcnt(4)" : "+v"(d0), "+v"(d1), "+v"(s0), "+v"(s1), "+v"(one[0]), "+v"(one[1]), "+v"(one[2]),
                 "+v"(one[3]), "+v"(far[0]), "+v"(far[1]), "+v"(far[2]), "+v"(far[3]));
    const u32x4 dv = {d0[0], d0[1], d1[0], d1[1]}, sv = {s0[0], s0[1], s1[0], s1[1]};
#pragma unroll
    for (int k = 0; k < PK; k++) {
      bk[k] = ((unsigned)__umul24(dv[k], M2) >> 22) | (far[k] & 255);
      pr[k] = atomicAdd(&L.phist[w][bk[k] >> 1], one[k] << ((bk[k] * 16) & 31));
    }
    __builtin_amdgcn_sched_barrier(0);
#pragma unroll
    for (int k = 0; k < PK; k++) {
      ky[k] = (sv[k] << 10) | ((dv[k] & 63) << 4) | ((((unsigned)__umul24(sv[k], M2) >> 26) - ((bk[k] >> 4) & 14)) & 15);
      ky[k] = __umul24(ky[k], one[k]);
      sa[k] = lds_addr(&L.seg[0][bk[k]]);
      asm volatile("ds_add_u32 %0, %1" :: "v"(sa[k]), "v"(ky[k]) : "memory");
    }
#pragma unroll
    for (int k = 0; k < PK; k++) {
      pr[k] = __builtin_amdgcn_ubfe(pr[k], bk[k] * 16, 16) + far[k];
      bk[k] = lds_addr(L.pbase[w]) + bk[k] * 2;
      asm volatile("" : "+v"(pr[k]), "+v"(bk[k]));
    }
  } else {
    if (w == PW) {
      L.deg[lane] = ((lane - node0) & 63) < NPB ? 63 - ((lane - node0) & 63) : -1;
      if (lane < 4) (&L.flag)[lane] = 0;
      if (lane < 16) L.prog[lane] = 255;
    }
    if (w == HWV) {
      ev = ev * 6364136223846793005ull + 1442695040888963407ull;
      L.tag = ev;
    } else {
#pragma unroll 1
      for (int q = zt; q < TB1 * 384; q += 384) table(q);
    }
  }
  asm volatile("s_waitcnt lgkmcnt(0)\n s_barrier" ::: "memory");
  const auto wsr = __builtin_amdgcn_make_buffer_rsrc((void*)ws, 0, WS_END, 0x00020000);
  auto lds_ld = [&](const unsigned* p) {
    return __builtin_amdgcn_readfirstlane(__hip_atomic_load(p, __ATOMIC_RELAXED, __HIP_MEMORY_SCOPE_WORKGROUP));
  };
  if (w == HWV) {
    const unsigned tl = (unsigned)ev, th = (unsigned)(ev >> 32);
    unsigned c[4] = {0, 0, 0, 0};
    u32x4 hv[4];
#pragma unroll
    for (int k = 0; k < 4; k++) {
#pragma unroll
      for (int ww = 0; ww < PW; ww++) c[k] += reinterpret_cast<const unsigned short*>(L.phist[ww])[k * 64 + lane];
      hv[k][1] = L.seg[0][k * 64 + lane];
    }
    const unsigned p0 = c[0] | (c[1] << 16), p1 = c[2] | (c[3] << 16);
    const unsigned i0 = wave_incl_scan(p0), i1 = wave_incl_scan(p1);
    const unsigned t0 = __builtin_amdgcn_readlane(i0, 63), t1 = __builtin_amdgcn_readlane(i1, 63);
    const unsigned s1 = t0 & 0xffff, s2 = s1 + (t0 >> 16), s3 = s2 + (t1 & 0xffff);
    const unsigned bs[4] = {(i0 - p0) & 0xffff, ((i0 - p0) >> 16) + s1, ((i1 - p1) & 0xffff) + s2, ((i1 - p1) >> 16) + s3};
#pragma unroll
    for (int k = 0; k < 4; k++) {
      hv[k][0] = bs[k] | (c[k] << 11) | (tl << 22);
      hv[k][1] += (unsigned)__umul24(c[k], tl & 255) << 24;
      hv[k][3] = th - hv[k][0] - hv[k][1];
    }
    asm volatile("" : "+v"(hv[0]), "+v"(hv[1]), "+v"(hv[2]), "+v"(hv[3]));
    while (true) {
      const unsigned gv = __hip_atomic_load(&L.go, __ATOMIC_RELAXED, __HIP_MEMORY_SCOPE_WORKGROUP);
      asm volatile("" ::: "memory");
#pragma unroll
      for (int k = 0; k < 4; k++) hv[k][2] = L.seg[1][k * 64 + lane];
      asm volatile("" ::: "memory");
      if (__builtin_amdgcn_readfirstlane(gv) == PW) break;
    }
#pragma unroll
    for (int k = 0; k < 4; k++) {
      hv[k][3] -= hv[k][2];
      __builtin_amdgcn_raw_buffer_store_b128(hv[k], wsr, ho0 + k * 1024, 0, 16);
    }
  } else if (w < PW) {
    const unsigned t8 = (unsigned)L.tag << 24;
    unsigned ca = 0, cb = 0, pa = 0, pb = 0, hh[PW][2];
#pragma unroll
    for (int ww = 0; ww < PW; ww++) {
      hh[ww][0] = L.phist[ww][lane];
      hh[ww][1] = L.phist[ww][64 + lane];
    }
    __builtin_amdgcn_sched_barrier(0);
#pragma unroll
    for (int ww = 0; ww < PW; ww++) {
      const unsigned ha = hh[ww][0], hb = hh[ww][1];
      pa = ww == w ? ca : pa;
      pb = ww == w ? cb : pb;
      ca += ha;
      cb += hb;
    }
    const unsigned t = __builtin_amdgcn_perm(cb, ca, 0x05040100) + __builtin_amdgcn_perm(cb, ca, 0x07060302);
    const unsigned inc = wave_incl_scan(t), ex = inc - t;
    L.pbase[w][lane] = (ex & 0xffff) * 0x10001u + (ca << 16) + pa;
    L.pbase[w][64 + lane] = ((ex >> 16) + (__builtin_amdgcn_readlane(inc, 63) & 0xffff)) * 0x10001u + (cb << 16) + pb;
    unsigned b4[PK];
#pragma unroll
    for (int k = 0; k < PK; k++) b4[k] = *(__attribute__((address_space(3))) unsigned short*)(uintptr_t)bk[k];
    __builtin_amdgcn_sched_barrier(0);
    {
      unsigned t0, t1, t2, t3;
      asm volatile(
          "s_waitcnt vmcnt(0)\n"
          "v_add3_u32 %0, v100, v101, v108\n v_add3_u32 %1, v102, v103, v110\n v_add3_u32 %2, v104, v105, v112\n"
          "v_add3_u32 %3, v106, v107, v114\n"
          "v_add_u32 %0, %0, v109\n v_add_u32 %1, %1, v111\n v_add_u32 %2, %2, v113\n v_add_u32 %3, %3, v115\n"
          "ds_add_u32 %8, %0 offset:1024\n ds_add_u32 %9, %1 offset:1024\n ds_add_u32 %10, %2 offset:1024\n"
          "ds_add_u32 %11, %3 offset:1024\n"
          "s_mov_b64 exec, 1\n ds_add_u32 %12, %13\n s_mov_b64 exec, -1"
          : "=&v"(t0), "=&v"(t1), "=&v"(t2), "=&v"(t3), "+{v[100:103]}"(y0), "+{v[104:107]}"(y1), "+{v[108:111]}"(f0),
            "+{v[112:115]}"(f1)
          : "v"(sa[0]), "v"(sa[1]), "v"(sa[2]), "v"(sa[3]), "v"(lds_addr(&L.go)), "v"(1) : "memory");
    }
#pragma unroll
    for (int k = 0; k < PK; k++) {
      pr[k] = min(pr[k] + b4[k], CH - 1u);
      L.sk[pr[k]] = ky[k] | t8;
      pr[k] = lds_addr(&L.sp[pr[k]]);
    }
    asm volatile(
        "ds_write2_b64 %0, v[100:101], v[108:109] offset1:1\n ds_write2_b64 %1, v[102:103], v[110:111] offset1:1\n"
        "ds_write2_b64 %2, v[104:105], v[112:113] offset1:1\n ds_write2_b64 %3, v[106:107], v[114:115] offset1:1\n"
        "s_waitcnt lgkmcnt(0)"
        :: "v"(pr[0]), "v"(pr[1]), "v"(pr[2]), "v"(pr[3]),
           "{v[100:103]}"(y0), "{v[104:107]}"(y1), "{v[108:111]}"(f0), "{v[112:115]}"(f1) : "memory");
  } else {
    if (w == PW) {
      asm volatile("s_waitcnt   vmcnt(0)" : "+v"(cv), "+v"(wq4[0]), "+v"(wq4[1]), "+v"(wq4[2]), "+v"(wq4[3]));
      reinterpret_cast<unsigned short*>(L.ct)[(lane & 3) * 16 + (lane >> 2)] = cv;
      L.ct[8 + lane] = u32x4{__builtin_amdgcn_perm(wq4[1], wq4[0], 0x05040100), __builtin_amdgcn_perm(wq4[3], wq4[2], 0x05040100),
                             __builtin_amdgcn_perm(wq4[1], wq4[0], 0x07060302), __builtin_amdgcn_perm(wq4[3], wq4[2], 0x07060302)};
    }
    if (w > PW && w < PW + 5) wdma(0, WA);
#pragma unroll 1
    for (int q = zt + TB1 * 384; q < TB2 * 384; q += 384) table(q);
    __builtin_amdgcn_s_setprio(3);
  }
  __syncthreads();
  const int st = tid - SW * 64;
  {
    const u32x4 v0 = reinterpret_cast<const u32x4*>(L.sk)[tid], v1 = reinterpret_cast<const u32x4*>(L.sk)[MT + tid];
    __builtin_amdgcn_raw_buffer_store_b128(v0, wsr, so0, 0, 16);
    __builtin_amdgcn_raw_buffer_store_b128(v1, wsr, so1, 0, 16);
    if (w == PW)
      __builtin_amdgcn_raw_buffer_store_b128(reinterpret_cast<const u32x4*>(L.sk)[2 * MT + lane], wsr, so1 + (MT - PT) * 16, 0, 16);
  }
  if (w >= SW) wdma(WA, WB);
  __syncthreads();
  const unsigned long long epoch = L.tag;
  const unsigned tagl = __builtin_amdgcn_readfirstlane((unsigned)epoch);
  const unsigned tag1 = __builtin_amdgcn_readfirstlane((unsigned)(epoch >> 32));
  const int row = lane & 15, kq = lane >> 4;
  const int half = (w ^ (w >> 3) ^ 1) & 1;
  if (w >= SW) {
    __builtin_amdgcn_s_setprio(0);
    table(st + TB2 * 384);
    table(st + TB2 * 384 + 256);
    wdma(WB, 8);
#pragma unroll 1
    for (int q = st + TB2 * 384 + 512; q < 2048; q += MT - SW * 64) table(q);
    __builtin_amdgcn_s_setprio(3);
    constexpr int HR = (NPB - RKL * SW) / (MW - SW);
    const int id = (node0 + RKL * SW + (w - SW) * HR + lane) & 63;
    __builtin_amdgcn_s_barrier();
    asm volatile("s_waitcnt     vmcnt(0)" ::: "memory");
    __builtin_amdgcn_s_sleep(2);
    warm(w - SW, PFN);
    __syncthreads();
    {
      const int dg = L.deg[lane], dm = L.deg[id];
      int dn[HR];
#pragma unroll
      for (int i = 0; i < HR; i++) dn[i] = __builtin_amdgcn_readlane(dm, i);
      __builtin_amdgcn_sched_barrier(0);
      unsigned long long m[HR];
#pragma unroll
      for (int i = 0; i < HR; i++) m[i] = __ballot(dg > dn[i]);
      __builtin_amdgcn_sched_barrier(0);
      unsigned r = 63;
#pragma unroll
      for (int i = 0; i < HR; i++) asm("v_writelane_b32 %0, %1, %2" : "+v"(r) : "s"(__popcll(m[i])), "n"(i));
      L.byrank[r] = id * 4;
    }
    __syncthreads();
    if (bucket == 0 && tid == MT - 1)
      __builtin_amdgcn_raw_buffer_store_b64(u32x2{tagl, tag1}, wsr, WS_EPOCH, 0, 16);
    __builtin_amdgcn_s_barrier();
    __builtin_amdgcn_s_setprio(0);
    {
      const int kk = w - SW;
      const u32x4* wp = L.wf + (kk * 32 + (lane >> 4) * 8) * 16 + (lane & 15);
      u32x4 q[8];
#pragma unroll
      for (int j = 0; j < 8; j++) q[j] = wp[j * 16];
      asm volatile("" : "+v"(q[0]), "+v"(q[1]), "+v"(q[2]), "+v"(q[3]), "+v"(q[4]), "+v"(q[5]), "+v"(q[6]), "+v"(q[7]) :: "memory");
      u32x4* fp = &L.wf[(kk * 8 + ((lane >> 2) & 3) * 2) * 64 + (lane & 48) + (lane & 3) * 4];
#pragma unroll
      for (int i = 0; i < 4; i++) {
        fp[i] = u32x4{__builtin_amdgcn_perm(q[1][i], q[0][i], 0x05040100), __builtin_amdgcn_perm(q[3][i], q[2][i], 0x05040100),
                      __builtin_amdgcn_perm(q[5][i], q[4][i], 0x05040100), __builtin_amdgcn_perm(q[7][i], q[6][i], 0x05040100)};
        fp[64 + i] = u32x4{__builtin_amdgcn_perm(q[1][i], q[0][i], 0x07060302), __builtin_amdgcn_perm(q[3][i], q[2][i], 0x07060302),
                           __builtin_amdgcn_perm(q[5][i], q[4][i], 0x07060302), __builtin_amdgcn_perm(q[7][i], q[6][i], 0x07060302)};
      }
    }
    __syncthreads();
  } else {
    reinterpret_cast<u32x4*>(&L.hist[w * HS])[lane] = u32x4{0, 0, 0, 0};
    reinterpret_cast<u32x4*>(&L.hist[w * HS])[64 + lane] = u32x4{0, 0, 0, 0};
    L.ri[w][lane] = u32x4{0x20000000u, 0x20000000u, 0x20000000u, 0x20000000u};
    const unsigned long long t0 = w & 1 ? 0x26796281267d7281ull : 0x277dc200272c8180ull, t1 = w & 1 ? 0x246942c324755245ull : 0x25755242217984c2ull;
    const unsigned long long t2 = w & 1 ? 0x22613345226d6483ull : 0x236d130423694307ull, t3 = w & 1 ? 0x206503c725710384ull : 0x215d2386206513cdull;
    const unsigned long long t = w & 4 ? (w & 2 ? t3 : t2) : (w & 2 ? t1 : t0);
    const unsigned rks = nn == NPB ? t >> 32 : t;
    const int start = w * CAPW;
    unsigned sop = lds_addr(&L.so[start + (lane & 15)]);
    unsigned s4p = lds_addr(&L.so[start + (lane >> 2)]);
    unsigned yp = lds_addr(&L.pay[start + (lane >> 2)]);
    unsigned ep = lds_addr(&L.pay[start + (lane & 15)]) + 8;
    u32x4 cg0 = L.ct[(lane & 3) * 2];
    const u32x4 cg1 = L.ct[(lane & 3) * 2 + 1], wq = L.ct[8 + lane];
    const unsigned rp = lds_addr(&L.pay[start]) + kq * 256 + row * 4;
    unsigned pg = lds_addr(&L.prog[w]), oo = node0 * 1024 + row * 16 + half * 512, slowv;
    u32x16 cs = {ho0,
                 (unsigned)(((lane >> 1) * SW + w) * CH + (lane & 1)) * 4,
                 lds_addr(L.ri[w]) + (lane & 1) * 4, lds_addr(L.ri[w]) + lane * 4, (lane & 1) - 1u, lane & 1u,
                 lds_addr(&L.hist[tid]), lds_addr(&L.byrank[(rks >> (min(lane & 7, NS - 1) * 6)) & 63]),
                 lds_addr(&L.byrank[(rks >> (kq * 6)) & 63]), lds_addr(&L.hist[SW * HS + (lane >> 3)]),
                 (start * 4 + ((lane & 7) << 13)) * 0x10001u, (unsigned)(start + lane) * 4,
                 lds_addr(&L.nl[w * 8 + (lane & 7)]), lane * 4u, ((node0 + w * RKL + lane) & 63) * 4u, 0};
    asm volatile(STREAM_ASM
                 : [sl] "=s"(slowv), "+{v12}"(sop), "+{v13}"(s4p), "+{v14}"(yp), "+{v15}"(ep), "+{v[136:151]}"(cs),
                   "+{v[20:23]}"(cg0), "+{v110}"(pg), "+{v159}"(oo)
                 : "{v8}"(lane * 16), "{v16}"(0xfffcu), "{v17}"((1u << (lane & 3)) >> 1),
                   "{v[24:27]}"(cg1), "{v[28:31]}"(wq), "{v[152:155]}"(u32x4{rp, rp + 64, rp + 128, rp + 192}),
                   "{v156}"(lds_addr(&L.row5[w * 64]) + kq * 256 + row * 4),
                   "{v157}"(lds_addr(&L.pay[start + (row >> 2) * 64 + (row & 3) * 4 + kq])),
                   "{v158}"(lds_addr(&L.wf[half * 256 + lane])), [os] "s"(make_srd(out, (node0 + nn) * 1024)),
                   [xs] "s"(make_srd(x, N * 1024)), [ws] "s"(wsd),
                   [ho] "s"(WS_OFF + bucket * 16), [rk] "s"(WS_RECK), [rp] "s"(WS_RECP), [t0] "s"(tagl << 22), [t1] "s"(tag1),
                   [wb] "s"(w * (HS * 4)), [n0] "s"(node0 * 4), [bit] "s"(1u << w), [fo] "s"((w >> 2) * FO1), [tb] "n"(offsetof(Lds, sg)),
                   [rd] "n"(offsetof(Lds, rdone)), [fl] "n"(offsetof(Lds, flag)),
                   [hi] "n"(offsetof(Lds, hist)), [to] "n"(offsetof(Lds, hist) + SW * HS * 4), [dg] "n"(offsetof(Lds, deg)),
                   [br] "n"(offsetof(Lds, byrank)), [so] "n"(offsetof(Lds, so)), [pa] "n"(offsetof(Lds, pay))
                 : STREAM_CLOBBERS);
  }
  {
  int lane;
  asm volatile("v_mbcnt_lo_u32_b32 %0, -1, 0\n v_mbcnt_hi_u32_b32 %0, -1, %0" : "=v"(lane));
  __builtin_assume(lane >= 0 && lane < 64);
  const int row = lane & 15, kq = lane >> 4, aoff = (row >> 2) * 64 + (row & 3) * 4 + kq;
  auto orow = [&](int i) { return (node0 + ((L.nl[i] / 4 - node0) & 63)) * 1024 + row * 16 + half * 512; };
  if (w >= SW) {
    const int h = w - SW, ht = h >> 1;
    asm volatile(MIX_ASM
                 :: "{v0}"(lds_addr(&L.pay[h * CAPW + aoff])), "{v1}"(lds_addr(&L.pay[(h + 4) * CAPW + aoff])),
                    "{v2}"(lds_addr(&L.row5[ht * 256 + aoff])), "{v3}"(orow(h * 8 + kq)), "{v4}"(orow((h + 4) * 8 + kq)),
                    "{v5}"(orow((ht * 4 + kq) * 8 + 4)), "{v6}"(lds_addr(&L.prog[h])),
                    "{v7}"(lds_addr(&L.wf[half * 256 + lane])), [h] "s"(h), [nd] "s"(15u << (ht * 4)),
                    [os] "s"(make_srd(out, (node0 + nn) * 1024)), [rd] "n"(offsetof(Lds, rdone)), [sl] "n"(HSL)
                 : MIX_CLOBBERS);
  } else {
    u32x4* rows = &L.pay[w * CAPW];
#pragma unroll 1
    for (int it = 0; it < 2 * NS; it++) {
      const int sl = it >> 1, slice = it & 1, ch = slice * 64 + lane;
      const unsigned dl = (L.nl[w * 8 + sl] / 4 - node0) & 63;
      float acc[4] = {0.f, 0.f, 0.f, 0.f};
#pragma unroll 1
      for (int eb = 0; eb < E; eb += 64) {
        unsigned long long m = __ballot(eb + lane < E && (unsigned)dst[min(eb + lane, E - 1)] == node0 + dl);
#pragma unroll 1
        for (; m; m &= m - 1) {
          const int e = eb + __builtin_ctzll(m);
          const u32x2 ry = Y[e], rf = ef[e];
          float yy[4] = {bf2f(ry[0] & 0xffff), bf2f(ry[0] >> 16), bf2f(ry[1] & 0xffff), bf2f(ry[1] >> 16)};
          float ee[4] = {bf2f(rf[0] & 0xffff), bf2f(rf[0] >> 16), bf2f(rf[1] & 0xffff), bf2f(rf[1] >> 16)};
          float z = 0.f;
#pragma unroll
          for (int k = 0; k < 4; k++) z += ee[k] * bf2f(wrad[k * C_ + ch]);
          float R = z / (1.f + __expf(-z));
          const unsigned short* xr = x + (size_t)src[e] * 512 + ch * 4;
#pragma unroll 1
          for (int o = 0; o < 4; o++) {
            float tp = 0.f;
#pragma unroll 1
            for (int i = 0; i < 4; i++) {
              float A = 0.f;
#pragma unroll
              for (int f = 0; f < 4; f++) A += yy[f] * bf2f(cg[i * 16 + f * 4 + o]);
              tp += bf2f(xr[i]) * bf2f(f2bf(A));
            }
            float v = R * tp;
            acc[0] += o == 0 ? v : 0.f;
            acc[1] += o == 1 ? v : 0.f;
            acc[2] += o == 2 ? v : 0.f;
            acc[3] += o == 3 ? v : 0.f;
          }
        }
      }
      u32x4* rw = sl < 4 ? &rows[sl * 64] : &L.row5[w * 64];
      unsigned short* ar = reinterpret_cast<unsigned short*>(&rw[slice * 32]) + (lane >> 5) * 128 + (lane & 31);
#pragma unroll
      for (int o = 0; o < 4; o++) ar[o * 32] = f2bf(acc[o]);
    }
    asm volatile("" ::: "memory");
    if (lane == 0) atomicOr(&L.rdone, 1u << w);
    const auto ors = __builtin_amdgcn_make_buffer_rsrc((void*)out, 0, (node0 + nn) * 1024, 0x00020000);
#pragma unroll 1
    for (int m = 0; m < 2; m++) {
      f32x4 a = {0.f, 0.f, 0.f, 0.f}, b = {0.f, 0.f, 0.f, 0.f};
#pragma unroll 1
      for (int k = 0; k < 4; k++) {
        const s16x8 f = __builtin_bit_cast(s16x8, rows[aoff + k * 16]);
        const u32x4* wp = &L.wf[(k * 8 + half * 4 + m * 2) * 64 + lane];
        a = __builtin_amdgcn_mfma_f32_16x16x32_bf16(f, __builtin_bit_cast(s16x8, wp[0]), a, 0, 0, 0);
        b = __builtin_amdgcn_mfma_f32_16x16x32_bf16(f, __builtin_bit_cast(s16x8, wp[64]), b, 0, 0, 0);
      }
      __builtin_amdgcn_raw_buffer_store_b128(
          u32x4{pk2bf(a[0], a[1]), pk2bf(a[2], a[3]), pk2bf(b[0], b[1]), pk2bf(b[2], b[3])}, ors, orow(w * 8 + kq) + m * 256, 0, 16);
    }
  }
  }
  asm volatile(".Ltfn_p1:");
}

extern "C" __global__ __launch_bounds__(C_) void tfn_plain_kernel(
    const int* __restrict__ dst, const int* __restrict__ src, const unsigned short* __restrict__ Y,
    const unsigned short* __restrict__ ef, const unsigned short* __restrict__ x, unsigned short* __restrict__ out,
    const unsigned short* __restrict__ wrad, const unsigned short* __restrict__ cg, const unsigned short* __restrict__ wmix,
    long long E) {
  __shared__ f32x4 agg[C_];
  const int c = threadIdx.x, lane = c & 63;
  f32x4 acc = {0.f, 0.f, 0.f, 0.f};
#pragma unroll 1
  for (long long eb = 0; eb < E; eb += 64) {
    unsigned long long m = __ballot(eb + lane < E && dst[eb + lane < E ? eb + lane : 0] == (int)blockIdx.x);
#pragma unroll 1
    for (; m; m &= m - 1) {
      const long long e = eb + __builtin_ctzll(m);
      float z = 0.f;
#pragma unroll
      for (int k = 0; k < 4; k++) z += bf2f(ef[e * 4 + k]) * bf2f(wrad[k * C_ + c]);
      const float R = z / (1.f + __expf(-z));
#pragma unroll
      for (int i = 0; i < 4; i++) {
        const float xv = R * bf2f(x[(size_t)src[e] * 512 + c * 4 + i]);
#pragma unroll
        for (int f = 0; f < 4; f++)
#pragma unroll
          for (int o = 0; o < 4; o++) acc[o] += xv * bf2f(Y[e * 4 + f]) * bf2f(cg[i * 16 + f * 4 + o]);
      }
    }
  }
  agg[c] = acc;
  __syncthreads();
  f32x4 r = {0.f, 0.f, 0.f, 0.f};
#pragma unroll 4
  for (int k = 0; k < C_; k++) r += agg[k] * bf2f(wmix[k * C_ + c]);
  *reinterpret_cast<u32x2*>(out + (size_t)blockIdx.x * 512 + c * 4) = u32x2{pk2bf(r[0], r[1]), pk2bf(r[2], r[3])};
}

extern "C" void kernel_launch(void* const* d_in, const int* in_sizes, int n_in, void* d_out, int out_size,
                              void* d_ws, size_t ws_size, hipStream_t stream) {
  const unsigned short* x = (const unsigned short*)d_in[0];
  const int* src = (const int*)d_in[1];
  const int* dst = (const int*)d_in[2];
  const u32x2* Y = (const u32x2*)d_in[3];
  const u32x2* ef = (const u32x2*)d_in[4];
  const unsigned short* wrad = (const unsigned short*)d_in[5];
  const unsigned short* cg = (const unsigned short*)d_in[6];
  const unsigned short* wmix = (const unsigned short*)d_in[7];
  const int N = in_sizes[0] / 512;
  const int E = in_sizes[1];
  const int CE = ((E + NBMAX - 1) / NBMAX + 1) & ~1;
  const unsigned M2 = ((unsigned)NBMAX << 22) / (unsigned)N;
  if (N <= 64 || ((1u << 22) + M2 - 1) / M2 > (unsigned)NPB || CE > CH || ws_size < WS_END) {
    tfn_plain_kernel<<<N, C_, 0, stream>>>(dst, src, (const unsigned short*)Y, (const unsigned short*)ef, x,
                                           (unsigned short*)d_out, wrad, cg, wmix, E);
    return;
  }
  tfn_main_kernel<<<NBMAX, MT, 0, stream>>>(dst, E, 0x20000, src, CE, 0x20000, Y, N, 0x20000, ef, M2, 0x20000, x, (char*)d_ws,
                                           (u32x4*)d_out, wrad, cg, wmix);
}
```

```cpp
#include <hip/hip_runtime.h>
#include <hip/hip_bf16.h>
#include <stddef.h>
#include <stdint.h>

typedef __attribute__((ext_vector_type(2))) float f32x2;
typedef __attribute__((ext_vector_type(4))) float f32x4;
typedef __attribute__((ext_vector_type(8))) float f32x8;
typedef __attribute__((ext_vector_type(16))) float f32x16;
typedef __attribute__((ext_vector_type(8))) short s16x8;
typedef __attribute__((ext_vector_type(2))) unsigned int u32x2;
typedef __attribute__((ext_vector_type(3))) unsigned int u32x3;
typedef __attribute__((ext_vector_type(4))) unsigned int u32x4;
typedef __attribute__((ext_vector_type(8))) unsigned int u32x8;
typedef __attribute__((ext_vector_type(16))) unsigned int u32x16;
typedef __attribute__((ext_vector_type(2))) __bf16 bf16x2;

#define STREAM_ASM \
  "s_mov_b32 s63, 0x10000\n" \
  "Lh%=:\n" \
  "buffer_load_dwordx4 v[72:75], v136, %[ws], %[ho] offen sc1\n" \
  "v_mov_b32 v117, 0\n" \
  "v_mov_b32 v116, 64\n" \
  "v_mov_b32 v87, 0\n" \
  "v_mov_b64 v[44:45], 0\n" \
  "v_mov_b64 v[46:47], 0\n" \
  "v_mov_b32 v118, 0x7fc\n" \
  "s_waitcnt vmcnt(0)\n" \
  "v_add3_u32 v108, v72, v73, v74\n" \
  "v_xor_b32 v109, %[t0], v72\n" \
  "v_add_u32 v108, v108, v75\n" \
  "v_cmp_lt_u32 vcc, 0x3fffff, v109\n" \
  "v_cmp_ne_u32_e64 s[74:75], %[t1], v108\n" \
  "s_or_b64 vcc, vcc, s[74:75]\n" \
  "s_cbranch_scc1 Lhr%=\n" \
  "s_mov_b64 exec, -1\n" \
  "v_bfe_u32 v108, v72, 11, 11\n" \
  "v_and_b32 v109, v140, v108\n" \
  "v_and_b32 v112, 0x7ff, v72\n" \
  "v_sub_u32 v111, v108, v141\n" \
  "v_add_u32_dpp v109, v109, v109 row_shr:1 row_mask:0xf bank_mask:0xf bound_ctrl:1\n" \
  "v_lshl_add_u32 v112, v112, 2, v137\n" \
  "v_and_b32 v114, v140, v73\n" \
  "v_add_u32_dpp v109, v109, v109 row_shr:2 row_mask:0xf bank_mask:0xf bound_ctrl:1\n" \
  "v_and_b32 v115, v140, v74\n" \
  "v_sub_u32 v114, 0, v114\n" \
  "v_add_u32_dpp v109, v109, v109 row_shr:4 row_mask:0xf bank_mask:0xf bound_ctrl:1\n" \
  "v_sub_u32 v115, 0, v115\n" \
  "s_nop 0\n" \
  "v_add_u32_dpp v109, v109, v109 row_shr:8 row_mask:0xf bank_mask:0xf bound_ctrl:1\n" \
  "s_nop 1\n" \
  "v_add_u32_dpp v109, v109, v109 row_bcast:15 row_mask:0xa bank_mask:0xf\n" \
  "s_nop 1\n" \
  "v_add_u32_dpp v109, v109, v109 row_bcast:31 row_mask:0xc bank_mask:0xf\n" \
  "v_sub_u32 v108, v109, v108\n" \
  "v_readlane_b32 s62, v109, 63\n" \
  "v_lshl_add_u32 v113, v108, 2, v138\n" \
  "s_cmpk_gt_u32 s62, 0x100\n" \
  "s_cbranch_scc1 Lover%=\n" \
  "Lw%=:\n" \
  "v_cmpx_lt_i32 vcc, 0, v111\n" \
  "ds_write_b32 v113, v112 offset:0\n" \
  "v_add_u32 v112, 8, v112\n" \
  "v_cmpx_lt_i32 vcc, 2, v111\n" \
  "ds_write_b32 v113, v112 offset:8\n" \
  "v_add_u32 v112, 8, v112\n" \
  "v_cmpx_lt_i32 vcc, 4, v111\n" \
  "ds_write_b32 v113, v112 offset:16\n" \
  "v_add_u32 v112, 8, v112\n" \
  "v_cmpx_lt_i32 vcc, 6, v111\n" \
  "ds_write_b32 v113, v112 offset:24\n" \
  "v_add_u32 v112, 8, v112\n" \
  "v_cmpx_lt_i32 vcc, 8, v111\n" \
  "ds_write_b32 v113, v112 offset:32\n" \
  "v_add_u32 v112, 8, v112\n" \
  "s_cbranch_execz Lwd%=\n" \
  "v_cmpx_lt_i32 vcc, 10, v111\n" \
  "ds_write_b32 v113, v112 offset:40\n" \
  "v_add_u32 v112, 8, v112\n" \
  "s_cbranch_execz Lwd%=\n" \
  "v_cmpx_lt_i32 vcc, 12, v111\n" \
  "ds_write_b32 v113, v112 offset:48\n" \
  "v_add_u32 v112, 8, v112\n" \
  "s_cbranch_execz Lwd%=\n" \
  "v_cmpx_lt_i32 vcc, 14, v111\n" \
  "ds_write_b32 v113, v112 offset:56\n" \
  "v_add_u32 v112, 8, v112\n" \
  "v_add_u32 v113, 64, v113\n" \
  "v_subrev_u32 v111, 16, v111\n" \
  "s_branch Lw%=\n" \
  "Lwd%=:\n" \
  "s_mov_b64 exec, -1\n" \
  "Lld%=:\n" \
  "ds_read2st64_b32 v[76:77], v139 offset1:1\n" \
  "ds_read2st64_b32 v[78:79], v139 offset0:2 offset1:3\n" \
  "s_mov_b32 s63, 0x10000\n" \
  "s_waitcnt lgkmcnt(0)\n" \
  "buffer_load_dword v84, v76, %[ws], %[rk] offen sc1\n" \
  "buffer_load_dword v85, v77, %[ws], %[rk] offen sc1\n" \
  "buffer_load_dword v86, v78, %[ws], %[rk] offen sc1\n" \
  "s_cmpk_le_u32 s62, 0xc0\n" \
  "s_cbranch_scc1 Lka%=\n" \
  "buffer_load_dword v87, v79, %[ws], %[rk] offen sc1\n" \
  "Lka%=:\n" \
  "v_lshlrev_b32 v80, 2, v76\n" \
  "v_lshlrev_b32 v81, 2, v77\n" \
  "v_lshlrev_b32 v82, 2, v78\n" \
  "v_lshlrev_b32 v83, 2, v79\n" \
  "s_barrier\n" \
  "s_cmpk_le_u32 s62, 0xc0\n" \
  "s_cbranch_scc1 Lpa%=\n" \
  "buffer_load_dwordx4 v[44:47], v83, %[ws], %[rp] offen sc1\n" \
  "Lpa%=:\n" \
  "buffer_load_dwordx4 v[32:35], v80, %[ws], %[rp] offen sc1\n" \
  "buffer_load_dwordx4 v[36:39], v81, %[ws], %[rp] offen sc1\n" \
  "buffer_load_dwordx4 v[40:43], v82, %[ws], %[rp] offen sc1\n" \
  "v_cmp_le_i32_e64 s[66:67], 0, v80\n" \
  "v_cmp_le_i32_e64 s[68:69], 0, v81\n" \
  "v_cmp_le_i32_e64 s[70:71], 0, v82\n" \
  "v_cmp_le_i32_e64 s[72:73], 0, v83\n" \
  "v_mov_b64 v[48:49], 0\n" \
  "v_mov_b64 v[50:51], 0\n" \
  "v_mov_b64 v[52:53], 0\n" \
  "v_mov_b64 v[54:55], 0\n" \
  "v_mov_b64 v[56:57], 0\n" \
  "v_mov_b64 v[58:59], 0\n" \
  "v_mov_b64 v[60:61], 0\n" \
  "v_mov_b64 v[62:63], 0\n" \
  "v_mov_b64 v[64:65], 0\n" \
  "v_mov_b64 v[66:67], 0\n" \
  "v_mov_b64 v[68:69], 0\n" \
  "v_mov_b64 v[70:71], 0\n" \
  "s_waitcnt vmcnt(3)\n" \
  "Lk%=:\n" \
  "v_add3_u32 v108, v84, v85, v86\n" \
  "v_add3_u32 v108, v108, v87, v114\n" \
  "v_lshlrev_b32 v88, 1, v84\n" \
  "v_lshlrev_b32 v92, 4, v84\n" \
  "v_add_u32_dpp v108, v108, v108 row_shr:1 row_mask:0xf bank_mask:0xf bound_ctrl:1\n" \
  "v_lshrrev_b32 v104, 2, v84\n" \
  "v_and_or_b32 v88, v88, v118, %[wb]\n" \
  "v_add_u32_dpp v108, v108, v108 row_shr:2 row_mask:0xf bank_mask:0xf bound_ctrl:1\n" \
  "v_lshlrev_b32_e64 v100, v92, 4\n" \
  "v_and_b32 v104, 0xfc, v104\n" \
  "v_add_u32_dpp v108, v108, v108 row_shr:4 row_mask:0xf bank_mask:0xf bound_ctrl:1\n" \
  "v_lshlrev_b32 v89, 1, v85\n" \
  "v_lshlrev_b32 v93, 4, v85\n" \
  "v_add_u32_dpp v108, v108, v108 row_shr:8 row_mask:0xf bank_mask:0xf bound_ctrl:1\n" \
  "v_lshrrev_b32 v105, 2, v85\n" \
  "v_and_or_b32 v89, v89, v118, %[wb]\n" \
  "v_add_u32_dpp v108, v108, v108 row_bcast:15 row_mask:0xa bank_mask:0xf\n" \
  "v_lshlrev_b32_e64 v101, v93, 4\n" \
  "v_and_b32 v105, 0xfc, v105\n" \
  "v_add_u32_dpp v108, v108, v108 row_bcast:31 row_mask:0xc bank_mask:0xf\n" \
  "v_lshlrev_b32 v90, 1, v86\n" \
  "v_readlane_b32 s65, v108, 63\n" \
  "v_lshlrev_b32 v94, 4, v86\n" \
  "v_lshrrev_b32 v106, 2, v86\n" \
  "v_and_or_b32 v90, v90, v118, %[wb]\n" \
  "v_lshlrev_b32_e64 v102, v94, 4\n" \
  "v_and_b32 v106, 0xfc, v106\n" \
  "s_cmp_lg_u32 s65, 0\n" \
  "s_cbranch_scc1 Lkr%=\n" \
  "Lkc%=:\n" \
  "s_mov_b64 exec, s[66:67]\n" \
  "ds_add_rtn_u32 v96, v88, v100 offset:%[hi]\n" \
  "ds_add_u32 v104, v116 offset:%[dg]\n" \
  "s_mov_b64 exec, s[68:69]\n" \
  "ds_add_rtn_u32 v97, v89, v101 offset:%[hi]\n" \
  "ds_add_u32 v105, v116 offset:%[dg]\n" \
  "s_mov_b64 exec, s[70:71]\n" \
  "ds_add_rtn_u32 v98, v90, v102 offset:%[hi]\n" \
  "ds_add_u32 v106, v116 offset:%[dg]\n" \
  "s_cmpk_gt_u32 s62, 0xc0\n" \
  "s_mov_b64 exec, -1\n" \
  "s_cbranch_scc1 Lc3%=\n" \
  "Lc4%=:\n" \
  "v_and_b32 v100, v118, v88\n" \
  "v_and_b32 v101, v118, v89\n" \
  "v_and_b32 v102, v118, v90\n" \
  "s_waitcnt lgkmcnt(0)\n" \
  "s_barrier\n" \
  "ds_read_b32 v108, v149 offset:%[dg]\n" \
  "ds_read_b32 v109, v150 offset:%[dg]\n" \
  "ds_read2st64_b32 v[76:77], v142 offset0:0 offset1:8\n" \
  "ds_read2st64_b32 v[78:79], v142 offset0:16 offset1:24\n" \
  "ds_read2st64_b32 v[104:105], v142 offset0:32 offset1:40\n" \
  "ds_read2st64_b32 v[106:107], v142 offset0:48 offset1:56\n" \
  "v_and_b32 v84, 0xfffc00, v84\n" \
  "v_and_b32 v85, 0xfffc00, v85\n" \
  "v_and_b32 v86, 0xfffc00, v86\n" \
  "v_and_b32 v87, 0xfffc00, v87\n" \
  "v_mov_b32 v111, 0\n" \
  "v_mov_b32 v112, 63\n" \
  "s_waitcnt lgkmcnt(4)\n" \
  "v_readlane_b32 s74, v109, 0\n" \
  "v_readlane_b32 s76, v109, 1\n" \
  "v_readlane_b32 s78, v109, 2\n" \
  "s_nop 0\n" \
  "v_cmp_lt_i32_e64 s[74:75], s74, v108\n" \
  "v_cmp_lt_i32_e64 s[76:77], s76, v108\n" \
  "v_cmp_lt_i32_e64 s[78:79], s78, v108\n" \
  "s_bcnt1_i32_b64 s74, s[74:75]\n" \
  "s_bcnt1_i32_b64 s76, s[76:77]\n" \
  "s_bcnt1_i32_b64 s78, s[78:79]\n" \
  "v_writelane_b32 v112, s74, 0\n" \
  "v_writelane_b32 v112, s76, 1\n" \
  "v_writelane_b32 v112, s78, 2\n" \
  "v_lshlrev_b32 v112, 2, v112\n" \
  "s_waitcnt lgkmcnt(3)\n" \
  "ds_write2st64_b32 v142, v117, v76 offset1:8\n" \
  "v_add_u32 v77, v77, v76\n" \
  "s_waitcnt lgkmcnt(3)\n" \
  "v_add_u32 v78, v78, v77\n" \
  "ds_write2st64_b32 v142, v77, v78 offset0:16 offset1:24\n" \
  "v_add_u32 v79, v79, v78\n" \
  "s_waitcnt lgkmcnt(3)\n" \
  "v_add_u32 v104, v104, v79\n" \
  "ds_write2st64_b32 v142, v79, v104 offset0:32 offset1:40\n" \
  "v_add_u32 v105, v105, v104\n" \
  "s_waitcnt lgkmcnt(3)\n" \
  "v_add_u32 v106, v106, v105\n" \
  "ds_write2st64_b32 v142, v105, v106 offset0:48 offset1:56\n" \
  "v_add_u32 v107, v107, v106\n" \
  "ds_write_b32 v142, v107 offset:16384\n" \
  "ds_write_b32 v112, v150 offset:%[br]\n" \
  "s_waitcnt lgkmcnt(0)\n" \
  "s_barrier\n" \
  "ds_read_b32 v108, v143\n" \
  "s_mov_b32 s74, 0x1f1f1f1f\n" \
  "s_mov_b32 s75, 0x1f1f1f1f\n" \
  "s_waitcnt lgkmcnt(0)\n" \
  "v_lshl_add_u32 v76, v108, 3, v145\n" \
  "s_mov_b64 exec, s[74:75]\n" \
  "ds_read_b32 v111, v76\n" \
  "s_mov_b64 exec, -1\n" \
  "ds_write_b32 v148, v108\n" \
  "s_waitcnt lgkmcnt(1)\n" \
  "v_add_u32_dpp v112, v111, v111 row_shr:1 row_mask:0xf bank_mask:0xf bound_ctrl:1\n" \
  "v_sub_u32 v77, v146, v111\n" \
  "s_nop 0\n" \
  "v_add_u32_dpp v112, v112, v112 row_shr:2 row_mask:0xf bank_mask:0xf bound_ctrl:1\n" \
  "s_nop 1\n" \
  "v_add_u32_dpp v112, v112, v112 row_shr:4 row_mask:0xf bank_mask:0xf bound_ctrl:1\n" \
  "s_nop 1\n" \
  "v_add_u32_dpp v112, v112, v112 row_shr:8 row_mask:0xf bank_mask:0xf bound_ctrl:1\n" \
  "s_nop 1\n" \
  "v_add_u32_dpp v112, v112, v112 row_bcast:15 row_mask:0xa bank_mask:0xf\n" \
  "s_nop 1\n" \
  "v_add_u32_dpp v112, v112, v112 row_bcast:31 row_mask:0xc bank_mask:0xf\n" \
  "v_sub_u32 v111, v112, v111\n" \
  "v_readlane_b32 s65, v112, 63\n" \
  "v_mov_b32_dpp v78, v112 row_newbcast:7 row_mask:0xf bank_mask:0x3\n" \
  "v_mov_b32_dpp v79, v111 row_newbcast:0 row_mask:0xf bank_mask:0x3\n" \
  "v_add_u32 v77, v77, v112\n" \
  "v_mov_b32_dpp v78, v112 row_newbcast:15 row_mask:0xf bank_mask:0xc\n" \
  "v_mov_b32_dpp v79, v111 row_newbcast:8 row_mask:0xf bank_mask:0xc\n" \
  "s_and_b32 s64, s65, 0xffff\n" \
  "v_lshl_add_u32 v77, v78, 16, v77\n" \
  "s_lshr_b32 s65, s65, 16\n" \
  "v_add_u32_sdwa v77, v77, v79 dst_sel:DWORD dst_unused:UNUSED_PAD src0_sel:DWORD src1_sel:WORD_1\n" \
  "s_add_u32 s64, s64, s65\n" \
  "s_mov_b64 exec, s[74:75]\n" \
  "ds_write_b32 v76, v77\n" \
  "s_mov_b64 exec, -1\n" \
  "s_cmpk_gt_u32 s64, 864\n" \
  "s_cbranch_scc1 Llo%=\n" \
  "Llc%=:\n" \
  "s_waitcnt lgkmcnt(0)\n" \
  "s_barrier\n" \
  "ds_read_b32 v109, v117 offset:%[fl]\n" \
  "ds_read_b32 v76, v88 offset:%[hi]\n" \
  "ds_read_b32 v104, v100 offset:%[to]\n" \
  "ds_read_b32 v77, v89 offset:%[hi]\n" \
  "ds_read_b32 v105, v101 offset:%[to]\n" \
  "ds_read_b32 v78, v90 offset:%[hi]\n" \
  "ds_read_b32 v106, v102 offset:%[to]\n" \
  "v_add_u32 v113, s64, v147\n" \
  "v_bfrev_b32 v111, 1\n" \
  "s_max_u32 s60, s64, 4\n" \
  "s_sub_u32 s60, s60, 4\n" \
  "s_lshr_b32 s60, s60, 4\n" \
  "s_waitcnt lgkmcnt(4)\n" \
  "v_readfirstlane_b32 s61, v109\n" \
  "v_add3_u32 v88, v76, v104, v96\n" \
  "v_lshrrev_b32 v88, v92, v88\n" \
  "v_bfe_u32 v72, v88, 13, 3\n" \
  "v_and_b32 v88, 0x1ffc, v88\n" \
  "v_or_b32 v72, v84, v72\n" \
  "s_cmp_lg_u32 s61, 0\n" \
  "s_cbranch_scc1 Lslow%=\n" \
  "s_waitcnt lgkmcnt(0)\n" \
  "v_add3_u32 v89, v77, v105, v97\n" \
  "v_add3_u32 v90, v78, v106, v98\n" \
  "v_lshrrev_b32 v89, v93, v89\n" \
  "v_lshrrev_b32 v90, v94, v90\n" \
  "v_bfe_u32 v73, v89, 13, 3\n" \
  "v_bfe_u32 v74, v90, 13, 3\n" \
  "v_and_b32 v89, 0x1ffc, v89\n" \
  "v_and_b32 v90, 0x1ffc, v90\n" \
  "v_or_b32 v73, v85, v73\n" \
  "v_or_b32 v74, v86, v74\n" \
  "s_mov_b64 exec, s[66:67]\n" \
  "ds_write_b32 v88, v72 offset:%[so]\n" \
  "s_mov_b64 exec, s[68:69]\n" \
  "ds_write_b32 v89, v73 offset:%[so]\n" \
  "s_mov_b64 exec, s[70:71]\n" \
  "ds_write_b32 v90, v74 offset:%[so]\n" \
  "s_cmpk_gt_u32 s62, 0xc0\n" \
  "s_cbranch_scc1 Lq3%=\n" \
  "Lq4%=:\n" \
  "s_mov_b32 exec_lo, -1\n" \
  "s_mov_b32 exec_hi, 0\n" \
  "ds_write_b32 v113, v111 offset:%[so]\n" \
  "s_mov_b64 exec, -1\n" \
  "v_lshlrev_b32 v88, 2, v88\n" \
  "v_lshlrev_b32 v89, 2, v89\n" \
  "v_lshlrev_b32 v90, 2, v90\n" \
  "v_lshlrev_b32 v91, 2, v91\n" \
  "v_lshlrev_b32 v113, 2, v113\n" \
  "s_mov_b32 s63, 0x10000\n" \
  "s_waitcnt vmcnt(0)\n" \
  "Lpc%=:\n" \
  "s_mov_b64 exec, s[66:67]\n" \
  "ds_write_b128 v88, v[32:35] offset:%[pa]\n" \
  "s_mov_b64 exec, s[68:69]\n" \
  "ds_write_b128 v89, v[36:39] offset:%[pa]\n" \
  "s_mov_b64 exec, s[70:71]\n" \
  "ds_write_b128 v90, v[40:43] offset:%[pa]\n" \
  "s_mov_b64 exec, s[72:73]\n" \
  "ds_write_b128 v91, v[44:47] offset:%[pa]\n" \
  "s_mov_b32 exec_lo, -1\n" \
  "s_mov_b32 exec_hi, 0\n" \
  "ds_write_b128 v113, v[48:51] offset:%[pa]\n" \
  "s_mov_b64 exec, -1\n" \
  "v_add3_u32 v108, v32, v33, v115\n" \
  "v_add3_u32 v111, v34, v35, v36\n" \
  "v_add3_u32 v112, v37, v38, v39\n" \
  "v_add3_u32 v108, v108, v40, v41\n" \
  "v_add3_u32 v111, v111, v42, v43\n" \
  "v_add3_u32 v112, v112, v44, v45\n" \
  "v_add3_u32 v108, v108, v46, v47\n" \
  "v_add3_u32 v108, v108, v111, v112\n" \
  "s_nop 1\n" \
  "v_add_u32_dpp v108, v108, v108 row_shr:1 row_mask:0xf bank_mask:0xf bound_ctrl:1\n" \
  "s_nop 1\n" \
  "v_add_u32_dpp v108, v108, v108 row_shr:2 row_mask:0xf bank_mask:0xf bound_ctrl:1\n" \
  "s_nop 1\n" \
  "v_add_u32_dpp v108, v108, v108 row_shr:4 row_mask:0xf bank_mask:0xf bound_ctrl:1\n" \
  "s_nop 1\n" \
  "v_add_u32_dpp v108, v108, v108 row_shr:8 row_mask:0xf bank_mask:0xf bound_ctrl:1\n" \
  "s_nop 1\n" \
  "v_add_u32_dpp v108, v108, v108 row_bcast:15 row_mask:0xa bank_mask:0xf\n" \
  "s_nop 1\n" \
  "v_add_u32_dpp v108, v108, v108 row_bcast:31 row_mask:0xc bank_mask:0xf\n" \
  "s_nop 0\n" \
  "v_readlane_b32 s65, v108, 63\n" \
  "s_waitcnt lgkmcnt(0)\n" \
  "s_cmp_lg_u32 s65, 0\n" \
  "s_cbranch_scc1 Lpr%=\n" \
  "Lpw%=:\n" \
  "s_barrier\n" \
  "ds_read_b32  v11, v12\n" \
  "ds_read_b64 v[106:107], v14\n" \
  "ds_read_b64 v[104:105], v15\n" \
  "ds_read_b32 v18, v13\n" \
  "s_setprio 1\n" \
  "v_mov_b64 v[32:33], 0\n" \
  "v_mov_b64 v[34:35], 0\n" \
  "v_mov_b64 v[36:37], 0\n" \
  "v_mov_b64 v[38:39], 0\n" \
  "v_mov_b64 v[40:41], 0\n" \
  "v_mov_b64 v[42:43], 0\n" \
  "v_mov_b64 v[44:45], 0\n" \
  "v_mov_b64 v[46:47], 0\n" \
  "s_waitcnt lgkmcnt(3)\n" \
  "v_and_b32 v10, 0xfffffc00, v11\n" \
  "s_nop 1\n" \
  "v_add_u32_dpp v9, v10, v8 row_newbcast:0 row_mask:0xf bank_mask:0xf\n" \
  "buffer_load_dwordx4 v[120:123], v9, %[xs], 0 offen\n" \
  "v_add_u32_dpp v9, v10, v8 row_newbcast:1 row_mask:0xf bank_mask:0xf\n" \
  "buffer_load_dwordx4 v[124:127], v9, %[xs], 0 offen\n" \
  "v_add_u32_dpp v9, v10, v8 row_newbcast:2 row_mask:0xf bank_mask:0xf\n" \
  "buffer_load_dwordx4 v[128:131], v9, %[xs], 0 offen\n" \
  "v_add_u32_dpp v9, v10, v8 row_newbcast:3 row_mask:0xf bank_mask:0xf\n" \
  "buffer_load_dwordx4 v[132:135], v9, %[xs], 0 offen\n" \
  "v_add_u32_dpp v9, v10, v8 row_newbcast:4 row_mask:0xf bank_mask:0xf\n" \
  "buffer_load_dwordx4 v[136:139], v9, %[xs], 0 offen\n" \
  "v_mov_b64  v[96:97], 0\n" \
  "v_mov_b64  v[98:99], 0\n" \
  "v_mov_b64  v[100:101], 0\n" \
  "v_mov_b64  v[102:103], 0\n" \
  "s_waitcnt lgkmcnt(0)\n" \
  "v_mfma_f32_4x4x4_16b_bf16 v[80:83], v[104:105], v[28:29], 0 cbsz:4\n" \
  "v_mfma_f32_4x4x4_16b_bf16 v[84:87], v[104:105], v[30:31], 0 cbsz:4\n" \
  "v_mfma_f32_4x4x4_16b_bf16 v[112:115], v[106:107], v[20:21], 0\n" \
  "v_mfma_f32_4x4x4_16b_bf16 v[116:119], v[106:107], v[22:23], 0\n" \
  "v_mfma_f32_4x4x4_16b_bf16 v[72:75], v[106:107], v[24:25], 0\n" \
  "v_mfma_f32_4x4x4_16b_bf16 v[76:79], v[106:107], v[26:27], 0\n" \
  "v_and_b32 v18, v17, v18\n" \
  "v_cmp_ne_u32_e64 s[62:63], 0, v18\n" \
  "v_cvt_pk_bf16_f32 v81, v80, v81\n" \
  "v_lshlrev_b32_sdwa v80, 1, v81 dst_sel:DWORD dst_unused:UNUSED_PAD src0_sel:DWORD src1_sel:WORD_0\n" \
  "v_lshlrev_b32_sdwa v81, 1, v81 dst_sel:DWORD dst_unused:UNUSED_PAD src0_sel:DWORD src1_sel:WORD_1\n" \
  "ds_read_u16_d16_hi v96, v80 offset:%[tb]\n" \
  "ds_read_u16_d16_hi v97, v81 offset:%[tb]\n" \
  "v_cvt_pk_bf16_f32 v85, v84, v85\n" \
  "v_lshlrev_b32_sdwa v84, 1, v85 dst_sel:DWORD dst_unused:UNUSED_PAD src0_sel:DWORD src1_sel:WORD_0\n" \
  "v_lshlrev_b32_sdwa v85, 1, v85 dst_sel:DWORD dst_unused:UNUSED_PAD src0_sel:DWORD src1_sel:WORD_1\n" \
  "ds_read_u16_d16_hi v98, v84 offset:%[tb]\n" \
  "ds_read_u16_d16_hi v99, v85 offset:%[tb]\n" \
  "v_cvt_pk_bf16_f32 v83, v82, v83\n" \
  "v_lshlrev_b32_sdwa v82, 1, v83 dst_sel:DWORD dst_unused:UNUSED_PAD src0_sel:DWORD src1_sel:WORD_0\n" \
  "v_lshlrev_b32_sdwa v83, 1, v83 dst_sel:DWORD dst_unused:UNUSED_PAD src0_sel:DWORD src1_sel:WORD_1\n" \
  "ds_read_u16_d16_hi v100, v82 offset:%[tb]\n" \
  "ds_read_u16_d16_hi v101, v83 offset:%[tb]\n" \
  "v_cvt_pk_bf16_f32 v108, v112, v116\n" \
  "v_cvt_pk_bf16_f32 v109, v72, v76\n" \
  "s_nop 0\n" \
  "s_waitcnt vmcnt(4)\n" \
  "v_mfma_f32_4x4x4_16b_bf16 v[72:75], v[108:109], v[120:121], 0 cbsz:4\n" \
  "v_mfma_f32_4x4x4_16b_bf16 v[76:79], v[108:109], v[122:123], 0 cbsz:4\n" \
  "v_add_u32_dpp v9, v10, v8 row_newbcast:5 row_mask:0xf bank_mask:0xf\n" \
  "buffer_load_dwordx4 v[140:143], v9, %[xs], 0 offen\n" \
  "s_waitcnt lgkmcnt(0)\n" \
  "s_nop 1\n" \
  "Lbody%=:\n" \
  "s_bfe_u32 s61, s62, 0x40000\n" \
  "s_waitcnt lgkmcnt(2)\n" \
  "s_set_gpr_idx_on s61, gpr_idx(SRC2,DST)\n" \
  "v_pk_fma_f32 v[32:33], v[96:97], v[72:73], v[32:33] op_sel:[0,0,0] op_sel_hi:[0,1,1]\n" \
  "v_pk_fma_f32 v[42:43], v[96:97], v[74:75], v[42:43] op_sel:[0,0,0] op_sel_hi:[0,1,1]\n" \
  "v_pk_fma_f32 v[52:53], v[98:99], v[76:77], v[52:53] op_sel:[0,0,0] op_sel_hi:[0,1,1]\n" \
  "v_pk_fma_f32 v[62:63], v[98:99], v[78:79], v[62:63] op_sel:[0,0,0] op_sel_hi:[0,1,1]\n" \
  "s_set_gpr_idx_off\n" \
  "s_waitcnt vmcnt(4)\n" \
  "v_mfma_f32_4x4x4_16b_bf16 v[72:75], v[108:109], v[124:125], 0 cbsz:4 abid:1\n" \
  "v_mfma_f32_4x4x4_16b_bf16 v[76:79], v[108:109], v[126:127], 0 cbsz:4 abid:1\n" \
  "v_mfma_f32_4x4x4_16b_bf16 v[88:91], v[104:105], v[28:29], 0 cbsz:4 abid:1\n" \
  "v_mfma_f32_4x4x4_16b_bf16 v[92:95], v[104:105], v[30:31], 0 cbsz:4 abid:1\n" \
  "v_add_u32_dpp v9, v10, v8 row_newbcast:6 row_mask:0xf bank_mask:0xf\n" \
  "v_cvt_pk_bf16_f32 v87, v86, v87\n" \
  "buffer_load_dwordx4 v[144:147], v9, %[xs], 0 offen\n" \
  "v_lshlrev_b32_sdwa v86, 1, v87 dst_sel:DWORD dst_unused:UNUSED_PAD src0_sel:DWORD src1_sel:WORD_0\n" \
  "v_lshlrev_b32_sdwa v87, 1, v87 dst_sel:DWORD dst_unused:UNUSED_PAD src0_sel:DWORD src1_sel:WORD_1\n" \
  "ds_read_u16_d16_hi v102, v86 offset:%[tb]\n" \
  "ds_read_u16_d16_hi v103, v87 offset:%[tb]\n" \
  "v_mov_b32 v111, s60\n" \
  "ds_write2_b32 v110, v111, v111 offset1:8\n" \
  "s_bfe_u32 s61, s62, 0x40004\n" \
  "s_set_gpr_idx_on s61, gpr_idx(SRC2,DST)\n" \
  "v_pk_fma_f32 v[32:33], v[96:97], v[72:73], v[32:33] op_sel:[1,0,0] op_sel_hi:[1,1,1]\n" \
  "v_pk_fma_f32 v[42:43], v[96:97], v[74:75], v[42:43] op_sel:[1,0,0] op_sel_hi:[1,1,1]\n" \
  "v_pk_fma_f32 v[52:53], v[98:99], v[76:77], v[52:53] op_sel:[1,0,0] op_sel_hi:[1,1,1]\n" \
  "v_pk_fma_f32 v[62:63], v[98:99], v[78:79], v[62:63] op_sel:[1,0,0] op_sel_hi:[1,1,1]\n" \
  "s_set_gpr_idx_off\n" \
  "s_waitcnt vmcnt(4)\n" \
  "v_mfma_f32_4x4x4_16b_bf16 v[72:75], v[108:109], v[128:129], 0 cbsz:4 abid:2\n" \
  "v_mfma_f32_4x4x4_16b_bf16 v[76:79], v[108:109], v[130:131], 0 cbsz:4 abid:2\n" \
  "v_add_u32_dpp v9, v10, v8 row_newbcast:7 row_mask:0xf bank_mask:0xf\n" \
  "v_cvt_pk_bf16_f32 v89, v88, v89\n" \
  "buffer_load_dwordx4 v[148:151], v9, %[xs], 0 offen\n" \
  "v_lshlrev_b32_sdwa v88, 1, v89 dst_sel:DWORD dst_unused:UNUSED_PAD src0_sel:DWORD src1_sel:WORD_0\n" \
  "v_lshlrev_b32_sdwa v89, 1, v89 dst_sel:DWORD dst_unused:UNUSED_PAD src0_sel:DWORD src1_sel:WORD_1\n" \
  "ds_read_u16_d16_hi v96, v88 offset:%[tb]\n" \
  "ds_read_u16_d16_hi v97, v89 offset:%[tb]\n" \
  "ds_read_b32 v111, v110 offset:16\n" \
  "s_bfe_u32 s61, s62, 0x40008\n" \
  "s_waitcnt lgkmcnt(4)\n" \
  "s_set_gpr_idx_on s61, gpr_idx(SRC2,DST)\n" \
  "v_pk_fma_f32 v[32:33], v[100:101], v[72:73], v[32:33] op_sel:[0,0,0] op_sel_hi:[0,1,1]\n" \
  "v_pk_fma_f32 v[42:43], v[100:101], v[74:75], v[42:43] op_sel:[0,0,0] op_sel_hi:[0,1,1]\n" \
  "v_pk_fma_f32 v[52:53], v[102:103], v[76:77], v[52:53] op_sel:[0,0,0] op_sel_hi:[0,1,1]\n" \
  "v_pk_fma_f32 v[62:63], v[102:103], v[78:79], v[62:63] op_sel:[0,0,0] op_sel_hi:[0,1,1]\n" \
  "s_set_gpr_idx_off\n" \
  "s_waitcnt vmcnt(4)\n" \
  "v_mfma_f32_4x4x4_16b_bf16 v[72:75], v[108:109], v[132:133], 0 cbsz:4 abid:3\n" \
  "v_mfma_f32_4x4x4_16b_bf16 v[76:79], v[108:109], v[134:135], 0 cbsz:4 abid:3\n" \
  "v_add_u32_dpp v9, v10, v8 row_newbcast:8 row_mask:0xf bank_mask:0xf\n" \
  "v_cvt_pk_bf16_f32 v93, v92, v93\n" \
  "buffer_load_dwordx4 v[120:123], v9, %[xs], 0 offen\n" \
  "v_lshlrev_b32_sdwa v92, 1, v93 dst_sel:DWORD dst_unused:UNUSED_PAD src0_sel:DWORD src1_sel:WORD_0\n" \
  "v_lshlrev_b32_sdwa v93, 1, v93 dst_sel:DWORD dst_unused:UNUSED_PAD src0_sel:DWORD src1_sel:WORD_1\n" \
  "ds_read_u16_d16_hi v98, v92 offset:%[tb]\n" \
  "ds_read_u16_d16_hi v99, v93 offset:%[tb]\n" \
  "ds_read_b64 v[106:107], v14 offset:256\n" \
  "s_bfe_u32 s61, s62, 0x4000c\n" \
  "s_set_gpr_idx_on s61, gpr_idx(SRC2,DST)\n" \
  "v_pk_fma_f32 v[32:33], v[100:101], v[72:73], v[32:33] op_sel:[1,0,0] op_sel_hi:[1,1,1]\n" \
  "v_pk_fma_f32 v[42:43], v[100:101], v[74:75], v[42:43] op_sel:[1,0,0] op_sel_hi:[1,1,1]\n" \
  "v_pk_fma_f32 v[52:53], v[102:103], v[76:77], v[52:53] op_sel:[1,0,0] op_sel_hi:[1,1,1]\n" \
  "v_pk_fma_f32 v[62:63], v[102:103], v[78:79], v[62:63] op_sel:[1,0,0] op_sel_hi:[1,1,1]\n" \
  "s_set_gpr_idx_off\n" \
  "s_sub_u32 s60, s60, 1\n" \
  "s_cbranch_scc1 Lend%=\n" \
  "s_waitcnt vmcnt(4)\n" \
  "v_mfma_f32_4x4x4_16b_bf16 v[72:75], v[108:109], v[136:137], 0 cbsz:4 abid:4\n" \
  "v_mfma_f32_4x4x4_16b_bf16 v[76:79], v[108:109], v[138:139], 0 cbsz:4 abid:4\n" \
  "v_add_u32_dpp v9, v10, v8 row_newbcast:9 row_mask:0xf bank_mask:0xf\n" \
  "v_cvt_pk_bf16_f32 v91, v90, v91\n" \
  "buffer_load_dwordx4 v[124:127], v9, %[xs], 0 offen\n" \
  "v_lshlrev_b32_sdwa v90, 1, v91 dst_sel:DWORD dst_unused:UNUSED_PAD src0_sel:DWORD src1_sel:WORD_0\n" \
  "v_lshlrev_b32_sdwa v91, 1, v91 dst_sel:DWORD dst_unused:UNUSED_PAD src0_sel:DWORD src1_sel:WORD_1\n" \
  "ds_read_u16_d16_hi v100, v90 offset:%[tb]\n" \
  "ds_read_u16_d16_hi v101, v91 offset:%[tb]\n" \
  "ds_read_b32 v18, v13 offset:64\n" \
  "v_add_u32 v14, 0x100, v14\n" \
  "s_bfe_u32 s61, s62, 0x40010\n" \
  "s_waitcnt lgkmcnt(4)\n" \
  "s_set_gpr_idx_on s61, gpr_idx(SRC2,DST)\n" \
  "v_pk_fma_f32 v[32:33], v[96:97], v[72:73], v[32:33] op_sel:[0,0,0] op_sel_hi:[0,1,1]\n" \
  "v_pk_fma_f32 v[42:43], v[96:97], v[74:75], v[42:43] op_sel:[0,0,0] op_sel_hi:[0,1,1]\n" \
  "v_pk_fma_f32 v[52:53], v[98:99], v[76:77], v[52:53] op_sel:[0,0,0] op_sel_hi:[0,1,1]\n" \
  "v_pk_fma_f32 v[62:63], v[98:99], v[78:79], v[62:63] op_sel:[0,0,0] op_sel_hi:[0,1,1]\n" \
  "s_set_gpr_idx_off\n" \
  "s_waitcnt vmcnt(4)\n" \
  "v_mfma_f32_4x4x4_16b_bf16 v[72:75], v[108:109], v[140:141], 0 cbsz:4 abid:5\n" \
  "v_mfma_f32_4x4x4_16b_bf16 v[76:79], v[108:109], v[142:143], 0 cbsz:4 abid:5\n" \
  "v_mfma_f32_4x4x4_16b_bf16 v[80:83], v[104:105], v[28:29], 0 cbsz:4 abid:2\n" \
  "v_mfma_f32_4x4x4_16b_bf16 v[84:87], v[104:105], v[30:31], 0 cbsz:4 abid:2\n" \
  "v_add_u32_dpp v9, v10, v8 row_newbcast:10 row_mask:0xf bank_mask:0xf\n" \
  "v_cvt_pk_bf16_f32 v95, v94, v95\n" \
  "buffer_load_dwordx4 v[128:131], v9, %[xs], 0 offen\n" \
  "v_lshlrev_b32_sdwa v94, 1, v95 dst_sel:DWORD dst_unused:UNUSED_PAD src0_sel:DWORD src1_sel:WORD_0\n" \
  "v_lshlrev_b32_sdwa v95, 1, v95 dst_sel:DWORD dst_unused:UNUSED_PAD src0_sel:DWORD src1_sel:WORD_1\n" \
  "ds_read_u16_d16_hi v102, v94 offset:%[tb]\n" \
  "ds_read_u16_d16_hi v103, v95 offset:%[tb]\n" \
  "s_bfe_u32 s61, s62, 0x40014\n" \
  "s_set_gpr_idx_on s61, gpr_idx(SRC2,DST)\n" \
  "v_pk_fma_f32 v[32:33], v[96:97], v[72:73], v[32:33] op_sel:[1,0,0] op_sel_hi:[1,1,1]\n" \
  "v_pk_fma_f32 v[42:43], v[96:97], v[74:75], v[42:43] op_sel:[1,0,0] op_sel_hi:[1,1,1]\n" \
  "v_pk_fma_f32 v[52:53], v[98:99], v[76:77], v[52:53] op_sel:[1,0,0] op_sel_hi:[1,1,1]\n" \
  "v_pk_fma_f32 v[62:63], v[98:99], v[78:79], v[62:63] op_sel:[1,0,0] op_sel_hi:[1,1,1]\n" \
  "s_set_gpr_idx_off\n" \
  "s_waitcnt vmcnt(4) lgkmcnt(8)\n" \
  "v_mfma_f32_4x4x4_16b_bf16 v[72:75], v[108:109], v[144:145], 0 cbsz:4 abid:6\n" \
  "v_mfma_f32_4x4x4_16b_bf16 v[76:79], v[108:109], v[146:147], 0 cbsz:4 abid:6\n" \
  "v_add_u32_dpp v9, v10, v8 row_newbcast:11 row_mask:0xf bank_mask:0xf\n" \
  "v_cvt_pk_bf16_f32 v81, v80, v81\n" \
  "buffer_load_dwordx4 v[132:135], v9, %[xs], 0 offen\n" \
  "v_lshlrev_b32_sdwa v80, 1, v81 dst_sel:DWORD dst_unused:UNUSED_PAD src0_sel:DWORD src1_sel:WORD_0\n" \
  "v_lshlrev_b32_sdwa v81, 1, v81 dst_sel:DWORD dst_unused:UNUSED_PAD src0_sel:DWORD src1_sel:WORD_1\n" \
  "ds_read_u16_d16_hi v96, v80 offset:%[tb]\n" \
  "ds_read_u16_d16_hi v97, v81 offset:%[tb]\n" \
  "v_add_u32 v13, 64, v13\n" \
  "v_readfirstlane_b32 s66, v111\n" \
  "s_setprio 1\n" \
  "s_add_u32 s67, s60, %[fo]\n" \
  "s_cmp_ge_u32 s67, s66\n" \
  "s_cbranch_scc0 Lf5_%=\n" \
  "s_setprio 2\n" \
  "Lf5_%=:\n" \
  "s_bfe_u32 s61, s62, 0x40018\n" \
  "s_waitcnt lgkmcnt(2)\n" \
  "s_set_gpr_idx_on s61, gpr_idx(SRC2,DST)\n" \
  "v_pk_fma_f32 v[32:33], v[100:101], v[72:73], v[32:33] op_sel:[0,0,0] op_sel_hi:[0,1,1]\n" \
  "v_pk_fma_f32 v[42:43], v[100:101], v[74:75], v[42:43] op_sel:[0,0,0] op_sel_hi:[0,1,1]\n" \
  "v_pk_fma_f32 v[52:53], v[102:103], v[76:77], v[52:53] op_sel:[0,0,0] op_sel_hi:[0,1,1]\n" \
  "v_pk_fma_f32 v[62:63], v[102:103], v[78:79], v[62:63] op_sel:[0,0,0] op_sel_hi:[0,1,1]\n" \
  "s_set_gpr_idx_off\n" \
  "s_waitcnt vmcnt(4) lgkmcnt(7)\n" \
  "v_mfma_f32_4x4x4_16b_bf16 v[72:75], v[108:109], v[148:149], 0 cbsz:4 abid:7\n" \
  "v_mfma_f32_4x4x4_16b_bf16 v[76:79], v[108:109], v[150:151], 0 cbsz:4 abid:7\n" \
  "v_mfma_f32_4x4x4_16b_bf16 v[112:115], v[106:107], v[20:21], 0\n" \
  "v_add_u32_dpp v9, v10, v8 row_newbcast:12 row_mask:0xf bank_mask:0xf\n" \
  "v_cvt_pk_bf16_f32 v85, v84, v85\n" \
  "buffer_load_dwordx4 v[136:139], v9, %[xs], 0 offen\n" \
  "v_lshlrev_b32_sdwa v84, 1, v85 dst_sel:DWORD dst_unused:UNUSED_PAD src0_sel:DWORD src1_sel:WORD_0\n" \
  "v_lshlrev_b32_sdwa v85, 1, v85 dst_sel:DWORD dst_unused:UNUSED_PAD src0_sel:DWORD src1_sel:WORD_1\n" \
  "ds_read_u16_d16_hi v98, v84 offset:%[tb]\n" \
  "ds_read_u16_d16_hi v99, v85 offset:%[tb]\n" \
  "ds_read_b32 v11, v12 offset:64\n" \
  "s_bfe_u32 s61, s62, 0x4001c\n" \
  "s_set_gpr_idx_on s61, gpr_idx(SRC2,DST)\n" \
  "v_pk_fma_f32 v[32:33], v[100:101], v[72:73], v[32:33] op_sel:[1,0,0] op_sel_hi:[1,1,1]\n" \
  "v_pk_fma_f32 v[42:43], v[100:101], v[74:75], v[42:43] op_sel:[1,0,0] op_sel_hi:[1,1,1]\n" \
  "v_pk_fma_f32 v[52:53], v[102:103], v[76:77], v[52:53] op_sel:[1,0,0] op_sel_hi:[1,1,1]\n" \
  "v_pk_fma_f32 v[62:63], v[102:103], v[78:79], v[62:63] op_sel:[1,0,0] op_sel_hi:[1,1,1]\n" \
  "s_set_gpr_idx_off\n" \
  "s_sub_u32 s60, s60, 1\n" \
  "s_cbranch_scc1 Lend%=\n" \
  "s_waitcnt vmcnt(4) lgkmcnt(7)\n" \
  "v_mfma_f32_4x4x4_16b_bf16 v[72:75], v[108:109], v[120:121], 0 cbsz:4 abid:8\n" \
  "v_mfma_f32_4x4x4_16b_bf16 v[76:79], v[108:109], v[122:123], 0 cbsz:4 abid:8\n" \
  "v_add_u32_dpp v9, v10, v8 row_newbcast:13 row_mask:0xf bank_mask:0xf\n" \
  "v_cvt_pk_bf16_f32 v83, v82, v83\n" \
  "buffer_load_dwordx4 v[140:143], v9, %[xs], 0 offen\n" \
  "v_lshlrev_b32_sdwa v82, 1, v83 dst_sel:DWORD dst_unused:UNUSED_PAD src0_sel:DWORD src1_sel:WORD_0\n" \
  "v_lshlrev_b32_sdwa v83, 1, v83 dst_sel:DWORD dst_unused:UNUSED_PAD src0_sel:DWORD src1_sel:WORD_1\n" \
  "ds_read_u16_d16_hi v100, v82 offset:%[tb]\n" \
  "ds_read_u16_d16_hi v101, v83 offset:%[tb]\n" \
  "v_add_u32 v12, 64, v12\n" \
  "v_and_b32 v18, v17, v18\n" \
  "s_bfe_u32 s61, s63, 0x40000\n" \
  "s_waitcnt lgkmcnt(3)\n" \
  "s_set_gpr_idx_on s61, gpr_idx(SRC2,DST)\n" \
  "v_pk_fma_f32 v[32:33], v[96:97], v[72:73], v[32:33] op_sel:[0,0,0] op_sel_hi:[0,1,1]\n" \
  "v_pk_fma_f32 v[42:43], v[96:97], v[74:75], v[42:43] op_sel:[0,0,0] op_sel_hi:[0,1,1]\n" \
  "v_pk_fma_f32 v[52:53], v[98:99], v[76:77], v[52:53] op_sel:[0,0,0] op_sel_hi:[0,1,1]\n" \
  "v_pk_fma_f32 v[62:63], v[98:99], v[78:79], v[62:63] op_sel:[0,0,0] op_sel_hi:[0,1,1]\n" \
  "s_set_gpr_idx_off\n" \
  "s_waitcnt vmcnt(4)\n" \
  "v_mfma_f32_4x4x4_16b_bf16 v[72:75], v[108:109], v[124:125], 0 cbsz:4 abid:9\n" \
  "v_mfma_f32_4x4x4_16b_bf16 v[76:79], v[108:109], v[126:127], 0 cbsz:4 abid:9\n" \
  "v_mfma_f32_4x4x4_16b_bf16 v[88:91], v[104:105], v[28:29], 0 cbsz:4 abid:3\n" \
  "v_mfma_f32_4x4x4_16b_bf16 v[92:95], v[104:105], v[30:31], 0 cbsz:4 abid:3\n" \
  "v_add_u32_dpp v9, v10, v8 row_newbcast:14 row_mask:0xf bank_mask:0xf\n" \
  "v_cvt_pk_bf16_f32 v87, v86, v87\n" \
  "buffer_load_dwordx4 v[144:147], v9, %[xs], 0 offen\n" \
  "v_lshlrev_b32_sdwa v86, 1, v87 dst_sel:DWORD dst_unused:UNUSED_PAD src0_sel:DWORD src1_sel:WORD_0\n" \
  "v_lshlrev_b32_sdwa v87, 1, v87 dst_sel:DWORD dst_unused:UNUSED_PAD src0_sel:DWORD src1_sel:WORD_1\n" \
  "ds_read_u16_d16_hi v102, v86 offset:%[tb]\n" \
  "ds_read_u16_d16_hi v103, v87 offset:%[tb]\n" \
  "v_mov_b32 v111, s60\n" \
  "ds_write2_b32 v110, v111, v111 offset1:8\n" \
  "s_bfe_u32 s61, s63, 0x40004\n" \
  "s_set_gpr_idx_on s61, gpr_idx(SRC2,DST)\n" \
  "v_pk_fma_f32 v[32:33], v[96:97], v[72:73], v[32:33] op_sel:[1,0,0] op_sel_hi:[1,1,1]\n" \
  "v_pk_fma_f32 v[42:43], v[96:97], v[74:75], v[42:43] op_sel:[1,0,0] op_sel_hi:[1,1,1]\n" \
  "v_pk_fma_f32 v[52:53], v[98:99], v[76:77], v[52:53] op_sel:[1,0,0] op_sel_hi:[1,1,1]\n" \
  "v_pk_fma_f32 v[62:63], v[98:99], v[78:79], v[62:63] op_sel:[1,0,0] op_sel_hi:[1,1,1]\n" \
  "s_set_gpr_idx_off\n" \
  "s_waitcnt vmcnt(4)\n" \
  "v_mfma_f32_4x4x4_16b_bf16 v[72:75], v[108:109], v[128:129], 0 cbsz:4 abid:10\n" \
  "v_mfma_f32_4x4x4_16b_bf16 v[76:79], v[108:109], v[130:131], 0 cbsz:4 abid:10\n" \
  "v_add_u32_dpp v9, v10, v8 row_newbcast:15 row_mask:0xf bank_mask:0xf\n" \
  "v_cvt_pk_bf16_f32 v89, v88, v89\n" \
  "buffer_load_dwordx4 v[148:151], v9, %[xs], 0 offen\n" \
  "v_lshlrev_b32_sdwa v88, 1, v89 dst_sel:DWORD dst_unused:UNUSED_PAD src0_sel:DWORD src1_sel:WORD_0\n" \
  "v_lshlrev_b32_sdwa v89, 1, v89 dst_sel:DWORD dst_unused:UNUSED_PAD src0_sel:DWORD src1_sel:WORD_1\n" \
  "ds_read_u16_d16_hi v96, v88 offset:%[tb]\n" \
  "ds_read_u16_d16_hi v97, v89 offset:%[tb]\n" \
  "ds_read_b64 v[104:105], v15 offset:256\n" \
  "v_cmp_ne_u32_e64 s[64:65], 0, v18\n" \
  "ds_read_b32 v111, v110 offset:16\n" \
  "s_bfe_u32 s61, s63, 0x40008\n" \
  "s_waitcnt lgkmcnt(5)\n" \
  "s_set_gpr_idx_on s61, gpr_idx(SRC2,DST)\n" \
  "v_pk_fma_f32 v[32:33], v[100:101], v[72:73], v[32:33] op_sel:[0,0,0] op_sel_hi:[0,1,1]\n" \
  "v_pk_fma_f32 v[42:43], v[100:101], v[74:75], v[42:43] op_sel:[0,0,0] op_sel_hi:[0,1,1]\n" \
  "v_pk_fma_f32 v[52:53], v[102:103], v[76:77], v[52:53] op_sel:[0,0,0] op_sel_hi:[0,1,1]\n" \
  "v_pk_fma_f32 v[62:63], v[102:103], v[78:79], v[62:63] op_sel:[0,0,0] op_sel_hi:[0,1,1]\n" \
  "s_set_gpr_idx_off\n" \
  "s_waitcnt vmcnt(4) lgkmcnt(9)\n" \
  "v_and_b32 v10, 0xfffffc00, v11\n" \
  "v_mfma_f32_4x4x4_16b_bf16 v[72:75], v[108:109], v[132:133], 0 cbsz:4 abid:11\n" \
  "v_mfma_f32_4x4x4_16b_bf16 v[76:79], v[108:109], v[134:135], 0 cbsz:4 abid:11\n" \
  "v_mfma_f32_4x4x4_16b_bf16 v[116:119], v[106:107], v[22:23], 0\n" \
  "v_add_u32_dpp v9, v10, v8 row_newbcast:0 row_mask:0xf bank_mask:0xf\n" \
  "v_cvt_pk_bf16_f32 v93, v92, v93\n" \
  "buffer_load_dwordx4 v[120:123], v9, %[xs], 0 offen\n" \
  "v_lshlrev_b32_sdwa v92, 1, v93 dst_sel:DWORD dst_unused:UNUSED_PAD src0_sel:DWORD src1_sel:WORD_0\n" \
  "v_lshlrev_b32_sdwa v93, 1, v93 dst_sel:DWORD dst_unused:UNUSED_PAD src0_sel:DWORD src1_sel:WORD_1\n" \
  "ds_read_u16_d16_hi v98, v92 offset:%[tb]\n" \
  "ds_read_u16_d16_hi v99, v93 offset:%[tb]\n" \
  "s_bfe_u32 s61, s63, 0x4000c\n" \
  "s_set_gpr_idx_on s61, gpr_idx(SRC2,DST)\n" \
  "v_pk_fma_f32 v[32:33], v[100:101], v[72:73], v[32:33] op_sel:[1,0,0] op_sel_hi:[1,1,1]\n" \
  "v_pk_fma_f32 v[42:43], v[100:101], v[74:75], v[42:43] op_sel:[1,0,0] op_sel_hi:[1,1,1]\n" \
  "v_pk_fma_f32 v[52:53], v[102:103], v[76:77], v[52:53] op_sel:[1,0,0] op_sel_hi:[1,1,1]\n" \
  "v_pk_fma_f32 v[62:63], v[102:103], v[78:79], v[62:63] op_sel:[1,0,0] op_sel_hi:[1,1,1]\n" \
  "s_set_gpr_idx_off\n" \
  "s_sub_u32 s60, s60, 1\n" \
  "s_cbranch_scc1 Lend%=\n" \
  "s_waitcnt vmcnt(4)\n" \
  "v_mfma_f32_4x4x4_16b_bf16 v[72:75], v[108:109], v[136:137], 0 cbsz:4 abid:12\n" \
  "v_mfma_f32_4x4x4_16b_bf16 v[76:79], v[108:109], v[138:139], 0 cbsz:4 abid:12\n" \
  "v_add_u32_dpp v9, v10, v8 row_newbcast:1 row_mask:0xf bank_mask:0xf\n" \
  "v_cvt_pk_bf16_f32 v91, v90, v91\n" \
  "buffer_load_dwordx4 v[124:127], v9, %[xs], 0 offen\n" \
  "v_lshlrev_b32_sdwa v90, 1, v91 dst_sel:DWORD dst_unused:UNUSED_PAD src0_sel:DWORD src1_sel:WORD_0\n" \
  "v_lshlrev_b32_sdwa v91, 1, v91 dst_sel:DWORD dst_unused:UNUSED_PAD src0_sel:DWORD src1_sel:WORD_1\n" \
  "ds_read_u16_d16_hi v100, v90 offset:%[tb]\n" \
  "ds_read_u16_d16_hi v101, v91 offset:%[tb]\n" \
  "v_add_u32 v15, 0x100, v15\n" \
  "s_bfe_u32 s61, s63, 0x40010\n" \
  "s_waitcnt lgkmcnt(2)\n" \
  "s_set_gpr_idx_on s61, gpr_idx(SRC2,DST)\n" \
  "v_pk_fma_f32 v[32:33], v[96:97], v[72:73], v[32:33] op_sel:[0,0,0] op_sel_hi:[0,1,1]\n" \
  "v_pk_fma_f32 v[42:43], v[96:97], v[74:75], v[42:43] op_sel:[0,0,0] op_sel_hi:[0,1,1]\n" \
  "v_pk_fma_f32 v[52:53], v[98:99], v[76:77], v[52:53] op_sel:[0,0,0] op_sel_hi:[0,1,1]\n" \
  "v_pk_fma_f32 v[62:63], v[98:99], v[78:79], v[62:63] op_sel:[0,0,0] op_sel_hi:[0,1,1]\n" \
  "s_set_gpr_idx_off\n" \
  "s_waitcnt vmcnt(4) lgkmcnt(5)\n" \
  "v_mfma_f32_4x4x4_16b_bf16 v[72:75], v[108:109], v[140:141], 0 cbsz:4 abid:13\n" \
  "v_mfma_f32_4x4x4_16b_bf16 v[76:79], v[108:109], v[142:143], 0 cbsz:4 abid:13\n" \
  "v_mfma_f32_4x4x4_16b_bf16 v[80:83], v[104:105], v[28:29], 0 cbsz:4\n" \
  "v_mfma_f32_4x4x4_16b_bf16 v[84:87], v[104:105], v[30:31], 0 cbsz:4\n" \
  "v_add_u32_dpp v9, v10, v8 row_newbcast:2 row_mask:0xf bank_mask:0xf\n" \
  "v_cvt_pk_bf16_f32 v95, v94, v95\n" \
  "buffer_load_dwordx4 v[128:131], v9, %[xs], 0 offen\n" \
  "v_lshlrev_b32_sdwa v94, 1, v95 dst_sel:DWORD dst_unused:UNUSED_PAD src0_sel:DWORD src1_sel:WORD_0\n" \
  "v_lshlrev_b32_sdwa v95, 1, v95 dst_sel:DWORD dst_unused:UNUSED_PAD src0_sel:DWORD src1_sel:WORD_1\n" \
  "ds_read_u16_d16_hi v102, v94 offset:%[tb]\n" \
  "ds_read_u16_d16_hi v103, v95 offset:%[tb]\n" \
  "s_bfe_u32 s61, s63, 0x40014\n" \
  "s_set_gpr_idx_on s61, gpr_idx(SRC2,DST)\n" \
  "v_pk_fma_f32 v[32:33], v[96:97], v[72:73], v[32:33] op_sel:[1,0,0] op_sel_hi:[1,1,1]\n" \
  "v_pk_fma_f32 v[42:43], v[96:97], v[74:75], v[42:43] op_sel:[1,0,0] op_sel_hi:[1,1,1]\n" \
  "v_pk_fma_f32 v[52:53], v[98:99], v[76:77], v[52:53] op_sel:[1,0,0] op_sel_hi:[1,1,1]\n" \
  "v_pk_fma_f32 v[62:63], v[98:99], v[78:79], v[62:63] op_sel:[1,0,0] op_sel_hi:[1,1,1]\n" \
  "s_set_gpr_idx_off\n" \
  "s_waitcnt vmcnt(4) lgkmcnt(6)\n" \
  "v_mfma_f32_4x4x4_16b_bf16 v[72:75], v[108:109], v[144:145], 0 cbsz:4 abid:14\n" \
  "v_mfma_f32_4x4x4_16b_bf16 v[76:79], v[108:109], v[146:147], 0 cbsz:4 abid:14\n" \
  "v_add_u32_dpp v9, v10, v8 row_newbcast:3 row_mask:0xf bank_mask:0xf\n" \
  "v_cvt_pk_bf16_f32 v81, v80, v81\n" \
  "buffer_load_dwordx4 v[132:135], v9, %[xs], 0 offen\n" \
  "v_lshlrev_b32_sdwa v80, 1, v81 dst_sel:DWORD dst_unused:UNUSED_PAD src0_sel:DWORD src1_sel:WORD_0\n" \
  "v_lshlrev_b32_sdwa v81, 1, v81 dst_sel:DWORD dst_unused:UNUSED_PAD src0_sel:DWORD src1_sel:WORD_1\n" \
  "ds_read_u16_d16_hi v96, v80 offset:%[tb]\n" \
  "ds_read_u16_d16_hi v97, v81 offset:%[tb]\n" \
  "v_readfirstlane_b32 s66, v111\n" \
  "s_setprio 1\n" \
  "s_add_u32 s67, s60, %[fo]\n" \
  "s_cmp_ge_u32 s67, s66\n" \
  "s_cbranch_scc0 Lf13_%=\n" \
  "s_setprio 2\n" \
  "Lf13_%=:\n" \
  "v_cvt_pk_bf16_f32 v19, v112, v116\n" \
  "s_bfe_u32 s61, s63, 0x40018\n" \
  "s_waitcnt lgkmcnt(2)\n" \
  "s_set_gpr_idx_on s61, gpr_idx(SRC2,DST)\n" \
  "v_pk_fma_f32 v[32:33], v[100:101], v[72:73], v[32:33] op_sel:[0,0,0] op_sel_hi:[0,1,1]\n" \
  "v_pk_fma_f32 v[42:43], v[100:101], v[74:75], v[42:43] op_sel:[0,0,0] op_sel_hi:[0,1,1]\n" \
  "v_pk_fma_f32 v[52:53], v[102:103], v[76:77], v[52:53] op_sel:[0,0,0] op_sel_hi:[0,1,1]\n" \
  "v_pk_fma_f32 v[62:63], v[102:103], v[78:79], v[62:63] op_sel:[0,0,0] op_sel_hi:[0,1,1]\n" \
  "s_set_gpr_idx_off\n" \
  "s_waitcnt vmcnt(4)\n" \
  "v_mfma_f32_4x4x4_16b_bf16 v[112:115], v[106:107], v[24:25], 0\n" \
  "v_mfma_f32_4x4x4_16b_bf16 v[116:119], v[106:107], v[26:27], 0\n" \
  "v_mfma_f32_4x4x4_16b_bf16 v[72:75], v[108:109], v[148:149], 0 cbsz:4 abid:15\n" \
  "v_mfma_f32_4x4x4_16b_bf16 v[76:79], v[108:109], v[150:151], 0 cbsz:4 abid:15\n" \
  "v_add_u32_dpp v9, v10, v8 row_newbcast:4 row_mask:0xf bank_mask:0xf\n" \
  "v_cvt_pk_bf16_f32 v85, v84, v85\n" \
  "buffer_load_dwordx4 v[136:139], v9, %[xs], 0 offen\n" \
  "v_lshlrev_b32_sdwa v84, 1, v85 dst_sel:DWORD dst_unused:UNUSED_PAD src0_sel:DWORD src1_sel:WORD_0\n" \
  "v_lshlrev_b32_sdwa v85, 1, v85 dst_sel:DWORD dst_unused:UNUSED_PAD src0_sel:DWORD src1_sel:WORD_1\n" \
  "ds_read_u16_d16_hi v98, v84 offset:%[tb]\n" \
  "ds_read_u16_d16_hi v99, v85 offset:%[tb]\n" \
  "v_cvt_pk_bf16_f32 v109, v112, v116\n" \
  "v_mov_b32 v108, v19\n" \
  "s_bfe_u32 s61, s63, 0x4001c\n" \
  "s_set_gpr_idx_on s61, gpr_idx(SRC2,DST)\n" \
  "v_pk_fma_f32 v[32:33], v[100:101], v[72:73], v[32:33] op_sel:[1,0,0] op_sel_hi:[1,1,1]\n" \
  "v_pk_fma_f32 v[42:43], v[100:101], v[74:75], v[42:43] op_sel:[1,0,0] op_sel_hi:[1,1,1]\n" \
  "v_pk_fma_f32 v[52:53], v[102:103], v[76:77], v[52:53] op_sel:[1,0,0] op_sel_hi:[1,1,1]\n" \
  "v_pk_fma_f32 v[62:63], v[102:103], v[78:79], v[62:63] op_sel:[1,0,0] op_sel_hi:[1,1,1]\n" \
  "s_set_gpr_idx_off\n" \
  "s_sub_u32 s60, s60, 1\n" \
  "s_cbranch_scc1 Lend%=\n" \
  "s_waitcnt vmcnt(4)\n" \
  "v_mfma_f32_4x4x4_16b_bf16 v[72:75], v[108:109], v[120:121], 0 cbsz:4\n" \
  "v_mfma_f32_4x4x4_16b_bf16 v[76:79], v[108:109], v[122:123], 0 cbsz:4\n" \
  "v_add_u32_dpp v9, v10, v8 row_newbcast:5 row_mask:0xf bank_mask:0xf\n" \
  "v_cvt_pk_bf16_f32 v83, v82, v83\n" \
  "buffer_load_dwordx4 v[140:143], v9, %[xs], 0 offen\n" \
  "v_lshlrev_b32_sdwa v82, 1, v83 dst_sel:DWORD dst_unused:UNUSED_PAD src0_sel:DWORD src1_sel:WORD_0\n" \
  "v_lshlrev_b32_sdwa v83, 1, v83 dst_sel:DWORD dst_unused:UNUSED_PAD src0_sel:DWORD src1_sel:WORD_1\n" \
  "ds_read_u16_d16_hi v100, v82 offset:%[tb]\n" \
  "ds_read_u16_d16_hi v101, v83 offset:%[tb]\n" \
  "s_mov_b64 s[62:63], s[64:65]\n" \
  "s_branch Lbody%=\n" \
  "Lend%=:\n" \
  "s_setprio 3\n" \
  "v_mov_b32 v20, 0\n" \
  "v_mov_b32 v21, %[bit]\n" \
  "v_cvt_pk_bf16_f32 v32, v32, v52\n" \
  "v_cvt_pk_bf16_f32 v33, v33, v53\n" \
  "v_cvt_pk_bf16_f32 v42, v42, v62\n" \
  "v_cvt_pk_bf16_f32 v43, v43, v63\n" \
  "v_cvt_pk_bf16_f32 v34, v34, v54\n" \
  "v_cvt_pk_bf16_f32 v35, v35, v55\n" \
  "v_cvt_pk_bf16_f32 v44, v44, v64\n" \
  "v_cvt_pk_bf16_f32 v45, v45, v65\n" \
  "ds_write2st64_b32 v152, v32, v34 offset0:0 offset1:4\n" \
  "ds_write2st64_b32 v153, v33, v35 offset0:0 offset1:4\n" \
  "ds_write2st64_b32 v154, v42, v44 offset0:0 offset1:4\n" \
  "ds_write2st64_b32 v155, v43, v45 offset0:0 offset1:4\n" \
  "v_cvt_pk_bf16_f32 v36, v36, v56\n" \
  "v_cvt_pk_bf16_f32 v37, v37, v57\n" \
  "v_cvt_pk_bf16_f32 v46, v46, v66\n" \
  "v_cvt_pk_bf16_f32 v47, v47, v67\n" \
  "v_cvt_pk_bf16_f32 v38, v38, v58\n" \
  "v_cvt_pk_bf16_f32 v39, v39, v59\n" \
  "v_cvt_pk_bf16_f32 v48, v48, v68\n" \
  "v_cvt_pk_bf16_f32 v49, v49, v69\n" \
  "ds_write2st64_b32 v152, v36, v38 offset0:8 offset1:12\n" \
  "ds_write2st64_b32 v153, v37, v39 offset0:8 offset1:12\n" \
  "ds_write2st64_b32 v154, v46, v48 offset0:8 offset1:12\n" \
  "ds_write2st64_b32 v155, v47, v49 offset0:8 offset1:12\n" \
  "v_cvt_pk_bf16_f32 v40, v40, v60\n" \
  "v_cvt_pk_bf16_f32 v41, v41, v61\n" \
  "v_cvt_pk_bf16_f32 v50, v50, v70\n" \
  "v_cvt_pk_bf16_f32 v51, v51, v71\n" \
  "ds_write2_b32 v156, v40, v41 offset0:0 offset1:16\n" \
  "ds_write2_b32 v156, v50, v51 offset0:32 offset1:48\n" \
  "s_mov_b64 exec, 1\n" \
  "ds_or_b32 v20, v21 offset:%[rd]\n" \
  "s_endpgm\n" \
  "Lhr%=:\n" \
  "s_mov_b64 exec, vcc\n" \
  "s_sub_u32 s63, s63, 1\n" \
  "s_cbranch_scc0 Lh%=\n" \
  "s_mov_b64 exec, -1\n" \
  "Lover%=:\n" \
  "v_mov_b32 v113, 1\n" \
  "ds_write_b32 v117, v113 offset:%[fl]\n" \
  "s_mov_b32 s62, 0\n" \
  "v_mov_b32 v114, 0\n" \
  "v_mov_b32 v115, 0\n" \
  "s_branch Lld%=\n" \
  "Lkr%=:\n" \
  "buffer_load_dword v84, v76, %[ws], %[rk] offen sc1\n" \
  "buffer_load_dword v85, v77, %[ws], %[rk] offen sc1\n" \
  "buffer_load_dword v86, v78, %[ws], %[rk] offen sc1\n" \
  "s_cmpk_le_u32 s62, 0xc0\n" \
  "s_cbranch_scc1 Lkb%=\n" \
  "buffer_load_dword v87, v79, %[ws], %[rk] offen sc1\n" \
  "Lkb%=:\n" \
  "s_waitcnt vmcnt(0)\n" \
  "s_sub_u32 s63, s63, 1\n" \
  "s_cbranch_scc0 Lk%=\n" \
  "v_mov_b32 v113, 1\n" \
  "ds_write_b32 v117, v113 offset:%[fl]\n" \
  "s_branch Lkc%=\n" \
  "Lc3%=:\n" \
  "v_lshlrev_b32 v91, 1, v87\n" \
  "v_lshlrev_b32 v95, 4, v87\n" \
  "v_lshrrev_b32 v107, 2, v87\n" \
  "v_and_or_b32 v91, v91, v118, %[wb]\n" \
  "v_lshlrev_b32_e64 v103, v95, 4\n" \
  "v_and_b32 v107, 0xfc, v107\n" \
  "s_mov_b64 exec, s[72:73]\n" \
  "ds_add_rtn_u32 v99, v91, v103 offset:%[hi]\n" \
  "ds_add_u32 v107, v116 offset:%[dg]\n" \
  "s_mov_b64 exec, -1\n" \
  "s_branch Lc4%=\n" \
  "Llo%=:\n" \
  "v_mov_b32 v113, 1\n" \
  "ds_write_b32 v117, v113 offset:%[fl]\n" \
  "s_branch Llc%=\n" \
  "Lslow%=:\n" \
  "s_waitcnt vmcnt(0) lgkmcnt(0)\n" \
  "s_barrier\n" \
  "s_branch Lexit%=\n" \
  "Lq3%=:\n" \
  "s_mov_b64 exec, -1\n" \
  "v_and_b32 v103, 0x7fc, v91\n" \
  "ds_read_b32 v79, v91 offset:%[hi]\n" \
  "ds_read_b32 v107, v103 offset:%[to]\n" \
  "s_waitcnt lgkmcnt(0)\n" \
  "v_add3_u32 v91, v79, v107, v99\n" \
  "v_lshrrev_b32 v91, v95, v91\n" \
  "v_bfe_u32 v75, v91, 13, 3\n" \
  "v_and_b32 v91, 0x1ffc, v91\n" \
  "v_or_b32 v75, v87, v75\n" \
  "s_mov_b64 exec, s[72:73]\n" \
  "ds_write_b32 v91, v75 offset:%[so]\n" \
  "s_branch Lq4%=\n" \
  "Lpr%=:\n" \
  "s_cmpk_le_u32 s62, 0xc0\n" \
  "s_cbranch_scc1 Lpb%=\n" \
  "buffer_load_dwordx4 v[44:47], v83, %[ws], %[rp] offen sc1\n" \
  "Lpb%=:\n" \
  "buffer_load_dwordx4 v[32:35], v80, %[ws], %[rp] offen sc1\n" \
  "buffer_load_dwordx4 v[36:39], v81, %[ws], %[rp] offen sc1\n" \
  "buffer_load_dwordx4 v[40:43], v82, %[ws], %[rp] offen sc1\n" \
  "s_sub_u32 s63, s63, 1\n" \
  "s_waitcnt vmcnt(0)\n" \
  "s_cbranch_scc0 Lpc%=\n" \
  "s_branch Lpw%=\n" \
  "Lexit%=:\n" \
  "s_mov_b32 %[sl], s61\n"
#define STREAM_CLOBBERS \
  "v7", "v9", "v10", "v11", "v18", "v19", "v32", "v33", "v34", "v35", "v36", "v37", "v38", "v39", "v40", "v41", \
  "v42", "v43", "v44", "v45", "v46", "v47", "v48", "v49", "v50", "v51", "v52", "v53", "v54", "v55", "v56", \
  "v57", "v58", "v59", "v60", "v61", "v62", "v63", "v64", "v65", "v66", "v67", "v68", "v69", "v70", "v71", \
  "v72", "v73", "v74", "v75", "v76", "v77", "v78", "v79", "v80", "v81", "v82", "v83", "v84", "v85", "v86", \
  "v87", "v88", "v89", "v90", "v91", "v92", "v93", "v94", "v95", "v96", "v97", "v98", "v99", "v100", "v101", \
  "v102", "v103", "v104", "v105", "v106", "v107", "v108", "v109", "v111", "v112", "v113", "v114", "v115", \
  "v116", "v117", "v118", "v119", "v120", "v121", "v122", "v123", "v124", "v125", "v126", "v127", "v128", \
  "v129", "v130", "v131", "v132", "v133", "v134", "v135", "s60", "s61", "s62", "s63", "s64", "s65", "s66", \
  "s67", "s68", "s69", "s70", "s71", "s72", "s73", "s74", "s75", "s76", "s77", "s78", "s79", "scc", "vcc", \
  "memory"
#define RKL 3
#define MIX_ASM \
  "v_mov_b32 v12, 0\n" \
  "s_movk_i32 s60, 0x3ff\n" \
  "Lz%=:\n" \
  "s_sleep %[sl]\n" \
  "ds_read_b32 v13, v1\n" \
  "ds_read_b32 v14, v12 offset:%[rd]\n" \
  "s_waitcnt lgkmcnt(0)\n" \
  "v_readfirstlane_b32 s61, v13\n" \
  "v_readfirstlane_b32 s62, v14\n" \
  "s_cmp_gt_u32 s61, 4\n" \
  "s_cselect_b32 s61, s62, 1\n" \
  "s_cmp_eq_u32 s61, 0\n" \
  "s_cbranch_scc1 Lz%=\n" \
  "s_setprio 1\n" \
  "Lp%=:\n" \
  "ds_read_b32 v13, v12 offset:%[rd]\n" \
  "s_waitcnt lgkmcnt(0)\n" \
  "v_readfirstlane_b32 s61, v13\n" \
  "s_andn2_b32 s62, 15, s61\n" \
  "s_cselect_b32 s62, 0, 0x100\n" \
  "s_andn2_b32 s63, 0xf0, s61\n" \
  "s_cselect_b32 s63, 0, 0x200\n" \
  "s_or_b32 s61, s61, s62\n" \
  "s_or_b32 s61, s61, s63\n" \
  "s_and_b32 s61, s61, s60\n" \
  "s_cbranch_scc1 Lgo%=\n" \
  "s_sleep 1\n" \
  "s_branch Lp%=\n" \
  "Lgo%=:\n" \
  "s_andn2_b32 s60, s60, s61\n" \
  "s_ff1_i32_b32 s62, s61\n" \
  "s_bitset0_b32 s61, s62\n" \
  "s_lshl_b32 s63, s62, 12\n" \
  "s_cmp_ge_u32 s62, 8\n" \
  "s_cselect_b32 s64, %[r5], 0\n" \
  "s_add_u32 s63, s63, s64\n" \
  "v_add_u32 v14, s63, v0\n" \
  "ds_read_b128 v[64:67], v14 offset:0\n" \
  "ds_read_b128 v[68:71], v14 offset:256\n" \
  "ds_read_b128 v[72:75], v14 offset:512\n" \
  "ds_read_b128 v[76:79], v14 offset:768\n" \
  "s_set_gpr_idx_on s62, gpr_idx(SRC0)\n" \
  "v_mov_b32 v16, v2\n" \
  "s_set_gpr_idx_off\n" \
  "s_cmp_eq_u32 s61, 0\n" \
  "s_cbranch_scc1 Ls%=\n" \
  "s_ff1_i32_b32 s62, s61\n" \
  "s_bitset0_b32 s61, s62\n" \
  "s_lshl_b32 s63, s62, 12\n" \
  "s_cmp_ge_u32 s62, 8\n" \
  "s_cselect_b32 s64, %[r5], 0\n" \
  "s_add_u32 s63, s63, s64\n" \
  "v_add_u32 v14, s63, v0\n" \
  "ds_read_b128 v[80:83], v14 offset:0\n" \
  "ds_read_b128 v[84:87], v14 offset:256\n" \
  "ds_read_b128 v[88:91], v14 offset:512\n" \
  "ds_read_b128 v[92:95], v14 offset:768\n" \
  "s_set_gpr_idx_on s62, gpr_idx(SRC0)\n" \
  "v_mov_b32 v17, v2\n" \
  "s_set_gpr_idx_off\n" \
  "s_waitcnt lgkmcnt(4)\n" \
  "v_mfma_f32_16x16x32_bf16 v[96:99], v[64:67], v[32:35], 0\n" \
  "v_mfma_f32_16x16x32_bf16 v[100:103], v[64:67], v[36:39], 0\n" \
  "v_mfma_f32_16x16x32_bf16 v[96:99], v[68:71], v[40:43], v[96:99]\n" \
  "v_mfma_f32_16x16x32_bf16 v[100:103], v[68:71], v[44:47], v[100:103]\n" \
  "v_mfma_f32_16x16x32_bf16 v[96:99], v[72:75], v[48:51], v[96:99]\n" \
  "v_mfma_f32_16x16x32_bf16 v[100:103], v[72:75], v[52:55], v[100:103]\n" \
  "v_mfma_f32_16x16x32_bf16 v[96:99], v[76:79], v[56:59], v[96:99]\n" \
  "v_mfma_f32_16x16x32_bf16 v[100:103], v[76:79], v[60:63], v[100:103]\n" \
  "Lb1_%=:\n" \
  "s_cmp_eq_u32 s61, 0\n" \
  "s_cbranch_scc1 Ll1_%=\n" \
  "s_ff1_i32_b32 s62, s61\n" \
  "s_bitset0_b32 s61, s62\n" \
  "s_lshl_b32 s63, s62, 12\n" \
  "s_cmp_ge_u32 s62, 8\n" \
  "s_cselect_b32 s64, %[r5], 0\n" \
  "s_add_u32 s63, s63, s64\n" \
  "v_add_u32 v14, s63, v0\n" \
  "ds_read_b128 v[64:67], v14 offset:0\n" \
  "ds_read_b128 v[68:71], v14 offset:256\n" \
  "ds_read_b128 v[72:75], v14 offset:512\n" \
  "ds_read_b128 v[76:79], v14 offset:768\n" \
  "s_waitcnt lgkmcnt(4)\n" \
  "v_mfma_f32_16x16x32_bf16 v[104:107], v[80:83], v[32:35], 0\n" \
  "v_mfma_f32_16x16x32_bf16 v[108:111], v[80:83], v[36:39], 0\n" \
  "v_cvt_pk_bf16_f32 v96, v96, v97\n" \
  "v_cvt_pk_bf16_f32 v97, v98, v99\n" \
  "v_mfma_f32_16x16x32_bf16 v[104:107], v[84:87], v[40:43], v[104:107]\n" \
  "v_cvt_pk_bf16_f32 v98, v100, v101\n" \
  "v_cvt_pk_bf16_f32 v99, v102, v103\n" \
  "v_mfma_f32_16x16x32_bf16 v[108:111], v[84:87], v[44:47], v[108:111]\n" \
  "buffer_store_dwordx4 v[96:99], v16, %[os], 0 offen sc1\n" \
  "v_mfma_f32_16x16x32_bf16 v[104:107], v[88:91], v[48:51], v[104:107]\n" \
  "v_mfma_f32_16x16x32_bf16 v[108:111], v[88:91], v[52:55], v[108:111]\n" \
  "s_set_gpr_idx_on s62, gpr_idx(SRC0)\n" \
  "v_mov_b32 v16, v2\n" \
  "s_set_gpr_idx_off\n" \
  "v_mfma_f32_16x16x32_bf16 v[104:107], v[92:95], v[56:59], v[104:107]\n" \
  "v_mfma_f32_16x16x32_bf16 v[108:111], v[92:95], v[60:63], v[108:111]\n" \
  "Lb0_%=:\n" \
  "s_cmp_eq_u32 s61, 0\n" \
  "s_cbranch_scc1 Ll0_%=\n" \
  "s_ff1_i32_b32 s62, s61\n" \
  "s_bitset0_b32 s61, s62\n" \
  "s_lshl_b32 s63, s62, 12\n" \
  "s_cmp_ge_u32 s62, 8\n" \
  "s_cselect_b32 s64, %[r5], 0\n" \
  "s_add_u32 s63, s63, s64\n" \
  "v_add_u32 v14, s63, v0\n" \
  "ds_read_b128 v[80:83], v14 offset:0\n" \
  "ds_read_b128 v[84:87], v14 offset:256\n" \
  "ds_read_b128 v[88:91], v14 offset:512\n" \
  "ds_read_b128 v[92:95], v14 offset:768\n" \
  "s_waitcnt lgkmcnt(4)\n" \
  "v_mfma_f32_16x16x32_bf16 v[96:99], v[64:67], v[32:35], 0\n" \
  "v_mfma_f32_16x16x32_bf16 v[100:103], v[64:67], v[36:39], 0\n" \
  "v_cvt_pk_bf16_f32 v104, v104, v105\n" \
  "v_cvt_pk_bf16_f32 v105, v106, v107\n" \
  "v_mfma_f32_16x16x32_bf16 v[96:99], v[68:71], v[40:43], v[96:99]\n" \
  "v_cvt_pk_bf16_f32 v106, v108, v109\n" \
  "v_cvt_pk_bf16_f32 v107, v110, v111\n" \
  "v_mfma_f32_16x16x32_bf16 v[100:103], v[68:71], v[44:47], v[100:103]\n" \
  "buffer_store_dwordx4 v[104:107], v17, %[os], 0 offen sc1\n" \
  "v_mfma_f32_16x16x32_bf16 v[96:99], v[72:75], v[48:51], v[96:99]\n" \
  "v_mfma_f32_16x16x32_bf16 v[100:103], v[72:75], v[52:55], v[100:103]\n" \
  "s_set_gpr_idx_on s62, gpr_idx(SRC0)\n" \
  "v_mov_b32 v17, v2\n" \
  "s_set_gpr_idx_off\n" \
  "v_mfma_f32_16x16x32_bf16 v[96:99], v[76:79], v[56:59], v[96:99]\n" \
  "v_mfma_f32_16x16x32_bf16 v[100:103], v[76:79], v[60:63], v[100:103]\n" \
  "s_branch Lb1_%=\n" \
  "Ll1_%=:\n" \
  "s_waitcnt lgkmcnt(0)\n" \
  "v_mfma_f32_16x16x32_bf16 v[104:107], v[80:83], v[32:35], 0\n" \
  "v_mfma_f32_16x16x32_bf16 v[108:111], v[80:83], v[36:39], 0\n" \
  "v_mfma_f32_16x16x32_bf16 v[104:107], v[84:87], v[40:43], v[104:107]\n" \
  "v_mfma_f32_16x16x32_bf16 v[108:111], v[84:87], v[44:47], v[108:111]\n" \
  "v_mfma_f32_16x16x32_bf16 v[104:107], v[88:91], v[48:51], v[104:107]\n" \
  "v_cvt_pk_bf16_f32 v96, v96, v97\n" \
  "v_cvt_pk_bf16_f32 v97, v98, v99\n" \
  "v_mfma_f32_16x16x32_bf16 v[108:111], v[88:91], v[52:55], v[108:111]\n" \
  "v_cvt_pk_bf16_f32 v98, v100, v101\n" \
  "v_cvt_pk_bf16_f32 v99, v102, v103\n" \
  "v_mfma_f32_16x16x32_bf16 v[104:107], v[92:95], v[56:59], v[104:107]\n" \
  "buffer_store_dwordx4 v[96:99], v16, %[os], 0 offen sc1\n" \
  "v_mfma_f32_16x16x32_bf16 v[108:111], v[92:95], v[60:63], v[108:111]\n" \
  "s_cmp_lg_u32 s60, 0\n" \
  "s_nop 5\n" \
  "v_cvt_pk_bf16_f32 v104, v104, v105\n" \
  "v_cvt_pk_bf16_f32 v105, v106, v107\n" \
  "v_cvt_pk_bf16_f32 v106, v108, v109\n" \
  "v_cvt_pk_bf16_f32 v107, v110, v111\n" \
  "buffer_store_dwordx4 v[104:107], v17, %[os], 0 offen sc1\n" \
  "s_cbranch_scc1 Lp%=\n" \
  "s_branch Le%=\n" \
  "Ll0_%=:\n" \
  "s_waitcnt lgkmcnt(0)\n" \
  "v_mfma_f32_16x16x32_bf16 v[96:99], v[64:67], v[32:35], 0\n" \
  "v_mfma_f32_16x16x32_bf16 v[100:103], v[64:67], v[36:39], 0\n" \
  "v_mfma_f32_16x16x32_bf16 v[96:99], v[68:71], v[40:43], v[96:99]\n" \
  "v_mfma_f32_16x16x32_bf16 v[100:103], v[68:71], v[44:47], v[100:103]\n" \
  "v_mfma_f32_16x16x32_bf16 v[96:99], v[72:75], v[48:51], v[96:99]\n" \
  "v_cvt_pk_bf16_f32 v104, v104, v105\n" \
  "v_cvt_pk_bf16_f32 v105, v106, v107\n" \
  "v_mfma_f32_16x16x32_bf16 v[100:103], v[72:75], v[52:55], v[100:103]\n" \
  "v_cvt_pk_bf16_f32 v106, v108, v109\n" \
  "v_cvt_pk_bf16_f32 v107, v110, v111\n" \
  "v_mfma_f32_16x16x32_bf16 v[96:99], v[76:79], v[56:59], v[96:99]\n" \
  "buffer_store_dwordx4 v[104:107], v17, %[os], 0 offen sc1\n" \
  "v_mfma_f32_16x16x32_bf16 v[100:103], v[76:79], v[60:63], v[100:103]\n" \
  "s_cmp_lg_u32 s60, 0\n" \
  "s_nop 5\n" \
  "v_cvt_pk_bf16_f32 v96, v96, v97\n" \
  "v_cvt_pk_bf16_f32 v97, v98, v99\n" \
  "v_cvt_pk_bf16_f32 v98, v100, v101\n" \
  "v_cvt_pk_bf16_f32 v99, v102, v103\n" \
  "buffer_store_dwordx4 v[96:99], v16, %[os], 0 offen sc1\n" \
  "s_cbranch_scc1 Lp%=\n" \
  "s_branch Le%=\n" \
  "Ls%=:\n" \
  "s_waitcnt lgkmcnt(0)\n" \
  "v_mfma_f32_16x16x32_bf16 v[96:99], v[64:67], v[32:35], 0\n" \
  "v_mfma_f32_16x16x32_bf16 v[100:103], v[64:67], v[36:39], 0\n" \
  "v_mfma_f32_16x16x32_bf16 v[96:99], v[68:71], v[40:43], v[96:99]\n" \
  "v_mfma_f32_16x16x32_bf16 v[100:103], v[68:71], v[44:47], v[100:103]\n" \
  "v_mfma_f32_16x16x32_bf16 v[96:99], v[72:75], v[48:51], v[96:99]\n" \
  "v_mfma_f32_16x16x32_bf16 v[100:103], v[72:75], v[52:55], v[100:103]\n" \
  "v_mfma_f32_16x16x32_bf16 v[96:99], v[76:79], v[56:59], v[96:99]\n" \
  "v_mfma_f32_16x16x32_bf16 v[100:103], v[76:79], v[60:63], v[100:103]\n" \
  "s_cmp_lg_u32 s60, 0\n" \
  "s_nop 5\n" \
  "v_cvt_pk_bf16_f32 v96, v96, v97\n" \
  "v_cvt_pk_bf16_f32 v97, v98, v99\n" \
  "v_cvt_pk_bf16_f32 v98, v100, v101\n" \
  "v_cvt_pk_bf16_f32 v99, v102, v103\n" \
  "buffer_store_dwordx4 v[96:99], v16, %[os], 0 offen sc1\n" \
  "s_cbranch_scc1 Lp%=\n" \
  "Le%=:\n"
#define MIX_CLOBBERS \
  "v16", "v17", "v64", "v65", "v66", "v67", "v68", "v69", "v70", "v71", "v72", "v73", "v74", "v75", "v76", \
  "v77", "v78", "v79", "v80", "v81", "v82", "v83", "v84", "v85", "v86", "v87", "v88", "v89", "v90", "v91", \
  "v92", "v93", "v94", "v95", "v96", "v97", "v98", "v99", "v100", "v101", "v102", "v103", "v104", "v105", \
  "v106", "v107", "v108", "v109", "v110", "v111", "s60", "s61", "s62", "s63", "s64", "scc", "vcc", "memory"


namespace {

constexpr int C_ = 128;
constexpr int MW = 12;
constexpr int MT = MW * 64;
constexpr int SW = 8;
constexpr int NS = 5;
constexpr int NPB = SW * NS;
constexpr int NPASS = 16;
constexpr int HS = 64 * NPASS / 2;
constexpr int NBMAX = 256;
constexpr int PW = 5;
constexpr int PT = PW * 64;
constexpr int PK = 4;
constexpr int CH = PT * PK;
constexpr int MK = 4;
constexpr int CAPW = 256;
constexpr int HWV = 10;
constexpr int PFN = 5;
constexpr int TB2 = 1;
constexpr int WA = 3;
constexpr int WB = 6;
constexpr int FO1 = 2;
constexpr int WSL = 10;
constexpr int TB3 = 1;
constexpr int HSL = 24;
constexpr unsigned WS_EPOCH = 0;
constexpr unsigned WS_OFF = 4096;
constexpr unsigned WS_RECK = WS_OFF + NBMAX * NBMAX * 16;
constexpr unsigned WS_RECP = WS_RECK + NBMAX * CH * 4;
constexpr unsigned WS_END = WS_RECP + NBMAX * CH * 16;

__device__ __forceinline__ float bf2f(unsigned short v) { return __uint_as_float(((unsigned)v) << 16); }
__device__ __forceinline__ unsigned short f2bf(float f) {
  __hip_bfloat16 b = __float2bfloat16(f);
  return *reinterpret_cast<unsigned short*>(&b);
}
__device__ __forceinline__ unsigned pk2bf(float lo, float hi) {
  return __builtin_bit_cast(unsigned, __builtin_convertvector(f32x2{lo, hi}, bf16x2));
}
__device__ __forceinline__ unsigned wave_incl_scan(unsigned x) {
  x += __builtin_amdgcn_update_dpp(0u, x, 0x111, 0xf, 0xf, false);
  x += __builtin_amdgcn_update_dpp(0u, x, 0x112, 0xf, 0xf, false);
  x += __builtin_amdgcn_update_dpp(0u, x, 0x114, 0xf, 0xf, false);
  x += __builtin_amdgcn_update_dpp(0u, x, 0x118, 0xf, 0xf, false);
  x += __builtin_amdgcn_update_dpp(0u, x, 0x142, 0xa, 0xf, false);
  x += __builtin_amdgcn_update_dpp(0u, x, 0x143, 0xc, 0xf, false);
  return x;
}

struct LdsLow {
  u32x4 sink[16];
  unsigned flag, rdone, go, pad_;
  unsigned long long tag, pad2_;
  unsigned prog[16];
  int deg[64];
  unsigned byrank[64];
  unsigned nl[64];
  u32x4 ct[72];
  union {
    struct {
      __attribute__((aligned(16))) unsigned so[SW * CAPW];
      union {
        __attribute__((aligned(16))) unsigned hist[(SW + 1) * HS];
        u32x4 row5[SW * 64];
      };
    };
    struct {
      __attribute__((aligned(16))) unsigned sk[CH];
      u32x4 sp[CH];
    };
  };
  union {
    struct {
      __attribute__((aligned(16))) unsigned seg[2][NBMAX];
      unsigned phist[PW][NBMAX / 2];
      unsigned pbase[PW][NBMAX / 2];
      u32x4 ri[SW][MK * 16];
    };
    u32x4 pay[SW * CAPW];
  };
};
constexpr int TBLK = 0xc300 / 8;
struct Lds : LdsLow {
  char pad_end[163840 - TBLK * 16 - sizeof(LdsLow)];
  u32x4 sg[TBLK];
};
constexpr int TOFF = 163840 - TBLK * 16;
constexpr int WBLK = 0x4180 / 8;
static_assert(sizeof(Lds) == 163840 && TOFF < 65536, "LDS layout");

__device__ __forceinline__ unsigned lds_addr(const void* p) {
  return (unsigned)(uintptr_t)(__attribute__((address_space(3))) const void*)p;
}
__device__ __forceinline__ u32x4 make_srd(const void* p, unsigned num) {
  unsigned long long a = (unsigned long long)p;
  return u32x4{(unsigned)a, (unsigned)(a >> 32), num, 0x00020000u};
}

}

extern "C" __global__ __launch_bounds__(MT, 3) void tfn_main_kernel(
    const int* dst_, int E_, int c0_, const int* src_, int CE_, int c1_, const u32x2* Y_, int N_, int c2_, const u32x2* ef_,
    unsigned M2_, int c3_, const unsigned short* x_, char* ws_, u32x4* out_, const unsigned short* wrad_,
    const unsigned short* cg_, const unsigned short* wmix_) {
  __shared__ Lds L;
  const int tid = threadIdx.x, lane = tid & 63;
  u32x2 d0, d1, s0, s1;
  u32x4 y0, y1, f0, f1;
  u32x16 ka;
  u32x8 kb, kc;
  u32x4 kd;
  unsigned long long ev;
  asm volatile(
      "s_load_dwordx16 s[8:23], %[kp], 0\n s_load_dwordx8 s[24:31], %[kp], 0x40\n s_load_dwordx4 s[32:35], %[kp], 0x60\n"
      "v_readfirstlane_b32 s36, %[tid]\n v_lshlrev_b32 v90, 3, %[tid]\n v_mov_b64 v[92:93], 0\n"
      "s_and_b32 s37, %[bid], 7\n s_lshr_b32 s38, %[bid], 3\n s_lshr_b32 s36, s36, 6\n"
      "ds_write_b64 v90, v[92:93] offset:%[zo]\n"
      "s_cmp_lg_u32 s36, %[pw]\n s_cbranch_scc1 1f\n"
      "s_getpc_b64 s[44:45]\n .Ltfn_p0:\n s_max_i32 s46, 0, .Ltfn_p1-.Ltfn_p0\n s_mov_b32 m0, %[sk]\n s_mov_b32 s47, 0x20000\n"
      "v_lshlrev_b32 v91, 12, %[tid]\n s_lshl_b32 s39, s38, 7\n v_and_b32 v91, 0x3f000, v91\n"
      "buffer_load_dword v91, s[44:47], s39 offen lds\n"
      "1:\n s_lshl_b32 s37, s37, 5\n s_add_u32 s37, s37, s38\n v_lshlrev_b32 v91, 4, %[tid]\n"
      "s_waitcnt lgkmcnt(0)\n"
      "s_mul_i32 s38, s37, s14\n s_mov_b32 s40, s10\n s_add_i32 s39, s38, s14\n s_lshl_b32 s44, s38, 2\n"
      "s_min_i32 s39, s39, s10\n s_add_i32 s45, s44, %[h4]\n s_lshl_b32 s10, s39, 2\n s_lshl_b32 s14, s39, 2\n"
      "s_cmp_ge_u32 s36, %[pw]\n s_cbranch_scc1 2f\n"
      "buffer_load_dwordx2 %0, v90, s[8:11], s44 offen\n buffer_load_dwordx2 %1, v90, s[8:11], s45 offen\n"
      "buffer_load_dwordx2 %2, v90, s[12:15], s44 offen\n buffer_load_dwordx2 %3, v90, s[12:15], s45 offen\n"
      "2:\n s_mov_b32 s41, s18\n s_mov_b32 s42, s22\n s_lshl_b32 s18, s39, 3\n s_lshl_b32 s22, s39, 3\n"
      "s_lshl_b32 s44, s44, 1\n s_lshl_b32 s45, s45, 1\n s_cmp_ge_u32 s36, %[pw]\n"
      "s_barrier\n s_cbranch_scc1 3f\n s_setprio 3\n"
      "buffer_load_dwordx4 v[100:103], v91, s[16:19], s44 offen\n buffer_load_dwordx4 v[104:107], v91, s[16:19], s45 offen\n"
      "buffer_load_dwordx4 v[108:111], v91, s[20:23], s44 offen\n buffer_load_dwordx4 v[112:115], v91, s[20:23], s45 offen\n"
      "3:\n s_cmp_lg_u32 s36, %[hw]\n s_cbranch_scc1 4f\n s_load_dwordx2 s[46:47], s[26:27], 0\n s_setprio 3\n"
      "s_waitcnt lgkmcnt(0)\n 4:"
      : "=&v"(d0), "=&v"(d1), "=&v"(s0), "=&v"(s1), "=&{v[100:103]}"(y0), "=&{v[104:107]}"(y1), "=&{v[108:111]}"(f0),
        "=&{v[112:115]}"(f1), "=&{s[8:23]}"(ka), "=&{s[24:31]}"(kb), "=&{s[32:35]}"(kd), "=&{s[36:43]}"(kc),
        "=&{s[46:47]}"(ev)
      : [kp] "s"(__builtin_amdgcn_kernarg_segment_ptr()), [tid] "v"(tid), [bid] "s"(blockIdx.x), [pw] "n"(PW), [hw] "n"(HWV),
        [zo] "n"(offsetof(LdsLow, seg)), [sk] "n"(offsetof(LdsLow, sink)), [h4] "n"(CH * 2)
      : "v90", "v91", "v92", "v93", "s44", "s45", "scc", "memory");
  typedef __attribute__((address_space(1))) char gchar;
  auto ptr = [](unsigned lo, unsigned hi) { return (char*)(gchar*)(lo | (unsigned long long)hi << 32); };
  const int* __restrict__ dst = (const int*)ptr(ka[0], ka[1]);
  const int* __restrict__ src = (const int*)ptr(ka[4], ka[5]);
  const u32x2* __restrict__ Y = (const u32x2*)ptr(ka[8], ka[9]);
  const u32x2* __restrict__ ef = (const u32x2*)ptr(ka[12], ka[13]);
  const unsigned short* __restrict__ x = (const unsigned short*)ptr(kb[0], kb[1]);
  char* ws = ptr(kb[2], kb[3]);
  u32x4* __restrict__ out = (u32x4*)ptr(kb[4], kb[5]);
  const unsigned short* __restrict__ wrad = (const unsigned short*)ptr(kb[6], kb[7]);
  const unsigned short* __restrict__ cg = (const unsigned short*)ptr(kd[0], kd[1]);
  const unsigned short* __restrict__ wmix = (const unsigned short*)ptr(kd[2], kd[3]);
  const int w = kc[0], bucket = kc[1], e0 = kc[2] + tid * 2, e1 = kc[3], E = kc[4], N = kc[5];
  const unsigned M2 = kc[6];
  __builtin_assume(w >= 0 && w < MW && bucket >= 0 && bucket < NBMAX);
  const int xcd = blockIdx.x & 7;
  const u32x4 wsd = make_srd(ws, WS_END);
  auto codes = [](unsigned q) { return q * 0x80008u + u32x4{0x10000u, 0x30002u, 0x50004u, 0x70006u}; };
  auto table = [&](int q) {
    u32x4 p, n;
#pragma unroll
    for (int i = 0; i < 4; i++) {
      const f32x2 z = {__uint_as_float((q * 8 + 2 * i) << 16), __uint_as_float((q * 8 + 2 * i + 1) << 16)};
      const f32x2 e = {__builtin_amdgcn_exp2f(-1.4426950408889634f * z[0]), __builtin_amdgcn_exp2f(-1.4426950408889634f * z[1])};
      const f32x2 r = z * f32x2{__builtin_amdgcn_rcpf(1.f + e[0]), __builtin_amdgcn_rcpf(1.f + e[1])};
      p[i] = pk2bf(r[0], r[1]);
      n[i] = pk2bf(-e[0] * r[0], -e[1] * r[1]);
    }
    if (q < WBLK) L.sg[q] = p;
    return n;
  };
  auto halves = [&](int q0, int q1, int st) {
#pragma unroll 1
    for (int q = q0; q < q1; q += st) {
      const u32x4 c = codes(q - 16);
      L.sg[q] = c;
      if (q >= 0x30) L.sg[q + 0x1000] = c | 0x80008000u;
    }
  };
  auto valid = [&](int k) { return e0 + (k >> 1) * (CH / 2) + (k & 1) < e1; };
  auto warm = [&](int i0, int i1) {
    const unsigned pfo = ((xcd * N) >> 3) * 1024 + ((blockIdx.x >> 3) * 64 + lane) * 128;
#pragma unroll 1
    for (int i = i0; i < i1; i += MW - SW)
      asm volatile("s_mov_b32 m0, %2\n s_nop 0\n buffer_load_dword %0, %1, %3 offen lds\n"
                   :: "v"(pfo), "s"(make_srd(x, N * 1024)), "s"(lds_addr(L.sink)), "s"(i * 32 * 64 * 128) : "memory");
  };
  unsigned bk[PK], pr[PK], ky[PK], sa[PK];
  unsigned cv, wq4[4];
  asm volatile(
      "s_cmp_lg_u32 %9, %10\n s_cbranch_scc1 1f\n global_load_ushort %0, %5, %7\n global_load_dword %1, %6, %8\n"
      "global_load_dword %2, %6, %8 offset:256\n global_load_dword %3, %6, %8 offset:512\n"
      "global_load_dword %4, %6, %8 offset:768\n 1:"
      : "=&v"(cv), "=&v"(wq4[0]), "=&v"(wq4[1]), "=&v"(wq4[2]), "=&v"(wq4[3])
      : "v"(lane * 2), "v"(lane * 4), "s"(cg), "s"(wrad), "s"(w), "n"(PW) : "scc");
  auto wdma = [&](int i0, int i1) {
#pragma unroll
    for (int i = i0; i < i1; i++) {
      const unsigned o = (i + (blockIdx.x >> 3)) & 7;
      asm volatile("s_mov_b32 m0, %2\n s_nop 0\n buffer_load_dwordx4 %0, %1, %3 offen lds\n"
                   :: "v"((lane >> 2) * 256 + (lane & 3) * 16), "s"(make_srd(wmix, C_ * C_ * 2)),
                      "s"(lds_addr(&L.sg[WBLK]) + ((w - SW) * 8 + o) * 1024), "s"(o * 4096 + (w - SW) * 64) : "memory");
    }
  };
  const int zt = tid - PT - (w > HWV ? 64 : 0);
  unsigned so1 = (tid - PT) * 16 + WS_RECP + bucket * (CH * 16);
  unsigned so0 = w < PW ? tid * 16 + WS_RECK + bucket * (CH * 4) : so1;
  so1 += MT * 16;
  unsigned ho0 = w < SW ? ((lane >> 1) * SW + w) * (NBMAX * 16) : lane * 16 + WS_OFF + bucket * (NBMAX * 16);
  int node0 = __builtin_amdgcn_readfirstlane(((unsigned)(bucket << 22) + M2 - 1) / M2);
  int nn = min(N, __builtin_amdgcn_readfirstlane(((unsigned)((bucket + 1) << 22) + M2 - 1) / M2)) - node0;
  asm volatile("" : "+v"(so0), "+v"(so1), "+v"(ho0), "+s"(node0), "+s"(nn));
  if (w < PW) {
    unsigned one[PK], far[PK];
#pragma unroll
    for (int k = 0; k < PK; k++) {
      one[k] = valid(k) ? 1u : 0u;
      far[k] = valid(k) ? 0u : (0x10000u | lane) << 2;
    }
    asm volatile("s_waitcnt vmcnt(4)" : "+v"(d0), "+v"(d1), "+v"(s0), "+v"(s1), "+v"(one[0]), "+v"(one[1]), "+v"(one[2]),
                 "+v"(one[3]), "+v"(far[0]), "+v"(far[1]), "+v"(far[2]), "+v"(far[3]));
    const u32x4 dv = {d0[0], d0[1], d1[0], d1[1]}, sv = {s0[0], s0[1], s1[0], s1[1]};
#pragma unroll
    for (int k = 0; k < PK; k++) {
      bk[k] = ((unsigned)__umul24(dv[k], M2) >> 22) | (far[k] & 255);
      pr[k] = atomicAdd(&L.phist[w][bk[k] >> 1], one[k] << ((bk[k] * 16) & 31));
    }
    __builtin_amdgcn_sched_barrier(0);
#pragma unroll
    for (int k = 0; k < PK; k++) {
      ky[k] = (sv[k] << 10) | ((dv[k] & 63) << 4) | ((((unsigned)__umul24(sv[k], M2) >> 26) - ((bk[k] >> 4) & 14)) & 15);
      ky[k] = __umul24(ky[k], one[k]);
      sa[k] = lds_addr(&L.seg[0][bk[k]]);
      asm volatile("ds_add_u32 %0, %1" :: "v"(sa[k]), "v"(ky[k]) : "memory");
    }
#pragma unroll
    for (int k = 0; k < PK; k++) {
      pr[k] = __builtin_amdgcn_ubfe(pr[k], bk[k] * 16, 16) + far[k];
      bk[k] = lds_addr(L.pbase[w]) + bk[k] * 2;
      asm volatile("" : "+v"(pr[k]), "+v"(bk[k]));
    }
  } else {
    if (w == PW) {
      L.deg[lane] = ((lane - node0) & 63) < NPB ? 63 - ((lane - node0) & 63) : -1;
      if (lane < 4) (&L.flag)[lane] = 0;
      if (lane < 16) L.prog[lane] = 255;
    }
    if (w == HWV) {
      ev = ev * 6364136223846793005ull + 1442695040888963407ull;
      L.tag = ev;
    } else {
      if (zt < (0x4300 - 0x3a80) / 8) L.sg[0xba80 / 8 + zt] = table(0x3a80 / 8 + zt);
    }
  }
  asm volatile("s_waitcnt lgkmcnt(0)\n s_barrier" ::: "memory");
  const auto wsr = __builtin_amdgcn_make_buffer_rsrc((void*)ws, 0, WS_END, 0x00020000);
  auto lds_ld = [&](const unsigned* p) {
    return __builtin_amdgcn_readfirstlane(__hip_atomic_load(p, __ATOMIC_RELAXED, __HIP_MEMORY_SCOPE_WORKGROUP));
  };
  if (w == HWV) {
    const unsigned tl = (unsigned)ev, th = (unsigned)(ev >> 32);
    unsigned c[4] = {0, 0, 0, 0};
    u32x4 hv[4];
#pragma unroll
    for (int k = 0; k < 4; k++) {
#pragma unroll
      for (int ww = 0; ww < PW; ww++) c[k] += reinterpret_cast<const unsigned short*>(L.phist[ww])[k * 64 + lane];
      hv[k][1] = L.seg[0][k * 64 + lane];
    }
    const unsigned p0 = c[0] | (c[1] << 16), p1 = c[2] | (c[3] << 16);
    const unsigned i0 = wave_incl_scan(p0), i1 = wave_incl_scan(p1);
    const unsigned t0 = __builtin_amdgcn_readlane(i0, 63), t1 = __builtin_amdgcn_readlane(i1, 63);
    const unsigned s1 = t0 & 0xffff, s2 = s1 + (t0 >> 16), s3 = s2 + (t1 & 0xffff);
    const unsigned bs[4] = {(i0 - p0) & 0xffff, ((i0 - p0) >> 16) + s1, ((i1 - p1) & 0xffff) + s2, ((i1 - p1) >> 16) + s3};
#pragma unroll
    for (int k = 0; k < 4; k++) {
      hv[k][0] = bs[k] | (c[k] << 11) | (tl << 22);
      hv[k][1] += (unsigned)__umul24(c[k], tl & 255) << 24;
      hv[k][3] = th - hv[k][0] - hv[k][1];
    }
    asm volatile("" : "+v"(hv[0]), "+v"(hv[1]), "+v"(hv[2]), "+v"(hv[3]));
    while (true) {
      const unsigned gv = __hip_atomic_load(&L.go, __ATOMIC_RELAXED, __HIP_MEMORY_SCOPE_WORKGROUP);
      asm volatile("" ::: "memory");
#pragma unroll
      for (int k = 0; k < 4; k++) hv[k][2] = L.seg[1][k * 64 + lane];
      asm volatile("" ::: "memory");
      if (__builtin_amdgcn_readfirstlane(gv) == PW) break;
    }
#pragma unroll
    for (int k = 0; k < 4; k++) {
      hv[k][3] -= hv[k][2];
      __builtin_amdgcn_raw_buffer_store_b128(hv[k], wsr, ho0 + k * 1024, 0, 16);
    }
    wdma(0, WA);
  } else if (w < PW) {
    const unsigned t8 = (unsigned)L.tag << 24;
    unsigned ca = 0, cb = 0, pa = 0, pb = 0, hh[PW][2];
#pragma unroll
    for (int ww = 0; ww < PW; ww++) {
      hh[ww][0] = L.phist[ww][lane];
      hh[ww][1] = L.phist[ww][64 + lane];
    }
    __builtin_amdgcn_sched_barrier(0);
#pragma unroll
    for (int ww = 0; ww < PW; ww++) {
      const unsigned ha = hh[ww][0], hb = hh[ww][1];
      pa = ww == w ? ca : pa;
      pb = ww == w ? cb : pb;
      ca += ha;
      cb += hb;
    }
    const unsigned t = __builtin_amdgcn_perm(cb, ca, 0x05040100) + __builtin_amdgcn_perm(cb, ca, 0x07060302);
    const unsigned inc = wave_incl_scan(t), ex = inc - t;
    L.pbase[w][lane] = (ex & 0xffff) * 0x10001u + (ca << 16) + pa;
    L.pbase[w][64 + lane] = ((ex >> 16) + (__builtin_amdgcn_readlane(inc, 63) & 0xffff)) * 0x10001u + (cb << 16) + pb;
    unsigned b4[PK];
#pragma unroll
    for (int k = 0; k < PK; k++) b4[k] = *(__attribute__((address_space(3))) unsigned short*)(uintptr_t)bk[k];
    __builtin_amdgcn_sched_barrier(0);
    {
      unsigned t0, t1, t2, t3;
      asm volatile(
          "s_waitcnt vmcnt(0)\n"
          "v_add3_u32 %0, v100, v101, v108\n v_add3_u32 %1, v102, v103, v110\n v_add3_u32 %2, v104, v105, v112\n"
          "v_add3_u32 %3, v106, v107, v114\n"
          "v_add_u32 %0, %0, v109\n v_add_u32 %1, %1, v111\n v_add_u32 %2, %2, v113\n v_add_u32 %3, %3, v115\n"
          "ds_add_u32 %8, %0 offset:1024\n ds_add_u32 %9, %1 offset:1024\n ds_add_u32 %10, %2 offset:1024\n"
          "ds_add_u32 %11, %3 offset:1024\n"
          "s_mov_b64 exec, 1\n ds_add_u32 %12, %13\n s_mov_b64 exec, -1"
          : "=&v"(t0), "=&v"(t1), "=&v"(t2), "=&v"(t3), "+{v[100:103]}"(y0), "+{v[104:107]}"(y1), "+{v[108:111]}"(f0),
            "+{v[112:115]}"(f1)
          : "v"(sa[0]), "v"(sa[1]), "v"(sa[2]), "v"(sa[3]), "v"(lds_addr(&L.go)), "v"(1) : "memory");
    }
#pragma unroll
    for (int k = 0; k < PK; k++) {
      pr[k] = min(pr[k] + b4[k], CH - 1u);
      L.sk[pr[k]] = ky[k] | t8;
      pr[k] = lds_addr(&L.sp[pr[k]]);
    }
    asm volatile(
        "ds_write2_b64 %0, v[100:101], v[108:109] offset1:1\n ds_write2_b64 %1, v[102:103], v[110:111] offset1:1\n"
        "ds_write2_b64 %2, v[104:105], v[112:113] offset1:1\n ds_write2_b64 %3, v[106:107], v[114:115] offset1:1\n"
        "s_waitcnt lgkmcnt(0)"
        :: "v"(pr[0]), "v"(pr[1]), "v"(pr[2]), "v"(pr[3]),
           "{v[100:103]}"(y0), "{v[104:107]}"(y1), "{v[108:111]}"(f0), "{v[112:115]}"(f1) : "memory");
  } else {
    if (w == PW) {
      asm volatile("s_waitcnt   vmcnt(0)" : "+v"(cv), "+v"(wq4[0]), "+v"(wq4[1]), "+v"(wq4[2]), "+v"(wq4[3]));
      reinterpret_cast<unsigned short*>(L.ct)[(lane & 3) * 16 + (lane >> 2)] = cv;
      L.ct[8 + lane] = u32x4{__builtin_amdgcn_perm(wq4[1], wq4[0], 0x05040100), __builtin_amdgcn_perm(wq4[3], wq4[2], 0x05040100),
                             __builtin_amdgcn_perm(wq4[1], wq4[0], 0x07060302), __builtin_amdgcn_perm(wq4[3], wq4[2], 0x07060302)};
    }
    if (w >= SW) wdma(0, WA);
    halves(0x20 + zt, 0x20 + TB2 * 384, 384);
    __builtin_amdgcn_s_setprio(3);
  }
  __syncthreads();
  {
    const u32x4 v0 = reinterpret_cast<const u32x4*>(L.sk)[tid], v1 = reinterpret_cast<const u32x4*>(L.sk)[MT + tid];
    __builtin_amdgcn_raw_buffer_store_b128(v0, wsr, so0, 0, 16);
    __builtin_amdgcn_raw_buffer_store_b128(v1, wsr, so1, 0, 16);
    if (w == PW)
      __builtin_amdgcn_raw_buffer_store_b128(reinterpret_cast<const u32x4*>(L.sk)[2 * MT + lane], wsr, so1 + (MT - PT) * 16, 0, 16);
  }
  if (w >= SW) wdma(WA, WB);
  __syncthreads();
  const unsigned long long epoch = L.tag;
  const unsigned tagl = __builtin_amdgcn_readfirstlane((unsigned)epoch);
  const unsigned tag1 = __builtin_amdgcn_readfirstlane((unsigned)(epoch >> 32));
  const int row = lane & 15, kq = lane >> 4;
  if (w >= SW) {
    int lane;
    asm volatile("v_mbcnt_lo_u32_b32 %0, -1, 0\n v_mbcnt_hi_u32_b32 %0, -1, %0" : "=v"(lane));
    __builtin_assume(lane >= 0 && lane < 64);
    const int h = w - SW, st = h * 64 + lane, row = lane & 15, kq = lane >> 4;
    __builtin_amdgcn_s_sleep(WSL);
    wdma(WB, 8);
    halves(0x20 + TB2 * 384 + TB3 * 512 + st, 0x3a80 / 8, 256);
    const int wq0 = WBLK + h * 512 + lane;
    u32x4 last = codes(wq0 + 448 < 0x1000 ? wq0 + 448 : wq0 + 432);
    if ((unsigned)(wq0 + 448 - 0x1000) < 0x20u) last = table(wq0 + 448 - 0x1000);
    constexpr int HR = (NPB - RKL * SW) / (MW - SW);
    const int id = (node0 + RKL * SW + h * HR + lane) & 63;
    asm volatile("s_waitcnt     vmcnt(0)" ::: "memory");
    u32x16 wa, wb;
    {
      const unsigned* wp = reinterpret_cast<const unsigned*>(&L.sg[WBLK + h * 512]) + kq * 128 + row;
#pragma unroll
      for (int i = 0; i < 16; i++) {
        const unsigned q0 = wp[(i >> 2) * 512 + (i & 3) * 32], q1 = wp[(i >> 2) * 512 + (i & 3) * 32 + 16];
        u32x16& d = i < 8 ? wa : wb;
        d[(i & 4) * 2 + (i & 3)] = __builtin_amdgcn_perm(q1, q0, 0x05040100);
        d[(i & 4) * 2 + (i & 3) + 4] = __builtin_amdgcn_perm(q1, q0, 0x07060302);
      }
    }
    asm volatile("" : "+v"(wa), "+v"(wb) :: "memory");
    asm volatile("s_barrier" ::: "memory");
    __builtin_amdgcn_s_sleep(2);
    warm(h, PFN);
    __syncthreads();
    {
      const int dg = L.deg[lane], dm = L.deg[id];
      int dn[HR];
#pragma unroll
      for (int i = 0; i < HR; i++) dn[i] = __builtin_amdgcn_readlane(dm, i);
      __builtin_amdgcn_sched_barrier(0);
      unsigned long long m[HR];
#pragma unroll
      for (int i = 0; i < HR; i++) m[i] = __ballot(dg > dn[i]);
      __builtin_amdgcn_sched_barrier(0);
      unsigned r = 63;
#pragma unroll
      for (int i = 0; i < HR; i++) asm("v_writelane_b32 %0, %1, %2" : "+v"(r) : "s"(__popcll(m[i])), "n"(i));
      L.byrank[r] = id * 4;
    }
    __syncthreads();
    if (bucket == 0 && st == 0)
      __builtin_amdgcn_raw_buffer_store_b64(u32x2{tagl, tag1}, wsr, WS_EPOCH, 0, 16);
    __builtin_amdgcn_s_setprio(0);
    __syncthreads();
#pragma unroll
    for (int i = 0; i < 8; i++) L.sg[wq0 + i * 64] = i < 7 ? codes(wq0 + i * 64) : last;
    __syncthreads();
    u32x16 oo;
#pragma unroll
    for (int t = 0; t < 10; t++)
      oo[t + 2] = (node0 + ((L.nl[t < SW ? t * 8 + kq : ((t - SW) * 4 + kq) * 8 + 4] / 4 - node0) & 63)) * 1024 + row * 16 + h * 256;
    oo[0] = lds_addr(&L.pay[(row >> 2) * 64 + (row & 3) * 4 + kq]);
    oo[1] = lds_addr(&L.prog[h]);
    asm volatile(MIX_ASM
                 : "+{v[0:15]}"(oo)
                 : "{v[32:47]}"(wa), "{v[48:63]}"(wb), [os] "s"(make_srd(out, (node0 + nn) * 1024)),
                   [r5] "n"((unsigned)(offsetof(LdsLow, row5) - offsetof(LdsLow, pay) - SW * CAPW * 16)),
                   [rd] "n"(offsetof(LdsLow, rdone)), [sl] "n"(HSL)
                 : MIX_CLOBBERS);
    asm volatile(".Ltfn_p1:");
  } else {
    {
    halves(0x20 + TB2 * 384 + tid, 0x20 + TB2 * 384 + TB3 * 512, 512);
    reinterpret_cast<u32x4*>(&L.hist[w * HS])[lane] = u32x4{0, 0, 0, 0};
    reinterpret_cast<u32x4*>(&L.hist[w * HS])[64 + lane] = u32x4{0, 0, 0, 0};
    L.ri[w][lane] = u32x4{0x20000000u, 0x20000000u, 0x20000000u, 0x20000000u};
    const unsigned long long t0 = w & 1 ? 0x26796281267d7281ull : 0x277dc200272c8180ull, t1 = w & 1 ? 0x246942c324755245ull : 0x25755242217984c2ull;
    const unsigned long long t2 = w & 1 ? 0x22613345226d6483ull : 0x236d130423694307ull, t3 = w & 1 ? 0x206503c725710384ull : 0x215d2386206513cdull;
    const unsigned long long t = w & 4 ? (w & 2 ? t3 : t2) : (w & 2 ? t1 : t0);
    const unsigned rks = nn == NPB ? t >> 32 : t;
    const int start = w * CAPW;
    unsigned sop = lds_addr(&L.so[start + (lane & 15)]);
    unsigned s4p = lds_addr(&L.so[start + (lane >> 2)]);
    unsigned yp = lds_addr(&L.pay[start + (lane >> 2)]);
    unsigned ep = lds_addr(&L.pay[start + (lane & 15)]) + 8;
    u32x4 cg0 = L.ct[(lane & 3) * 2];
    const u32x4 cg1 = L.ct[(lane & 3) * 2 + 1], wq = L.ct[8 + lane];
    const unsigned rp = lds_addr(&L.pay[start]) + kq * 256 + row * 4;
    unsigned pg = lds_addr(&L.prog[w]), slowv;
    u32x16 cs = {ho0,
                 (unsigned)(((lane >> 1) * SW + w) * CH + (lane & 1)) * 4,
                 lds_addr(L.ri[w]) + (lane & 1) * 4, lds_addr(L.ri[w]) + lane * 4, (lane & 1) - 1u, lane & 1u,
                 lds_addr(&L.hist[tid]), lds_addr(&L.byrank[(rks >> (min(lane & 7, NS - 1) * 6)) & 63]),
                 0, lds_addr(&L.hist[SW * HS + (lane >> 3)]),
                 (start * 4 + ((lane & 7) << 13)) * 0x10001u, (unsigned)(start + lane) * 4,
                 lds_addr(&L.nl[w * 8 + (lane & 7)]), lane * 4u, ((node0 + w * RKL + lane) & 63) * 4u, 0};
    asm volatile(STREAM_ASM
                 : [sl] "=s"(slowv), "+{v12}"(sop), "+{v13}"(s4p), "+{v14}"(yp), "+{v15}"(ep), "+{v[136:151]}"(cs),
                   "+{v[20:23]}"(cg0), "+{v110}"(pg)
                 : "{v8}"(lane * 16), "{v17}"((1u << (lane & 3)) >> 1),
                   "{v[24:27]}"(cg1), "{v[28:31]}"(wq), "{v[152:155]}"(u32x4{rp, rp + 64, rp + 128, rp + 192}),
                   "{v156}"(lds_addr(&L.row5[w * 64]) + kq * 256 + row * 4),
                   [xs] "s"(make_srd(x, N * 1024)), [ws] "s"(wsd),
                   [ho] "s"(WS_OFF + bucket * 16), [rk] "s"(WS_RECK), [rp] "s"(WS_RECP), [t0] "s"(tagl << 22), [t1] "s"(tag1),
                   [wb] "s"(w * (HS * 4)), [bit] "s"(1u << w), [fo] "s"((w >> 2) * FO1), [tb] "n"(TOFF),
                   [rd] "n"(offsetof(LdsLow, rdone)), [fl] "n"(offsetof(LdsLow, flag)),
                   [hi] "n"(offsetof(LdsLow, hist)), [to] "n"(offsetof(LdsLow, hist) + SW * HS * 4), [dg] "n"(offsetof(LdsLow, deg)),
                   [br] "n"(offsetof(LdsLow, byrank)), [so] "n"(offsetof(LdsLow, so)), [pa] "n"(offsetof(LdsLow, pay))
                 : STREAM_CLOBBERS);
    }
    int lane;
    asm volatile("v_mbcnt_lo_u32_b32 %0, -1, 0\n v_mbcnt_hi_u32_b32 %0, -1, %0" : "=v"(lane));
    __builtin_assume(lane >= 0 && lane < 64);
    u32x4* rows = &L.pay[w * CAPW];
#pragma unroll 1
    for (int it = 0; it < 2 * NS; it++) {
      const int sl = it >> 1, slice = it & 1, ch = slice * 64 + lane;
      const unsigned dl = (L.nl[w * 8 + sl] / 4 - node0) & 63;
      float acc[4] = {0.f, 0.f, 0.f, 0.f};
#pragma unroll 1
      for (int eb = 0; eb < E; eb += 64) {
        unsigned long long m = __ballot(eb + lane < E && (unsigned)dst[min(eb + lane, E - 1)] == node0 + dl);
#pragma unroll 1
        for (; m; m &= m - 1) {
          const int e = eb + __builtin_ctzll(m);
          const u32x2 ry = Y[e], rf = ef[e];
          float yy[4] = {bf2f(ry[0] & 0xffff), bf2f(ry[0] >> 16), bf2f(ry[1] & 0xffff), bf2f(ry[1] >> 16)};
          float ee[4] = {bf2f(rf[0] & 0xffff), bf2f(rf[0] >> 16), bf2f(rf[1] & 0xffff), bf2f(rf[1] >> 16)};
          float z = 0.f;
#pragma unroll
          for (int k = 0; k < 4; k++) z += ee[k] * bf2f(wrad[k * C_ + ch]);
          float R = z / (1.f + __expf(-z));
          const unsigned short* xr = x + (size_t)src[e] * 512 + ch * 4;
#pragma unroll 1
          for (int o = 0; o < 4; o++) {
            float tp = 0.f;
#pragma unroll 1
            for (int i = 0; i < 4; i++) {
              float A = 0.f;
#pragma unroll
              for (int f = 0; f < 4; f++) A += yy[f] * bf2f(cg[i * 16 + f * 4 + o]);
              tp += bf2f(xr[i]) * bf2f(f2bf(A));
            }
            float v = R * tp;
            acc[0] += o == 0 ? v : 0.f;
            acc[1] += o == 1 ? v : 0.f;
            acc[2] += o == 2 ? v : 0.f;
            acc[3] += o == 3 ? v : 0.f;
          }
        }
      }
      u32x4* rw = sl < 4 ? &rows[sl * 64] : &L.row5[w * 64];
      unsigned short* ar = reinterpret_cast<unsigned short*>(&rw[slice * 32]) + (lane >> 5) * 128 + (lane & 31);
#pragma unroll
      for (int o = 0; o < 4; o++) ar[o * 32] = f2bf(acc[o]);
    }
    asm volatile("" ::: "memory");
    if (lane == 0) atomicOr(&L.rdone, 1u << w);
  }
}

extern "C" __global__ __launch_bounds__(C_) void tfn_plain_kernel(
    const int* __restrict__ dst, const int* __restrict__ src, const unsigned short* __restrict__ Y,
    const unsigned short* __restrict__ ef, const unsigned short* __restrict__ x, unsigned short* __restrict__ out,
    const unsigned short* __restrict__ wrad, const unsigned short* __restrict__ cg, const unsigned short* __restrict__ wmix,
    long long E) {
  __shared__ f32x4 agg[C_];
  const int c = threadIdx.x, lane = c & 63;
  f32x4 acc = {0.f, 0.f, 0.f, 0.f};
#pragma unroll 1
  for (long long eb = 0; eb < E; eb += 64) {
    unsigned long long m = __ballot(eb + lane < E && dst[eb + lane < E ? eb + lane : 0] == (int)blockIdx.x);
#pragma unroll 1
    for (; m; m &= m - 1) {
      const long long e = eb + __builtin_ctzll(m);
      float z = 0.f;
#pragma unroll
      for (int k = 0; k < 4; k++) z += bf2f(ef[e * 4 + k]) * bf2f(wrad[k * C_ + c]);
      const float R = z / (1.f + __expf(-z));
#pragma unroll
      for (int i = 0; i < 4; i++) {
        const float xv = R * bf2f(x[(size_t)src[e] * 512 + c * 4 + i]);
#pragma unroll
        for (int f = 0; f < 4; f++)
#pragma unroll
          for (int o = 0; o < 4; o++) acc[o] += xv * bf2f(Y[e * 4 + f]) * bf2f(cg[i * 16 + f * 4 + o]);
      }
    }
  }
  agg[c] = acc;
  __syncthreads();
  f32x4 r = {0.f, 0.f, 0.f, 0.f};
#pragma unroll 4
  for (int k = 0; k < C_; k++) r += agg[k] * bf2f(wmix[k * C_ + c]);
  *reinterpret_cast<u32x2*>(out + (size_t)blockIdx.x * 512 + c * 4) = u32x2{pk2bf(r[0], r[1]), pk2bf(r[2], r[3])};
}

extern "C" void kernel_launch(void* const* d_in, const int* in_sizes, int n_in, void* d_out, int out_size,
                              void* d_ws, size_t ws_size, hipStream_t stream) {
  const unsigned short* x = (const unsigned short*)d_in[0];
  const int* src = (const int*)d_in[1];
  const int* dst = (const int*)d_in[2];
  const u32x2* Y = (const u32x2*)d_in[3];
  const u32x2* ef = (const u32x2*)d_in[4];
  const unsigned short* wrad = (const unsigned short*)d_in[5];
  const unsigned short* cg = (const unsigned short*)d_in[6];
  const unsigned short* wmix = (const unsigned short*)d_in[7];
  const int N = in_sizes[0] / 512;
  const int E = in_sizes[1];
  const int CE = ((E + NBMAX - 1) / NBMAX + 1) & ~1;
  const unsigned M2 = ((unsigned)NBMAX << 22) / (unsigned)N;
  if (N <= 64 || ((1u << 22) + M2 - 1) / M2 > (unsigned)NPB || CE > CH || ws_size < WS_END) {
    tfn_plain_kernel<<<N, C_, 0, stream>>>(dst, src, (const unsigned short*)Y, (const unsigned short*)ef, x,
                                           (unsigned short*)d_out, wrad, cg, wmix, E);
    return;
  }
  tfn_main_kernel<<<NBMAX, MT, 0, stream>>>(dst, E, 0x20000, src, CE, 0x20000, Y, N, 0x20000, ef, M2, 0x20000, x, (char*)d_ws,
                                           (u32x4*)d_out, wrad, cg, wmix);
}
```

```cpp
#include <hip/hip_runtime.h>
#include <hip/hip_bf16.h>
#include <stddef.h>
#include <stdint.h>

typedef __attribute__((ext_vector_type(2))) float f32x2;
typedef __attribute__((ext_vector_type(4))) float f32x4;
typedef __attribute__((ext_vector_type(2))) unsigned int u32x2;
typedef __attribute__((ext_vector_type(4))) unsigned int u32x4;
typedef __attribute__((ext_vector_type(8))) unsigned int u32x8;
typedef __attribute__((ext_vector_type(16))) unsigned int u32x16;
typedef __attribute__((ext_vector_type(2))) __bf16 bf16x2;

#define STREAM_ASM \
  "s_mov_b32 s63, 0x10000\n" \
  "Lh%=:\n" \
  "buffer_load_dwordx4 v[72:75], v136, %[ws], %[ho] offen sc1\n" \
  "v_mov_b32 v117, 0\n" \
  "v_mov_b32 v116, 64\n" \
  "v_mov_b32 v87, 0\n" \
  "v_mov_b64 v[44:45], 0\n" \
  "v_mov_b64 v[46:47], 0\n" \
  "v_mov_b32 v118, 0x7fc\n" \
  "s_waitcnt vmcnt(0)\n" \
  "v_add3_u32 v108, v72, v73, v74\n" \
  "v_xor_b32 v109, %[t0], v72\n" \
  "v_add_u32 v108, v108, v75\n" \
  "v_cmp_lt_u32 vcc, 0x3fffff, v109\n" \
  "v_cmp_ne_u32_e64 s[74:75], %[t1], v108\n" \
  "s_or_b64 vcc, vcc, s[74:75]\n" \
  "s_cbranch_scc1 Lhr%=\n" \
  "s_mov_b64 exec, -1\n" \
  "v_bfe_u32 v108, v72, 11, 11\n" \
  "v_and_b32 v109, v140, v108\n" \
  "v_and_b32 v112, 0x7ff, v72\n" \
  "v_sub_u32 v111, v108, v141\n" \
  "v_add_u32_dpp v109, v109, v109 row_shr:1 row_mask:0xf bank_mask:0xf bound_ctrl:1\n" \
  "v_lshl_add_u32 v112, v112, 2, v137\n" \
  "v_and_b32 v114, v140, v73\n" \
  "v_add_u32_dpp v109, v109, v109 row_shr:2 row_mask:0xf bank_mask:0xf bound_ctrl:1\n" \
  "v_and_b32 v115, v140, v74\n" \
  "v_sub_u32 v114, 0, v114\n" \
  "v_add_u32_dpp v109, v109, v109 row_shr:4 row_mask:0xf bank_mask:0xf bound_ctrl:1\n" \
  "v_sub_u32 v115, 0, v115\n" \
  "s_nop 0\n" \
  "v_add_u32_dpp v109, v109, v109 row_shr:8 row_mask:0xf bank_mask:0xf bound_ctrl:1\n" \
  "s_nop 1\n" \
  "v_add_u32_dpp v109, v109, v109 row_bcast:15 row_mask:0xa bank_mask:0xf\n" \
  "s_nop 1\n" \
  "v_add_u32_dpp v109, v109, v109 row_bcast:31 row_mask:0xc bank_mask:0xf\n" \
  "v_sub_u32 v108, v109, v108\n" \
  "v_readlane_b32 s62, v109, 63\n" \
  "v_lshl_add_u32 v113, v108, 2, v138\n" \
  "s_cmpk_gt_u32 s62, 0x100\n" \
  "s_cbranch_scc1 Lover%=\n" \
  "Lw%=:\n" \
  "v_cmpx_lt_i32 vcc, 0, v111\n" \
  "ds_write_b32 v113, v112 offset:0\n" \
  "v_add_u32 v112, 8, v112\n" \
  "v_cmpx_lt_i32 vcc, 2, v111\n" \
  "ds_write_b32 v113, v112 offset:8\n" \
  "v_add_u32 v112, 8, v112\n" \
  "v_cmpx_lt_i32 vcc, 4, v111\n" \
  "ds_write_b32 v113, v112 offset:16\n" \
  "v_add_u32 v112, 8, v112\n" \
  "v_cmpx_lt_i32 vcc, 6, v111\n" \
  "ds_write_b32 v113, v112 offset:24\n" \
  "v_add_u32 v112, 8, v112\n" \
  "v_cmpx_lt_i32 vcc, 8, v111\n" \
  "ds_write_b32 v113, v112 offset:32\n" \
  "v_add_u32 v112, 8, v112\n" \
  "s_cbranch_execz Lwd%=\n" \
  "v_cmpx_lt_i32 vcc, 10, v111\n" \
  "ds_write_b32 v113, v112 offset:40\n" \
  "v_add_u32 v112, 8, v112\n" \
  "s_cbranch_execz Lwd%=\n" \
  "v_cmpx_lt_i32 vcc, 12, v111\n" \
  "ds_write_b32 v113, v112 offset:48\n" \
  "v_add_u32 v112, 8, v112\n" \
  "s_cbranch_execz Lwd%=\n" \
  "v_cmpx_lt_i32 vcc, 14, v111\n" \
  "ds_write_b32 v113, v112 offset:56\n" \
  "v_add_u32 v112, 8, v112\n" \
  "v_add_u32 v113, 64, v113\n" \
  "v_subrev_u32 v111, 16, v111\n" \
  "s_branch Lw%=\n" \
  "Lwd%=:\n" \
  "s_mov_b64 exec, -1\n" \
  "Lld%=:\n" \
  "ds_read2st64_b32 v[76:77], v139 offset1:1\n" \
  "ds_read2st64_b32 v[78:79], v139 offset0:2 offset1:3\n" \
  "s_mov_b32 s63, 0x10000\n" \
  "s_waitcnt lgkmcnt(0)\n" \
  "buffer_load_dword v84, v76, %[ws], %[rk] offen sc1\n" \
  "buffer_load_dword v85, v77, %[ws], %[rk] offen sc1\n" \
  "buffer_load_dword v86, v78, %[ws], %[rk] offen sc1\n" \
  "s_cmpk_le_u32 s62, 0xc0\n" \
  "s_cbranch_scc1 Lka%=\n" \
  "buffer_load_dword v87, v79, %[ws], %[rk] offen sc1\n" \
  "Lka%=:\n" \
  "v_lshlrev_b32 v80, 2, v76\n" \
  "v_lshlrev_b32 v81, 2, v77\n" \
  "v_lshlrev_b32 v82, 2, v78\n" \
  "v_lshlrev_b32 v83, 2, v79\n" \
  "s_barrier\n" \
  "s_cmpk_le_u32 s62, 0xc0\n" \
  "s_cbranch_scc1 Lpa%=\n" \
  "buffer_load_dwordx4 v[44:47], v83, %[ws], %[rp] offen sc1\n" \
  "Lpa%=:\n" \
  "buffer_load_dwordx4 v[32:35], v80, %[ws], %[rp] offen sc1\n" \
  "buffer_load_dwordx4 v[36:39], v81, %[ws], %[rp] offen sc1\n" \
  "buffer_load_dwordx4 v[40:43], v82, %[ws], %[rp] offen sc1\n" \
  "v_cmp_le_i32_e64 s[66:67], 0, v80\n" \
  "v_cmp_le_i32_e64 s[68:69], 0, v81\n" \
  "v_cmp_le_i32_e64 s[70:71], 0, v82\n" \
  "v_cmp_le_i32_e64 s[72:73], 0, v83\n" \
  "v_mov_b64 v[48:49], 0\n" \
  "v_mov_b64 v[50:51], 0\n" \
  "v_mov_b64 v[52:53], 0\n" \
  "v_mov_b64 v[54:55], 0\n" \
  "v_mov_b64 v[56:57], 0\n" \
  "v_mov_b64 v[58:59], 0\n" \
  "v_mov_b64 v[60:61], 0\n" \
  "v_mov_b64 v[62:63], 0\n" \
  "v_mov_b64 v[64:65], 0\n" \
  "v_mov_b64 v[66:67], 0\n" \
  "v_mov_b64 v[68:69], 0\n" \
  "v_mov_b64 v[70:71], 0\n" \
  "s_waitcnt vmcnt(3)\n" \
  "Lk%=:\n" \
  "v_add3_u32 v108, v84, v85, v86\n" \
  "v_add3_u32 v108, v108, v87, v114\n" \
  "v_lshlrev_b32 v88, 1, v84\n" \
  "v_lshlrev_b32 v92, 4, v84\n" \
  "v_add_u32_dpp v108, v108, v108 row_shr:1 row_mask:0xf bank_mask:0xf bound_ctrl:1\n" \
  "v_lshrrev_b32 v104, 2, v84\n" \
  "v_and_or_b32 v88, v88, v118, %[wb]\n" \
  "v_add_u32_dpp v108, v108, v108 row_shr:2 row_mask:0xf bank_mask:0xf bound_ctrl:1\n" \
  "v_lshlrev_b32_e64 v100, v92, 4\n" \
  "v_and_b32 v104, 0xfc, v104\n" \
  "v_add_u32_dpp v108, v108, v108 row_shr:4 row_mask:0xf bank_mask:0xf bound_ctrl:1\n" \
  "v_lshlrev_b32 v89, 1, v85\n" \
  "v_lshlrev_b32 v93, 4, v85\n" \
  "v_add_u32_dpp v108, v108, v108 row_shr:8 row_mask:0xf bank_mask:0xf bound_ctrl:1\n" \
  "v_lshrrev_b32 v105, 2, v85\n" \
  "v_and_or_b32 v89, v89, v118, %[wb]\n" \
  "v_add_u32_dpp v108, v108, v108 row_bcast:15 row_mask:0xa bank_mask:0xf\n" \
  "v_lshlrev_b32_e64 v101, v93, 4\n" \
  "v_and_b32 v105, 0xfc, v105\n" \
  "v_add_u32_dpp v108, v108, v108 row_bcast:31 row_mask:0xc bank_mask:0xf\n" \
  "v_lshlrev_b32 v90, 1, v86\n" \
  "v_readlane_b32 s65, v108, 63\n" \
  "v_lshlrev_b32 v94, 4, v86\n" \
  "v_lshrrev_b32 v106, 2, v86\n" \
  "v_and_or_b32 v90, v90, v118, %[wb]\n" \
  "v_lshlrev_b32_e64 v102, v94, 4\n" \
  "v_and_b32 v106, 0xfc, v106\n" \
  "s_cmp_lg_u32 s65, 0\n" \
  "s_cbranch_scc1 Lkr%=\n" \
  "Lkc%=:\n" \
  "s_mov_b64 exec, s[66:67]\n" \
  "ds_add_rtn_u32 v96, v88, v100 offset:%[hi]\n" \
  "ds_add_u32 v104, v116 offset:%[dg]\n" \
  "s_mov_b64 exec, s[68:69]\n" \
  "ds_add_rtn_u32 v97, v89, v101 offset:%[hi]\n" \
  "ds_add_u32 v105, v116 offset:%[dg]\n" \
  "s_mov_b64 exec, s[70:71]\n" \
  "ds_add_rtn_u32 v98, v90, v102 offset:%[hi]\n" \
  "ds_add_u32 v106, v116 offset:%[dg]\n" \
  "s_cmpk_gt_u32 s62, 0xc0\n" \
  "s_mov_b64 exec, -1\n" \
  "s_cbranch_scc1 Lc3%=\n" \
  "Lc4%=:\n" \
  "v_and_b32 v100, v118, v88\n" \
  "v_and_b32 v101, v118, v89\n" \
  "v_and_b32 v102, v118, v90\n" \
  "s_waitcnt lgkmcnt(0)\n" \
  "s_barrier\n" \
  "ds_read_b32 v108, v149 offset:%[dg]\n" \
  "ds_read_b32 v109, v150 offset:%[dg]\n" \
  "ds_read2st64_b32 v[76:77], v142 offset0:0 offset1:8\n" \
  "ds_read2st64_b32 v[78:79], v142 offset0:16 offset1:24\n" \
  "ds_read2st64_b32 v[104:105], v142 offset0:32 offset1:40\n" \
  "ds_read2st64_b32 v[106:107], v142 offset0:48 offset1:56\n" \
  "v_and_b32 v84, 0xfffc00, v84\n" \
  "v_and_b32 v85, 0xfffc00, v85\n" \
  "v_and_b32 v86, 0xfffc00, v86\n" \
  "v_and_b32 v87, 0xfffc00, v87\n" \
  "v_mov_b32 v111, 0\n" \
  "v_mov_b32 v112, 63\n" \
  "s_waitcnt lgkmcnt(4)\n" \
  "v_readlane_b32 s74, v109, 0\n" \
  "v_readlane_b32 s76, v109, 1\n" \
  "v_readlane_b32 s78, v109, 2\n" \
  "s_nop 0\n" \
  "v_cmp_lt_i32_e64 s[74:75], s74, v108\n" \
  "v_cmp_lt_i32_e64 s[76:77], s76, v108\n" \
  "v_cmp_lt_i32_e64 s[78:79], s78, v108\n" \
  "s_bcnt1_i32_b64 s74, s[74:75]\n" \
  "s_bcnt1_i32_b64 s76, s[76:77]\n" \
  "s_bcnt1_i32_b64 s78, s[78:79]\n" \
  "v_writelane_b32 v112, s74, 0\n" \
  "v_writelane_b32 v112, s76, 1\n" \
  "v_writelane_b32 v112, s78, 2\n" \
  "v_lshlrev_b32 v112, 2, v112\n" \
  "s_waitcnt lgkmcnt(3)\n" \
  "ds_write2st64_b32 v142, v117, v76 offset1:8\n" \
  "v_add_u32 v77, v77, v76\n" \
  "s_waitcnt lgkmcnt(3)\n" \
  "v_add_u32 v78, v78, v77\n" \
  "ds_write2st64_b32 v142, v77, v78 offset0:16 offset1:24\n" \
  "v_add_u32 v79, v79, v78\n" \
  "s_waitcnt lgkmcnt(3)\n" \
  "v_add_u32 v104, v104, v79\n" \
  "ds_write2st64_b32 v142, v79, v104 offset0:32 offset1:40\n" \
  "v_add_u32 v105, v105, v104\n" \
  "s_waitcnt lgkmcnt(3)\n" \
  "v_add_u32 v106, v106, v105\n" \
  "ds_write2st64_b32 v142, v105, v106 offset0:48 offset1:56\n" \
  "v_add_u32 v107, v107, v106\n" \
  "ds_write_b32 v142, v107 offset:16384\n" \
  "ds_write_b32 v112, v150 offset:%[br]\n" \
  "s_waitcnt lgkmcnt(0)\n" \
  "s_barrier\n" \
  "ds_read_b32 v108, v143\n" \
  "s_mov_b32 s74, 0x1f1f1f1f\n" \
  "s_mov_b32 s75, 0x1f1f1f1f\n" \
  "s_waitcnt lgkmcnt(0)\n" \
  "v_lshl_add_u32 v76, v108, 3, v145\n" \
  "s_mov_b64 exec, s[74:75]\n" \
  "ds_read_b32 v111, v76\n" \
  "s_mov_b64 exec, -1\n" \
  "ds_write_b32 v148, v108\n" \
  "s_waitcnt lgkmcnt(1)\n" \
  "v_add_u32_dpp v112, v111, v111 row_shr:1 row_mask:0xf bank_mask:0xf bound_ctrl:1\n" \
  "v_sub_u32 v77, v146, v111\n" \
  "s_nop 0\n" \
  "v_add_u32_dpp v112, v112, v112 row_shr:2 row_mask:0xf bank_mask:0xf bound_ctrl:1\n" \
  "s_nop 1\n" \
  "v_add_u32_dpp v112, v112, v112 row_shr:4 row_mask:0xf bank_mask:0xf bound_ctrl:1\n" \
  "s_nop 1\n" \
  "v_add_u32_dpp v112, v112, v112 row_shr:8 row_mask:0xf bank_mask:0xf bound_ctrl:1\n" \
  "s_nop 1\n" \
  "v_add_u32_dpp v112, v112, v112 row_bcast:15 row_mask:0xa bank_mask:0xf\n" \
  "s_nop 1\n" \
  "v_add_u32_dpp v112, v112, v112 row_bcast:31 row_mask:0xc bank_mask:0xf\n" \
  "v_sub_u32 v111, v112, v111\n" \
  "v_readlane_b32 s65, v112, 63\n" \
  "v_mov_b32_dpp v78, v112 row_newbcast:7 row_mask:0xf bank_mask:0x3\n" \
  "v_mov_b32_dpp v79, v111 row_newbcast:0 row_mask:0xf bank_mask:0x3\n" \
  "v_add_u32 v77, v77, v112\n" \
  "v_mov_b32_dpp v78, v112 row_newbcast:15 row_mask:0xf bank_mask:0xc\n" \
  "v_mov_b32_dpp v79, v111 row_newbcast:8 row_mask:0xf bank_mask:0xc\n" \
  "s_and_b32 s64, s65, 0xffff\n" \
  "v_lshl_add_u32 v77, v78, 16, v77\n" \
  "s_lshr_b32 s65, s65, 16\n" \
  "v_add_u32_sdwa v77, v77, v79 dst_sel:DWORD dst_unused:UNUSED_PAD src0_sel:DWORD src1_sel:WORD_1\n" \
  "s_add_u32 s64, s64, s65\n" \
  "s_mov_b64 exec, s[74:75]\n" \
  "ds_write_b32 v76, v77\n" \
  "s_mov_b64 exec, -1\n" \
  "s_cmpk_gt_u32 s64, 864\n" \
  "s_cbranch_scc1 Llo%=\n" \
  "Llc%=:\n" \
  "s_waitcnt lgkmcnt(0)\n" \
  "s_barrier\n" \
  "ds_read_b32 v109, v117 offset:%[fl]\n" \
  "ds_read_b32 v76, v88 offset:%[hi]\n" \
  "ds_read_b32 v104, v100 offset:%[to]\n" \
  "ds_read_b32 v77, v89 offset:%[hi]\n" \
  "ds_read_b32 v105, v101 offset:%[to]\n" \
  "ds_read_b32 v78, v90 offset:%[hi]\n" \
  "ds_read_b32 v106, v102 offset:%[to]\n" \
  "v_add_u32 v113, s64, v147\n" \
  "v_bfrev_b32 v111, 1\n" \
  "s_max_u32 s60, s64, 4\n" \
  "s_sub_u32 s60, s60, 4\n" \
  "s_lshr_b32 s60, s60, 4\n" \
  "s_waitcnt lgkmcnt(4)\n" \
  "v_readfirstlane_b32 s61, v109\n" \
  "v_add3_u32 v88, v76, v104, v96\n" \
  "v_lshrrev_b32 v88, v92, v88\n" \
  "v_bfe_u32 v72, v88, 13, 3\n" \
  "v_and_b32 v88, 0x1ffc, v88\n" \
  "v_lshl_or_b32 v72, v72, 1, v84\n" \
  "s_cmp_lg_u32 s61, 0\n" \
  "s_cbranch_scc1 Lslow%=\n" \
  "s_waitcnt lgkmcnt(0)\n" \
  "v_add3_u32 v89, v77, v105, v97\n" \
  "v_add3_u32 v90, v78, v106, v98\n" \
  "v_lshrrev_b32 v89, v93, v89\n" \
  "v_lshrrev_b32 v90, v94, v90\n" \
  "v_bfe_u32 v73, v89, 13, 3\n" \
  "v_bfe_u32 v74, v90, 13, 3\n" \
  "v_and_b32 v89, 0x1ffc, v89\n" \
  "v_and_b32 v90, 0x1ffc, v90\n" \
  "v_lshl_or_b32 v73, v73, 1, v85\n" \
  "v_lshl_or_b32 v74, v74, 1, v86\n" \
  "s_mov_b64 exec, s[66:67]\n" \
  "ds_write_b32 v88, v72 offset:%[so]\n" \
  "s_mov_b64 exec, s[68:69]\n" \
  "ds_write_b32 v89, v73 offset:%[so]\n" \
  "s_mov_b64 exec, s[70:71]\n" \
  "ds_write_b32 v90, v74 offset:%[so]\n" \
  "s_cmpk_gt_u32 s62, 0xc0\n" \
  "s_cbranch_scc1 Lq3%=\n" \
  "Lq4%=:\n" \
  "s_mov_b32 exec_lo, -1\n" \
  "s_mov_b32 exec_hi, 0\n" \
  "ds_write_b32 v113, v111 offset:%[so]\n" \
  "s_mov_b64 exec, -1\n" \
  "v_lshlrev_b32 v88, 2, v88\n" \
  "v_lshlrev_b32 v89, 2, v89\n" \
  "v_lshlrev_b32 v90, 2, v90\n" \
  "v_lshlrev_b32 v91, 2, v91\n" \
  "v_lshlrev_b32 v113, 2, v113\n" \
  "s_mov_b32 s63, 0x10000\n" \
  "s_waitcnt vmcnt(0)\n" \
  "Lpc%=:\n" \
  "s_mov_b64 exec, s[66:67]\n" \
  "ds_write_b128 v88, v[32:35] offset:%[pa]\n" \
  "s_mov_b64 exec, s[68:69]\n" \
  "ds_write_b128 v89, v[36:39] offset:%[pa]\n" \
  "s_mov_b64 exec, s[70:71]\n" \
  "ds_write_b128 v90, v[40:43] offset:%[pa]\n" \
  "s_mov_b64 exec, s[72:73]\n" \
  "ds_write_b128 v91, v[44:47] offset:%[pa]\n" \
  "s_mov_b32 exec_lo, -1\n" \
  "s_mov_b32 exec_hi, 0\n" \
  "ds_write_b128 v113, v[48:51] offset:%[pa]\n" \
  "s_mov_b64 exec, -1\n" \
  "v_add3_u32 v108, v32, v33, v115\n" \
  "v_add3_u32 v111, v34, v35, v36\n" \
  "v_add3_u32 v112, v37, v38, v39\n" \
  "v_add3_u32 v108, v108, v40, v41\n" \
  "v_add3_u32 v111, v111, v42, v43\n" \
  "v_add3_u32 v112, v112, v44, v45\n" \
  "v_add3_u32 v108, v108, v46, v47\n" \
  "v_add3_u32 v108, v108, v111, v112\n" \
  "s_nop 1\n" \
  "v_add_u32_dpp v108, v108, v108 row_shr:1 row_mask:0xf bank_mask:0xf bound_ctrl:1\n" \
  "s_nop 1\n" \
  "v_add_u32_dpp v108, v108, v108 row_shr:2 row_mask:0xf bank_mask:0xf bound_ctrl:1\n" \
  "s_nop 1\n" \
  "v_add_u32_dpp v108, v108, v108 row_shr:4 row_mask:0xf bank_mask:0xf bound_ctrl:1\n" \
  "s_nop 1\n" \
  "v_add_u32_dpp v108, v108, v108 row_shr:8 row_mask:0xf bank_mask:0xf bound_ctrl:1\n" \
  "s_nop 1\n" \
  "v_add_u32_dpp v108, v108, v108 row_bcast:15 row_mask:0xa bank_mask:0xf\n" \
  "s_nop 1\n" \
  "v_add_u32_dpp v108, v108, v108 row_bcast:31 row_mask:0xc bank_mask:0xf\n" \
  "s_nop 0\n" \
  "v_readlane_b32 s65, v108, 63\n" \
  "s_waitcnt lgkmcnt(0)\n" \
  "s_cmp_lg_u32 s65, 0\n" \
  "s_cbranch_scc1 Lpr%=\n" \
  "Lpw%=:\n" \
  "s_barrier\n" \
  "ds_read2_b32  v[10:11], v12 offset1:8\n" \
  "ds_read_b64 v[106:107], v14\n" \
  "ds_read_b64 v[104:105], v15\n" \
  "s_nop 0\n" \
  "s_setprio 1\n" \
  "v_mov_b64 v[32:33], 0\n" \
  "v_mov_b64 v[34:35], 0\n" \
  "v_mov_b64 v[36:37], 0\n" \
  "v_mov_b64 v[38:39], 0\n" \
  "v_mov_b64 v[40:41], 0\n" \
  "v_mov_b64 v[42:43], 0\n" \
  "v_mov_b64 v[44:45], 0\n" \
  "v_mov_b64 v[46:47], 0\n" \
  "s_waitcnt lgkmcnt(2)\n" \
  "v_readlane_b32 s80, v10, 0\n" \
  "v_readlane_b32 s81, v10, 1\n" \
  "v_readlane_b32 s82, v10, 2\n" \
  "v_readlane_b32 s83, v10, 3\n" \
  "v_readlane_b32 s84, v10, 4\n" \
  "v_readlane_b32 s85, v10, 5\n" \
  "s_andn2_b32 s61, s80, 15\n" \
  "buffer_load_dwordx4 v[120:123], v8, %[xs], s61 offen\n" \
  "s_andn2_b32 s61, s81, 15\n" \
  "buffer_load_dwordx4 v[124:127], v8, %[xs], s61 offen\n" \
  "s_andn2_b32 s61, s82, 15\n" \
  "buffer_load_dwordx4 v[128:131], v8, %[xs], s61 offen\n" \
  "s_andn2_b32 s61, s83, 15\n" \
  "buffer_load_dwordx4 v[132:135], v8, %[xs], s61 offen\n" \
  "s_andn2_b32 s61, s84, 15\n" \
  "buffer_load_dwordx4 v[136:139], v8, %[xs], s61 offen\n" \
  "v_mov_b64  v[96:97], 0\n" \
  "v_mov_b64  v[98:99], 0\n" \
  "v_mov_b64  v[100:101], 0\n" \
  "v_mov_b64  v[102:103], 0\n" \
  "s_waitcnt lgkmcnt(0)\n" \
  "v_mfma_f32_4x4x4_16b_bf16 v[80:83], v[104:105], v[28:29], 0 cbsz:4\n" \
  "v_mfma_f32_4x4x4_16b_bf16 v[84:87], v[104:105], v[30:31], 0 cbsz:4\n" \
  "v_mfma_f32_4x4x4_16b_bf16 v[112:115], v[106:107], v[20:21], 0\n" \
  "v_mfma_f32_4x4x4_16b_bf16 v[116:119], v[106:107], v[22:23], 0\n" \
  "v_mfma_f32_4x4x4_16b_bf16 v[72:75], v[106:107], v[24:25], 0\n" \
  "v_mfma_f32_4x4x4_16b_bf16 v[76:79], v[106:107], v[26:27], 0\n" \
  "v_cvt_pk_bf16_f32 v81, v80, v81\n" \
  "v_lshlrev_b32_sdwa v80, 1, v81 dst_sel:DWORD dst_unused:UNUSED_PAD src0_sel:DWORD src1_sel:WORD_0\n" \
  "v_lshlrev_b32_sdwa v81, 1, v81 dst_sel:DWORD dst_unused:UNUSED_PAD src0_sel:DWORD src1_sel:WORD_1\n" \
  "ds_read_u16_d16_hi v96, v80 offset:%[tb]\n" \
  "ds_read_u16_d16_hi v97, v81 offset:%[tb]\n" \
  "v_cvt_pk_bf16_f32 v85, v84, v85\n" \
  "v_lshlrev_b32_sdwa v84, 1, v85 dst_sel:DWORD dst_unused:UNUSED_PAD src0_sel:DWORD src1_sel:WORD_0\n" \
  "v_lshlrev_b32_sdwa v85, 1, v85 dst_sel:DWORD dst_unused:UNUSED_PAD src0_sel:DWORD src1_sel:WORD_1\n" \
  "ds_read_u16_d16_hi v98, v84 offset:%[tb]\n" \
  "ds_read_u16_d16_hi v99, v85 offset:%[tb]\n" \
  "v_cvt_pk_bf16_f32 v83, v82, v83\n" \
  "v_lshlrev_b32_sdwa v82, 1, v83 dst_sel:DWORD dst_unused:UNUSED_PAD src0_sel:DWORD src1_sel:WORD_0\n" \
  "v_lshlrev_b32_sdwa v83, 1, v83 dst_sel:DWORD dst_unused:UNUSED_PAD src0_sel:DWORD src1_sel:WORD_1\n" \
  "ds_read_u16_d16_hi v100, v82 offset:%[tb]\n" \
  "ds_read_u16_d16_hi v101, v83 offset:%[tb]\n" \
  "v_cvt_pk_bf16_f32 v108, v112, v116\n" \
  "v_cvt_pk_bf16_f32 v109, v72, v76\n" \
  "s_nop 0\n" \
  "s_waitcnt vmcnt(4)\n" \
  "v_mfma_f32_4x4x4_16b_bf16 v[72:75], v[108:109], v[120:121], 0 cbsz:4\n" \
  "v_mfma_f32_4x4x4_16b_bf16 v[76:79], v[108:109], v[122:123], 0 cbsz:4\n" \
  "s_andn2_b32 s61, s85, 15\n" \
  "buffer_load_dwordx4 v[140:143], v8, %[xs], s61 offen\n" \
  "s_waitcnt lgkmcnt(0)\n" \
  "s_nop 1\n" \
  "Lbody%=:\n" \
  "s_waitcnt lgkmcnt(2)\n" \
  "s_set_gpr_idx_on s80, gpr_idx(SRC2,DST)\n" \
  "v_pk_fma_f32 v[32:33], v[96:97], v[72:73], v[32:33] op_sel:[0,0,0] op_sel_hi:[0,1,1]\n" \
  "v_pk_fma_f32 v[42:43], v[96:97], v[74:75], v[42:43] op_sel:[0,0,0] op_sel_hi:[0,1,1]\n" \
  "v_pk_fma_f32 v[52:53], v[98:99], v[76:77], v[52:53] op_sel:[0,0,0] op_sel_hi:[0,1,1]\n" \
  "v_pk_fma_f32 v[62:63], v[98:99], v[78:79], v[62:63] op_sel:[0,0,0] op_sel_hi:[0,1,1]\n" \
  "s_set_gpr_idx_off\n" \
  "s_waitcnt vmcnt(4)\n" \
  "v_mfma_f32_4x4x4_16b_bf16 v[72:75], v[108:109], v[124:125], 0 cbsz:4 abid:1\n" \
  "v_mfma_f32_4x4x4_16b_bf16 v[76:79], v[108:109], v[126:127], 0 cbsz:4 abid:1\n" \
  "v_mfma_f32_4x4x4_16b_bf16 v[88:91], v[104:105], v[28:29], 0 cbsz:4 abid:1\n" \
  "v_mfma_f32_4x4x4_16b_bf16 v[92:95], v[104:105], v[30:31], 0 cbsz:4 abid:1\n" \
  "v_readlane_b32 s86, v10, 6\n" \
  "v_cvt_pk_bf16_f32 v87, v86, v87\n" \
  "v_lshlrev_b32_sdwa v86, 1, v87 dst_sel:DWORD dst_unused:UNUSED_PAD src0_sel:DWORD src1_sel:WORD_0\n" \
  "v_lshlrev_b32_sdwa v87, 1, v87 dst_sel:DWORD dst_unused:UNUSED_PAD src0_sel:DWORD src1_sel:WORD_1\n" \
  "s_andn2_b32 s61, s86, 15\n" \
  "buffer_load_dwordx4 v[144:147], v8, %[xs], s61 offen\n" \
  "ds_read_u16_d16_hi v102, v86 offset:%[tb]\n" \
  "ds_read_u16_d16_hi v103, v87 offset:%[tb]\n" \
  "v_mov_b32 v111, s60\n" \
  "ds_write2_b32 v110, v111, v111 offset1:8\n" \
  "s_set_gpr_idx_on s81, gpr_idx(SRC2,DST)\n" \
  "v_pk_fma_f32 v[32:33], v[96:97], v[72:73], v[32:33] op_sel:[1,0,0] op_sel_hi:[1,1,1]\n" \
  "v_pk_fma_f32 v[42:43], v[96:97], v[74:75], v[42:43] op_sel:[1,0,0] op_sel_hi:[1,1,1]\n" \
  "v_pk_fma_f32 v[52:53], v[98:99], v[76:77], v[52:53] op_sel:[1,0,0] op_sel_hi:[1,1,1]\n" \
  "v_pk_fma_f32 v[62:63], v[98:99], v[78:79], v[62:63] op_sel:[1,0,0] op_sel_hi:[1,1,1]\n" \
  "s_set_gpr_idx_off\n" \
  "s_waitcnt vmcnt(4)\n" \
  "v_mfma_f32_4x4x4_16b_bf16 v[72:75], v[108:109], v[128:129], 0 cbsz:4 abid:2\n" \
  "v_mfma_f32_4x4x4_16b_bf16 v[76:79], v[108:109], v[130:131], 0 cbsz:4 abid:2\n" \
  "v_readlane_b32 s87, v10, 7\n" \
  "v_cvt_pk_bf16_f32 v89, v88, v89\n" \
  "v_lshlrev_b32_sdwa v88, 1, v89 dst_sel:DWORD dst_unused:UNUSED_PAD src0_sel:DWORD src1_sel:WORD_0\n" \
  "v_lshlrev_b32_sdwa v89, 1, v89 dst_sel:DWORD dst_unused:UNUSED_PAD src0_sel:DWORD src1_sel:WORD_1\n" \
  "s_andn2_b32 s61, s87, 15\n" \
  "buffer_load_dwordx4 v[148:151], v8, %[xs], s61 offen\n" \
  "ds_read_u16_d16_hi v96, v88 offset:%[tb]\n" \
  "ds_read_u16_d16_hi v97, v89 offset:%[tb]\n" \
  "ds_read_b32 v111, v110 offset:16\n" \
  "s_waitcnt lgkmcnt(4)\n" \
  "s_set_gpr_idx_on s82, gpr_idx(SRC2,DST)\n" \
  "v_pk_fma_f32 v[32:33], v[100:101], v[72:73], v[32:33] op_sel:[0,0,0] op_sel_hi:[0,1,1]\n" \
  "v_pk_fma_f32 v[42:43], v[100:101], v[74:75], v[42:43] op_sel:[0,0,0] op_sel_hi:[0,1,1]\n" \
  "v_pk_fma_f32 v[52:53], v[102:103], v[76:77], v[52:53] op_sel:[0,0,0] op_sel_hi:[0,1,1]\n" \
  "v_pk_fma_f32 v[62:63], v[102:103], v[78:79], v[62:63] op_sel:[0,0,0] op_sel_hi:[0,1,1]\n" \
  "s_set_gpr_idx_off\n" \
  "s_waitcnt vmcnt(4) lgkmcnt(10)\n" \
  "v_mfma_f32_4x4x4_16b_bf16 v[72:75], v[108:109], v[132:133], 0 cbsz:4 abid:3\n" \
  "v_mfma_f32_4x4x4_16b_bf16 v[76:79], v[108:109], v[134:135], 0 cbsz:4 abid:3\n" \
  "v_readlane_b32 s80, v11, 0\n" \
  "v_cvt_pk_bf16_f32 v93, v92, v93\n" \
  "v_lshlrev_b32_sdwa v92, 1, v93 dst_sel:DWORD dst_unused:UNUSED_PAD src0_sel:DWORD src1_sel:WORD_0\n" \
  "v_lshlrev_b32_sdwa v93, 1, v93 dst_sel:DWORD dst_unused:UNUSED_PAD src0_sel:DWORD src1_sel:WORD_1\n" \
  "s_andn2_b32 s61, s80, 15\n" \
  "buffer_load_dwordx4 v[120:123], v8, %[xs], s61 offen\n" \
  "ds_read_u16_d16_hi v98, v92 offset:%[tb]\n" \
  "ds_read_u16_d16_hi v99, v93 offset:%[tb]\n" \
  "ds_read_b64 v[106:107], v14 offset:256\n" \
  "s_set_gpr_idx_on s83, gpr_idx(SRC2,DST)\n" \
  "v_pk_fma_f32 v[32:33], v[100:101], v[72:73], v[32:33] op_sel:[1,0,0] op_sel_hi:[1,1,1]\n" \
  "v_pk_fma_f32 v[42:43], v[100:101], v[74:75], v[42:43] op_sel:[1,0,0] op_sel_hi:[1,1,1]\n" \
  "v_pk_fma_f32 v[52:53], v[102:103], v[76:77], v[52:53] op_sel:[1,0,0] op_sel_hi:[1,1,1]\n" \
  "v_pk_fma_f32 v[62:63], v[102:103], v[78:79], v[62:63] op_sel:[1,0,0] op_sel_hi:[1,1,1]\n" \
  "s_set_gpr_idx_off\n" \
  "s_sub_u32 s60, s60, 1\n" \
  "s_cbranch_scc1 Lend%=\n" \
  "s_waitcnt vmcnt(4)\n" \
  "v_mfma_f32_4x4x4_16b_bf16 v[72:75], v[108:109], v[136:137], 0 cbsz:4 abid:4\n" \
  "v_mfma_f32_4x4x4_16b_bf16 v[76:79], v[108:109], v[138:139], 0 cbsz:4 abid:4\n" \
  "v_readlane_b32 s81, v11, 1\n" \
  "v_cvt_pk_bf16_f32 v91, v90, v91\n" \
  "v_lshlrev_b32_sdwa v90, 1, v91 dst_sel:DWORD dst_unused:UNUSED_PAD src0_sel:DWORD src1_sel:WORD_0\n" \
  "v_lshlrev_b32_sdwa v91, 1, v91 dst_sel:DWORD dst_unused:UNUSED_PAD src0_sel:DWORD src1_sel:WORD_1\n" \
  "s_andn2_b32 s61, s81, 15\n" \
  "buffer_load_dwordx4 v[124:127], v8, %[xs], s61 offen\n" \
  "ds_read_u16_d16_hi v100, v90 offset:%[tb]\n" \
  "ds_read_u16_d16_hi v101, v91 offset:%[tb]\n" \
  "v_add_u32 v14, 0x100, v14\n" \
  "s_waitcnt lgkmcnt(3)\n" \
  "s_set_gpr_idx_on s84, gpr_idx(SRC2,DST)\n" \
  "v_pk_fma_f32 v[32:33], v[96:97], v[72:73], v[32:33] op_sel:[0,0,0] op_sel_hi:[0,1,1]\n" \
  "v_pk_fma_f32 v[42:43], v[96:97], v[74:75], v[42:43] op_sel:[0,0,0] op_sel_hi:[0,1,1]\n" \
  "v_pk_fma_f32 v[52:53], v[98:99], v[76:77], v[52:53] op_sel:[0,0,0] op_sel_hi:[0,1,1]\n" \
  "v_pk_fma_f32 v[62:63], v[98:99], v[78:79], v[62:63] op_sel:[0,0,0] op_sel_hi:[0,1,1]\n" \
  "s_set_gpr_idx_off\n" \
  "s_waitcnt vmcnt(4)\n" \
  "v_mfma_f32_4x4x4_16b_bf16 v[72:75], v[108:109], v[140:141], 0 cbsz:4 abid:5\n" \
  "v_mfma_f32_4x4x4_16b_bf16 v[76:79], v[108:109], v[142:143], 0 cbsz:4 abid:5\n" \
  "v_mfma_f32_4x4x4_16b_bf16 v[80:83], v[104:105], v[28:29], 0 cbsz:4 abid:2\n" \
  "v_mfma_f32_4x4x4_16b_bf16 v[84:87], v[104:105], v[30:31], 0 cbsz:4 abid:2\n" \
  "v_readlane_b32 s82, v11, 2\n" \
  "v_cvt_pk_bf16_f32 v95, v94, v95\n" \
  "v_lshlrev_b32_sdwa v94, 1, v95 dst_sel:DWORD dst_unused:UNUSED_PAD src0_sel:DWORD src1_sel:WORD_0\n" \
  "v_lshlrev_b32_sdwa v95, 1, v95 dst_sel:DWORD dst_unused:UNUSED_PAD src0_sel:DWORD src1_sel:WORD_1\n" \
  "s_andn2_b32 s61, s82, 15\n" \
  "buffer_load_dwordx4 v[128:131], v8, %[xs], s61 offen\n" \
  "ds_read_u16_d16_hi v102, v94 offset:%[tb]\n" \
  "ds_read_u16_d16_hi v103, v95 offset:%[tb]\n" \
  "s_set_gpr_idx_on s85, gpr_idx(SRC2,DST)\n" \
  "v_pk_fma_f32 v[32:33], v[96:97], v[72:73], v[32:33] op_sel:[1,0,0] op_sel_hi:[1,1,1]\n" \
  "v_pk_fma_f32 v[42:43], v[96:97], v[74:75], v[42:43] op_sel:[1,0,0] op_sel_hi:[1,1,1]\n" \
  "v_pk_fma_f32 v[52:53], v[98:99], v[76:77], v[52:53] op_sel:[1,0,0] op_sel_hi:[1,1,1]\n" \
  "v_pk_fma_f32 v[62:63], v[98:99], v[78:79], v[62:63] op_sel:[1,0,0] op_sel_hi:[1,1,1]\n" \
  "s_set_gpr_idx_off\n" \
  "s_waitcnt vmcnt(4) lgkmcnt(7)\n" \
  "v_mfma_f32_4x4x4_16b_bf16 v[72:75], v[108:109], v[144:145], 0 cbsz:4 abid:6\n" \
  "v_mfma_f32_4x4x4_16b_bf16 v[76:79], v[108:109], v[146:147], 0 cbsz:4 abid:6\n" \
  "v_readlane_b32 s83, v11, 3\n" \
  "v_cvt_pk_bf16_f32 v81, v80, v81\n" \
  "v_lshlrev_b32_sdwa v80, 1, v81 dst_sel:DWORD dst_unused:UNUSED_PAD src0_sel:DWORD src1_sel:WORD_0\n" \
  "v_lshlrev_b32_sdwa v81, 1, v81 dst_sel:DWORD dst_unused:UNUSED_PAD src0_sel:DWORD src1_sel:WORD_1\n" \
  "s_andn2_b32 s61, s83, 15\n" \
  "buffer_load_dwordx4 v[132:135], v8, %[xs], s61 offen\n" \
  "ds_read_u16_d16_hi v96, v80 offset:%[tb]\n" \
  "ds_read_u16_d16_hi v97, v81 offset:%[tb]\n" \
  "v_readfirstlane_b32 s66, v111\n" \
  "s_setprio 1\n" \
  "s_add_u32 s67, s60, %[fo]\n" \
  "s_cmp_ge_u32 s67, s66\n" \
  "s_cbranch_scc0 Lf5_%=\n" \
  "s_setprio 2\n" \
  "Lf5_%=:\n" \
  "s_waitcnt lgkmcnt(2)\n" \
  "s_set_gpr_idx_on s86, gpr_idx(SRC2,DST)\n" \
  "v_pk_fma_f32 v[32:33], v[100:101], v[72:73], v[32:33] op_sel:[0,0,0] op_sel_hi:[0,1,1]\n" \
  "v_pk_fma_f32 v[42:43], v[100:101], v[74:75], v[42:43] op_sel:[0,0,0] op_sel_hi:[0,1,1]\n" \
  "v_pk_fma_f32 v[52:53], v[102:103], v[76:77], v[52:53] op_sel:[0,0,0] op_sel_hi:[0,1,1]\n" \
  "v_pk_fma_f32 v[62:63], v[102:103], v[78:79], v[62:63] op_sel:[0,0,0] op_sel_hi:[0,1,1]\n" \
  "s_set_gpr_idx_off\n" \
  "s_waitcnt vmcnt(4) lgkmcnt(6)\n" \
  "v_mfma_f32_4x4x4_16b_bf16 v[72:75], v[108:109], v[148:149], 0 cbsz:4 abid:7\n" \
  "v_mfma_f32_4x4x4_16b_bf16 v[76:79], v[108:109], v[150:151], 0 cbsz:4 abid:7\n" \
  "v_mfma_f32_4x4x4_16b_bf16 v[112:115], v[106:107], v[20:21], 0\n" \
  "v_readlane_b32 s84, v11, 4\n" \
  "v_cvt_pk_bf16_f32 v85, v84, v85\n" \
  "v_lshlrev_b32_sdwa v84, 1, v85 dst_sel:DWORD dst_unused:UNUSED_PAD src0_sel:DWORD src1_sel:WORD_0\n" \
  "v_lshlrev_b32_sdwa v85, 1, v85 dst_sel:DWORD dst_unused:UNUSED_PAD src0_sel:DWORD src1_sel:WORD_1\n" \
  "s_andn2_b32 s61, s84, 15\n" \
  "buffer_load_dwordx4 v[136:139], v8, %[xs], s61 offen\n" \
  "ds_read_u16_d16_hi v98, v84 offset:%[tb]\n" \
  "ds_read_u16_d16_hi v99, v85 offset:%[tb]\n" \
  "ds_read_b32 v10, v12 offset:64\n" \
  "s_set_gpr_idx_on s87, gpr_idx(SRC2,DST)\n" \
  "v_pk_fma_f32 v[32:33], v[100:101], v[72:73], v[32:33] op_sel:[1,0,0] op_sel_hi:[1,1,1]\n" \
  "v_pk_fma_f32 v[42:43], v[100:101], v[74:75], v[42:43] op_sel:[1,0,0] op_sel_hi:[1,1,1]\n" \
  "v_pk_fma_f32 v[52:53], v[102:103], v[76:77], v[52:53] op_sel:[1,0,0] op_sel_hi:[1,1,1]\n" \
  "v_pk_fma_f32 v[62:63], v[102:103], v[78:79], v[62:63] op_sel:[1,0,0] op_sel_hi:[1,1,1]\n" \
  "s_set_gpr_idx_off\n" \
  "s_sub_u32 s60, s60, 1\n" \
  "s_cbranch_scc1 Lend%=\n" \
  "s_waitcnt vmcnt(4)\n" \
  "v_mfma_f32_4x4x4_16b_bf16 v[72:75], v[108:109], v[120:121], 0 cbsz:4 abid:8\n" \
  "v_mfma_f32_4x4x4_16b_bf16 v[76:79], v[108:109], v[122:123], 0 cbsz:4 abid:8\n" \
  "v_readlane_b32 s85, v11, 5\n" \
  "v_cvt_pk_bf16_f32 v83, v82, v83\n" \
  "v_lshlrev_b32_sdwa v82, 1, v83 dst_sel:DWORD dst_unused:UNUSED_PAD src0_sel:DWORD src1_sel:WORD_0\n" \
  "v_lshlrev_b32_sdwa v83, 1, v83 dst_sel:DWORD dst_unused:UNUSED_PAD src0_sel:DWORD src1_sel:WORD_1\n" \
  "s_andn2_b32 s61, s85, 15\n" \
  "buffer_load_dwordx4 v[140:143], v8, %[xs], s61 offen\n" \
  "ds_read_u16_d16_hi v100, v82 offset:%[tb]\n" \
  "ds_read_u16_d16_hi v101, v83 offset:%[tb]\n" \
  "v_add_u32 v12, 64, v12\n" \
  "s_waitcnt lgkmcnt(3)\n" \
  "s_set_gpr_idx_on s80, gpr_idx(SRC2,DST)\n" \
  "v_pk_fma_f32 v[32:33], v[96:97], v[72:73], v[32:33] op_sel:[0,0,0] op_sel_hi:[0,1,1]\n" \
  "v_pk_fma_f32 v[42:43], v[96:97], v[74:75], v[42:43] op_sel:[0,0,0] op_sel_hi:[0,1,1]\n" \
  "v_pk_fma_f32 v[52:53], v[98:99], v[76:77], v[52:53] op_sel:[0,0,0] op_sel_hi:[0,1,1]\n" \
  "v_pk_fma_f32 v[62:63], v[98:99], v[78:79], v[62:63] op_sel:[0,0,0] op_sel_hi:[0,1,1]\n" \
  "s_set_gpr_idx_off\n" \
  "s_waitcnt vmcnt(4)\n" \
  "v_mfma_f32_4x4x4_16b_bf16 v[72:75], v[108:109], v[124:125], 0 cbsz:4 abid:9\n" \
  "v_mfma_f32_4x4x4_16b_bf16 v[76:79], v[108:109], v[126:127], 0 cbsz:4 abid:9\n" \
  "v_mfma_f32_4x4x4_16b_bf16 v[88:91], v[104:105], v[28:29], 0 cbsz:4 abid:3\n" \
  "v_mfma_f32_4x4x4_16b_bf16 v[92:95], v[104:105], v[30:31], 0 cbsz:4 abid:3\n" \
  "v_readlane_b32 s86, v11, 6\n" \
  "v_cvt_pk_bf16_f32 v87, v86, v87\n" \
  "v_lshlrev_b32_sdwa v86, 1, v87 dst_sel:DWORD dst_unused:UNUSED_PAD src0_sel:DWORD src1_sel:WORD_0\n" \
  "v_lshlrev_b32_sdwa v87, 1, v87 dst_sel:DWORD dst_unused:UNUSED_PAD src0_sel:DWORD src1_sel:WORD_1\n" \
  "s_andn2_b32 s61, s86, 15\n" \
  "buffer_load_dwordx4 v[144:147], v8, %[xs], s61 offen\n" \
  "ds_read_u16_d16_hi v102, v86 offset:%[tb]\n" \
  "ds_read_u16_d16_hi v103, v87 offset:%[tb]\n" \
  "v_mov_b32 v111, s60\n" \
  "ds_write2_b32 v110, v111, v111 offset1:8\n" \
  "s_set_gpr_idx_on s81, gpr_idx(SRC2,DST)\n" \
  "v_pk_fma_f32 v[32:33], v[96:97], v[72:73], v[32:33] op_sel:[1,0,0] op_sel_hi:[1,1,1]\n" \
  "v_pk_fma_f32 v[42:43], v[96:97], v[74:75], v[42:43] op_sel:[1,0,0] op_sel_hi:[1,1,1]\n" \
  "v_pk_fma_f32 v[52:53], v[98:99], v[76:77], v[52:53] op_sel:[1,0,0] op_sel_hi:[1,1,1]\n" \
  "v_pk_fma_f32 v[62:63], v[98:99], v[78:79], v[62:63] op_sel:[1,0,0] op_sel_hi:[1,1,1]\n" \
  "s_set_gpr_idx_off\n" \
  "s_waitcnt vmcnt(4)\n" \
  "v_mfma_f32_4x4x4_16b_bf16 v[72:75], v[108:109], v[128:129], 0 cbsz:4 abid:10\n" \
  "v_mfma_f32_4x4x4_16b_bf16 v[76:79], v[108:109], v[130:131], 0 cbsz:4 abid:10\n" \
  "v_readlane_b32 s87, v11, 7\n" \
  "v_cvt_pk_bf16_f32 v89, v88, v89\n" \
  "v_lshlrev_b32_sdwa v88, 1, v89 dst_sel:DWORD dst_unused:UNUSED_PAD src0_sel:DWORD src1_sel:WORD_0\n" \
  "v_lshlrev_b32_sdwa v89, 1, v89 dst_sel:DWORD dst_unused:UNUSED_PAD src0_sel:DWORD src1_sel:WORD_1\n" \
  "s_andn2_b32 s61, s87, 15\n" \
  "buffer_load_dwordx4 v[148:151], v8, %[xs], s61 offen\n" \
  "ds_read_u16_d16_hi v96, v88 offset:%[tb]\n" \
  "ds_read_u16_d16_hi v97, v89 offset:%[tb]\n" \
  "ds_read_b64 v[104:105], v15 offset:256\n" \
  "ds_read_b32 v111, v110 offset:16\n" \
  "s_waitcnt lgkmcnt(5)\n" \
  "s_set_gpr_idx_on s82, gpr_idx(SRC2,DST)\n" \
  "v_pk_fma_f32 v[32:33], v[100:101], v[72:73], v[32:33] op_sel:[0,0,0] op_sel_hi:[0,1,1]\n" \
  "v_pk_fma_f32 v[42:43], v[100:101], v[74:75], v[42:43] op_sel:[0,0,0] op_sel_hi:[0,1,1]\n" \
  "v_pk_fma_f32 v[52:53], v[102:103], v[76:77], v[52:53] op_sel:[0,0,0] op_sel_hi:[0,1,1]\n" \
  "v_pk_fma_f32 v[62:63], v[102:103], v[78:79], v[62:63] op_sel:[0,0,0] op_sel_hi:[0,1,1]\n" \
  "s_set_gpr_idx_off\n" \
  "s_waitcnt vmcnt(4) lgkmcnt(9)\n" \
  "v_mfma_f32_4x4x4_16b_bf16 v[72:75], v[108:109], v[132:133], 0 cbsz:4 abid:11\n" \
  "v_mfma_f32_4x4x4_16b_bf16 v[76:79], v[108:109], v[134:135], 0 cbsz:4 abid:11\n" \
  "v_mfma_f32_4x4x4_16b_bf16 v[116:119], v[106:107], v[22:23], 0\n" \
  "v_readlane_b32 s80, v10, 0\n" \
  "v_cvt_pk_bf16_f32 v93, v92, v93\n" \
  "v_lshlrev_b32_sdwa v92, 1, v93 dst_sel:DWORD dst_unused:UNUSED_PAD src0_sel:DWORD src1_sel:WORD_0\n" \
  "v_lshlrev_b32_sdwa v93, 1, v93 dst_sel:DWORD dst_unused:UNUSED_PAD src0_sel:DWORD src1_sel:WORD_1\n" \
  "s_andn2_b32 s61, s80, 15\n" \
  "buffer_load_dwordx4 v[120:123], v8, %[xs], s61 offen\n" \
  "ds_read_u16_d16_hi v98, v92 offset:%[tb]\n" \
  "ds_read_u16_d16_hi v99, v93 offset:%[tb]\n" \
  "s_set_gpr_idx_on s83, gpr_idx(SRC2,DST)\n" \
  "v_pk_fma_f32 v[32:33], v[100:101], v[72:73], v[32:33] op_sel:[1,0,0] op_sel_hi:[1,1,1]\n" \
  "v_pk_fma_f32 v[42:43], v[100:101], v[74:75], v[42:43] op_sel:[1,0,0] op_sel_hi:[1,1,1]\n" \
  "v_pk_fma_f32 v[52:53], v[102:103], v[76:77], v[52:53] op_sel:[1,0,0] op_sel_hi:[1,1,1]\n" \
  "v_pk_fma_f32 v[62:63], v[102:103], v[78:79], v[62:63] op_sel:[1,0,0] op_sel_hi:[1,1,1]\n" \
  "s_set_gpr_idx_off\n" \
  "s_sub_u32 s60, s60, 1\n" \
  "s_cbranch_scc1 Lend%=\n" \
  "s_waitcnt vmcnt(4)\n" \
  "v_mfma_f32_4x4x4_16b_bf16 v[72:75], v[108:109], v[136:137], 0 cbsz:4 abid:12\n" \
  "v_mfma_f32_4x4x4_16b_bf16 v[76:79], v[108:109], v[138:139], 0 cbsz:4 abid:12\n" \
  "v_readlane_b32 s81, v10, 1\n" \
  "v_cvt_pk_bf16_f32 v91, v90, v91\n" \
  "v_lshlrev_b32_sdwa v90, 1, v91 dst_sel:DWORD dst_unused:UNUSED_PAD src0_sel:DWORD src1_sel:WORD_0\n" \
  "v_lshlrev_b32_sdwa v91, 1, v91 dst_sel:DWORD dst_unused:UNUSED_PAD src0_sel:DWORD src1_sel:WORD_1\n" \
  "s_andn2_b32 s61, s81, 15\n" \
  "buffer_load_dwordx4 v[124:127], v8, %[xs], s61 offen\n" \
  "ds_read_u16_d16_hi v100, v90 offset:%[tb]\n" \
  "ds_read_u16_d16_hi v101, v91 offset:%[tb]\n" \
  "v_add_u32 v15, 0x100, v15\n" \
  "s_waitcnt lgkmcnt(2)\n" \
  "s_set_gpr_idx_on s84, gpr_idx(SRC2,DST)\n" \
  "v_pk_fma_f32 v[32:33], v[96:97], v[72:73], v[32:33] op_sel:[0,0,0] op_sel_hi:[0,1,1]\n" \
  "v_pk_fma_f32 v[42:43], v[96:97], v[74:75], v[42:43] op_sel:[0,0,0] op_sel_hi:[0,1,1]\n" \
  "v_pk_fma_f32 v[52:53], v[98:99], v[76:77], v[52:53] op_sel:[0,0,0] op_sel_hi:[0,1,1]\n" \
  "v_pk_fma_f32 v[62:63], v[98:99], v[78:79], v[62:63] op_sel:[0,0,0] op_sel_hi:[0,1,1]\n" \
  "s_set_gpr_idx_off\n" \
  "s_waitcnt vmcnt(4) lgkmcnt(5)\n" \
  "v_mfma_f32_4x4x4_16b_bf16 v[72:75], v[108:109], v[140:141], 0 cbsz:4 abid:13\n" \
  "v_mfma_f32_4x4x4_16b_bf16 v[76:79], v[108:109], v[142:143], 0 cbsz:4 abid:13\n" \
  "v_mfma_f32_4x4x4_16b_bf16 v[80:83], v[104:105], v[28:29], 0 cbsz:4\n" \
  "v_mfma_f32_4x4x4_16b_bf16 v[84:87], v[104:105], v[30:31], 0 cbsz:4\n" \
  "v_readlane_b32 s82, v10, 2\n" \
  "v_cvt_pk_bf16_f32 v95, v94, v95\n" \
  "v_lshlrev_b32_sdwa v94, 1, v95 dst_sel:DWORD dst_unused:UNUSED_PAD src0_sel:DWORD src1_sel:WORD_0\n" \
  "v_lshlrev_b32_sdwa v95, 1, v95 dst_sel:DWORD dst_unused:UNUSED_PAD src0_sel:DWORD src1_sel:WORD_1\n" \
  "s_andn2_b32 s61, s82, 15\n" \
  "buffer_load_dwordx4 v[128:131], v8, %[xs], s61 offen\n" \
  "ds_read_u16_d16_hi v102, v94 offset:%[tb]\n" \
  "ds_read_u16_d16_hi v103, v95 offset:%[tb]\n" \
  "s_set_gpr_idx_on s85, gpr_idx(SRC2,DST)\n" \
  "v_pk_fma_f32 v[32:33], v[96:97], v[72:73], v[32:33] op_sel:[1,0,0] op_sel_hi:[1,1,1]\n" \
  "v_pk_fma_f32 v[42:43], v[96:97], v[74:75], v[42:43] op_sel:[1,0,0] op_sel_hi:[1,1,1]\n" \
  "v_pk_fma_f32 v[52:53], v[98:99], v[76:77], v[52:53] op_sel:[1,0,0] op_sel_hi:[1,1,1]\n" \
  "v_pk_fma_f32 v[62:63], v[98:99], v[78:79], v[62:63] op_sel:[1,0,0] op_sel_hi:[1,1,1]\n" \
  "s_set_gpr_idx_off\n" \
  "s_waitcnt vmcnt(4) lgkmcnt(6)\n" \
  "v_mfma_f32_4x4x4_16b_bf16 v[72:75], v[108:109], v[144:145], 0 cbsz:4 abid:14\n" \
  "v_mfma_f32_4x4x4_16b_bf16 v[76:79], v[108:109], v[146:147], 0 cbsz:4 abid:14\n" \
  "v_readlane_b32 s83, v10, 3\n" \
  "v_cvt_pk_bf16_f32 v81, v80, v81\n" \
  "v_lshlrev_b32_sdwa v80, 1, v81 dst_sel:DWORD dst_unused:UNUSED_PAD src0_sel:DWORD src1_sel:WORD_0\n" \
  "v_lshlrev_b32_sdwa v81, 1, v81 dst_sel:DWORD dst_unused:UNUSED_PAD src0_sel:DWORD src1_sel:WORD_1\n" \
  "s_andn2_b32 s61, s83, 15\n" \
  "buffer_load_dwordx4 v[132:135], v8, %[xs], s61 offen\n" \
  "ds_read_u16_d16_hi v96, v80 offset:%[tb]\n" \
  "ds_read_u16_d16_hi v97, v81 offset:%[tb]\n" \
  "ds_read_b32 v11, v12 offset:32\n" \
  "v_readfirstlane_b32 s66, v111\n" \
  "s_setprio 1\n" \
  "s_add_u32 s67, s60, %[fo]\n" \
  "s_cmp_ge_u32 s67, s66\n" \
  "s_cbranch_scc0 Lf13_%=\n" \
  "s_setprio 2\n" \
  "Lf13_%=:\n" \
  "v_cvt_pk_bf16_f32 v19, v112, v116\n" \
  "s_waitcnt lgkmcnt(3)\n" \
  "s_set_gpr_idx_on s86, gpr_idx(SRC2,DST)\n" \
  "v_pk_fma_f32 v[32:33], v[100:101], v[72:73], v[32:33] op_sel:[0,0,0] op_sel_hi:[0,1,1]\n" \
  "v_pk_fma_f32 v[42:43], v[100:101], v[74:75], v[42:43] op_sel:[0,0,0] op_sel_hi:[0,1,1]\n" \
  "v_pk_fma_f32 v[52:53], v[102:103], v[76:77], v[52:53] op_sel:[0,0,0] op_sel_hi:[0,1,1]\n" \
  "v_pk_fma_f32 v[62:63], v[102:103], v[78:79], v[62:63] op_sel:[0,0,0] op_sel_hi:[0,1,1]\n" \
  "s_set_gpr_idx_off\n" \
  "s_waitcnt vmcnt(4)\n" \
  "v_mfma_f32_4x4x4_16b_bf16 v[112:115], v[106:107], v[24:25], 0\n" \
  "v_mfma_f32_4x4x4_16b_bf16 v[116:119], v[106:107], v[26:27], 0\n" \
  "v_mfma_f32_4x4x4_16b_bf16 v[72:75], v[108:109], v[148:149], 0 cbsz:4 abid:15\n" \
  "v_mfma_f32_4x4x4_16b_bf16 v[76:79], v[108:109], v[150:151], 0 cbsz:4 abid:15\n" \
  "v_readlane_b32 s84, v10, 4\n" \
  "v_cvt_pk_bf16_f32 v85, v84, v85\n" \
  "v_lshlrev_b32_sdwa v84, 1, v85 dst_sel:DWORD dst_unused:UNUSED_PAD src0_sel:DWORD src1_sel:WORD_0\n" \
  "v_lshlrev_b32_sdwa v85, 1, v85 dst_sel:DWORD dst_unused:UNUSED_PAD src0_sel:DWORD src1_sel:WORD_1\n" \
  "s_andn2_b32 s61, s84, 15\n" \
  "buffer_load_dwordx4 v[136:139], v8, %[xs], s61 offen\n" \
  "ds_read_u16_d16_hi v98, v84 offset:%[tb]\n" \
  "ds_read_u16_d16_hi v99, v85 offset:%[tb]\n" \
  "v_cvt_pk_bf16_f32 v109, v112, v116\n" \
  "v_mov_b32 v108, v19\n" \
  "s_set_gpr_idx_on s87, gpr_idx(SRC2,DST)\n" \
  "v_pk_fma_f32 v[32:33], v[100:101], v[72:73], v[32:33] op_sel:[1,0,0] op_sel_hi:[1,1,1]\n" \
  "v_pk_fma_f32 v[42:43], v[100:101], v[74:75], v[42:43] op_sel:[1,0,0] op_sel_hi:[1,1,1]\n" \
  "v_pk_fma_f32 v[52:53], v[102:103], v[76:77], v[52:53] op_sel:[1,0,0] op_sel_hi:[1,1,1]\n" \
  "v_pk_fma_f32 v[62:63], v[102:103], v[78:79], v[62:63] op_sel:[1,0,0] op_sel_hi:[1,1,1]\n" \
  "s_set_gpr_idx_off\n" \
  "s_sub_u32 s60, s60, 1\n" \
  "s_cbranch_scc1 Lend%=\n" \
  "s_waitcnt vmcnt(4)\n" \
  "v_mfma_f32_4x4x4_16b_bf16 v[72:75], v[108:109], v[120:121], 0 cbsz:4\n" \
  "v_mfma_f32_4x4x4_16b_bf16 v[76:79], v[108:109], v[122:123], 0 cbsz:4\n" \
  "v_readlane_b32 s85, v10, 5\n" \
  "v_cvt_pk_bf16_f32 v83, v82, v83\n" \
  "v_lshlrev_b32_sdwa v82, 1, v83 dst_sel:DWORD dst_unused:UNUSED_PAD src0_sel:DWORD src1_sel:WORD_0\n" \
  "v_lshlrev_b32_sdwa v83, 1, v83 dst_sel:DWORD dst_unused:UNUSED_PAD src0_sel:DWORD src1_sel:WORD_1\n" \
  "s_andn2_b32 s61, s85, 15\n" \
  "buffer_load_dwordx4 v[140:143], v8, %[xs], s61 offen\n" \
  "ds_read_u16_d16_hi v100, v82 offset:%[tb]\n" \
  "ds_read_u16_d16_hi v101, v83 offset:%[tb]\n" \
  "s_branch Lbody%=\n" \
  "Lend%=:\n" \
  "s_setprio 3\n" \
  "v_mov_b32 v20, 0\n" \
  "v_mov_b32 v21, %[bit]\n" \
  "v_cvt_pk_bf16_f32 v32, v32, v52\n" \
  "v_cvt_pk_bf16_f32 v33, v33, v53\n" \
  "v_cvt_pk_bf16_f32 v42, v42, v62\n" \
  "v_cvt_pk_bf16_f32 v43, v43, v63\n" \
  "v_cvt_pk_bf16_f32 v34, v34, v54\n" \
  "v_cvt_pk_bf16_f32 v35, v35, v55\n" \
  "v_cvt_pk_bf16_f32 v44, v44, v64\n" \
  "v_cvt_pk_bf16_f32 v45, v45, v65\n" \
  "ds_write2st64_b32 v152, v32, v34 offset0:0 offset1:4\n" \
  "ds_write2st64_b32 v153, v33, v35 offset0:0 offset1:4\n" \
  "ds_write2st64_b32 v154, v42, v44 offset0:0 offset1:4\n" \
  "ds_write2st64_b32 v155, v43, v45 offset0:0 offset1:4\n" \
  "v_cvt_pk_bf16_f32 v36, v36, v56\n" \
  "v_cvt_pk_bf16_f32 v37, v37, v57\n" \
  "v_cvt_pk_bf16_f32 v46, v46, v66\n" \
  "v_cvt_pk_bf16_f32 v47, v47, v67\n" \
  "v_cvt_pk_bf16_f32 v38, v38, v58\n" \
  "v_cvt_pk_bf16_f32 v39, v39, v59\n" \
  "v_cvt_pk_bf16_f32 v48, v48, v68\n" \
  "v_cvt_pk_bf16_f32 v49, v49, v69\n" \
  "ds_write2st64_b32 v152, v36, v38 offset0:8 offset1:12\n" \
  "ds_write2st64_b32 v153, v37, v39 offset0:8 offset1:12\n" \
  "ds_write2st64_b32 v154, v46, v48 offset0:8 offset1:12\n" \
  "ds_write2st64_b32 v155, v47, v49 offset0:8 offset1:12\n" \
  "v_cvt_pk_bf16_f32 v40, v40, v60\n" \
  "v_cvt_pk_bf16_f32 v41, v41, v61\n" \
  "v_cvt_pk_bf16_f32 v50, v50, v70\n" \
  "v_cvt_pk_bf16_f32 v51, v51, v71\n" \
  "ds_write2_b32 v156, v40, v41 offset0:0 offset1:16\n" \
  "ds_write2_b32 v156, v50, v51 offset0:32 offset1:48\n" \
  "s_mov_b64 exec, 1\n" \
  "ds_or_b32 v20, v21 offset:%[rd]\n" \
  "s_endpgm\n" \
  "Lhr%=:\n" \
  "s_mov_b64 exec, vcc\n" \
  "s_sub_u32 s63, s63, 1\n" \
  "s_cbranch_scc0 Lh%=\n" \
  "s_mov_b64 exec, -1\n" \
  "Lover%=:\n" \
  "v_mov_b32 v113, 1\n" \
  "ds_write_b32 v117, v113 offset:%[fl]\n" \
  "s_mov_b32 s62, 0\n" \
  "v_mov_b32 v114, 0\n" \
  "v_mov_b32 v115, 0\n" \
  "s_branch Lld%=\n" \
  "Lkr%=:\n" \
  "buffer_load_dword v84, v76, %[ws], %[rk] offen sc1\n" \
  "buffer_load_dword v85, v77, %[ws], %[rk] offen sc1\n" \
  "buffer_load_dword v86, v78, %[ws], %[rk] offen sc1\n" \
  "s_cmpk_le_u32 s62, 0xc0\n" \
  "s_cbranch_scc1 Lkb%=\n" \
  "buffer_load_dword v87, v79, %[ws], %[rk] offen sc1\n" \
  "Lkb%=:\n" \
  "s_waitcnt vmcnt(0)\n" \
  "s_sub_u32 s63, s63, 1\n" \
  "s_cbranch_scc0 Lk%=\n" \
  "v_mov_b32 v113, 1\n" \
  "ds_write_b32 v117, v113 offset:%[fl]\n" \
  "s_branch Lkc%=\n" \
  "Lc3%=:\n" \
  "v_lshlrev_b32 v91, 1, v87\n" \
  "v_lshlrev_b32 v95, 4, v87\n" \
  "v_lshrrev_b32 v107, 2, v87\n" \
  "v_and_or_b32 v91, v91, v118, %[wb]\n" \
  "v_lshlrev_b32_e64 v103, v95, 4\n" \
  "v_and_b32 v107, 0xfc, v107\n" \
  "s_mov_b64 exec, s[72:73]\n" \
  "ds_add_rtn_u32 v99, v91, v103 offset:%[hi]\n" \
  "ds_add_u32 v107, v116 offset:%[dg]\n" \
  "s_mov_b64 exec, -1\n" \
  "s_branch Lc4%=\n" \
  "Llo%=:\n" \
  "v_mov_b32 v113, 1\n" \
  "ds_write_b32 v117, v113 offset:%[fl]\n" \
  "s_branch Llc%=\n" \
  "Lslow%=:\n" \
  "s_waitcnt vmcnt(0) lgkmcnt(0)\n" \
  "s_barrier\n" \
  "s_branch Lexit%=\n" \
  "Lq3%=:\n" \
  "s_mov_b64 exec, -1\n" \
  "v_and_b32 v103, 0x7fc, v91\n" \
  "ds_read_b32 v79, v91 offset:%[hi]\n" \
  "ds_read_b32 v107, v103 offset:%[to]\n" \
  "s_waitcnt lgkmcnt(0)\n" \
  "v_add3_u32 v91, v79, v107, v99\n" \
  "v_lshrrev_b32 v91, v95, v91\n" \
  "v_bfe_u32 v75, v91, 13, 3\n" \
  "v_and_b32 v91, 0x1ffc, v91\n" \
  "v_lshl_or_b32 v75, v75, 1, v87\n" \
  "s_mov_b64 exec, s[72:73]\n" \
  "ds_write_b32 v91, v75 offset:%[so]\n" \
  "s_branch Lq4%=\n" \
  "Lpr%=:\n" \
  "s_cmpk_le_u32 s62, 0xc0\n" \
  "s_cbranch_scc1 Lpb%=\n" \
  "buffer_load_dwordx4 v[44:47], v83, %[ws], %[rp] offen sc1\n" \
  "Lpb%=:\n" \
  "buffer_load_dwordx4 v[32:35], v80, %[ws], %[rp] offen sc1\n" \
  "buffer_load_dwordx4 v[36:39], v81, %[ws], %[rp] offen sc1\n" \
  "buffer_load_dwordx4 v[40:43], v82, %[ws], %[rp] offen sc1\n" \
  "s_sub_u32 s63, s63, 1\n" \
  "s_waitcnt vmcnt(0)\n" \
  "s_cbranch_scc0 Lpc%=\n" \
  "s_branch Lpw%=\n" \
  "Lexit%=:\n" \
  "s_mov_b32 %[sl], s61\n"
#define STREAM_CLOBBERS \
  "v7", "v9", "v10", "v11", "v18", "v19", "v32", "v33", "v34", "v35", "v36", "v37", "v38", "v39", "v40", "v41", \
  "v42", "v43", "v44", "v45", "v46", "v47", "v48", "v49", "v50", "v51", "v52", "v53", "v54", "v55", "v56", \
  "v57", "v58", "v59", "v60", "v61", "v62", "v63", "v64", "v65", "v66", "v67", "v68", "v69", "v70", "v71", \
  "v72", "v73", "v74", "v75", "v76", "v77", "v78", "v79", "v80", "v81", "v82", "v83", "v84", "v85", "v86", \
  "v87", "v88", "v89", "v90", "v91", "v92", "v93", "v94", "v95", "v96", "v97", "v98", "v99", "v100", "v101", \
  "v102", "v103", "v104", "v105", "v106", "v107", "v108", "v109", "v111", "v112", "v113", "v114", "v115", \
  "v116", "v117", "v118", "v119", "v120", "v121", "v122", "v123", "v124", "v125", "v126", "v127", "v128", \
  "v129", "v130", "v131", "v132", "v133", "v134", "v135", "s60", "s61", "s62", "s63", "s64", "s65", "s66", \
  "s67", "s68", "s69", "s70", "s71", "s72", "s73", "s74", "s75", "s76", "s77", "s78", "s79", "s80", "s81", \
  "s82", "s83", "s84", "s85", "s86", "s87", "scc", "vcc", "memory"
#define RKL 3
#define MIX_ASM \
  "v_mov_b32 v12, 0\n" \
  "s_movk_i32 s60, 0x3ff\n" \
  "Lz%=:\n" \
  "s_sleep %[sl]\n" \
  "ds_read_b32 v13, v1\n" \
  "ds_read_b32 v14, v12 offset:%[rd]\n" \
  "s_waitcnt lgkmcnt(0)\n" \
  "v_readfirstlane_b32 s61, v13\n" \
  "v_readfirstlane_b32 s62, v14\n" \
  "s_cmp_gt_u32 s61, 4\n" \
  "s_cselect_b32 s61, s62, 1\n" \
  "s_cmp_eq_u32 s61, 0\n" \
  "s_cbranch_scc1 Lz%=\n" \
  "s_setprio 1\n" \
  "Lp%=:\n" \
  "ds_read_b32 v13, v12 offset:%[rd]\n" \
  "s_waitcnt lgkmcnt(0)\n" \
  "v_readfirstlane_b32 s61, v13\n" \
  "s_andn2_b32 s62, 15, s61\n" \
  "s_cselect_b32 s62, 0, 0x100\n" \
  "s_andn2_b32 s63, 0xf0, s61\n" \
  "s_cselect_b32 s63, 0, 0x200\n" \
  "s_or_b32 s61, s61, s62\n" \
  "s_or_b32 s61, s61, s63\n" \
  "s_and_b32 s61, s61, s60\n" \
  "s_cbranch_scc1 Lgo%=\n" \
  "s_sleep 1\n" \
  "s_branch Lp%=\n" \
  "Lgo%=:\n" \
  "s_andn2_b32 s60, s60, s61\n" \
  "s_ff1_i32_b32 s62, s61\n" \
  "s_bitset0_b32 s61, s62\n" \
  "s_lshl_b32 s63, s62, 12\n" \
  "s_cmp_ge_u32 s62, 8\n" \
  "s_cselect_b32 s64, %[r5], 0\n" \
  "s_add_u32 s63, s63, s64\n" \
  "v_add_u32 v14, s63, v0\n" \
  "ds_read_b128 v[64:67], v14 offset:0\n" \
  "ds_read_b128 v[68:71], v14 offset:256\n" \
  "ds_read_b128 v[72:75], v14 offset:512\n" \
  "ds_read_b128 v[76:79], v14 offset:768\n" \
  "s_set_gpr_idx_on s62, gpr_idx(SRC0)\n" \
  "v_mov_b32 v16, v2\n" \
  "s_set_gpr_idx_off\n" \
  "s_cmp_eq_u32 s61, 0\n" \
  "s_cbranch_scc1 Ls%=\n" \
  "s_ff1_i32_b32 s62, s61\n" \
  "s_bitset0_b32 s61, s62\n" \
  "s_lshl_b32 s63, s62, 12\n" \
  "s_cmp_ge_u32 s62, 8\n" \
  "s_cselect_b32 s64, %[r5], 0\n" \
  "s_add_u32 s63, s63, s64\n" \
  "v_add_u32 v14, s63, v0\n" \
  "ds_read_b128 v[80:83], v14 offset:0\n" \
  "ds_read_b128 v[84:87], v14 offset:256\n" \
  "ds_read_b128 v[88:91], v14 offset:512\n" \
  "ds_read_b128 v[92:95], v14 offset:768\n" \
  "s_set_gpr_idx_on s62, gpr_idx(SRC0)\n" \
  "v_mov_b32 v17, v2\n" \
  "s_set_gpr_idx_off\n" \
  "s_waitcnt lgkmcnt(4)\n" \
  "v_mfma_f32_16x16x32_bf16 v[96:99], v[64:67], v[32:35], 0\n" \
  "v_mfma_f32_16x16x32_bf16 v[100:103], v[64:67], v[36:39], 0\n" \
  "v_mfma_f32_16x16x32_bf16 v[96:99], v[68:71], v[40:43], v[96:99]\n" \
  "v_mfma_f32_16x16x32_bf16 v[100:103], v[68:71], v[44:47], v[100:103]\n" \
  "v_mfma_f32_16x16x32_bf16 v[96:99], v[72:75], v[48:51], v[96:99]\n" \
  "v_mfma_f32_16x16x32_bf16 v[100:103], v[72:75], v[52:55], v[100:103]\n" \
  "v_mfma_f32_16x16x32_bf16 v[96:99], v[76:79], v[56:59], v[96:99]\n" \
  "v_mfma_f32_16x16x32_bf16 v[100:103], v[76:79], v[60:63], v[100:103]\n" \
  "Lb1_%=:\n" \
  "s_cmp_eq_u32 s61, 0\n" \
  "s_cbranch_scc1 Ll1_%=\n" \
  "s_ff1_i32_b32 s62, s61\n" \
  "s_bitset0_b32 s61, s62\n" \
  "s_lshl_b32 s63, s62, 12\n" \
  "s_cmp_ge_u32 s62, 8\n" \
  "s_cselect_b32 s64, %[r5], 0\n" \
  "s_add_u32 s63, s63, s64\n" \
  "v_add_u32 v14, s63, v0\n" \
  "ds_read_b128 v[64:67], v14 offset:0\n" \
  "ds_read_b128 v[68:71], v14 offset:256\n" \
  "ds_read_b128 v[72:75], v14 offset:512\n" \
  "ds_read_b128 v[76:79], v14 offset:768\n" \
  "s_waitcnt lgkmcnt(4)\n" \
  "v_mfma_f32_16x16x32_bf16 v[104:107], v[80:83], v[32:35], 0\n" \
  "v_mfma_f32_16x16x32_bf16 v[108:111], v[80:83], v[36:39], 0\n" \
  "v_cvt_pk_bf16_f32 v96, v96, v97\n" \
  "v_cvt_pk_bf16_f32 v97, v98, v99\n" \
  "v_mfma_f32_16x16x32_bf16 v[104:107], v[84:87], v[40:43], v[104:107]\n" \
  "v_cvt_pk_bf16_f32 v98, v100, v101\n" \
  "v_cvt_pk_bf16_f32 v99, v102, v103\n" \
  "v_mfma_f32_16x16x32_bf16 v[108:111], v[84:87], v[44:47], v[108:111]\n" \
  "buffer_store_dwordx4 v[96:99], v16, %[os], 0 offen sc1\n" \
  "v_mfma_f32_16x16x32_bf16 v[104:107], v[88:91], v[48:51], v[104:107]\n" \
  "v_mfma_f32_16x16x32_bf16 v[108:111], v[88:91], v[52:55], v[108:111]\n" \
  "s_set_gpr_idx_on s62, gpr_idx(SRC0)\n" \
  "v_mov_b32 v16, v2\n" \
  "s_set_gpr_idx_off\n" \
  "v_mfma_f32_16x16x32_bf16 v[104:107], v[92:95], v[56:59], v[104:107]\n" \
  "v_mfma_f32_16x16x32_bf16 v[108:111], v[92:95], v[60:63], v[108:111]\n" \
  "Lb0_%=:\n" \
  "s_cmp_eq_u32 s61, 0\n" \
  "s_cbranch_scc1 Ll0_%=\n" \
  "s_ff1_i32_b32 s62, s61\n" \
  "s_bitset0_b32 s61, s62\n" \
  "s_lshl_b32 s63, s62, 12\n" \
  "s_cmp_ge_u32 s62, 8\n" \
  "s_cselect_b32 s64, %[r5], 0\n" \
  "s_add_u32 s63, s63, s64\n" \
  "v_add_u32 v14, s63, v0\n" \
  "ds_read_b128 v[80:83], v14 offset:0\n" \
  "ds_read_b128 v[84:87], v14 offset:256\n" \
  "ds_read_b128 v[88:91], v14 offset:512\n" \
  "ds_read_b128 v[92:95], v14 offset:768\n" \
  "s_waitcnt lgkmcnt(4)\n" \
  "v_mfma_f32_16x16x32_bf16 v[96:99], v[64:67], v[32:35], 0\n" \
  "v_mfma_f32_16x16x32_bf16 v[100:103], v[64:67], v[36:39], 0\n" \
  "v_cvt_pk_bf16_f32 v104, v104, v105\n" \
  "v_cvt_pk_bf16_f32 v105, v106, v107\n" \
  "v_mfma_f32_16x16x32_bf16 v[96:99], v[68:71], v[40:43], v[96:99]\n" \
  "v_cvt_pk_bf16_f32 v106, v108, v109\n" \
  "v_cvt_pk_bf16_f32 v107, v110, v111\n" \
  "v_mfma_f32_16x16x32_bf16 v[100:103], v[68:71], v[44:47], v[100:103]\n" \
  "buffer_store_dwordx4 v[104:107], v17, %[os], 0 offen sc1\n" \
  "v_mfma_f32_16x16x32_bf16 v[96:99], v[72:75], v[48:51], v[96:99]\n" \
  "v_mfma_f32_16x16x32_bf16 v[100:103], v[72:75], v[52:55], v[100:103]\n" \
  "s_set_gpr_idx_on s62, gpr_idx(SRC0)\n" \
  "v_mov_b32 v17, v2\n" \
  "s_set_gpr_idx_off\n" \
  "v_mfma_f32_16x16x32_bf16 v[96:99], v[76:79], v[56:59], v[96:99]\n" \
  "v_mfma_f32_16x16x32_bf16 v[100:103], v[76:79], v[60:63], v[100:103]\n" \
  "s_branch Lb1_%=\n" \
  "Ll1_%=:\n" \
  "s_waitcnt lgkmcnt(0)\n" \
  "v_mfma_f32_16x16x32_bf16 v[104:107], v[80:83], v[32:35], 0\n" \
  "v_mfma_f32_16x16x32_bf16 v[108:111], v[80:83], v[36:39], 0\n" \
  "v_mfma_f32_16x16x32_bf16 v[104:107], v[84:87], v[40:43], v[104:107]\n" \
  "v_mfma_f32_16x16x32_bf16 v[108:111], v[84:87], v[44:47], v[108:111]\n" \
  "v_mfma_f32_16x16x32_bf16 v[104:107], v[88:91], v[48:51], v[104:107]\n" \
  "v_cvt_pk_bf16_f32 v96, v96, v97\n" \
  "v_cvt_pk_bf16_f32 v97, v98, v99\n" \
  "v_mfma_f32_16x16x32_bf16 v[108:111], v[88:91], v[52:55], v[108:111]\n" \
  "v_cvt_pk_bf16_f32 v98, v100, v101\n" \
  "v_cvt_pk_bf16_f32 v99, v102, v103\n" \
  "v_mfma_f32_16x16x32_bf16 v[104:107], v[92:95], v[56:59], v[104:107]\n" \
  "buffer_store_dwordx4 v[96:99], v16, %[os], 0 offen sc1\n" \
  "v_mfma_f32_16x16x32_bf16 v[108:111], v[92:95], v[60:63], v[108:111]\n" \
  "s_cmp_lg_u32 s60, 0\n" \
  "s_nop 5\n" \
  "v_cvt_pk_bf16_f32 v104, v104, v105\n" \
  "v_cvt_pk_bf16_f32 v105, v106, v107\n" \
  "v_cvt_pk_bf16_f32 v106, v108, v109\n" \
  "v_cvt_pk_bf16_f32 v107, v110, v111\n" \
  "buffer_store_dwordx4 v[104:107], v17, %[os], 0 offen sc1\n" \
  "s_cbranch_scc1 Lp%=\n" \
  "s_branch Le%=\n" \
  "Ll0_%=:\n" \
  "s_waitcnt lgkmcnt(0)\n" \
  "v_mfma_f32_16x16x32_bf16 v[96:99], v[64:67], v[32:35], 0\n" \
  "v_mfma_f32_16x16x32_bf16 v[100:103], v[64:67], v[36:39], 0\n" \
  "v_mfma_f32_16x16x32_bf16 v[96:99], v[68:71], v[40:43], v[96:99]\n" \
  "v_mfma_f32_16x16x32_bf16 v[100:103], v[68:71], v[44:47], v[100:103]\n" \
  "v_mfma_f32_16x16x32_bf16 v[96:99], v[72:75], v[48:51], v[96:99]\n" \
  "v_cvt_pk_bf16_f32 v104, v104, v105\n" \
  "v_cvt_pk_bf16_f32 v105, v106, v107\n" \
  "v_mfma_f32_16x16x32_bf16 v[100:103], v[72:75], v[52:55], v[100:103]\n" \
  "v_cvt_pk_bf16_f32 v106, v108, v109\n" \
  "v_cvt_pk_bf16_f32 v107, v110, v111\n" \
  "v_mfma_f32_16x16x32_bf16 v[96:99], v[76:79], v[56:59], v[96:99]\n" \
  "buffer_store_dwordx4 v[104:107], v17, %[os], 0 offen sc1\n" \
  "v_mfma_f32_16x16x32_bf16 v[100:103], v[76:79], v[60:63], v[100:103]\n" \
  "s_cmp_lg_u32 s60, 0\n" \
  "s_nop 5\n" \
  "v_cvt_pk_bf16_f32 v96, v96, v97\n" \
  "v_cvt_pk_bf16_f32 v97, v98, v99\n" \
  "v_cvt_pk_bf16_f32 v98, v100, v101\n" \
  "v_cvt_pk_bf16_f32 v99, v102, v103\n" \
  "buffer_store_dwordx4 v[96:99], v16, %[os], 0 offen sc1\n" \
  "s_cbranch_scc1 Lp%=\n" \
  "s_branch Le%=\n" \
  "Ls%=:\n" \
  "s_waitcnt lgkmcnt(0)\n" \
  "v_mfma_f32_16x16x32_bf16 v[96:99], v[64:67], v[32:35], 0\n" \
  "v_mfma_f32_16x16x32_bf16 v[100:103], v[64:67], v[36:39], 0\n" \
  "v_mfma_f32_16x16x32_bf16 v[96:99], v[68:71], v[40:43], v[96:99]\n" \
  "v_mfma_f32_16x16x32_bf16 v[100:103], v[68:71], v[44:47], v[100:103]\n" \
  "v_mfma_f32_16x16x32_bf16 v[96:99], v[72:75], v[48:51], v[96:99]\n" \
  "v_mfma_f32_16x16x32_bf16 v[100:103], v[72:75], v[52:55], v[100:103]\n" \
  "v_mfma_f32_16x16x32_bf16 v[96:99], v[76:79], v[56:59], v[96:99]\n" \
  "v_mfma_f32_16x16x32_bf16 v[100:103], v[76:79], v[60:63], v[100:103]\n" \
  "s_cmp_lg_u32 s60, 0\n" \
  "s_nop 5\n" \
  "v_cvt_pk_bf16_f32 v96, v96, v97\n" \
  "v_cvt_pk_bf16_f32 v97, v98, v99\n" \
  "v_cvt_pk_bf16_f32 v98, v100, v101\n" \
  "v_cvt_pk_bf16_f32 v99, v102, v103\n" \
  "buffer_store_dwordx4 v[96:99], v16, %[os], 0 offen sc1\n" \
  "s_cbranch_scc1 Lp%=\n" \
  "Le%=:\n"
#define MIX_CLOBBERS \
  "v16", "v17", "v64", "v65", "v66", "v67", "v68", "v69", "v70", "v71", "v72", "v73", "v74", "v75", "v76", \
  "v77", "v78", "v79", "v80", "v81", "v82", "v83", "v84", "v85", "v86", "v87", "v88", "v89", "v90", "v91", \
  "v92", "v93", "v94", "v95", "v96", "v97", "v98", "v99", "v100", "v101", "v102", "v103", "v104", "v105", \
  "v106", "v107", "v108", "v109", "v110", "v111", "s60", "s61", "s62", "s63", "s64", "scc", "vcc", "memory"


namespace {

constexpr int C_ = 128;
constexpr int MW = 12;
constexpr int MT = MW * 64;
constexpr int SW = 8;
constexpr int NS = 5;
constexpr int NPB = SW * NS;
constexpr int NPASS = 16;
constexpr int HS = 64 * NPASS / 2;
constexpr int NBMAX = 256;
constexpr int PW = 5;
constexpr int PT = PW * 64;
constexpr int PK = 4;
constexpr int CH = PT * PK;
constexpr int MK = 4;
constexpr int CAPW = 256;
constexpr int HWV = 10;
constexpr int PFN = 5;
constexpr int TB2 = 1;
constexpr int WA = 4;
constexpr int FO1 = 2;
constexpr int WSL = 10;
constexpr int TB3 = 1;
constexpr int HSL = 24;
constexpr unsigned WS_EPOCH = 0;
constexpr unsigned WS_OFF = 4096;
constexpr unsigned WS_RECK = WS_OFF + NBMAX * NBMAX * 16;
constexpr unsigned WS_RECP = WS_RECK + NBMAX * CH * 4;
constexpr unsigned WS_END = WS_RECP + NBMAX * CH * 16;

__device__ __forceinline__ float bf2f(unsigned short v) { return __uint_as_float(((unsigned)v) << 16); }
__device__ __forceinline__ unsigned short f2bf(float f) {
  __hip_bfloat16 b = __float2bfloat16(f);
  return *reinterpret_cast<unsigned short*>(&b);
}
__device__ __forceinline__ unsigned pk2bf(float lo, float hi) {
  return __builtin_bit_cast(unsigned, __builtin_convertvector(f32x2{lo, hi}, bf16x2));
}
__device__ __forceinline__ unsigned wave_incl_scan(unsigned x) {
  x += __builtin_amdgcn_update_dpp(0u, x, 0x111, 0xf, 0xf, false);
  x += __builtin_amdgcn_update_dpp(0u, x, 0x112, 0xf, 0xf, false);
  x += __builtin_amdgcn_update_dpp(0u, x, 0x114, 0xf, 0xf, false);
  x += __builtin_amdgcn_update_dpp(0u, x, 0x118, 0xf, 0xf, false);
  x += __builtin_amdgcn_update_dpp(0u, x, 0x142, 0xa, 0xf, false);
  x += __builtin_amdgcn_update_dpp(0u, x, 0x143, 0xc, 0xf, false);
  return x;
}

struct LdsLow {
  u32x4 sink[16];
  unsigned flag, rdone, go, pad_;
  unsigned long long tag, pad2_;
  unsigned prog[16];
  int deg[64];
  unsigned byrank[64];
  unsigned nl[64];
  u32x4 ct[72];
  union {
    struct {
      __attribute__((aligned(16))) unsigned so[SW * CAPW];
      union {
        __attribute__((aligned(16))) unsigned hist[(SW + 1) * HS];
        u32x4 row5[SW * 64];
      };
    };
    struct {
      __attribute__((aligned(16))) unsigned sk[CH];
      u32x4 sp[CH];
    };
  };
  union {
    struct {
      __attribute__((aligned(16))) unsigned seg[2][NBMAX];
      unsigned phist[PW][NBMAX / 2];
      unsigned pbase[PW][NBMAX / 2];
      u32x4 ri[SW][MK * 16];
    };
    u32x4 pay[SW * CAPW];
  };
};
constexpr int TBLK = 0xc300 / 8;
struct Lds : LdsLow {
  char pad_end[163840 - TBLK * 16 - sizeof(LdsLow)];
  u32x4 sg[TBLK];
};
constexpr int TOFF = 163840 - TBLK * 16;
constexpr int WBLK = 0x4180 / 8;
static_assert(sizeof(Lds) == 163840 && TOFF < 65536, "LDS layout");

__device__ __forceinline__ unsigned lds_addr(const void* p) {
  return (unsigned)(uintptr_t)(__attribute__((address_space(3))) const void*)p;
}
__device__ __forceinline__ u32x4 make_srd(const void* p, unsigned num) {
  unsigned long long a = (unsigned long long)p;
  return u32x4{(unsigned)a, (unsigned)(a >> 32), num, 0x00020000u};
}

}

extern "C" __global__ __launch_bounds__(MT, 3) void tfn_main_kernel(
    const int* dst_, int E_, int c0_, const int* src_, int CE_, int c1_, const u32x2* Y_, int N_, int c2_, const u32x2* ef_,
    unsigned M2_, int c3_, const unsigned short* x_, char* ws_, u32x4* out_, const unsigned short* wrad_,
    const unsigned short* cg_, const unsigned short* wmix_) {
  __shared__ Lds L;
  const int tid = threadIdx.x, lane = tid & 63;
  u32x2 d0, d1, s0, s1;
  u32x4 y0, y1, f0, f1;
  u32x16 ka;
  u32x8 kb, kc;
  u32x4 kd;
  unsigned long long ev;
  asm volatile(
      "s_load_dwordx16 s[8:23], %[kp], 0\n s_load_dwordx8 s[24:31], %[kp], 0x40\n s_load_dwordx4 s[32:35], %[kp], 0x60\n"
      "v_readfirstlane_b32 s36, %[tid]\n v_lshlrev_b32 v90, 3, %[tid]\n v_mov_b64 v[92:93], 0\n"
      "s_and_b32 s37, %[bid], 7\n s_lshr_b32 s38, %[bid], 3\n s_lshr_b32 s36, s36, 6\n"
      "ds_write_b64 v90, v[92:93] offset:%[zo]\n"
      "s_cmp_lg_u32 s36, %[pw]\n s_cbranch_scc1 1f\n"
      "s_getpc_b64 s[44:45]\n .Ltfn_p0:\n s_max_i32 s46, 0, .Ltfn_p1-.Ltfn_p0\n s_mov_b32 m0, %[sk]\n s_mov_b32 s47, 0x20000\n"
      "v_lshlrev_b32 v91, 12, %[tid]\n s_lshl_b32 s39, s38, 7\n v_and_b32 v91, 0x3f000, v91\n"
      "buffer_load_dword v91, s[44:47], s39 offen lds\n"
      "1:\n s_lshl_b32 s37, s37, 5\n s_add_u32 s37, s37, s38\n v_lshlrev_b32 v91, 4, %[tid]\n"
      "s_waitcnt lgkmcnt(0)\n"
      "s_mul_i32 s38, s37, s14\n s_mov_b32 s40, s10\n s_add_i32 s39, s38, s14\n s_lshl_b32 s44, s38, 2\n"
      "s_min_i32 s39, s39, s10\n s_add_i32 s45, s44, %[h4]\n s_lshl_b32 s10, s39, 2\n s_lshl_b32 s14, s39, 2\n"
      "s_cmp_ge_u32 s36, %[pw]\n s_cbranch_scc1 2f\n"
      "buffer_load_dwordx2 %0, v90, s[8:11], s44 offen\n buffer_load_dwordx2 %1, v90, s[8:11], s45 offen\n"
      "buffer_load_dwordx2 %2, v90, s[12:15], s44 offen\n buffer_load_dwordx2 %3, v90, s[12:15], s45 offen\n"
      "2:\n s_mov_b32 s41, s18\n s_mov_b32 s42, s22\n s_lshl_b32 s18, s39, 3\n s_lshl_b32 s22, s39, 3\n"
      "s_lshl_b32 s44, s44, 1\n s_lshl_b32 s45, s45, 1\n s_cmp_ge_u32 s36, %[pw]\n"
      "s_barrier\n s_cbranch_scc1 3f\n s_setprio 3\n"
      "buffer_load_dwordx4 v[100:103], v91, s[16:19], s44 offen\n buffer_load_dwordx4 v[104:107], v91, s[16:19], s45 offen\n"
      "buffer_load_dwordx4 v[108:111], v91, s[20:23], s44 offen\n buffer_load_dwordx4 v[112:115], v91, s[20:23], s45 offen\n"
      "3:\n s_cmp_lg_u32 s36, %[hw]\n s_cbranch_scc1 4f\n s_load_dwordx2 s[46:47], s[26:27], 0\n s_setprio 3\n"
      "s_waitcnt lgkmcnt(0)\n 4:"
      : "=&v"(d0), "=&v"(d1), "=&v"(s0), "=&v"(s1), "=&{v[100:103]}"(y0), "=&{v[104:107]}"(y1), "=&{v[108:111]}"(f0),
        "=&{v[112:115]}"(f1), "=&{s[8:23]}"(ka), "=&{s[24:31]}"(kb), "=&{s[32:35]}"(kd), "=&{s[36:43]}"(kc),
        "=&{s[46:47]}"(ev)
      : [kp] "s"(__builtin_amdgcn_kernarg_segment_ptr()), [tid] "v"(tid), [bid] "s"(blockIdx.x), [pw] "n"(PW), [hw] "n"(HWV),
        [zo] "n"(offsetof(LdsLow, seg)), [sk] "n"(offsetof(LdsLow, sink)), [h4] "n"(CH * 2)
      : "v90", "v91", "v92", "v93", "s44", "s45", "scc", "memory");
  typedef __attribute__((address_space(1))) char gchar;
  auto ptr = [](unsigned lo, unsigned hi) { return (char*)(gchar*)(lo | (unsigned long long)hi << 32); };
  const int* __restrict__ dst = (const int*)ptr(ka[0], ka[1]);
  const int* __restrict__ src = (const int*)ptr(ka[4], ka[5]);
  const u32x2* __restrict__ Y = (const u32x2*)ptr(ka[8], ka[9]);
  const u32x2* __restrict__ ef = (const u32x2*)ptr(ka[12], ka[13]);
  const unsigned short* __restrict__ x = (const unsigned short*)ptr(kb[0], kb[1]);
  char* ws = ptr(kb[2], kb[3]);
  u32x4* __restrict__ out = (u32x4*)ptr(kb[4], kb[5]);
  const unsigned short* __restrict__ wrad = (const unsigned short*)ptr(kb[6], kb[7]);
  const unsigned short* __restrict__ cg = (const unsigned short*)ptr(kd[0], kd[1]);
  const unsigned short* __restrict__ wmix = (const unsigned short*)ptr(kd[2], kd[3]);
  const int w = kc[0], bucket = kc[1], e0 = kc[2] + tid * 2, e1 = kc[3], E = kc[4], N = kc[5];
  const unsigned M2 = kc[6];
  __builtin_assume(w >= 0 && w < MW && bucket >= 0 && bucket < NBMAX);
  const int xcd = blockIdx.x & 7;
  const u32x4 wsd = make_srd(ws, WS_END);
  auto codes = [](unsigned q) { return q * 0x80008u + u32x4{0x10000u, 0x30002u, 0x50004u, 0x70006u}; };
  auto table = [&](int q) {
    u32x4 p, n;
#pragma unroll
    for (int i = 0; i < 4; i++) {
      const f32x2 z = {__uint_as_float((q * 8 + 2 * i) << 16), __uint_as_float((q * 8 + 2 * i + 1) << 16)};
      const f32x2 e = {__builtin_amdgcn_exp2f(-1.4426950408889634f * z[0]), __builtin_amdgcn_exp2f(-1.4426950408889634f * z[1])};
      const f32x2 r = z * f32x2{__builtin_amdgcn_rcpf(1.f + e[0]), __builtin_amdgcn_rcpf(1.f + e[1])};
      p[i] = pk2bf(r[0], r[1]);
      n[i] = pk2bf(-e[0] * r[0], -e[1] * r[1]);
    }
    if (q < WBLK) L.sg[q] = p;
    return n;
  };
  auto halves = [&](int q0, int q1, int st) {
#pragma unroll 1
    for (int q = q0; q < q1; q += st) {
      const u32x4 c = codes(q - 16);
      L.sg[q] = c;
      if (q >= 0x30) L.sg[q + 0x1000] = c | 0x80008000u;
    }
  };
  auto valid = [&](int k) { return e0 + (k >> 1) * (CH / 2) + (k & 1) < e1; };
  auto warm = [&](int i0, int i1) {
    const unsigned pfo = ((xcd * N) >> 3) * 1024 + ((blockIdx.x >> 3) * 64 + lane) * 128;
#pragma unroll 1
    for (int i = i0; i < i1; i += MW - SW)
      asm volatile("s_mov_b32 m0, %2\n s_nop 0\n buffer_load_dword %0, %1, %3 offen lds\n"
                   :: "v"(pfo), "s"(make_srd(x, N * 1024)), "s"(lds_addr(L.sink)), "s"(i * 32 * 64 * 128) : "memory");
  };
  unsigned bk[PK], pr[PK], ky[PK], sa[PK];
  unsigned cv, wq4[4];
  asm volatile(
      "s_cmp_lg_u32 %9, %10\n s_cbranch_scc1 1f\n global_load_ushort %0, %5, %7\n global_load_dword %1, %6, %8\n"
      "global_load_dword %2, %6, %8 offset:256\n global_load_dword %3, %6, %8 offset:512\n"
      "global_load_dword %4, %6, %8 offset:768\n 1:"
      : "=&v"(cv), "=&v"(wq4[0]), "=&v"(wq4[1]), "=&v"(wq4[2]), "=&v"(wq4[3])
      : "v"(lane * 2), "v"(lane * 4), "s"(cg), "s"(wrad), "s"(w), "n"(PW) : "scc");
  auto wdma = [&](int i0, int i1) {
#pragma unroll
    for (int i = i0; i < i1; i++) {
      const unsigned o = (i + (blockIdx.x >> 3)) & 7;
      asm volatile("s_mov_b32 m0, %2\n s_nop 0\n buffer_load_dwordx4 %0, %1, %3 offen lds\n"
                   :: "v"((lane >> 2) * 256 + (lane & 3) * 16), "s"(make_srd(wmix, C_ * C_ * 2)),
                      "s"(lds_addr(&L.sg[WBLK]) + ((w - SW) * 8 + o) * 1024), "s"(o * 4096 + (w - SW) * 64) : "memory");
    }
  };
  const int zt = tid - PT - (w > HWV ? 64 : 0);
  unsigned so1 = (tid - PT) * 16 + WS_RECP + bucket * (CH * 16);
  unsigned so0 = w < PW ? tid * 16 + WS_RECK + bucket * (CH * 4) : so1;
  so1 += MT * 16;
  unsigned ho0 = w < SW ? ((lane >> 1) * SW + w) * (NBMAX * 16) : lane * 16 + WS_OFF + bucket * (NBMAX * 16);
  int node0 = __builtin_amdgcn_readfirstlane(((unsigned)(bucket << 22) + M2 - 1) / M2);
  int nn = min(N, __builtin_amdgcn_readfirstlane(((unsigned)((bucket + 1) << 22) + M2 - 1) / M2)) - node0;
  asm volatile("" : "+v"(so0), "+v"(so1), "+v"(ho0), "+s"(node0), "+s"(nn));
  if (w < PW) {
    unsigned one[PK], far[PK];
#pragma unroll
    for (int k = 0; k < PK; k++) {
      one[k] = valid(k) ? 1u : 0u;
      far[k] = valid(k) ? 0u : (0x10000u | lane) << 2;
    }
    asm volatile("s_waitcnt vmcnt(4)" : "+v"(d0), "+v"(d1), "+v"(s0), "+v"(s1), "+v"(one[0]), "+v"(one[1]), "+v"(one[2]),
                 "+v"(one[3]), "+v"(far[0]), "+v"(far[1]), "+v"(far[2]), "+v"(far[3]));
    const u32x4 dv = {d0[0], d0[1], d1[0], d1[1]}, sv = {s0[0], s0[1], s1[0], s1[1]};
#pragma unroll
    for (int k = 0; k < PK; k++) {
      bk[k] = ((unsigned)__umul24(dv[k], M2) >> 22) | (far[k] & 255);
      pr[k] = atomicAdd(&L.phist[w][bk[k] >> 1], one[k] << ((bk[k] * 16) & 31));
    }
    __builtin_amdgcn_sched_barrier(0);
#pragma unroll
    for (int k = 0; k < PK; k++) {
      ky[k] = (sv[k] << 10) | ((dv[k] & 63) << 4) | ((((unsigned)__umul24(sv[k], M2) >> 26) - ((bk[k] >> 4) & 14)) & 15);
      ky[k] = __umul24(ky[k], one[k]);
      sa[k] = lds_addr(&L.seg[0][bk[k]]);
      asm volatile("ds_add_u32 %0, %1" :: "v"(sa[k]), "v"(ky[k]) : "memory");
    }
#pragma unroll
    for (int k = 0; k < PK; k++) {
      pr[k] = __builtin_amdgcn_ubfe(pr[k], bk[k] * 16, 16) + far[k];
      bk[k] = lds_addr(L.pbase[w]) + bk[k] * 2;
      asm volatile("" : "+v"(pr[k]), "+v"(bk[k]));
    }
  } else {
    if (w == PW) {
      L.deg[lane] = ((lane - node0) & 63) < NPB ? 63 - ((lane - node0) & 63) : -1;
      if (lane < 4) (&L.flag)[lane] = 0;
      if (lane < 16) L.prog[lane] = 255;
    }
    if (w == HWV) {
      ev = ev * 6364136223846793005ull + 1442695040888963407ull;
      L.tag = ev;
    } else {
      if (zt < (0x4300 - 0x3a80) / 8) L.sg[0xba80 / 8 + zt] = table(0x3a80 / 8 + zt);
    }
  }
  asm volatile("s_waitcnt lgkmcnt(0)\n s_barrier" ::: "memory");
  const auto wsr = __builtin_amdgcn_make_buffer_rsrc((void*)ws, 0, WS_END, 0x00020000);
  if (w == HWV) {
    const unsigned tl = (unsigned)ev, th = (unsigned)(ev >> 32);
    unsigned c[4] = {0, 0, 0, 0};
    u32x4 hv[4];
#pragma unroll
    for (int k = 0; k < 4; k++) {
#pragma unroll
      for (int ww = 0; ww < PW; ww++) c[k] += reinterpret_cast<const unsigned short*>(L.phist[ww])[k * 64 + lane];
      hv[k][1] = L.seg[0][k * 64 + lane];
    }
    const unsigned p0 = c[0] | (c[1] << 16), p1 = c[2] | (c[3] << 16);
    const unsigned i0 = wave_incl_scan(p0), i1 = wave_incl_scan(p1);
    const unsigned t0 = __builtin_amdgcn_readlane(i0, 63), t1 = __builtin_amdgcn_readlane(i1, 63);
    const unsigned s1 = t0 & 0xffff, s2 = s1 + (t0 >> 16), s3 = s2 + (t1 & 0xffff);
    const unsigned bs[4] = {(i0 - p0) & 0xffff, ((i0 - p0) >> 16) + s1, ((i1 - p1) & 0xffff) + s2, ((i1 - p1) >> 16) + s3};
#pragma unroll
    for (int k = 0; k < 4; k++) {
      hv[k][0] = bs[k] | (c[k] << 11) | (tl << 22);
      hv[k][1] += (unsigned)__umul24(c[k], tl & 255) << 24;
      hv[k][3] = th - hv[k][0] - hv[k][1];
    }
    asm volatile("" : "+v"(hv[0]), "+v"(hv[1]), "+v"(hv[2]), "+v"(hv[3]));
    while (true) {
      const unsigned gv = __hip_atomic_load(&L.go, __ATOMIC_RELAXED, __HIP_MEMORY_SCOPE_WORKGROUP);
      asm volatile("" ::: "memory");
#pragma unroll
      for (int k = 0; k < 4; k++) hv[k][2] = L.seg[1][k * 64 + lane];
      asm volatile("" ::: "memory");
      if (__builtin_amdgcn_readfirstlane(gv) == PW) break;
    }
#pragma unroll
    for (int k = 0; k < 4; k++) {
      hv[k][3] -= hv[k][2];
      __builtin_amdgcn_raw_buffer_store_b128(hv[k], wsr, ho0 + k * 1024, 0, 16);
    }
    wdma(0, WA);
  } else if (w < PW) {
    const unsigned t8 = (unsigned)L.tag << 24;
    unsigned ca = 0, cb = 0, pa = 0, pb = 0, hh[PW][2];
#pragma unroll
    for (int ww = 0; ww < PW; ww++) {
      hh[ww][0] = L.phist[ww][lane];
      hh[ww][1] = L.phist[ww][64 + lane];
    }
    __builtin_amdgcn_sched_barrier(0);
#pragma unroll
    for (int ww = 0; ww < PW; ww++) {
      const unsigned ha = hh[ww][0], hb = hh[ww][1];
      pa = ww == w ? ca : pa;
      pb = ww == w ? cb : pb;
      ca += ha;
      cb += hb;
    }
    const unsigned t = __builtin_amdgcn_perm(cb, ca, 0x05040100) + __builtin_amdgcn_perm(cb, ca, 0x07060302);
    const unsigned inc = wave_incl_scan(t), ex = inc - t;
    L.pbase[w][lane] = (ex & 0xffff) * 0x10001u + (ca << 16) + pa;
    L.pbase[w][64 + lane] = ((ex >> 16) + (__builtin_amdgcn_readlane(inc, 63) & 0xffff)) * 0x10001u + (cb << 16) + pb;
    unsigned b4[PK];
#pragma unroll
    for (int k = 0; k < PK; k++) b4[k] = *(__attribute__((address_space(3))) unsigned short*)(uintptr_t)bk[k];
    __builtin_amdgcn_sched_barrier(0);
    {
      unsigned t0, t1, t2, t3;
      asm volatile(
          "s_waitcnt vmcnt(0)\n"
          "v_add3_u32 %0, v100, v101, v108\n v_add3_u32 %1, v102, v103, v110\n v_add3_u32 %2, v104, v105, v112\n"
          "v_add3_u32 %3, v106, v107, v114\n"
          "v_add_u32 %0, %0, v109\n v_add_u32 %1, %1, v111\n v_add_u32 %2, %2, v113\n v_add_u32 %3, %3, v115\n"
          "ds_add_u32 %8, %0 offset:1024\n ds_add_u32 %9, %1 offset:1024\n ds_add_u32 %10, %2 offset:1024\n"
          "ds_add_u32 %11, %3 offset:1024\n"
          "s_mov_b64 exec, 1\n ds_add_u32 %12, %13\n s_mov_b64 exec, -1"
          : "=&v"(t0), "=&v"(t1), "=&v"(t2), "=&v"(t3), "+{v[100:103]}"(y0), "+{v[104:107]}"(y1), "+{v[108:111]}"(f0),
            "+{v[112:115]}"(f1)
          : "v"(sa[0]), "v"(sa[1]), "v"(sa[2]), "v"(sa[3]), "v"(lds_addr(&L.go)), "v"(1) : "memory");
    }
#pragma unroll
    for (int k = 0; k < PK; k++) {
      pr[k] = min(pr[k] + b4[k], CH - 1u);
      L.sk[pr[k]] = ky[k] | t8;
      pr[k] = lds_addr(&L.sp[pr[k]]);
    }
    asm volatile(
        "ds_write2_b64 %0, v[100:101], v[108:109] offset1:1\n ds_write2_b64 %1, v[102:103], v[110:111] offset1:1\n"
        "ds_write2_b64 %2, v[104:105], v[112:113] offset1:1\n ds_write2_b64 %3, v[106:107], v[114:115] offset1:1\n"
        "s_waitcnt lgkmcnt(0)"
        :: "v"(pr[0]), "v"(pr[1]), "v"(pr[2]), "v"(pr[3]),
           "{v[100:103]}"(y0), "{v[104:107]}"(y1), "{v[108:111]}"(f0), "{v[112:115]}"(f1) : "memory");
  } else {
    if (w == PW) {
      asm volatile("s_waitcnt   vmcnt(0)" : "+v"(cv), "+v"(wq4[0]), "+v"(wq4[1]), "+v"(wq4[2]), "+v"(wq4[3]));
      reinterpret_cast<unsigned short*>(L.ct)[(lane & 3) * 16 + (lane >> 2)] = cv;
      L.ct[8 + lane] = u32x4{__builtin_amdgcn_perm(wq4[1], wq4[0], 0x05040100), __builtin_amdgcn_perm(wq4[3], wq4[2], 0x05040100),
                             __builtin_amdgcn_perm(wq4[1], wq4[0], 0x07060302), __builtin_amdgcn_perm(wq4[3], wq4[2], 0x07060302)};
    }
    if (w >= SW) wdma(0, WA);
    halves(0x20 + zt, 0x20 + TB2 * 384, 384);
    __builtin_amdgcn_s_setprio(3);
  }
  __syncthreads();
  {
    const u32x4 v0 = reinterpret_cast<const u32x4*>(L.sk)[tid], v1 = reinterpret_cast<const u32x4*>(L.sk)[MT + tid];
    __builtin_amdgcn_raw_buffer_store_b128(v0, wsr, so0, 0, 16);
    __builtin_amdgcn_raw_buffer_store_b128(v1, wsr, so1, 0, 16);
    if (w == PW)
      __builtin_amdgcn_raw_buffer_store_b128(reinterpret_cast<const u32x4*>(L.sk)[2 * MT + lane], wsr, so1 + (MT - PT) * 16, 0, 16);
  }
  __syncthreads();
  const unsigned long long epoch = L.tag;
  const unsigned tagl = __builtin_amdgcn_readfirstlane((unsigned)epoch);
  const unsigned tag1 = __builtin_amdgcn_readfirstlane((unsigned)(epoch >> 32));
  const int row = lane & 15, kq = lane >> 4;
  if (w >= SW) {
    int lane;
    asm volatile("v_mbcnt_lo_u32_b32 %0, -1, 0\n v_mbcnt_hi_u32_b32 %0, -1, %0" : "=v"(lane));
    __builtin_assume(lane >= 0 && lane < 64);
    const int h = w - SW, st = h * 64 + lane, row = lane & 15, kq = lane >> 4;
    __builtin_amdgcn_s_sleep(WSL);
    wdma(WA, 8);
    halves(0x20 + TB2 * 384 + TB3 * 512 + st, 0x3a80 / 8, 256);
    const int wq0 = WBLK + h * 512 + lane;
    u32x4 last = codes(wq0 + 448 < 0x1000 ? wq0 + 448 : wq0 + 432);
    if ((unsigned)(wq0 + 448 - 0x1000) < 0x20u) last = table(wq0 + 448 - 0x1000);
    constexpr int HR = (NPB - RKL * SW) / (MW - SW);
    const int id = (node0 + RKL * SW + h * HR + lane) & 63;
    asm volatile("s_waitcnt     vmcnt(0)" ::: "memory");
    u32x16 wa, wb;
    {
      const unsigned* wp = reinterpret_cast<const unsigned*>(&L.sg[WBLK + h * 512]) + kq * 128 + row;
#pragma unroll
      for (int i = 0; i < 16; i++) {
        const unsigned q0 = wp[(i >> 2) * 512 + (i & 3) * 32], q1 = wp[(i >> 2) * 512 + (i & 3) * 32 + 16];
        u32x16& d = i < 8 ? wa : wb;
        d[(i & 4) * 2 + (i & 3)] = __builtin_amdgcn_perm(q1, q0, 0x05040100);
        d[(i & 4) * 2 + (i & 3) + 4] = __builtin_amdgcn_perm(q1, q0, 0x07060302);
      }
    }
    asm volatile("" : "+v"(wa), "+v"(wb) :: "memory");
    asm volatile("s_barrier" ::: "memory");
    __builtin_amdgcn_s_sleep(2);
    warm(h, PFN);
#pragma unroll
    for (int i = 0; i < 8; i++) L.sg[wq0 + i * 64] = i < 7 ? codes(wq0 + i * 64) : last;
    __syncthreads();
    {
      const int dg = L.deg[lane], dm = L.deg[id];
      int dn[HR];
#pragma unroll
      for (int i = 0; i < HR; i++) dn[i] = __builtin_amdgcn_readlane(dm, i);
      __builtin_amdgcn_sched_barrier(0);
      unsigned long long m[HR];
#pragma unroll
      for (int i = 0; i < HR; i++) m[i] = __ballot(dg > dn[i]);
      __builtin_amdgcn_sched_barrier(0);
      unsigned r = 63;
#pragma unroll
      for (int i = 0; i < HR; i++) asm("v_writelane_b32 %0, %1, %2" : "+v"(r) : "s"(__popcll(m[i])), "n"(i));
      L.byrank[r] = id * 4;
    }
    __syncthreads();
    if (bucket == 0 && st == 0)
      __builtin_amdgcn_raw_buffer_store_b64(u32x2{tagl, tag1}, wsr, WS_EPOCH, 0, 16);
    __builtin_amdgcn_s_setprio(0);
    __syncthreads();
    __syncthreads();
    u32x16 oo;
#pragma unroll
    for (int t = 0; t < 10; t++)
      oo[t + 2] = (node0 + ((L.nl[t < SW ? t * 8 + kq : ((t - SW) * 4 + kq) * 8 + 4] / 4 - node0) & 63)) * 1024 + row * 16 + h * 256;
    oo[0] = lds_addr(&L.pay[(row >> 2) * 64 + (row & 3) * 4 + kq]);
    oo[1] = lds_addr(&L.prog[h]);
    asm volatile(MIX_ASM
                 : "+{v[0:15]}"(oo)
                 : "{v[32:47]}"(wa), "{v[48:63]}"(wb), [os] "s"(make_srd(out, (node0 + nn) * 1024)),
                   [r5] "n"((unsigned)(offsetof(LdsLow, row5) - offsetof(LdsLow, pay) - SW * CAPW * 16)),
                   [rd] "n"(offsetof(LdsLow, rdone)), [sl] "n"(HSL)
                 : MIX_CLOBBERS);
    asm volatile(".Ltfn_p1:");
  } else {
    {
    halves(0x20 + TB2 * 384 + tid, 0x20 + TB2 * 384 + TB3 * 512, 512);
    reinterpret_cast<u32x4*>(&L.hist[w * HS])[lane] = u32x4{0, 0, 0, 0};
    reinterpret_cast<u32x4*>(&L.hist[w * HS])[64 + lane] = u32x4{0, 0, 0, 0};
    L.ri[w][lane] = u32x4{0x20000000u, 0x20000000u, 0x20000000u, 0x20000000u};
    const unsigned long long t0 = w & 1 ? 0x26796281267d7281ull : 0x277dc200272c8180ull, t1 = w & 1 ? 0x246942c324755245ull : 0x25755242217984c2ull;
    const unsigned long long t2 = w & 1 ? 0x22613345226d6483ull : 0x236d130423694307ull, t3 = w & 1 ? 0x206503c725710384ull : 0x215d2386206513cdull;
    const unsigned long long t = w & 4 ? (w & 2 ? t3 : t2) : (w & 2 ? t1 : t0);
    const unsigned rks = nn == NPB ? t >> 32 : t;
    const int start = w * CAPW;
    unsigned sop = lds_addr(&L.so[start + (lane & 7)]);
    unsigned yp = lds_addr(&L.pay[start + (lane >> 2)]);
    unsigned ep = lds_addr(&L.pay[start + (lane & 15)]) + 8;
    u32x4 cg0 = L.ct[(lane & 3) * 2];
    const u32x4 cg1 = L.ct[(lane & 3) * 2 + 1], wq = L.ct[8 + lane];
    const unsigned rp = lds_addr(&L.pay[start]) + kq * 256 + row * 4;
    unsigned pg = lds_addr(&L.prog[w]), slowv;
    u32x16 cs = {ho0,
                 (unsigned)(((lane >> 1) * SW + w) * CH + (lane & 1)) * 4,
                 lds_addr(L.ri[w]) + (lane & 1) * 4, lds_addr(L.ri[w]) + lane * 4, (lane & 1) - 1u, lane & 1u,
                 lds_addr(&L.hist[tid]), lds_addr(&L.byrank[(rks >> (min(lane & 7, NS - 1) * 6)) & 63]),
                 0, lds_addr(&L.hist[SW * HS + (lane >> 3)]),
                 (start * 4 + ((lane & 7) << 13)) * 0x10001u, (unsigned)(start + lane) * 4,
                 lds_addr(&L.nl[w * 8 + (lane & 7)]), lane * 4u, ((node0 + w * RKL + lane) & 63) * 4u, 0};
    asm volatile(STREAM_ASM
                 : [sl] "=s"(slowv), "+{v12}"(sop), "+{v14}"(yp), "+{v15}"(ep), "+{v[136:151]}"(cs),
                   "+{v[20:23]}"(cg0), "+{v110}"(pg)
                 : "{v8}"(lane * 16),
                   "{v[24:27]}"(cg1), "{v[28:31]}"(wq), "{v[152:155]}"(u32x4{rp, rp + 64, rp + 128, rp + 192}),
                   "{v156}"(lds_addr(&L.row5[w * 64]) + kq * 256 + row * 4),
                   [xs] "s"(make_srd(x, N * 1024)), [ws] "s"(wsd),
                   [ho] "s"(WS_OFF + bucket * 16), [rk] "s"(WS_RECK), [rp] "s"(WS_RECP), [t0] "s"(tagl << 22), [t1] "s"(tag1),
                   [wb] "s"(w * (HS * 4)), [bit] "s"(1u << w), [fo] "s"((w >> 2) * FO1), [tb] "n"(TOFF),
                   [rd] "n"(offsetof(LdsLow, rdone)), [fl] "n"(offsetof(LdsLow, flag)),
                   [hi] "n"(offsetof(LdsLow, hist)), [to] "n"(offsetof(LdsLow, hist) + SW * HS * 4), [dg] "n"(offsetof(LdsLow, deg)),
                   [br] "n"(offsetof(LdsLow, byrank)), [so] "n"(offsetof(LdsLow, so)), [pa] "n"(offsetof(LdsLow, pay))
                 : STREAM_CLOBBERS);
    }
    int lane;
    asm volatile("v_mbcnt_lo_u32_b32 %0, -1, 0\n v_mbcnt_hi_u32_b32 %0, -1, %0" : "=v"(lane));
    __builtin_assume(lane >= 0 && lane < 64);
    u32x4* rows = &L.pay[w * CAPW];
#pragma unroll 1
    for (int it = 0; it < 2 * NS; it++) {
      const int sl = it >> 1, slice = it & 1, ch = slice * 64 + lane;
      const unsigned dl = (L.nl[w * 8 + sl] / 4 - node0) & 63;
      float acc[4] = {0.f, 0.f, 0.f, 0.f};
#pragma unroll 1
      for (int eb = 0; eb < E; eb += 64) {
        unsigned long long m = __ballot(eb + lane < E && (unsigned)dst[min(eb + lane, E - 1)] == node0 + dl);
#pragma unroll 1
        for (; m; m &= m - 1) {
          const int e = eb + __builtin_ctzll(m);
          const u32x2 ry = Y[e], rf = ef[e];
          float yy[4] = {bf2f(ry[0] & 0xffff), bf2f(ry[0] >> 16), bf2f(ry[1] & 0xffff), bf2f(ry[1] >> 16)};
          float ee[4] = {bf2f(rf[0] & 0xffff), bf2f(rf[0] >> 16), bf2f(rf[1] & 0xffff), bf2f(rf[1] >> 16)};
          float z = 0.f;
#pragma unroll
          for (int k = 0; k < 4; k++) z += ee[k] * bf2f(wrad[k * C_ + ch]);
          float R = z / (1.f + __expf(-z));
          const unsigned short* xr = x + (size_t)src[e] * 512 + ch * 4;
#pragma unroll 1
          for (int o = 0; o < 4; o++) {
            float tp = 0.f;
#pragma unroll 1
            for (int i = 0; i < 4; i++) {
              float A = 0.f;
#pragma unroll
              for (int f = 0; f < 4; f++) A += yy[f] * bf2f(cg[i * 16 + f * 4 + o]);
              tp += bf2f(xr[i]) * bf2f(f2bf(A));
            }
            float v = R * tp;
            acc[0] += o == 0 ? v : 0.f;
            acc[1] += o == 1 ? v : 0.f;
            acc[2] += o == 2 ? v : 0.f;
            acc[3] += o == 3 ? v : 0.f;
          }
        }
      }
      u32x4* rw = sl < 4 ? &rows[sl * 64] : &L.row5[w * 64];
      unsigned short* ar = reinterpret_cast<unsigned short*>(&rw[slice * 32]) + (lane >> 5) * 128 + (lane & 31);
#pragma unroll
      for (int o = 0; o < 4; o++) ar[o * 32] = f2bf(acc[o]);
    }
    asm volatile("" ::: "memory");
    if (lane == 0) atomicOr(&L.rdone, 1u << w);
  }
}

extern "C" __global__ __launch_bounds__(C_) void tfn_plain_kernel(
    const int* __restrict__ dst, const int* __restrict__ src, const unsigned short* __restrict__ Y,
    const unsigned short* __restrict__ ef, const unsigned short* __restrict__ x, unsigned short* __restrict__ out,
    const unsigned short* __restrict__ wrad, const unsigned short* __restrict__ cg, const unsigned short* __restrict__ wmix,
    long long E) {
  __shared__ f32x4 agg[C_];
  const int c = threadIdx.x, lane = c & 63;
  f32x4 acc = {0.f, 0.f, 0.f, 0.f};
#pragma unroll 1
  for (long long eb = 0; eb < E; eb += 64) {
    unsigned long long m = __ballot(eb + lane < E && dst[eb + lane < E ? eb + lane : 0] == (int)blockIdx.x);
#pragma unroll 1
    for (; m; m &= m - 1) {
      const long long e = eb + __builtin_ctzll(m);
      float z = 0.f;
#pragma unroll
      for (int k = 0; k < 4; k++) z += bf2f(ef[e * 4 + k]) * bf2f(wrad[k * C_ + c]);
      const float R = z / (1.f + __expf(-z));
#pragma unroll
      for (int i = 0; i < 4; i++) {
        const float xv = R * bf2f(x[(size_t)src[e] * 512 + c * 4 + i]);
#pragma unroll
        for (int f = 0; f < 4; f++)
#pragma unroll
          for (int o = 0; o < 4; o++) acc[o] += xv * bf2f(Y[e * 4 + f]) * bf2f(cg[i * 16 + f * 4 + o]);
      }
    }
  }
  agg[c] = acc;
  __syncthreads();
  f32x4 r = {0.f, 0.f, 0.f, 0.f};
#pragma unroll 4
  for (int k = 0; k < C_; k++) r += agg[k] * bf2f(wmix[k * C_ + c]);
  *reinterpret_cast<u32x2*>(out + (size_t)blockIdx.x * 512 + c * 4) = u32x2{pk2bf(r[0], r[1]), pk2bf(r[2], r[3])};
}

extern "C" void kernel_launch(void* const* d_in, const int* in_sizes, int n_in, void* d_out, int out_size,
                              void* d_ws, size_t ws_size, hipStream_t stream) {
  const unsigned short* x = (const unsigned short*)d_in[0];
  const int* src = (const int*)d_in[1];
  const int* dst = (const int*)d_in[2];
  const u32x2* Y = (const u32x2*)d_in[3];
  const u32x2* ef = (const u32x2*)d_in[4];
  const unsigned short* wrad = (const unsigned short*)d_in[5];
  const unsigned short* cg = (const unsigned short*)d_in[6];
  const unsigned short* wmix = (const unsigned short*)d_in[7];
  const int N = in_sizes[0] / 512;
  const int E = in_sizes[1];
  const int CE = ((E + NBMAX - 1) / NBMAX + 1) & ~1;
  const unsigned M2 = ((unsigned)NBMAX << 22) / (unsigned)N;
  if (N <= 64 || ((1u << 22) + M2 - 1) / M2 > (unsigned)NPB || CE > CH || ws_size < WS_END) {
    tfn_plain_kernel<<<N, C_, 0, stream>>>(dst, src, (const unsigned short*)Y, (const unsigned short*)ef, x,
                                           (unsigned short*)d_out, wrad, cg, wmix, E);
    return;
  }
  tfn_main_kernel<<<NBMAX, MT, 0, stream>>>(dst, E, 0x20000, src, CE, 0x20000, Y, N, 0x20000, ef, M2, 0x20000, x, (char*)d_ws,
                                           (u32x4*)d_out, wrad, cg, wmix);
}
```

```cpp
#include <hip/hip_runtime.h>
#include <hip/hip_bf16.h>
#include <stddef.h>
#include <stdint.h>

typedef __attribute__((ext_vector_type(2))) float f32x2;
typedef __attribute__((ext_vector_type(4))) float f32x4;
typedef __attribute__((ext_vector_type(2))) unsigned int u32x2;
typedef __attribute__((ext_vector_type(4))) unsigned int u32x4;
typedef __attribute__((ext_vector_type(8))) unsigned int u32x8;
typedef __attribute__((ext_vector_type(16))) unsigned int u32x16;
typedef __attribute__((ext_vector_type(2))) __bf16 bf16x2;

#define STREAM_ASM \
  "s_mov_b32 s63, 0x10000\n" \
  "Lh%=:\n" \
  "buffer_load_dwordx4 v[72:75], v136, %[ws], %[ho] offen sc1\n" \
  "v_mov_b32 v117, 0\n" \
  "v_mov_b32 v116, 64\n" \
  "v_mov_b32 v87, 0\n" \
  "v_mov_b64 v[44:45], 0\n" \
  "v_mov_b64 v[46:47], 0\n" \
  "v_mov_b32 v118, 0x7fc\n" \
  "s_waitcnt vmcnt(0)\n" \
  "v_add3_u32 v108, v72, v73, v74\n" \
  "v_xor_b32 v109, %[t0], v72\n" \
  "v_add_u32 v108, v108, v75\n" \
  "v_cmp_lt_u32 vcc, 0x3fffff, v109\n" \
  "v_cmp_ne_u32_e64 s[74:75], %[t1], v108\n" \
  "s_or_b64 vcc, vcc, s[74:75]\n" \
  "s_cbranch_scc1 Lhr%=\n" \
  "s_mov_b64 exec, -1\n" \
  "v_bfe_u32 v108, v72, 11, 11\n" \
  "v_and_b32 v109, v140, v108\n" \
  "v_and_b32 v112, 0x7ff, v72\n" \
  "v_sub_u32 v111, v108, v141\n" \
  "v_add_u32_dpp v109, v109, v109 row_shr:1 row_mask:0xf bank_mask:0xf bound_ctrl:1\n" \
  "v_lshl_add_u32 v112, v112, 2, v137\n" \
  "v_and_b32 v114, v140, v73\n" \
  "v_add_u32_dpp v109, v109, v109 row_shr:2 row_mask:0xf bank_mask:0xf bound_ctrl:1\n" \
  "v_and_b32 v115, v140, v74\n" \
  "v_sub_u32 v114, 0, v114\n" \
  "v_add_u32_dpp v109, v109, v109 row_shr:4 row_mask:0xf bank_mask:0xf bound_ctrl:1\n" \
  "v_sub_u32 v115, 0, v115\n" \
  "s_nop 0\n" \
  "v_add_u32_dpp v109, v109, v109 row_shr:8 row_mask:0xf bank_mask:0xf bound_ctrl:1\n" \
  "s_nop 1\n" \
  "v_add_u32_dpp v109, v109, v109 row_bcast:15 row_mask:0xa bank_mask:0xf\n" \
  "s_nop 1\n" \
  "v_add_u32_dpp v109, v109, v109 row_bcast:31 row_mask:0xc bank_mask:0xf\n" \
  "v_sub_u32 v108, v109, v108\n" \
  "v_readlane_b32 s62, v109, 63\n" \
  "v_lshl_add_u32 v113, v108, 2, v138\n" \
  "s_cmpk_gt_u32 s62, 0x100\n" \
  "s_cbranch_scc1 Lover%=\n" \
  "Lw%=:\n" \
  "v_cmpx_lt_i32 vcc, 0, v111\n" \
  "ds_write_b32 v113, v112 offset:0\n" \
  "v_add_u32 v112, 8, v112\n" \
  "v_cmpx_lt_i32 vcc, 2, v111\n" \
  "ds_write_b32 v113, v112 offset:8\n" \
  "v_add_u32 v112, 8, v112\n" \
  "v_cmpx_lt_i32 vcc, 4, v111\n" \
  "ds_write_b32 v113, v112 offset:16\n" \
  "v_add_u32 v112, 8, v112\n" \
  "v_cmpx_lt_i32 vcc, 6, v111\n" \
  "ds_write_b32 v113, v112 offset:24\n" \
  "v_add_u32 v112, 8, v112\n" \
  "v_cmpx_lt_i32 vcc, 8, v111\n" \
  "ds_write_b32 v113, v112 offset:32\n" \
  "v_add_u32 v112, 8, v112\n" \
  "s_cbranch_execz Lwd%=\n" \
  "v_cmpx_lt_i32 vcc, 10, v111\n" \
  "ds_write_b32 v113, v112 offset:40\n" \
  "v_add_u32 v112, 8, v112\n" \
  "s_cbranch_execz Lwd%=\n" \
  "v_cmpx_lt_i32 vcc, 12, v111\n" \
  "ds_write_b32 v113, v112 offset:48\n" \
  "v_add_u32 v112, 8, v112\n" \
  "s_cbranch_execz Lwd%=\n" \
  "v_cmpx_lt_i32 vcc, 14, v111\n" \
  "ds_write_b32 v113, v112 offset:56\n" \
  "v_add_u32 v112, 8, v112\n" \
  "v_add_u32 v113, 64, v113\n" \
  "v_subrev_u32 v111, 16, v111\n" \
  "s_branch Lw%=\n" \
  "Lwd%=:\n" \
  "s_mov_b64 exec, -1\n" \
  "Lld%=:\n" \
  "ds_read2st64_b32 v[76:77], v139 offset1:1\n" \
  "ds_read2st64_b32 v[78:79], v139 offset0:2 offset1:3\n" \
  "s_mov_b32 s63, 0x10000\n" \
  "s_waitcnt lgkmcnt(0)\n" \
  "buffer_load_dword v84, v76, %[ws], %[rk] offen sc1\n" \
  "buffer_load_dword v85, v77, %[ws], %[rk] offen sc1\n" \
  "buffer_load_dword v86, v78, %[ws], %[rk] offen sc1\n" \
  "s_cmpk_le_u32 s62, 0xc0\n" \
  "s_cbranch_scc1 Lka%=\n" \
  "buffer_load_dword v87, v79, %[ws], %[rk] offen sc1\n" \
  "Lka%=:\n" \
  "v_lshlrev_b32 v80, 2, v76\n" \
  "v_lshlrev_b32 v81, 2, v77\n" \
  "v_lshlrev_b32 v82, 2, v78\n" \
  "v_lshlrev_b32 v83, 2, v79\n" \
  "s_barrier\n" \
  "s_cmpk_le_u32 s62, 0xc0\n" \
  "s_cbranch_scc1 Lpa%=\n" \
  "buffer_load_dwordx4 v[44:47], v83, %[ws], %[rp] offen sc1\n" \
  "Lpa%=:\n" \
  "buffer_load_dwordx4 v[32:35], v80, %[ws], %[rp] offen sc1\n" \
  "buffer_load_dwordx4 v[36:39], v81, %[ws], %[rp] offen sc1\n" \
  "buffer_load_dwordx4 v[40:43], v82, %[ws], %[rp] offen sc1\n" \
  "v_cmp_le_i32_e64 s[66:67], 0, v80\n" \
  "v_cmp_le_i32_e64 s[68:69], 0, v81\n" \
  "v_cmp_le_i32_e64 s[70:71], 0, v82\n" \
  "v_cmp_le_i32_e64 s[72:73], 0, v83\n" \
  "v_mov_b64 v[48:49], 0\n" \
  "v_mov_b64 v[50:51], 0\n" \
  "v_mov_b64 v[52:53], 0\n" \
  "v_mov_b64 v[54:55], 0\n" \
  "v_mov_b64 v[56:57], 0\n" \
  "v_mov_b64 v[58:59], 0\n" \
  "v_mov_b64 v[60:61], 0\n" \
  "v_mov_b64 v[62:63], 0\n" \
  "v_mov_b64 v[64:65], 0\n" \
  "v_mov_b64 v[66:67], 0\n" \
  "v_mov_b64 v[68:69], 0\n" \
  "v_mov_b64 v[70:71], 0\n" \
  "s_waitcnt vmcnt(3)\n" \
  "Lk%=:\n" \
  "v_add3_u32 v108, v84, v85, v86\n" \
  "v_add3_u32 v108, v108, v87, v114\n" \
  "v_lshlrev_b32 v88, 1, v84\n" \
  "v_lshlrev_b32 v92, 4, v84\n" \
  "v_add_u32_dpp v108, v108, v108 row_shr:1 row_mask:0xf bank_mask:0xf bound_ctrl:1\n" \
  "v_lshrrev_b32 v104, 2, v84\n" \
  "v_and_or_b32 v88, v88, v118, %[wb]\n" \
  "v_add_u32_dpp v108, v108, v108 row_shr:2 row_mask:0xf bank_mask:0xf bound_ctrl:1\n" \
  "v_lshlrev_b32_e64 v100, v92, 4\n" \
  "v_and_b32 v104, 0xfc, v104\n" \
  "v_add_u32_dpp v108, v108, v108 row_shr:4 row_mask:0xf bank_mask:0xf bound_ctrl:1\n" \
  "v_lshlrev_b32 v89, 1, v85\n" \
  "v_lshlrev_b32 v93, 4, v85\n" \
  "v_add_u32_dpp v108, v108, v108 row_shr:8 row_mask:0xf bank_mask:0xf bound_ctrl:1\n" \
  "v_lshrrev_b32 v105, 2, v85\n" \
  "v_and_or_b32 v89, v89, v118, %[wb]\n" \
  "v_add_u32_dpp v108, v108, v108 row_bcast:15 row_mask:0xa bank_mask:0xf\n" \
  "v_lshlrev_b32_e64 v101, v93, 4\n" \
  "v_and_b32 v105, 0xfc, v105\n" \
  "v_add_u32_dpp v108, v108, v108 row_bcast:31 row_mask:0xc bank_mask:0xf\n" \
  "v_lshlrev_b32 v90, 1, v86\n" \
  "v_readlane_b32 s65, v108, 63\n" \
  "v_lshlrev_b32 v94, 4, v86\n" \
  "v_lshrrev_b32 v106, 2, v86\n" \
  "v_and_or_b32 v90, v90, v118, %[wb]\n" \
  "v_lshlrev_b32_e64 v102, v94, 4\n" \
  "v_and_b32 v106, 0xfc, v106\n" \
  "s_cmp_lg_u32 s65, 0\n" \
  "s_cbranch_scc1 Lkr%=\n" \
  "Lkc%=:\n" \
  "s_mov_b64 exec, s[66:67]\n" \
  "ds_add_rtn_u32 v96, v88, v100 offset:%[hi]\n" \
  "ds_add_u32 v104, v116 offset:%[dg]\n" \
  "s_mov_b64 exec, s[68:69]\n" \
  "ds_add_rtn_u32 v97, v89, v101 offset:%[hi]\n" \
  "ds_add_u32 v105, v116 offset:%[dg]\n" \
  "s_mov_b64 exec, s[70:71]\n" \
  "ds_add_rtn_u32 v98, v90, v102 offset:%[hi]\n" \
  "ds_add_u32 v106, v116 offset:%[dg]\n" \
  "s_cmpk_gt_u32 s62, 0xc0\n" \
  "s_mov_b64 exec, -1\n" \
  "s_cbranch_scc1 Lc3%=\n" \
  "Lc4%=:\n" \
  "ds_read_b128 v[24:27], v20 offset:16\n" \
  "ds_read_b128 v[28:31], v8 offset:%[ct]+128\n" \
  "ds_read_b128 v[20:23], v20\n" \
  "v_and_b32 v100, v118, v88\n" \
  "v_and_b32 v101, v118, v89\n" \
  "v_and_b32 v102, v118, v90\n" \
  "s_waitcnt lgkmcnt(0)\n" \
  "s_barrier\n" \
  "ds_read_b32 v108, v149 offset:%[dg]\n" \
  "ds_read_b32 v109, v150 offset:%[dg]\n" \
  "ds_read2st64_b32 v[76:77], v142 offset0:0 offset1:8\n" \
  "ds_read2st64_b32 v[78:79], v142 offset0:16 offset1:24\n" \
  "ds_read2st64_b32 v[104:105], v142 offset0:32 offset1:40\n" \
  "ds_read2st64_b32 v[106:107], v142 offset0:48 offset1:56\n" \
  "v_and_b32 v84, 0xfffc00, v84\n" \
  "v_and_b32 v85, 0xfffc00, v85\n" \
  "v_and_b32 v86, 0xfffc00, v86\n" \
  "v_and_b32 v87, 0xfffc00, v87\n" \
  "v_mov_b32 v111, 0\n" \
  "v_mov_b32 v112, 63\n" \
  "s_waitcnt lgkmcnt(4)\n" \
  "v_readlane_b32 s74, v109, 0\n" \
  "v_readlane_b32 s76, v109, 1\n" \
  "v_readlane_b32 s78, v109, 2\n" \
  "s_nop 0\n" \
  "v_cmp_lt_i32_e64 s[74:75], s74, v108\n" \
  "v_cmp_lt_i32_e64 s[76:77], s76, v108\n" \
  "v_cmp_lt_i32_e64 s[78:79], s78, v108\n" \
  "s_bcnt1_i32_b64 s74, s[74:75]\n" \
  "s_bcnt1_i32_b64 s76, s[76:77]\n" \
  "s_bcnt1_i32_b64 s78, s[78:79]\n" \
  "v_writelane_b32 v112, s74, 0\n" \
  "v_writelane_b32 v112, s76, 1\n" \
  "v_writelane_b32 v112, s78, 2\n" \
  "v_lshlrev_b32 v112, 2, v112\n" \
  "s_waitcnt lgkmcnt(3)\n" \
  "ds_write2st64_b32 v142, v117, v76 offset1:8\n" \
  "v_add_u32 v77, v77, v76\n" \
  "s_waitcnt lgkmcnt(3)\n" \
  "v_add_u32 v78, v78, v77\n" \
  "ds_write2st64_b32 v142, v77, v78 offset0:16 offset1:24\n" \
  "v_add_u32 v79, v79, v78\n" \
  "s_waitcnt lgkmcnt(3)\n" \
  "v_add_u32 v104, v104, v79\n" \
  "ds_write2st64_b32 v142, v79, v104 offset0:32 offset1:40\n" \
  "v_add_u32 v105, v105, v104\n" \
  "s_waitcnt lgkmcnt(3)\n" \
  "v_add_u32 v106, v106, v105\n" \
  "ds_write2st64_b32 v142, v105, v106 offset0:48 offset1:56\n" \
  "v_add_u32 v107, v107, v106\n" \
  "ds_write_b32 v142, v107 offset:16384\n" \
  "ds_write_b32 v112, v150 offset:%[br]\n" \
  "s_waitcnt lgkmcnt(0)\n" \
  "s_barrier\n" \
  "ds_read_b32 v108, v143\n" \
  "s_mov_b32 s74, 0x1f1f1f1f\n" \
  "s_mov_b32 s75, 0x1f1f1f1f\n" \
  "s_waitcnt lgkmcnt(0)\n" \
  "v_lshl_add_u32 v76, v108, 3, v145\n" \
  "s_mov_b64 exec, s[74:75]\n" \
  "ds_read_b32 v111, v76\n" \
  "s_mov_b64 exec, -1\n" \
  "ds_write_b32 v148, v108\n" \
  "s_waitcnt lgkmcnt(1)\n" \
  "v_add_u32_dpp v112, v111, v111 row_shr:1 row_mask:0xf bank_mask:0xf bound_ctrl:1\n" \
  "v_sub_u32 v77, v146, v111\n" \
  "s_nop 0\n" \
  "v_add_u32_dpp v112, v112, v112 row_shr:2 row_mask:0xf bank_mask:0xf bound_ctrl:1\n" \
  "s_nop 1\n" \
  "v_add_u32_dpp v112, v112, v112 row_shr:4 row_mask:0xf bank_mask:0xf bound_ctrl:1\n" \
  "s_nop 1\n" \
  "v_add_u32_dpp v112, v112, v112 row_shr:8 row_mask:0xf bank_mask:0xf bound_ctrl:1\n" \
  "s_nop 1\n" \
  "v_add_u32_dpp v112, v112, v112 row_bcast:15 row_mask:0xa bank_mask:0xf\n" \
  "s_nop 1\n" \
  "v_add_u32_dpp v112, v112, v112 row_bcast:31 row_mask:0xc bank_mask:0xf\n" \
  "v_sub_u32 v111, v112, v111\n" \
  "v_readlane_b32 s65, v112, 63\n" \
  "v_mov_b32_dpp v78, v112 row_newbcast:7 row_mask:0xf bank_mask:0x3\n" \
  "v_mov_b32_dpp v79, v111 row_newbcast:0 row_mask:0xf bank_mask:0x3\n" \
  "v_add_u32 v77, v77, v112\n" \
  "v_mov_b32_dpp v78, v112 row_newbcast:15 row_mask:0xf bank_mask:0xc\n" \
  "v_mov_b32_dpp v79, v111 row_newbcast:8 row_mask:0xf bank_mask:0xc\n" \
  "s_and_b32 s64, s65, 0xffff\n" \
  "v_lshl_add_u32 v77, v78, 16, v77\n" \
  "s_lshr_b32 s65, s65, 16\n" \
  "v_add_u32_sdwa v77, v77, v79 dst_sel:DWORD dst_unused:UNUSED_PAD src0_sel:DWORD src1_sel:WORD_1\n" \
  "s_add_u32 s64, s64, s65\n" \
  "s_mov_b64 exec, s[74:75]\n" \
  "ds_write_b32 v76, v77\n" \
  "s_mov_b64 exec, -1\n" \
  "s_cmpk_gt_u32 s64, 864\n" \
  "s_cbranch_scc1 Llo%=\n" \
  "Llc%=:\n" \
  "s_waitcnt lgkmcnt(0)\n" \
  "s_barrier\n" \
  "ds_read_b32 v109, v117 offset:%[fl]\n" \
  "ds_read_b32 v76, v88 offset:%[hi]\n" \
  "ds_read_b32 v104, v100 offset:%[to]\n" \
  "ds_read_b32 v77, v89 offset:%[hi]\n" \
  "ds_read_b32 v105, v101 offset:%[to]\n" \
  "ds_read_b32 v78, v90 offset:%[hi]\n" \
  "ds_read_b32 v106, v102 offset:%[to]\n" \
  "v_add_u32 v113, s64, v147\n" \
  "v_bfrev_b32 v111, 1\n" \
  "s_max_u32 s60, s64, 4\n" \
  "s_sub_u32 s60, s60, 4\n" \
  "s_lshr_b32 s60, s60, 4\n" \
  "s_waitcnt lgkmcnt(4)\n" \
  "v_readfirstlane_b32 s61, v109\n" \
  "v_add3_u32 v88, v76, v104, v96\n" \
  "v_lshrrev_b32 v88, v92, v88\n" \
  "v_bfe_u32 v72, v88, 13, 3\n" \
  "v_and_b32 v88, 0x1ffc, v88\n" \
  "v_lshl_or_b32 v72, v72, 1, v84\n" \
  "s_cmp_lg_u32 s61, 0\n" \
  "s_cbranch_scc1 Lslow%=\n" \
  "s_waitcnt lgkmcnt(0)\n" \
  "v_add3_u32 v89, v77, v105, v97\n" \
  "v_add3_u32 v90, v78, v106, v98\n" \
  "v_lshrrev_b32 v89, v93, v89\n" \
  "v_lshrrev_b32 v90, v94, v90\n" \
  "v_bfe_u32 v73, v89, 13, 3\n" \
  "v_bfe_u32 v74, v90, 13, 3\n" \
  "v_and_b32 v89, 0x1ffc, v89\n" \
  "v_and_b32 v90, 0x1ffc, v90\n" \
  "v_lshl_or_b32 v73, v73, 1, v85\n" \
  "v_lshl_or_b32 v74, v74, 1, v86\n" \
  "s_mov_b64 exec, s[66:67]\n" \
  "ds_write_b32 v88, v72 offset:%[so]\n" \
  "s_mov_b64 exec, s[68:69]\n" \
  "ds_write_b32 v89, v73 offset:%[so]\n" \
  "s_mov_b64 exec, s[70:71]\n" \
  "ds_write_b32 v90, v74 offset:%[so]\n" \
  "s_cmpk_gt_u32 s62, 0xc0\n" \
  "s_cbranch_scc1 Lq3%=\n" \
  "Lq4%=:\n" \
  "s_mov_b32 exec_lo, -1\n" \
  "s_mov_b32 exec_hi, 0\n" \
  "ds_write_b32 v113, v111 offset:%[so]\n" \
  "s_mov_b64 exec, -1\n" \
  "v_lshlrev_b32 v88, 2, v88\n" \
  "v_lshlrev_b32 v89, 2, v89\n" \
  "v_lshlrev_b32 v90, 2, v90\n" \
  "v_lshlrev_b32 v91, 2, v91\n" \
  "v_lshlrev_b32 v113, 2, v113\n" \
  "s_mov_b32 s63, 0x10000\n" \
  "s_waitcnt vmcnt(0)\n" \
  "Lpc%=:\n" \
  "s_mov_b64 exec, s[66:67]\n" \
  "ds_write_b128 v88, v[32:35] offset:%[pa]\n" \
  "s_mov_b64 exec, s[68:69]\n" \
  "ds_write_b128 v89, v[36:39] offset:%[pa]\n" \
  "s_mov_b64 exec, s[70:71]\n" \
  "ds_write_b128 v90, v[40:43] offset:%[pa]\n" \
  "s_mov_b64 exec, s[72:73]\n" \
  "ds_write_b128 v91, v[44:47] offset:%[pa]\n" \
  "s_mov_b32 exec_lo, -1\n" \
  "s_mov_b32 exec_hi, 0\n" \
  "ds_write_b128 v113, v[48:51] offset:%[pa]\n" \
  "s_mov_b64 exec, -1\n" \
  "v_add3_u32 v108, v32, v33, v115\n" \
  "v_add3_u32 v111, v34, v35, v36\n" \
  "v_add3_u32 v112, v37, v38, v39\n" \
  "v_add3_u32 v108, v108, v40, v41\n" \
  "v_add3_u32 v111, v111, v42, v43\n" \
  "v_add3_u32 v112, v112, v44, v45\n" \
  "v_add3_u32 v108, v108, v46, v47\n" \
  "v_add3_u32 v108, v108, v111, v112\n" \
  "s_nop 1\n" \
  "v_add_u32_dpp v108, v108, v108 row_shr:1 row_mask:0xf bank_mask:0xf bound_ctrl:1\n" \
  "s_nop 1\n" \
  "v_add_u32_dpp v108, v108, v108 row_shr:2 row_mask:0xf bank_mask:0xf bound_ctrl:1\n" \
  "s_nop 1\n" \
  "v_add_u32_dpp v108, v108, v108 row_shr:4 row_mask:0xf bank_mask:0xf bound_ctrl:1\n" \
  "s_nop 1\n" \
  "v_add_u32_dpp v108, v108, v108 row_shr:8 row_mask:0xf bank_mask:0xf bound_ctrl:1\n" \
  "s_nop 1\n" \
  "v_add_u32_dpp v108, v108, v108 row_bcast:15 row_mask:0xa bank_mask:0xf\n" \
  "s_nop 1\n" \
  "v_add_u32_dpp v108, v108, v108 row_bcast:31 row_mask:0xc bank_mask:0xf\n" \
  "s_nop 0\n" \
  "v_readlane_b32 s65, v108, 63\n" \
  "s_waitcnt lgkmcnt(0)\n" \
  "s_cmp_lg_u32 s65, 0\n" \
  "s_cbranch_scc1 Lpr%=\n" \
  "Lpw%=:\n" \
  "s_barrier\n" \
  "ds_read2_b32  v[10:11], v12 offset1:8\n" \
  "ds_read_b64 v[106:107], v14\n" \
  "ds_read_b64 v[104:105], v15\n" \
  "s_nop 0\n" \
  "s_setprio 1\n" \
  "v_mov_b64 v[32:33], 0\n" \
  "v_mov_b64 v[34:35], 0\n" \
  "v_mov_b64 v[36:37], 0\n" \
  "v_mov_b64 v[38:39], 0\n" \
  "v_mov_b64 v[40:41], 0\n" \
  "v_mov_b64 v[42:43], 0\n" \
  "v_mov_b64 v[44:45], 0\n" \
  "v_mov_b64 v[46:47], 0\n" \
  "s_waitcnt lgkmcnt(2)\n" \
  "v_readlane_b32 s80, v10, 0\n" \
  "v_readlane_b32 s81, v10, 1\n" \
  "v_readlane_b32 s82, v10, 2\n" \
  "v_readlane_b32 s83, v10, 3\n" \
  "v_readlane_b32 s84, v10, 4\n" \
  "v_readlane_b32 s85, v10, 5\n" \
  "s_andn2_b32 s61, s80, 15\n" \
  "buffer_load_dwordx4 v[120:123], v8, %[xs], s61 offen\n" \
  "s_andn2_b32 s61, s81, 15\n" \
  "buffer_load_dwordx4 v[124:127], v8, %[xs], s61 offen\n" \
  "s_andn2_b32 s61, s82, 15\n" \
  "buffer_load_dwordx4 v[128:131], v8, %[xs], s61 offen\n" \
  "s_andn2_b32 s61, s83, 15\n" \
  "buffer_load_dwordx4 v[132:135], v8, %[xs], s61 offen\n" \
  "s_andn2_b32 s61, s84, 15\n" \
  "buffer_load_dwordx4 v[136:139], v8, %[xs], s61 offen\n" \
  "v_mov_b64  v[96:97], 0\n" \
  "v_mov_b64  v[98:99], 0\n" \
  "v_mov_b64  v[100:101], 0\n" \
  "v_mov_b64  v[102:103], 0\n" \
  "s_waitcnt lgkmcnt(0)\n" \
  "v_mfma_f32_4x4x4_16b_bf16 v[80:83], v[104:105], v[28:29], 0 cbsz:4\n" \
  "v_mfma_f32_4x4x4_16b_bf16 v[84:87], v[104:105], v[30:31], 0 cbsz:4\n" \
  "v_mfma_f32_4x4x4_16b_bf16 v[112:115], v[106:107], v[20:21], 0\n" \
  "v_mfma_f32_4x4x4_16b_bf16 v[116:119], v[106:107], v[22:23], 0\n" \
  "v_mfma_f32_4x4x4_16b_bf16 v[72:75], v[106:107], v[24:25], 0\n" \
  "v_mfma_f32_4x4x4_16b_bf16 v[76:79], v[106:107], v[26:27], 0\n" \
  "v_cvt_pk_bf16_f32 v81, v80, v81\n" \
  "v_lshlrev_b32_sdwa v80, 1, v81 dst_sel:DWORD dst_unused:UNUSED_PAD src0_sel:DWORD src1_sel:WORD_0\n" \
  "v_lshlrev_b32_sdwa v81, 1, v81 dst_sel:DWORD dst_unused:UNUSED_PAD src0_sel:DWORD src1_sel:WORD_1\n" \
  "ds_read_u16_d16_hi v96, v80 offset:%[tb]\n" \
  "ds_read_u16_d16_hi v97, v81 offset:%[tb]\n" \
  "v_cvt_pk_bf16_f32 v85, v84, v85\n" \
  "v_lshlrev_b32_sdwa v84, 1, v85 dst_sel:DWORD dst_unused:UNUSED_PAD src0_sel:DWORD src1_sel:WORD_0\n" \
  "v_lshlrev_b32_sdwa v85, 1, v85 dst_sel:DWORD dst_unused:UNUSED_PAD src0_sel:DWORD src1_sel:WORD_1\n" \
  "ds_read_u16_d16_hi v98, v84 offset:%[tb]\n" \
  "ds_read_u16_d16_hi v99, v85 offset:%[tb]\n" \
  "v_cvt_pk_bf16_f32 v83, v82, v83\n" \
  "v_lshlrev_b32_sdwa v82, 1, v83 dst_sel:DWORD dst_unused:UNUSED_PAD src0_sel:DWORD src1_sel:WORD_0\n" \
  "v_lshlrev_b32_sdwa v83, 1, v83 dst_sel:DWORD dst_unused:UNUSED_PAD src0_sel:DWORD src1_sel:WORD_1\n" \
  "ds_read_u16_d16_hi v100, v82 offset:%[tb]\n" \
  "ds_read_u16_d16_hi v101, v83 offset:%[tb]\n" \
  "v_cvt_pk_bf16_f32 v108, v112, v116\n" \
  "v_cvt_pk_bf16_f32 v109, v72, v76\n" \
  "s_nop 0\n" \
  "s_waitcnt vmcnt(4)\n" \
  "v_mfma_f32_4x4x4_16b_bf16 v[72:75], v[108:109], v[120:121], 0 cbsz:4\n" \
  "v_mfma_f32_4x4x4_16b_bf16 v[76:79], v[108:109], v[122:123], 0 cbsz:4\n" \
  "s_andn2_b32 s61, s85, 15\n" \
  "buffer_load_dwordx4 v[140:143], v8, %[xs], s61 offen\n" \
  "s_waitcnt lgkmcnt(0)\n" \
  "s_nop 1\n" \
  "Lbody%=:\n" \
  "s_waitcnt lgkmcnt(2)\n" \
  "s_set_gpr_idx_on s80, gpr_idx(SRC2,DST)\n" \
  "v_pk_fma_f32 v[32:33], v[96:97], v[72:73], v[32:33] op_sel:[0,0,0] op_sel_hi:[0,1,1]\n" \
  "v_pk_fma_f32 v[42:43], v[96:97], v[74:75], v[42:43] op_sel:[0,0,0] op_sel_hi:[0,1,1]\n" \
  "v_pk_fma_f32 v[52:53], v[98:99], v[76:77], v[52:53] op_sel:[0,0,0] op_sel_hi:[0,1,1]\n" \
  "v_pk_fma_f32 v[62:63], v[98:99], v[78:79], v[62:63] op_sel:[0,0,0] op_sel_hi:[0,1,1]\n" \
  "s_set_gpr_idx_off\n" \
  "s_waitcnt vmcnt(4)\n" \
  "v_mfma_f32_4x4x4_16b_bf16 v[72:75], v[108:109], v[124:125], 0 cbsz:4 abid:1\n" \
  "v_mfma_f32_4x4x4_16b_bf16 v[76:79], v[108:109], v[126:127], 0 cbsz:4 abid:1\n" \
  "v_mfma_f32_4x4x4_16b_bf16 v[88:91], v[104:105], v[28:29], 0 cbsz:4 abid:1\n" \
  "v_mfma_f32_4x4x4_16b_bf16 v[92:95], v[104:105], v[30:31], 0 cbsz:4 abid:1\n" \
  "v_readlane_b32 s86, v10, 6\n" \
  "v_cvt_pk_bf16_f32 v87, v86, v87\n" \
  "v_lshlrev_b32_sdwa v86, 1, v87 dst_sel:DWORD dst_unused:UNUSED_PAD src0_sel:DWORD src1_sel:WORD_0\n" \
  "v_lshlrev_b32_sdwa v87, 1, v87 dst_sel:DWORD dst_unused:UNUSED_PAD src0_sel:DWORD src1_sel:WORD_1\n" \
  "s_andn2_b32 s61, s86, 15\n" \
  "buffer_load_dwordx4 v[144:147], v8, %[xs], s61 offen\n" \
  "ds_read_u16_d16_hi v102, v86 offset:%[tb]\n" \
  "ds_read_u16_d16_hi v103, v87 offset:%[tb]\n" \
  "v_mov_b32 v111, s60\n" \
  "ds_write2_b32 v110, v111, v111 offset1:8\n" \
  "s_set_gpr_idx_on s81, gpr_idx(SRC2,DST)\n" \
  "v_pk_fma_f32 v[32:33], v[96:97], v[72:73], v[32:33] op_sel:[1,0,0] op_sel_hi:[1,1,1]\n" \
  "v_pk_fma_f32 v[42:43], v[96:97], v[74:75], v[42:43] op_sel:[1,0,0] op_sel_hi:[1,1,1]\n" \
  "v_pk_fma_f32 v[52:53], v[98:99], v[76:77], v[52:53] op_sel:[1,0,0] op_sel_hi:[1,1,1]\n" \
  "v_pk_fma_f32 v[62:63], v[98:99], v[78:79], v[62:63] op_sel:[1,0,0] op_sel_hi:[1,1,1]\n" \
  "s_set_gpr_idx_off\n" \
  "s_waitcnt vmcnt(4)\n" \
  "v_mfma_f32_4x4x4_16b_bf16 v[72:75], v[108:109], v[128:129], 0 cbsz:4 abid:2\n" \
  "v_mfma_f32_4x4x4_16b_bf16 v[76:79], v[108:109], v[130:131], 0 cbsz:4 abid:2\n" \
  "v_readlane_b32 s87, v10, 7\n" \
  "v_cvt_pk_bf16_f32 v89, v88, v89\n" \
  "v_lshlrev_b32_sdwa v88, 1, v89 dst_sel:DWORD dst_unused:UNUSED_PAD src0_sel:DWORD src1_sel:WORD_0\n" \
  "v_lshlrev_b32_sdwa v89, 1, v89 dst_sel:DWORD dst_unused:UNUSED_PAD src0_sel:DWORD src1_sel:WORD_1\n" \
  "s_andn2_b32 s61, s87, 15\n" \
  "buffer_load_dwordx4 v[148:151], v8, %[xs], s61 offen\n" \
  "ds_read_u16_d16_hi v96, v88 offset:%[tb]\n" \
  "ds_read_u16_d16_hi v97, v89 offset:%[tb]\n" \
  "ds_read_b32 v111, v110 offset:16\n" \
  "s_waitcnt lgkmcnt(4)\n" \
  "s_set_gpr_idx_on s82, gpr_idx(SRC2,DST)\n" \
  "v_pk_fma_f32 v[32:33], v[100:101], v[72:73], v[32:33] op_sel:[0,0,0] op_sel_hi:[0,1,1]\n" \
  "v_pk_fma_f32 v[42:43], v[100:101], v[74:75], v[42:43] op_sel:[0,0,0] op_sel_hi:[0,1,1]\n" \
  "v_pk_fma_f32 v[52:53], v[102:103], v[76:77], v[52:53] op_sel:[0,0,0] op_sel_hi:[0,1,1]\n" \
  "v_pk_fma_f32 v[62:63], v[102:103], v[78:79], v[62:63] op_sel:[0,0,0] op_sel_hi:[0,1,1]\n" \
  "s_set_gpr_idx_off\n" \
  "s_waitcnt vmcnt(4) lgkmcnt(10)\n" \
  "v_mfma_f32_4x4x4_16b_bf16 v[72:75], v[108:109], v[132:133], 0 cbsz:4 abid:3\n" \
  "v_mfma_f32_4x4x4_16b_bf16 v[76:79], v[108:109], v[134:135], 0 cbsz:4 abid:3\n" \
  "v_readlane_b32 s80, v11, 0\n" \
  "v_cvt_pk_bf16_f32 v93, v92, v93\n" \
  "v_lshlrev_b32_sdwa v92, 1, v93 dst_sel:DWORD dst_unused:UNUSED_PAD src0_sel:DWORD src1_sel:WORD_0\n" \
  "v_lshlrev_b32_sdwa v93, 1, v93 dst_sel:DWORD dst_unused:UNUSED_PAD src0_sel:DWORD src1_sel:WORD_1\n" \
  "s_andn2_b32 s61, s80, 15\n" \
  "buffer_load_dwordx4 v[120:123], v8, %[xs], s61 offen\n" \
  "ds_read_u16_d16_hi v98, v92 offset:%[tb]\n" \
  "ds_read_u16_d16_hi v99, v93 offset:%[tb]\n" \
  "ds_read_b64 v[106:107], v14 offset:256\n" \
  "s_set_gpr_idx_on s83, gpr_idx(SRC2,DST)\n" \
  "v_pk_fma_f32 v[32:33], v[100:101], v[72:73], v[32:33] op_sel:[1,0,0] op_sel_hi:[1,1,1]\n" \
  "v_pk_fma_f32 v[42:43], v[100:101], v[74:75], v[42:43] op_sel:[1,0,0] op_sel_hi:[1,1,1]\n" \
  "v_pk_fma_f32 v[52:53], v[102:103], v[76:77], v[52:53] op_sel:[1,0,0] op_sel_hi:[1,1,1]\n" \
  "v_pk_fma_f32 v[62:63], v[102:103], v[78:79], v[62:63] op_sel:[1,0,0] op_sel_hi:[1,1,1]\n" \
  "s_set_gpr_idx_off\n" \
  "s_sub_u32 s60, s60, 1\n" \
  "s_cbranch_scc1 Lend%=\n" \
  "s_waitcnt vmcnt(4)\n" \
  "v_mfma_f32_4x4x4_16b_bf16 v[72:75], v[108:109], v[136:137], 0 cbsz:4 abid:4\n" \
  "v_mfma_f32_4x4x4_16b_bf16 v[76:79], v[108:109], v[138:139], 0 cbsz:4 abid:4\n" \
  "v_readlane_b32 s81, v11, 1\n" \
  "v_cvt_pk_bf16_f32 v91, v90, v91\n" \
  "v_lshlrev_b32_sdwa v90, 1, v91 dst_sel:DWORD dst_unused:UNUSED_PAD src0_sel:DWORD src1_sel:WORD_0\n" \
  "v_lshlrev_b32_sdwa v91, 1, v91 dst_sel:DWORD dst_unused:UNUSED_PAD src0_sel:DWORD src1_sel:WORD_1\n" \
  "s_andn2_b32 s61, s81, 15\n" \
  "buffer_load_dwordx4 v[124:127], v8, %[xs], s61 offen\n" \
  "ds_read_u16_d16_hi v100, v90 offset:%[tb]\n" \
  "ds_read_u16_d16_hi v101, v91 offset:%[tb]\n" \
  "v_add_u32 v14, 0x100, v14\n" \
  "s_waitcnt lgkmcnt(3)\n" \
  "s_set_gpr_idx_on s84, gpr_idx(SRC2,DST)\n" \
  "v_pk_fma_f32 v[32:33], v[96:97], v[72:73], v[32:33] op_sel:[0,0,0] op_sel_hi:[0,1,1]\n" \
  "v_pk_fma_f32 v[42:43], v[96:97], v[74:75], v[42:43] op_sel:[0,0,0] op_sel_hi:[0,1,1]\n" \
  "v_pk_fma_f32 v[52:53], v[98:99], v[76:77], v[52:53] op_sel:[0,0,0] op_sel_hi:[0,1,1]\n" \
  "v_pk_fma_f32 v[62:63], v[98:99], v[78:79], v[62:63] op_sel:[0,0,0] op_sel_hi:[0,1,1]\n" \
  "s_set_gpr_idx_off\n" \
  "s_waitcnt vmcnt(4)\n" \
  "v_mfma_f32_4x4x4_16b_bf16 v[72:75], v[108:109], v[140:141], 0 cbsz:4 abid:5\n" \
  "v_mfma_f32_4x4x4_16b_bf16 v[76:79], v[108:109], v[142:143], 0 cbsz:4 abid:5\n" \
  "v_mfma_f32_4x4x4_16b_bf16 v[80:83], v[104:105], v[28:29], 0 cbsz:4 abid:2\n" \
  "v_mfma_f32_4x4x4_16b_bf16 v[84:87], v[104:105], v[30:31], 0 cbsz:4 abid:2\n" \
  "v_readlane_b32 s82, v11, 2\n" \
  "v_cvt_pk_bf16_f32 v95, v94, v95\n" \
  "v_lshlrev_b32_sdwa v94, 1, v95 dst_sel:DWORD dst_unused:UNUSED_PAD src0_sel:DWORD src1_sel:WORD_0\n" \
  "v_lshlrev_b32_sdwa v95, 1, v95 dst_sel:DWORD dst_unused:UNUSED_PAD src0_sel:DWORD src1_sel:WORD_1\n" \
  "s_andn2_b32 s61, s82, 15\n" \
  "buffer_load_dwordx4 v[128:131], v8, %[xs], s61 offen\n" \
  "ds_read_u16_d16_hi v102, v94 offset:%[tb]\n" \
  "ds_read_u16_d16_hi v103, v95 offset:%[tb]\n" \
  "s_set_gpr_idx_on s85, gpr_idx(SRC2,DST)\n" \
  "v_pk_fma_f32 v[32:33], v[96:97], v[72:73], v[32:33] op_sel:[1,0,0] op_sel_hi:[1,1,1]\n" \
  "v_pk_fma_f32 v[42:43], v[96:97], v[74:75], v[42:43] op_sel:[1,0,0] op_sel_hi:[1,1,1]\n" \
  "v_pk_fma_f32 v[52:53], v[98:99], v[76:77], v[52:53] op_sel:[1,0,0] op_sel_hi:[1,1,1]\n" \
  "v_pk_fma_f32 v[62:63], v[98:99], v[78:79], v[62:63] op_sel:[1,0,0] op_sel_hi:[1,1,1]\n" \
  "s_set_gpr_idx_off\n" \
  "s_waitcnt vmcnt(4) lgkmcnt(7)\n" \
  "v_mfma_f32_4x4x4_16b_bf16 v[72:75], v[108:109], v[144:145], 0 cbsz:4 abid:6\n" \
  "v_mfma_f32_4x4x4_16b_bf16 v[76:79], v[108:109], v[146:147], 0 cbsz:4 abid:6\n" \
  "v_readlane_b32 s83, v11, 3\n" \
  "v_cvt_pk_bf16_f32 v81, v80, v81\n" \
  "v_lshlrev_b32_sdwa v80, 1, v81 dst_sel:DWORD dst_unused:UNUSED_PAD src0_sel:DWORD src1_sel:WORD_0\n" \
  "v_lshlrev_b32_sdwa v81, 1, v81 dst_sel:DWORD dst_unused:UNUSED_PAD src0_sel:DWORD src1_sel:WORD_1\n" \
  "s_andn2_b32 s61, s83, 15\n" \
  "buffer_load_dwordx4 v[132:135], v8, %[xs], s61 offen\n" \
  "ds_read_u16_d16_hi v96, v80 offset:%[tb]\n" \
  "ds_read_u16_d16_hi v97, v81 offset:%[tb]\n" \
  "v_readfirstlane_b32 s66, v111\n" \
  "s_setprio 1\n" \
  "s_add_u32 s67, s60, %[fo]\n" \
  "s_cmp_ge_u32 s67, s66\n" \
  "s_cbranch_scc0 Lf5_%=\n" \
  "s_setprio 2\n" \
  "Lf5_%=:\n" \
  "s_waitcnt lgkmcnt(2)\n" \
  "s_set_gpr_idx_on s86, gpr_idx(SRC2,DST)\n" \
  "v_pk_fma_f32 v[32:33], v[100:101], v[72:73], v[32:33] op_sel:[0,0,0] op_sel_hi:[0,1,1]\n" \
  "v_pk_fma_f32 v[42:43], v[100:101], v[74:75], v[42:43] op_sel:[0,0,0] op_sel_hi:[0,1,1]\n" \
  "v_pk_fma_f32 v[52:53], v[102:103], v[76:77], v[52:53] op_sel:[0,0,0] op_sel_hi:[0,1,1]\n" \
  "v_pk_fma_f32 v[62:63], v[102:103], v[78:79], v[62:63] op_sel:[0,0,0] op_sel_hi:[0,1,1]\n" \
  "s_set_gpr_idx_off\n" \
  "s_waitcnt vmcnt(4) lgkmcnt(6)\n" \
  "v_mfma_f32_4x4x4_16b_bf16 v[72:75], v[108:109], v[148:149], 0 cbsz:4 abid:7\n" \
  "v_mfma_f32_4x4x4_16b_bf16 v[76:79], v[108:109], v[150:151], 0 cbsz:4 abid:7\n" \
  "v_mfma_f32_4x4x4_16b_bf16 v[112:115], v[106:107], v[20:21], 0\n" \
  "v_readlane_b32 s84, v11, 4\n" \
  "v_cvt_pk_bf16_f32 v85, v84, v85\n" \
  "v_lshlrev_b32_sdwa v84, 1, v85 dst_sel:DWORD dst_unused:UNUSED_PAD src0_sel:DWORD src1_sel:WORD_0\n" \
  "v_lshlrev_b32_sdwa v85, 1, v85 dst_sel:DWORD dst_unused:UNUSED_PAD src0_sel:DWORD src1_sel:WORD_1\n" \
  "s_andn2_b32 s61, s84, 15\n" \
  "buffer_load_dwordx4 v[136:139], v8, %[xs], s61 offen\n" \
  "ds_read_u16_d16_hi v98, v84 offset:%[tb]\n" \
  "ds_read_u16_d16_hi v99, v85 offset:%[tb]\n" \
  "ds_read_b32 v10, v12 offset:64\n" \
  "s_set_gpr_idx_on s87, gpr_idx(SRC2,DST)\n" \
  "v_pk_fma_f32 v[32:33], v[100:101], v[72:73], v[32:33] op_sel:[1,0,0] op_sel_hi:[1,1,1]\n" \
  "v_pk_fma_f32 v[42:43], v[100:101], v[74:75], v[42:43] op_sel:[1,0,0] op_sel_hi:[1,1,1]\n" \
  "v_pk_fma_f32 v[52:53], v[102:103], v[76:77], v[52:53] op_sel:[1,0,0] op_sel_hi:[1,1,1]\n" \
  "v_pk_fma_f32 v[62:63], v[102:103], v[78:79], v[62:63] op_sel:[1,0,0] op_sel_hi:[1,1,1]\n" \
  "s_set_gpr_idx_off\n" \
  "s_sub_u32 s60, s60, 1\n" \
  "s_cbranch_scc1 Lend%=\n" \
  "s_waitcnt vmcnt(4)\n" \
  "v_mfma_f32_4x4x4_16b_bf16 v[72:75], v[108:109], v[120:121], 0 cbsz:4 abid:8\n" \
  "v_mfma_f32_4x4x4_16b_bf16 v[76:79], v[108:109], v[122:123], 0 cbsz:4 abid:8\n" \
  "v_readlane_b32 s85, v11, 5\n" \
  "v_cvt_pk_bf16_f32 v83, v82, v83\n" \
  "v_lshlrev_b32_sdwa v82, 1, v83 dst_sel:DWORD dst_unused:UNUSED_PAD src0_sel:DWORD src1_sel:WORD_0\n" \
  "v_lshlrev_b32_sdwa v83, 1, v83 dst_sel:DWORD dst_unused:UNUSED_PAD src0_sel:DWORD src1_sel:WORD_1\n" \
  "s_andn2_b32 s61, s85, 15\n" \
  "buffer_load_dwordx4 v[140:143], v8, %[xs], s61 offen\n" \
  "ds_read_u16_d16_hi v100, v82 offset:%[tb]\n" \
  "ds_read_u16_d16_hi v101, v83 offset:%[tb]\n" \
  "v_add_u32 v12, 64, v12\n" \
  "s_waitcnt lgkmcnt(3)\n" \
  "s_set_gpr_idx_on s80, gpr_idx(SRC2,DST)\n" \
  "v_pk_fma_f32 v[32:33], v[96:97], v[72:73], v[32:33] op_sel:[0,0,0] op_sel_hi:[0,1,1]\n" \
  "v_pk_fma_f32 v[42:43], v[96:97], v[74:75], v[42:43] op_sel:[0,0,0] op_sel_hi:[0,1,1]\n" \
  "v_pk_fma_f32 v[52:53], v[98:99], v[76:77], v[52:53] op_sel:[0,0,0] op_sel_hi:[0,1,1]\n" \
  "v_pk_fma_f32 v[62:63], v[98:99], v[78:79], v[62:63] op_sel:[0,0,0] op_sel_hi:[0,1,1]\n" \
  "s_set_gpr_idx_off\n" \
  "s_waitcnt vmcnt(4)\n" \
  "v_mfma_f32_4x4x4_16b_bf16 v[72:75], v[108:109], v[124:125], 0 cbsz:4 abid:9\n" \
  "v_mfma_f32_4x4x4_16b_bf16 v[76:79], v[108:109], v[126:127], 0 cbsz:4 abid:9\n" \
  "v_mfma_f32_4x4x4_16b_bf16 v[88:91], v[104:105], v[28:29], 0 cbsz:4 abid:3\n" \
  "v_mfma_f32_4x4x4_16b_bf16 v[92:95], v[104:105], v[30:31], 0 cbsz:4 abid:3\n" \
  "v_readlane_b32 s86, v11, 6\n" \
  "v_cvt_pk_bf16_f32 v87, v86, v87\n" \
  "v_lshlrev_b32_sdwa v86, 1, v87 dst_sel:DWORD dst_unused:UNUSED_PAD src0_sel:DWORD src1_sel:WORD_0\n" \
  "v_lshlrev_b32_sdwa v87, 1, v87 dst_sel:DWORD dst_unused:UNUSED_PAD src0_sel:DWORD src1_sel:WORD_1\n" \
  "s_andn2_b32 s61, s86, 15\n" \
  "buffer_load_dwordx4 v[144:147], v8, %[xs], s61 offen\n" \
  "ds_read_u16_d16_hi v102, v86 offset:%[tb]\n" \
  "ds_read_u16_d16_hi v103, v87 offset:%[tb]\n" \
  "v_mov_b32 v111, s60\n" \
  "ds_write2_b32 v110, v111, v111 offset1:8\n" \
  "s_set_gpr_idx_on s81, gpr_idx(SRC2,DST)\n" \
  "v_pk_fma_f32 v[32:33], v[96:97], v[72:73], v[32:33] op_sel:[1,0,0] op_sel_hi:[1,1,1]\n" \
  "v_pk_fma_f32 v[42:43], v[96:97], v[74:75], v[42:43] op_sel:[1,0,0] op_sel_hi:[1,1,1]\n" \
  "v_pk_fma_f32 v[52:53], v[98:99], v[76:77], v[52:53] op_sel:[1,0,0] op_sel_hi:[1,1,1]\n" \
  "v_pk_fma_f32 v[62:63], v[98:99], v[78:79], v[62:63] op_sel:[1,0,0] op_sel_hi:[1,1,1]\n" \
  "s_set_gpr_idx_off\n" \
  "s_waitcnt vmcnt(4)\n" \
  "v_mfma_f32_4x4x4_16b_bf16 v[72:75], v[108:109], v[128:129], 0 cbsz:4 abid:10\n" \
  "v_mfma_f32_4x4x4_16b_bf16 v[76:79], v[108:109], v[130:131], 0 cbsz:4 abid:10\n" \
  "v_readlane_b32 s87, v11, 7\n" \
  "v_cvt_pk_bf16_f32 v89, v88, v89\n" \
  "v_lshlrev_b32_sdwa v88, 1, v89 dst_sel:DWORD dst_unused:UNUSED_PAD src0_sel:DWORD src1_sel:WORD_0\n" \
  "v_lshlrev_b32_sdwa v89, 1, v89 dst_sel:DWORD dst_unused:UNUSED_PAD src0_sel:DWORD src1_sel:WORD_1\n" \
  "s_andn2_b32 s61, s87, 15\n" \
  "buffer_load_dwordx4 v[148:151], v8, %[xs], s61 offen\n" \
  "ds_read_u16_d16_hi v96, v88 offset:%[tb]\n" \
  "ds_read_u16_d16_hi v97, v89 offset:%[tb]\n" \
  "ds_read_b64 v[104:105], v15 offset:256\n" \
  "ds_read_b32 v111, v110 offset:16\n" \
  "s_waitcnt lgkmcnt(5)\n" \
  "s_set_gpr_idx_on s82, gpr_idx(SRC2,DST)\n" \
  "v_pk_fma_f32 v[32:33], v[100:101], v[72:73], v[32:33] op_sel:[0,0,0] op_sel_hi:[0,1,1]\n" \
  "v_pk_fma_f32 v[42:43], v[100:101], v[74:75], v[42:43] op_sel:[0,0,0] op_sel_hi:[0,1,1]\n" \
  "v_pk_fma_f32 v[52:53], v[102:103], v[76:77], v[52:53] op_sel:[0,0,0] op_sel_hi:[0,1,1]\n" \
  "v_pk_fma_f32 v[62:63], v[102:103], v[78:79], v[62:63] op_sel:[0,0,0] op_sel_hi:[0,1,1]\n" \
  "s_set_gpr_idx_off\n" \
  "s_waitcnt vmcnt(4) lgkmcnt(9)\n" \
  "v_mfma_f32_4x4x4_16b_bf16 v[72:75], v[108:109], v[132:133], 0 cbsz:4 abid:11\n" \
  "v_mfma_f32_4x4x4_16b_bf16 v[76:79], v[108:109], v[134:135], 0 cbsz:4 abid:11\n" \
  "v_mfma_f32_4x4x4_16b_bf16 v[116:119], v[106:107], v[22:23], 0\n" \
  "v_readlane_b32 s80, v10, 0\n" \
  "v_cvt_pk_bf16_f32 v93, v92, v93\n" \
  "v_lshlrev_b32_sdwa v92, 1, v93 dst_sel:DWORD dst_unused:UNUSED_PAD src0_sel:DWORD src1_sel:WORD_0\n" \
  "v_lshlrev_b32_sdwa v93, 1, v93 dst_sel:DWORD dst_unused:UNUSED_PAD src0_sel:DWORD src1_sel:WORD_1\n" \
  "s_andn2_b32 s61, s80, 15\n" \
  "buffer_load_dwordx4 v[120:123], v8, %[xs], s61 offen\n" \
  "ds_read_u16_d16_hi v98, v92 offset:%[tb]\n" \
  "ds_read_u16_d16_hi v99, v93 offset:%[tb]\n" \
  "s_set_gpr_idx_on s83, gpr_idx(SRC2,DST)\n" \
  "v_pk_fma_f32 v[32:33], v[100:101], v[72:73], v[32:33] op_sel:[1,0,0] op_sel_hi:[1,1,1]\n" \
  "v_pk_fma_f32 v[42:43], v[100:101], v[74:75], v[42:43] op_sel:[1,0,0] op_sel_hi:[1,1,1]\n" \
  "v_pk_fma_f32 v[52:53], v[102:103], v[76:77], v[52:53] op_sel:[1,0,0] op_sel_hi:[1,1,1]\n" \
  "v_pk_fma_f32 v[62:63], v[102:103], v[78:79], v[62:63] op_sel:[1,0,0] op_sel_hi:[1,1,1]\n" \
  "s_set_gpr_idx_off\n" \
  "s_sub_u32 s60, s60, 1\n" \
  "s_cbranch_scc1 Lend%=\n" \
  "s_waitcnt vmcnt(4)\n" \
  "v_mfma_f32_4x4x4_16b_bf16 v[72:75], v[108:109], v[136:137], 0 cbsz:4 abid:12\n" \
  "v_mfma_f32_4x4x4_16b_bf16 v[76:79], v[108:109], v[138:139], 0 cbsz:4 abid:12\n" \
  "v_readlane_b32 s81, v10, 1\n" \
  "v_cvt_pk_bf16_f32 v91, v90, v91\n" \
  "v_lshlrev_b32_sdwa v90, 1, v91 dst_sel:DWORD dst_unused:UNUSED_PAD src0_sel:DWORD src1_sel:WORD_0\n" \
  "v_lshlrev_b32_sdwa v91, 1, v91 dst_sel:DWORD dst_unused:UNUSED_PAD src0_sel:DWORD src1_sel:WORD_1\n" \
  "s_andn2_b32 s61, s81, 15\n" \
  "buffer_load_dwordx4 v[124:127], v8, %[xs], s61 offen\n" \
  "ds_read_u16_d16_hi v100, v90 offset:%[tb]\n" \
  "ds_read_u16_d16_hi v101, v91 offset:%[tb]\n" \
  "v_add_u32 v15, 0x100, v15\n" \
  "s_waitcnt lgkmcnt(2)\n" \
  "s_set_gpr_idx_on s84, gpr_idx(SRC2,DST)\n" \
  "v_pk_fma_f32 v[32:33], v[96:97], v[72:73], v[32:33] op_sel:[0,0,0] op_sel_hi:[0,1,1]\n" \
  "v_pk_fma_f32 v[42:43], v[96:97], v[74:75], v[42:43] op_sel:[0,0,0] op_sel_hi:[0,1,1]\n" \
  "v_pk_fma_f32 v[52:53], v[98:99], v[76:77], v[52:53] op_sel:[0,0,0] op_sel_hi:[0,1,1]\n" \
  "v_pk_fma_f32 v[62:63], v[98:99], v[78:79], v[62:63] op_sel:[0,0,0] op_sel_hi:[0,1,1]\n" \
  "s_set_gpr_idx_off\n" \
  "s_waitcnt vmcnt(4) lgkmcnt(5)\n" \
  "v_mfma_f32_4x4x4_16b_bf16 v[72:75], v[108:109], v[140:141], 0 cbsz:4 abid:13\n" \
  "v_mfma_f32_4x4x4_16b_bf16 v[76:79], v[108:109], v[142:143], 0 cbsz:4 abid:13\n" \
  "v_mfma_f32_4x4x4_16b_bf16 v[80:83], v[104:105], v[28:29], 0 cbsz:4\n" \
  "v_mfma_f32_4x4x4_16b_bf16 v[84:87], v[104:105], v[30:31], 0 cbsz:4\n" \
  "v_readlane_b32 s82, v10, 2\n" \
  "v_cvt_pk_bf16_f32 v95, v94, v95\n" \
  "v_lshlrev_b32_sdwa v94, 1, v95 dst_sel:DWORD dst_unused:UNUSED_PAD src0_sel:DWORD src1_sel:WORD_0\n" \
  "v_lshlrev_b32_sdwa v95, 1, v95 dst_sel:DWORD dst_unused:UNUSED_PAD src0_sel:DWORD src1_sel:WORD_1\n" \
  "s_andn2_b32 s61, s82, 15\n" \
  "buffer_load_dwordx4 v[128:131], v8, %[xs], s61 offen\n" \
  "ds_read_u16_d16_hi v102, v94 offset:%[tb]\n" \
  "ds_read_u16_d16_hi v103, v95 offset:%[tb]\n" \
  "s_set_gpr_idx_on s85, gpr_idx(SRC2,DST)\n" \
  "v_pk_fma_f32 v[32:33], v[96:97], v[72:73], v[32:33] op_sel:[1,0,0] op_sel_hi:[1,1,1]\n" \
  "v_pk_fma_f32 v[42:43], v[96:97], v[74:75], v[42:43] op_sel:[1,0,0] op_sel_hi:[1,1,1]\n" \
  "v_pk_fma_f32 v[52:53], v[98:99], v[76:77], v[52:53] op_sel:[1,0,0] op_sel_hi:[1,1,1]\n" \
  "v_pk_fma_f32 v[62:63], v[98:99], v[78:79], v[62:63] op_sel:[1,0,0] op_sel_hi:[1,1,1]\n" \
  "s_set_gpr_idx_off\n" \
  "s_waitcnt vmcnt(4) lgkmcnt(6)\n" \
  "v_mfma_f32_4x4x4_16b_bf16 v[72:75], v[108:109], v[144:145], 0 cbsz:4 abid:14\n" \
  "v_mfma_f32_4x4x4_16b_bf16 v[76:79], v[108:109], v[146:147], 0 cbsz:4 abid:14\n" \
  "v_readlane_b32 s83, v10, 3\n" \
  "v_cvt_pk_bf16_f32 v81, v80, v81\n" \
  "v_lshlrev_b32_sdwa v80, 1, v81 dst_sel:DWORD dst_unused:UNUSED_PAD src0_sel:DWORD src1_sel:WORD_0\n" \
  "v_lshlrev_b32_sdwa v81, 1, v81 dst_sel:DWORD dst_unused:UNUSED_PAD src0_sel:DWORD src1_sel:WORD_1\n" \
  "s_andn2_b32 s61, s83, 15\n" \
  "buffer_load_dwordx4 v[132:135], v8, %[xs], s61 offen\n" \
  "ds_read_u16_d16_hi v96, v80 offset:%[tb]\n" \
  "ds_read_u16_d16_hi v97, v81 offset:%[tb]\n" \
  "ds_read_b32 v11, v12 offset:32\n" \
  "v_readfirstlane_b32 s66, v111\n" \
  "s_setprio 1\n" \
  "s_add_u32 s67, s60, %[fo]\n" \
  "s_cmp_ge_u32 s67, s66\n" \
  "s_cbranch_scc0 Lf13_%=\n" \
  "s_setprio 2\n" \
  "Lf13_%=:\n" \
  "v_cvt_pk_bf16_f32 v19, v112, v116\n" \
  "s_waitcnt lgkmcnt(3)\n" \
  "s_set_gpr_idx_on s86, gpr_idx(SRC2,DST)\n" \
  "v_pk_fma_f32 v[32:33], v[100:101], v[72:73], v[32:33] op_sel:[0,0,0] op_sel_hi:[0,1,1]\n" \
  "v_pk_fma_f32 v[42:43], v[100:101], v[74:75], v[42:43] op_sel:[0,0,0] op_sel_hi:[0,1,1]\n" \
  "v_pk_fma_f32 v[52:53], v[102:103], v[76:77], v[52:53] op_sel:[0,0,0] op_sel_hi:[0,1,1]\n" \
  "v_pk_fma_f32 v[62:63], v[102:103], v[78:79], v[62:63] op_sel:[0,0,0] op_sel_hi:[0,1,1]\n" \
  "s_set_gpr_idx_off\n" \
  "s_waitcnt vmcnt(4)\n" \
  "v_mfma_f32_4x4x4_16b_bf16 v[112:115], v[106:107], v[24:25], 0\n" \
  "v_mfma_f32_4x4x4_16b_bf16 v[116:119], v[106:107], v[26:27], 0\n" \
  "v_mfma_f32_4x4x4_16b_bf16 v[72:75], v[108:109], v[148:149], 0 cbsz:4 abid:15\n" \
  "v_mfma_f32_4x4x4_16b_bf16 v[76:79], v[108:109], v[150:151], 0 cbsz:4 abid:15\n" \
  "v_readlane_b32 s84, v10, 4\n" \
  "v_cvt_pk_bf16_f32 v85, v84, v85\n" \
  "v_lshlrev_b32_sdwa v84, 1, v85 dst_sel:DWORD dst_unused:UNUSED_PAD src0_sel:DWORD src1_sel:WORD_0\n" \
  "v_lshlrev_b32_sdwa v85, 1, v85 dst_sel:DWORD dst_unused:UNUSED_PAD src0_sel:DWORD src1_sel:WORD_1\n" \
  "s_andn2_b32 s61, s84, 15\n" \
  "buffer_load_dwordx4 v[136:139], v8, %[xs], s61 offen\n" \
  "ds_read_u16_d16_hi v98, v84 offset:%[tb]\n" \
  "ds_read_u16_d16_hi v99, v85 offset:%[tb]\n" \
  "v_cvt_pk_bf16_f32 v109, v112, v116\n" \
  "v_mov_b32 v108, v19\n" \
  "s_set_gpr_idx_on s87, gpr_idx(SRC2,DST)\n" \
  "v_pk_fma_f32 v[32:33], v[100:101], v[72:73], v[32:33] op_sel:[1,0,0] op_sel_hi:[1,1,1]\n" \
  "v_pk_fma_f32 v[42:43], v[100:101], v[74:75], v[42:43] op_sel:[1,0,0] op_sel_hi:[1,1,1]\n" \
  "v_pk_fma_f32 v[52:53], v[102:103], v[76:77], v[52:53] op_sel:[1,0,0] op_sel_hi:[1,1,1]\n" \
  "v_pk_fma_f32 v[62:63], v[102:103], v[78:79], v[62:63] op_sel:[1,0,0] op_sel_hi:[1,1,1]\n" \
  "s_set_gpr_idx_off\n" \
  "s_sub_u32 s60, s60, 1\n" \
  "s_cbranch_scc1 Lend%=\n" \
  "s_waitcnt vmcnt(4)\n" \
  "v_mfma_f32_4x4x4_16b_bf16 v[72:75], v[108:109], v[120:121], 0 cbsz:4\n" \
  "v_mfma_f32_4x4x4_16b_bf16 v[76:79], v[108:109], v[122:123], 0 cbsz:4\n" \
  "v_readlane_b32 s85, v10, 5\n" \
  "v_cvt_pk_bf16_f32 v83, v82, v83\n" \
  "v_lshlrev_b32_sdwa v82, 1, v83 dst_sel:DWORD dst_unused:UNUSED_PAD src0_sel:DWORD src1_sel:WORD_0\n" \
  "v_lshlrev_b32_sdwa v83, 1, v83 dst_sel:DWORD dst_unused:UNUSED_PAD src0_sel:DWORD src1_sel:WORD_1\n" \
  "s_andn2_b32 s61, s85, 15\n" \
  "buffer_load_dwordx4 v[140:143], v8, %[xs], s61 offen\n" \
  "ds_read_u16_d16_hi v100, v82 offset:%[tb]\n" \
  "ds_read_u16_d16_hi v101, v83 offset:%[tb]\n" \
  "s_branch Lbody%=\n" \
  "Lend%=:\n" \
  "s_setprio 3\n" \
  "v_mov_b32 v20, 0\n" \
  "v_mov_b32 v21, %[bit]\n" \
  "v_cvt_pk_bf16_f32 v32, v32, v52\n" \
  "v_cvt_pk_bf16_f32 v33, v33, v53\n" \
  "v_cvt_pk_bf16_f32 v42, v42, v62\n" \
  "v_cvt_pk_bf16_f32 v43, v43, v63\n" \
  "v_cvt_pk_bf16_f32 v34, v34, v54\n" \
  "v_cvt_pk_bf16_f32 v35, v35, v55\n" \
  "v_cvt_pk_bf16_f32 v44, v44, v64\n" \
  "v_cvt_pk_bf16_f32 v45, v45, v65\n" \
  "ds_write2st64_b32 v152, v32, v34 offset0:0 offset1:4\n" \
  "ds_write2st64_b32 v153, v33, v35 offset0:0 offset1:4\n" \
  "ds_write2st64_b32 v154, v42, v44 offset0:0 offset1:4\n" \
  "ds_write2st64_b32 v155, v43, v45 offset0:0 offset1:4\n" \
  "v_cvt_pk_bf16_f32 v36, v36, v56\n" \
  "v_cvt_pk_bf16_f32 v37, v37, v57\n" \
  "v_cvt_pk_bf16_f32 v46, v46, v66\n" \
  "v_cvt_pk_bf16_f32 v47, v47, v67\n" \
  "v_cvt_pk_bf16_f32 v38, v38, v58\n" \
  "v_cvt_pk_bf16_f32 v39, v39, v59\n" \
  "v_cvt_pk_bf16_f32 v48, v48, v68\n" \
  "v_cvt_pk_bf16_f32 v49, v49, v69\n" \
  "ds_write2st64_b32 v152, v36, v38 offset0:8 offset1:12\n" \
  "ds_write2st64_b32 v153, v37, v39 offset0:8 offset1:12\n" \
  "ds_write2st64_b32 v154, v46, v48 offset0:8 offset1:12\n" \
  "ds_write2st64_b32 v155, v47, v49 offset0:8 offset1:12\n" \
  "v_cvt_pk_bf16_f32 v40, v40, v60\n" \
  "v_cvt_pk_bf16_f32 v41, v41, v61\n" \
  "v_cvt_pk_bf16_f32 v50, v50, v70\n" \
  "v_cvt_pk_bf16_f32 v51, v51, v71\n" \
  "ds_write2_b32 v156, v40, v41 offset0:0 offset1:16\n" \
  "ds_write2_b32 v156, v50, v51 offset0:32 offset1:48\n" \
  "s_mov_b64 exec, 1\n" \
  "ds_or_b32 v20, v21 offset:%[rd]\n" \
  "s_endpgm\n" \
  "Lhr%=:\n" \
  "s_mov_b64 exec, vcc\n" \
  "s_sub_u32 s63, s63, 1\n" \
  "s_cbranch_scc0 Lh%=\n" \
  "s_mov_b64 exec, -1\n" \
  "Lover%=:\n" \
  "v_mov_b32 v113, 1\n" \
  "ds_write_b32 v117, v113 offset:%[fl]\n" \
  "s_mov_b32 s62, 0\n" \
  "v_mov_b32 v114, 0\n" \
  "v_mov_b32 v115, 0\n" \
  "s_branch Lld%=\n" \
  "Lkr%=:\n" \
  "buffer_load_dword v84, v76, %[ws], %[rk] offen sc1\n" \
  "buffer_load_dword v85, v77, %[ws], %[rk] offen sc1\n" \
  "buffer_load_dword v86, v78, %[ws], %[rk] offen sc1\n" \
  "s_cmpk_le_u32 s62, 0xc0\n" \
  "s_cbranch_scc1 Lkb%=\n" \
  "buffer_load_dword v87, v79, %[ws], %[rk] offen sc1\n" \
  "Lkb%=:\n" \
  "s_waitcnt vmcnt(0)\n" \
  "s_sub_u32 s63, s63, 1\n" \
  "s_cbranch_scc0 Lk%=\n" \
  "v_mov_b32 v113, 1\n" \
  "ds_write_b32 v117, v113 offset:%[fl]\n" \
  "s_branch Lkc%=\n" \
  "Lc3%=:\n" \
  "v_lshlrev_b32 v91, 1, v87\n" \
  "v_lshlrev_b32 v95, 4, v87\n" \
  "v_lshrrev_b32 v107, 2, v87\n" \
  "v_and_or_b32 v91, v91, v118, %[wb]\n" \
  "v_lshlrev_b32_e64 v103, v95, 4\n" \
  "v_and_b32 v107, 0xfc, v107\n" \
  "s_mov_b64 exec, s[72:73]\n" \
  "ds_add_rtn_u32 v99, v91, v103 offset:%[hi]\n" \
  "ds_add_u32 v107, v116 offset:%[dg]\n" \
  "s_mov_b64 exec, -1\n" \
  "s_branch Lc4%=\n" \
  "Llo%=:\n" \
  "v_mov_b32 v113, 1\n" \
  "ds_write_b32 v117, v113 offset:%[fl]\n" \
  "s_branch Llc%=\n" \
  "Lslow%=:\n" \
  "s_waitcnt vmcnt(0) lgkmcnt(0)\n" \
  "s_barrier\n" \
  "s_branch Lexit%=\n" \
  "Lq3%=:\n" \
  "s_mov_b64 exec, -1\n" \
  "v_and_b32 v103, 0x7fc, v91\n" \
  "ds_read_b32 v79, v91 offset:%[hi]\n" \
  "ds_read_b32 v107, v103 offset:%[to]\n" \
  "s_waitcnt lgkmcnt(0)\n" \
  "v_add3_u32 v91, v79, v107, v99\n" \
  "v_lshrrev_b32 v91, v95, v91\n" \
  "v_bfe_u32 v75, v91, 13, 3\n" \
  "v_and_b32 v91, 0x1ffc, v91\n" \
  "v_lshl_or_b32 v75, v75, 1, v87\n" \
  "s_mov_b64 exec, s[72:73]\n" \
  "ds_write_b32 v91, v75 offset:%[so]\n" \
  "s_branch Lq4%=\n" \
  "Lpr%=:\n" \
  "s_cmpk_le_u32 s62, 0xc0\n" \
  "s_cbranch_scc1 Lpb%=\n" \
  "buffer_load_dwordx4 v[44:47], v83, %[ws], %[rp] offen sc1\n" \
  "Lpb%=:\n" \
  "buffer_load_dwordx4 v[32:35], v80, %[ws], %[rp] offen sc1\n" \
  "buffer_load_dwordx4 v[36:39], v81, %[ws], %[rp] offen sc1\n" \
  "buffer_load_dwordx4 v[40:43], v82, %[ws], %[rp] offen sc1\n" \
  "s_sub_u32 s63, s63, 1\n" \
  "s_waitcnt vmcnt(0)\n" \
  "s_cbranch_scc0 Lpc%=\n" \
  "s_branch Lpw%=\n" \
  "Lexit%=:\n" \
  "s_mov_b32 %[sl], s61\n"
#define STREAM_CLOBBERS \
  "v10", "v11", "v19", "v21", "v22", "v23", "v24", "v25", "v26", "v27", "v28", "v29", "v30", "v31", "v32", \
  "v33", "v34", "v35", "v36", "v37", "v38", "v39", "v40", "v41", "v42", "v43", "v44", "v45", "v46", "v47", \
  "v48", "v49", "v50", "v51", "v52", "v53", "v54", "v55", "v56", "v57", "v58", "v59", "v60", "v61", "v62", \
  "v63", "v64", "v65", "v66", "v67", "v68", "v69", "v70", "v71", "v72", "v73", "v74", "v75", "v76", "v77", \
  "v78", "v79", "v80", "v81", "v82", "v83", "v84", "v85", "v86", "v87", "v88", "v89", "v90", "v91", "v92", \
  "v93", "v94", "v95", "v96", "v97", "v98", "v99", "v100", "v101", "v102", "v103", "v104", "v105", "v106", \
  "v107", "v108", "v109", "v111", "v112", "v113", "v114", "v115", "v116", "v117", "v118", "v119", "v120", \
  "v121", "v122", "v123", "v124", "v125", "v126", "v127", "v128", "v129", "v130", "v131", "v132", "v133", \
  "v134", "v135", "s60", "s61", "s62", "s63", "s64", "s65", "s66", "s67", "s68", "s69", "s70", "s71", "s72", \
  "s73", "s74", "s75", "s76", "s77", "s78", "s79", "s80", "s81", "s82", "s83", "s84", "s85", "s86", "s87", \
  "scc", "vcc", "memory"
#define RKL 3
#define MIX_ASM \
  "v_mov_b32 v12, 0\n" \
  "s_movk_i32 s60, 0x3ff\n" \
  "Lz%=:\n" \
  "s_sleep %[sl]\n" \
  "ds_read_b32 v13, v1\n" \
  "ds_read_b32 v14, v12 offset:%[rd]\n" \
  "s_waitcnt lgkmcnt(0)\n" \
  "v_readfirstlane_b32 s61, v13\n" \
  "v_readfirstlane_b32 s62, v14\n" \
  "s_cmp_gt_u32 s61, 4\n" \
  "s_cselect_b32 s61, s62, 1\n" \
  "s_cmp_eq_u32 s61, 0\n" \
  "s_cbranch_scc1 Lz%=\n" \
  "s_setprio 1\n" \
  "Lp%=:\n" \
  "ds_read_b32 v13, v12 offset:%[rd]\n" \
  "s_waitcnt lgkmcnt(0)\n" \
  "v_readfirstlane_b32 s61, v13\n" \
  "s_andn2_b32 s62, 15, s61\n" \
  "s_cselect_b32 s62, 0, 0x100\n" \
  "s_andn2_b32 s63, 0xf0, s61\n" \
  "s_cselect_b32 s63, 0, 0x200\n" \
  "s_or_b32 s61, s61, s62\n" \
  "s_or_b32 s61, s61, s63\n" \
  "s_and_b32 s61, s61, s60\n" \
  "s_cbranch_scc1 Lgo%=\n" \
  "s_sleep 1\n" \
  "s_branch Lp%=\n" \
  "Lgo%=:\n" \
  "s_andn2_b32 s60, s60, s61\n" \
  "s_ff1_i32_b32 s62, s61\n" \
  "s_bitset0_b32 s61, s62\n" \
  "s_lshl_b32 s63, s62, 12\n" \
  "s_cmp_ge_u32 s62, 8\n" \
  "s_cselect_b32 s64, %[r5], 0\n" \
  "s_add_u32 s63, s63, s64\n" \
  "v_add_u32 v14, s63, v0\n" \
  "ds_read_b128 v[64:67], v14 offset:0\n" \
  "ds_read_b128 v[68:71], v14 offset:256\n" \
  "ds_read_b128 v[72:75], v14 offset:512\n" \
  "ds_read_b128 v[76:79], v14 offset:768\n" \
  "s_set_gpr_idx_on s62, gpr_idx(SRC0)\n" \
  "v_mov_b32 v16, v2\n" \
  "s_set_gpr_idx_off\n" \
  "s_cmp_eq_u32 s61, 0\n" \
  "s_cbranch_scc1 Ls%=\n" \
  "s_ff1_i32_b32 s62, s61\n" \
  "s_bitset0_b32 s61, s62\n" \
  "s_lshl_b32 s63, s62, 12\n" \
  "s_cmp_ge_u32 s62, 8\n" \
  "s_cselect_b32 s64, %[r5], 0\n" \
  "s_add_u32 s63, s63, s64\n" \
  "v_add_u32 v14, s63, v0\n" \
  "ds_read_b128 v[80:83], v14 offset:0\n" \
  "ds_read_b128 v[84:87], v14 offset:256\n" \
  "ds_read_b128 v[88:91], v14 offset:512\n" \
  "ds_read_b128 v[92:95], v14 offset:768\n" \
  "s_set_gpr_idx_on s62, gpr_idx(SRC0)\n" \
  "v_mov_b32 v17, v2\n" \
  "s_set_gpr_idx_off\n" \
  "s_waitcnt lgkmcnt(4)\n" \
  "v_mfma_f32_16x16x32_bf16 v[96:99], v[64:67], v[32:35], 0\n" \
  "v_mfma_f32_16x16x32_bf16 v[100:103], v[64:67], v[36:39], 0\n" \
  "v_mfma_f32_16x16x32_bf16 v[96:99], v[68:71], v[40:43], v[96:99]\n" \
  "v_mfma_f32_16x16x32_bf16 v[100:103], v[68:71], v[44:47], v[100:103]\n" \
  "v_mfma_f32_16x16x32_bf16 v[96:99], v[72:75], v[48:51], v[96:99]\n" \
  "v_mfma_f32_16x16x32_bf16 v[100:103], v[72:75], v[52:55], v[100:103]\n" \
  "v_mfma_f32_16x16x32_bf16 v[96:99], v[76:79], v[56:59], v[96:99]\n" \
  "v_mfma_f32_16x16x32_bf16 v[100:103], v[76:79], v[60:63], v[100:103]\n" \
  "Lb1_%=:\n" \
  "s_cmp_eq_u32 s61, 0\n" \
  "s_cbranch_scc1 Ll1_%=\n" \
  "s_ff1_i32_b32 s62, s61\n" \
  "s_bitset0_b32 s61, s62\n" \
  "s_lshl_b32 s63, s62, 12\n" \
  "s_cmp_ge_u32 s62, 8\n" \
  "s_cselect_b32 s64, %[r5], 0\n" \
  "s_add_u32 s63, s63, s64\n" \
  "v_add_u32 v14, s63, v0\n" \
  "ds_read_b128 v[64:67], v14 offset:0\n" \
  "ds_read_b128 v[68:71], v14 offset:256\n" \
  "ds_read_b128 v[72:75], v14 offset:512\n" \
  "ds_read_b128 v[76:79], v14 offset:768\n" \
  "s_waitcnt lgkmcnt(4)\n" \
  "v_mfma_f32_16x16x32_bf16 v[104:107], v[80:83], v[32:35], 0\n" \
  "v_mfma_f32_16x16x32_bf16 v[108:111], v[80:83], v[36:39], 0\n" \
  "v_cvt_pk_bf16_f32 v96, v96, v97\n" \
  "v_cvt_pk_bf16_f32 v97, v98, v99\n" \
  "v_mfma_f32_16x16x32_bf16 v[104:107], v[84:87], v[40:43], v[104:107]\n" \
  "v_cvt_pk_bf16_f32 v98, v100, v101\n" \
  "v_cvt_pk_bf16_f32 v99, v102, v103\n" \
  "v_mfma_f32_16x16x32_bf16 v[108:111], v[84:87], v[44:47], v[108:111]\n" \
  "buffer_store_dwordx4 v[96:99], v16, %[os], 0 offen sc1\n" \
  "v_mfma_f32_16x16x32_bf16 v[104:107], v[88:91], v[48:51], v[104:107]\n" \
  "v_mfma_f32_16x16x32_bf16 v[108:111], v[88:91], v[52:55], v[108:111]\n" \
  "s_set_gpr_idx_on s62, gpr_idx(SRC0)\n" \
  "v_mov_b32 v16, v2\n" \
  "s_set_gpr_idx_off\n" \
  "v_mfma_f32_16x16x32_bf16 v[104:107], v[92:95], v[56:59], v[104:107]\n" \
  "v_mfma_f32_16x16x32_bf16 v[108:111], v[92:95], v[60:63], v[108:111]\n" \
  "Lb0_%=:\n" \
  "s_cmp_eq_u32 s61, 0\n" \
  "s_cbranch_scc1 Ll0_%=\n" \
  "s_ff1_i32_b32 s62, s61\n" \
  "s_bitset0_b32 s61, s62\n" \
  "s_lshl_b32 s63, s62, 12\n" \
  "s_cmp_ge_u32 s62, 8\n" \
  "s_cselect_b32 s64, %[r5], 0\n" \
  "s_add_u32 s63, s63, s64\n" \
  "v_add_u32 v14, s63, v0\n" \
  "ds_read_b128 v[80:83], v14 offset:0\n" \
  "ds_read_b128 v[84:87], v14 offset:256\n" \
  "ds_read_b128 v[88:91], v14 offset:512\n" \
  "ds_read_b128 v[92:95], v14 offset:768\n" \
  "s_waitcnt lgkmcnt(4)\n" \
  "v_mfma_f32_16x16x32_bf16 v[96:99], v[64:67], v[32:35], 0\n" \
  "v_mfma_f32_16x16x32_bf16 v[100:103], v[64:67], v[36:39], 0\n" \
  "v_cvt_pk_bf16_f32 v104, v104, v105\n" \
  "v_cvt_pk_bf16_f32 v105, v106, v107\n" \
  "v_mfma_f32_16x16x32_bf16 v[96:99], v[68:71], v[40:43], v[96:99]\n" \
  "v_cvt_pk_bf16_f32 v106, v108, v109\n" \
  "v_cvt_pk_bf16_f32 v107, v110, v111\n" \
  "v_mfma_f32_16x16x32_bf16 v[100:103], v[68:71], v[44:47], v[100:103]\n" \
  "buffer_store_dwordx4 v[104:107], v17, %[os], 0 offen sc1\n" \
  "v_mfma_f32_16x16x32_bf16 v[96:99], v[72:75], v[48:51], v[96:99]\n" \
  "v_mfma_f32_16x16x32_bf16 v[100:103], v[72:75], v[52:55], v[100:103]\n" \
  "s_set_gpr_idx_on s62, gpr_idx(SRC0)\n" \
  "v_mov_b32 v17, v2\n" \
  "s_set_gpr_idx_off\n" \
  "v_mfma_f32_16x16x32_bf16 v[96:99], v[76:79], v[56:59], v[96:99]\n" \
  "v_mfma_f32_16x16x32_bf16 v[100:103], v[76:79], v[60:63], v[100:103]\n" \
  "s_branch Lb1_%=\n" \
  "Ll1_%=:\n" \
  "s_waitcnt lgkmcnt(0)\n" \
  "v_mfma_f32_16x16x32_bf16 v[104:107], v[80:83], v[32:35], 0\n" \
  "v_mfma_f32_16x16x32_bf16 v[108:111], v[80:83], v[36:39], 0\n" \
  "v_mfma_f32_16x16x32_bf16 v[104:107], v[84:87], v[40:43], v[104:107]\n" \
  "v_mfma_f32_16x16x32_bf16 v[108:111], v[84:87], v[44:47], v[108:111]\n" \
  "v_mfma_f32_16x16x32_bf16 v[104:107], v[88:91], v[48:51], v[104:107]\n" \
  "v_cvt_pk_bf16_f32 v96, v96, v97\n" \
  "v_cvt_pk_bf16_f32 v97, v98, v99\n" \
  "v_mfma_f32_16x16x32_bf16 v[108:111], v[88:91], v[52:55], v[108:111]\n" \
  "v_cvt_pk_bf16_f32 v98, v100, v101\n" \
  "v_cvt_pk_bf16_f32 v99, v102, v103\n" \
  "v_mfma_f32_16x16x32_bf16 v[104:107], v[92:95], v[56:59], v[104:107]\n" \
  "buffer_store_dwordx4 v[96:99], v16, %[os], 0 offen sc1\n" \
  "v_mfma_f32_16x16x32_bf16 v[108:111], v[92:95], v[60:63], v[108:111]\n" \
  "s_cmp_lg_u32 s60, 0\n" \
  "s_nop 5\n" \
  "v_cvt_pk_bf16_f32 v104, v104, v105\n" \
  "v_cvt_pk_bf16_f32 v105, v106, v107\n" \
  "v_cvt_pk_bf16_f32 v106, v108, v109\n" \
  "v_cvt_pk_bf16_f32 v107, v110, v111\n" \
  "buffer_store_dwordx4 v[104:107], v17, %[os], 0 offen sc1\n" \
  "s_cbranch_scc1 Lp%=\n" \
  "s_branch Le%=\n" \
  "Ll0_%=:\n" \
  "s_waitcnt lgkmcnt(0)\n" \
  "v_mfma_f32_16x16x32_bf16 v[96:99], v[64:67], v[32:35], 0\n" \
  "v_mfma_f32_16x16x32_bf16 v[100:103], v[64:67], v[36:39], 0\n" \
  "v_mfma_f32_16x16x32_bf16 v[96:99], v[68:71], v[40:43], v[96:99]\n" \
  "v_mfma_f32_16x16x32_bf16 v[100:103], v[68:71], v[44:47], v[100:103]\n" \
  "v_mfma_f32_16x16x32_bf16 v[96:99], v[72:75], v[48:51], v[96:99]\n" \
  "v_cvt_pk_bf16_f32 v104, v104, v105\n" \
  "v_cvt_pk_bf16_f32 v105, v106, v107\n" \
  "v_mfma_f32_16x16x32_bf16 v[100:103], v[72:75], v[52:55], v[100:103]\n" \
  "v_cvt_pk_bf16_f32 v106, v108, v109\n" \
  "v_cvt_pk_bf16_f32 v107, v110, v111\n" \
  "v_mfma_f32_16x16x32_bf16 v[96:99], v[76:79], v[56:59], v[96:99]\n" \
  "buffer_store_dwordx4 v[104:107], v17, %[os], 0 offen sc1\n" \
  "v_mfma_f32_16x16x32_bf16 v[100:103], v[76:79], v[60:63], v[100:103]\n" \
  "s_cmp_lg_u32 s60, 0\n" \
  "s_nop 5\n" \
  "v_cvt_pk_bf16_f32 v96, v96, v97\n" \
  "v_cvt_pk_bf16_f32 v97, v98, v99\n" \
  "v_cvt_pk_bf16_f32 v98, v100, v101\n" \
  "v_cvt_pk_bf16_f32 v99, v102, v103\n" \
  "buffer_store_dwordx4 v[96:99], v16, %[os], 0 offen sc1\n" \
  "s_cbranch_scc1 Lp%=\n" \
  "s_branch Le%=\n" \
  "Ls%=:\n" \
  "s_waitcnt lgkmcnt(0)\n" \
  "v_mfma_f32_16x16x32_bf16 v[96:99], v[64:67], v[32:35], 0\n" \
  "v_mfma_f32_16x16x32_bf16 v[100:103], v[64:67], v[36:39], 0\n" \
  "v_mfma_f32_16x16x32_bf16 v[96:99], v[68:71], v[40:43], v[96:99]\n" \
  "v_mfma_f32_16x16x32_bf16 v[100:103], v[68:71], v[44:47], v[100:103]\n" \
  "v_mfma_f32_16x16x32_bf16 v[96:99], v[72:75], v[48:51], v[96:99]\n" \
  "v_mfma_f32_16x16x32_bf16 v[100:103], v[72:75], v[52:55], v[100:103]\n" \
  "v_mfma_f32_16x16x32_bf16 v[96:99], v[76:79], v[56:59], v[96:99]\n" \
  "v_mfma_f32_16x16x32_bf16 v[100:103], v[76:79], v[60:63], v[100:103]\n" \
  "s_cmp_lg_u32 s60, 0\n" \
  "s_nop 5\n" \
  "v_cvt_pk_bf16_f32 v96, v96, v97\n" \
  "v_cvt_pk_bf16_f32 v97, v98, v99\n" \
  "v_cvt_pk_bf16_f32 v98, v100, v101\n" \
  "v_cvt_pk_bf16_f32 v99, v102, v103\n" \
  "buffer_store_dwordx4 v[96:99], v16, %[os], 0 offen sc1\n" \
  "s_cbranch_scc1 Lp%=\n" \
  "Le%=:\n"
#define MIX_CLOBBERS \
  "v16", "v17", "v64", "v65", "v66", "v67", "v68", "v69", "v70", "v71", "v72", "v73", "v74", "v75", "v76", \
  "v77", "v78", "v79", "v80", "v81", "v82", "v83", "v84", "v85", "v86", "v87", "v88", "v89", "v90", "v91", \
  "v92", "v93", "v94", "v95", "v96", "v97", "v98", "v99", "v100", "v101", "v102", "v103", "v104", "v105", \
  "v106", "v107", "v108", "v109", "v110", "v111", "s60", "s61", "s62", "s63", "s64", "scc", "vcc", "memory"
#define PART_ASM \
  "s_load_dwordx16 s[8:23], %[kp], 0\n" \
  "s_load_dwordx8 s[24:31], %[kp], 0x40\n" \
  "s_load_dwordx4 s[32:35], %[kp], 0x60\n" \
  "v_readfirstlane_b32 s36, %[tid]\n" \
  "v_lshlrev_b32 v90, 3, %[tid]\n" \
  "v_mov_b64 v[92:93], 0\n" \
  "s_and_b32 s37, %[bid], 7\n" \
  "s_lshr_b32 s38, %[bid], 3\n" \
  "s_lshr_b32 s36, s36, 6\n" \
  "ds_write_b64 v90, v[92:93] offset:%[sg]\n" \
  "s_cmp_lg_u32 s36, %[pw]\n" \
  "s_cbranch_scc1 1f\n" \
  "s_getpc_b64 s[44:45]\n" \
  ".Ltfn_p0:\n" \
  "s_max_i32 s46, 0, .Ltfn_p1-.Ltfn_p0\n" \
  "s_mov_b32 m0, %[sn]\n" \
  "s_mov_b32 s47, 0x20000\n" \
  "v_lshlrev_b32 v91, 12, %[tid]\n" \
  "s_lshl_b32 s39, s38, 7\n" \
  "v_and_b32 v91, 0x3f000, v91\n" \
  "buffer_load_dword v91, s[44:47], s39 offen lds\n" \
  "1:\n" \
  "s_lshl_b32 s37, s37, 5\n" \
  "s_add_u32 s37, s37, s38\n" \
  "v_lshlrev_b32 v91, 4, %[tid]\n" \
  "s_waitcnt lgkmcnt(0)\n" \
  "s_mul_i32 s38, s37, s14\n" \
  "s_mov_b32 s40, s10\n" \
  "s_add_i32 s39, s38, s14\n" \
  "s_lshl_b32 s44, s38, 2\n" \
  "s_min_i32 s39, s39, s10\n" \
  "s_add_i32 s45, s44, %[h4]\n" \
  "s_lshl_b32 s10, s39, 2\n" \
  "s_lshl_b32 s14, s39, 2\n" \
  "s_cmp_ge_u32 s36, %[pw]\n" \
  "s_cbranch_scc1 2f\n" \
  "buffer_load_dwordx2 v[40:41], v90, s[8:11], s44 offen\n" \
  "buffer_load_dwordx2 v[42:43], v90, s[8:11], s45 offen\n" \
  "buffer_load_dwordx2 v[44:45], v90, s[12:15], s44 offen\n" \
  "buffer_load_dwordx2 v[46:47], v90, s[12:15], s45 offen\n" \
  "2:\n" \
  "s_mov_b32 s41, s18\n" \
  "s_mov_b32 s42, s22\n" \
  "s_lshl_b32 s18, s39, 3\n" \
  "s_lshl_b32 s22, s39, 3\n" \
  "s_lshl_b32 s44, s44, 1\n" \
  "s_lshl_b32 s45, s45, 1\n" \
  "s_cmp_ge_u32 s36, %[pw]\n" \
  "s_barrier\n" \
  "s_cbranch_scc0 3f\n" \
  "s_cmp_lg_u32 s36, %[hw]\n" \
  "s_cbranch_scc1 4f\n" \
  "s_load_dwordx2 s[46:47], s[26:27], 0\n" \
  "s_branch Lpx%=\n" \
  "4:\n" \
  "s_barrier\n" \
  "s_branch Lpx%=\n" \
  "3:\n" \
  "s_setprio 3\n" \
  "buffer_load_dwordx4 v[100:103], v91, s[16:19], s44 offen\n" \
  "buffer_load_dwordx4 v[104:107], v91, s[16:19], s45 offen\n" \
  "buffer_load_dwordx4 v[108:111], v91, s[20:23], s44 offen\n" \
  "buffer_load_dwordx4 v[112:115], v91, s[20:23], s45 offen\n" \
  "s_sub_i32 s56, s39, s38\n" \
  "v_lshlrev_b32 v72, 1, %[tid]\n" \
  "s_lshl_b32 s57, s36, 9\n" \
  "v_cmp_gt_i32_e64 s[48:49], s56, v72\n" \
  "s_sub_i32 s58, s56, 1\n" \
  "v_cmp_gt_i32_e64 s[50:51], s58, v72\n" \
  "s_sub_i32 s58, s56, %[h1]\n" \
  "v_cmp_gt_i32_e64 s[52:53], s58, v72\n" \
  "s_sub_i32 s58, s56, %[h1]+1\n" \
  "v_cmp_gt_i32_e64 s[54:55], s58, v72\n" \
  "v_and_b32 v88, 63, %[tid]\n" \
  "v_mov_b32 v95, 0x1fc\n" \
  "v_mov_b64 v[76:77], 0\n" \
  "v_lshlrev_b32 v88, 2, v88\n" \
  "v_mov_b32 v96, 0\n" \
  "v_mov_b32 v97, 1\n" \
  "v_add_u32 v89, s57, v88\n" \
  "v_add_u32 v88, %[ph], v88\n" \
  "v_add_u32 v89, %[pb], v89\n" \
  "s_lshl_b32 s58, s36, 1\n" \
  "s_mov_b32 s59, 0x38000000\n" \
  "s_waitcnt vmcnt(6)\n" \
  "v_mul_u32_u24 v48, s42, v40\n" \
  "v_mul_u32_u24 v49, s42, v41\n" \
  "v_mul_u32_u24 v50, s42, v42\n" \
  "v_mul_u32_u24 v51, s42, v43\n" \
  "v_lshrrev_b32 v52, 22, v48\n" \
  "v_lshrrev_b32 v53, 22, v49\n" \
  "v_lshrrev_b32 v54, 22, v50\n" \
  "v_lshrrev_b32 v55, 22, v51\n" \
  "v_lshrrev_b32 v56, 21, v48\n" \
  "v_lshrrev_b32 v57, 21, v49\n" \
  "v_lshrrev_b32 v58, 21, v50\n" \
  "v_lshrrev_b32 v59, 21, v51\n" \
  "v_lshlrev_b32 v60, 4, v52\n" \
  "v_lshlrev_b32 v61, 4, v53\n" \
  "v_lshlrev_b32 v62, 4, v54\n" \
  "v_lshlrev_b32 v63, 4, v55\n" \
  "v_and_or_b32 v56, v56, v95, s57\n" \
  "v_and_or_b32 v57, v57, v95, s57\n" \
  "v_and_or_b32 v58, v58, v95, s57\n" \
  "v_and_or_b32 v59, v59, v95, s57\n" \
  "v_lshlrev_b32 v72, v60, v97\n" \
  "v_lshlrev_b32 v73, v61, v97\n" \
  "v_lshlrev_b32 v74, v62, v97\n" \
  "v_lshlrev_b32 v75, v63, v97\n" \
  "s_mov_b64 exec, s[48:49]\n" \
  "ds_add_rtn_u32 v64, v56, v72 offset:%[ph]\n" \
  "s_mov_b64 exec, s[50:51]\n" \
  "ds_add_rtn_u32 v65, v57, v73 offset:%[ph]\n" \
  "s_mov_b64 exec, s[52:53]\n" \
  "ds_add_rtn_u32 v66, v58, v74 offset:%[ph]\n" \
  "s_mov_b64 exec, s[54:55]\n" \
  "ds_add_rtn_u32 v67, v59, v75 offset:%[ph]\n" \
  "s_mov_b64 exec, -1\n" \
  "v_and_b32 v48, s59, v48\n" \
  "v_and_b32 v49, s59, v49\n" \
  "v_and_b32 v50, s59, v50\n" \
  "v_and_b32 v51, s59, v51\n" \
  "v_and_b32 v40, 63, v40\n" \
  "v_and_b32 v41, 63, v41\n" \
  "v_and_b32 v42, 63, v42\n" \
  "v_and_b32 v43, 63, v43\n" \
  "s_waitcnt vmcnt(4)\n" \
  "v_mul_u32_u24 v68, s42, v44\n" \
  "v_mul_u32_u24 v69, s42, v45\n" \
  "v_mul_u32_u24 v70, s42, v46\n" \
  "v_mul_u32_u24 v71, s42, v47\n" \
  "v_lshl_or_b32 v40, v44, 6, v40\n" \
  "v_lshl_or_b32 v41, v45, 6, v41\n" \
  "v_lshl_or_b32 v42, v46, 6, v42\n" \
  "v_lshl_or_b32 v43, v47, 6, v43\n" \
  "v_sub_u32 v68, v68, v48\n" \
  "v_sub_u32 v69, v69, v49\n" \
  "v_sub_u32 v70, v70, v50\n" \
  "v_sub_u32 v71, v71, v51\n" \
  "v_lshl_add_u32 v56, v52, 1, s57\n" \
  "v_lshl_add_u32 v57, v53, 1, s57\n" \
  "v_lshl_add_u32 v58, v54, 1, s57\n" \
  "v_lshl_add_u32 v59, v55, 1, s57\n" \
  "v_bfe_u32 v68, v68, 26, 4\n" \
  "v_bfe_u32 v69, v69, 26, 4\n" \
  "v_bfe_u32 v70, v70, 26, 4\n" \
  "v_bfe_u32 v71, v71, 26, 4\n" \
  "v_lshlrev_b32 v52, 2, v52\n" \
  "v_lshlrev_b32 v53, 2, v53\n" \
  "v_lshlrev_b32 v54, 2, v54\n" \
  "v_lshlrev_b32 v55, 2, v55\n" \
  "v_lshl_or_b32 v68, v40, 4, v68\n" \
  "v_lshl_or_b32 v69, v41, 4, v69\n" \
  "v_lshl_or_b32 v70, v42, 4, v70\n" \
  "v_lshl_or_b32 v71, v43, 4, v71\n" \
  "s_mov_b64 exec, s[48:49]\n" \
  "ds_add_u32 v52, v68 offset:%[sg]\n" \
  "s_mov_b64 exec, s[50:51]\n" \
  "ds_add_u32 v53, v69 offset:%[sg]\n" \
  "s_mov_b64 exec, s[52:53]\n" \
  "ds_add_u32 v54, v70 offset:%[sg]\n" \
  "s_mov_b64 exec, s[54:55]\n" \
  "ds_add_u32 v55, v71 offset:%[sg]\n" \
  "s_mov_b64 exec, -1\n" \
  "s_waitcnt lgkmcnt(0)\n" \
  "s_barrier\n" \
  "ds_read2st64_b32 v[78:79], v88 offset0:0 offset1:1\n" \
  "ds_read2st64_b32 v[80:81], v88 offset0:2 offset1:3\n" \
  "ds_read2st64_b32 v[82:83], v88 offset0:4 offset1:5\n" \
  "ds_read2st64_b32 v[84:85], v88 offset0:6 offset1:7\n" \
  "ds_read2st64_b32 v[86:87], v88 offset0:8 offset1:9\n" \
  "ds_read_b32 v94, v96 offset:%[tg]\n" \
  "v_bfe_u32 v64, v64, v60, 16\n" \
  "v_bfe_u32 v65, v65, v61, 16\n" \
  "v_bfe_u32 v66, v66, v62, 16\n" \
  "v_bfe_u32 v67, v67, v63, 16\n" \
  "s_mov_b32 s56, 0x05040100\n" \
  "s_mov_b32 s59, 0x07060302\n" \
  "s_waitcnt lgkmcnt(4)\n" \
  "v_add_u32 v80, v80, v78\n" \
  "v_add_u32 v81, v81, v79\n" \
  "s_waitcnt lgkmcnt(3)\n" \
  "v_add_u32 v82, v82, v80\n" \
  "v_add_u32 v83, v83, v81\n" \
  "s_waitcnt lgkmcnt(2)\n" \
  "v_add_u32 v84, v84, v82\n" \
  "v_add_u32 v85, v85, v83\n" \
  "s_waitcnt lgkmcnt(1)\n" \
  "v_add_u32 v86, v86, v84\n" \
  "v_add_u32 v87, v87, v85\n" \
  "v_perm_b32 v72, v87, v86, s56\n" \
  "v_perm_b32 v73, v87, v86, s59\n" \
  "v_add_u32 v72, v72, v73\n" \
  "s_set_gpr_idx_on s58, gpr_idx(SRC0)\n" \
  "v_mov_b32 v74, v76\n" \
  "v_mov_b32 v75, v77\n" \
  "s_set_gpr_idx_off\n" \
  "s_mov_b32 s56, 0x10001\n" \
  "v_add_u32_dpp v73, v72, v72 row_shr:1 row_mask:0xf bank_mask:0xf bound_ctrl:1\n" \
  "v_lshl_add_u32 v74, v86, 16, v74\n" \
  "v_lshl_add_u32 v75, v87, 16, v75\n" \
  "v_add_u32_dpp v73, v73, v73 row_shr:2 row_mask:0xf bank_mask:0xf bound_ctrl:1\n" \
  "s_waitcnt lgkmcnt(0)\n" \
  "v_lshlrev_b32 v94, 24, v94\n" \
  "v_add_u32_dpp v73, v73, v73 row_shr:4 row_mask:0xf bank_mask:0xf bound_ctrl:1\n" \
  "v_or_b32 v68, v68, v94\n" \
  "v_or_b32 v69, v69, v94\n" \
  "v_add_u32_dpp v73, v73, v73 row_shr:8 row_mask:0xf bank_mask:0xf bound_ctrl:1\n" \
  "v_or_b32 v70, v70, v94\n" \
  "v_or_b32 v71, v71, v94\n" \
  "v_add_u32_dpp v73, v73, v73 row_bcast:15 row_mask:0xa bank_mask:0xf\n" \
  "s_nop 1\n" \
  "v_add_u32_dpp v73, v73, v73 row_bcast:31 row_mask:0xc bank_mask:0xf\n" \
  "v_sub_u32 v72, v73, v72\n" \
  "v_readlane_b32 s59, v73, 63\n" \
  "v_and_b32 v73, 0xffff, v72\n" \
  "v_lshrrev_b32 v72, 16, v72\n" \
  "s_and_b32 s59, s59, 0xffff\n" \
  "v_mad_u32_u24 v74, v73, s56, v74\n" \
  "v_add_u32 v72, s59, v72\n" \
  "v_mad_u32_u24 v75, v72, s56, v75\n" \
  "ds_write2st64_b32 v89, v74, v75 offset1:1\n" \
  "ds_read_u16 v48, v56 offset:%[pb]\n" \
  "ds_read_u16 v49, v57 offset:%[pb]\n" \
  "ds_read_u16 v50, v58 offset:%[pb]\n" \
  "ds_read_u16 v51, v59 offset:%[pb]\n" \
  "s_mov_b32 s56, %[sp]\n" \
  "s_waitcnt lgkmcnt(0)\n" \
  "v_add_lshl_u32 v48, v48, v64, 2\n" \
  "v_add_lshl_u32 v49, v49, v65, 2\n" \
  "v_add_lshl_u32 v50, v50, v66, 2\n" \
  "v_add_lshl_u32 v51, v51, v67, 2\n" \
  "v_lshl_add_u32 v56, v48, 2, s56\n" \
  "v_lshl_add_u32 v57, v49, 2, s56\n" \
  "v_lshl_add_u32 v58, v50, 2, s56\n" \
  "v_lshl_add_u32 v59, v51, 2, s56\n" \
  "s_mov_b64 exec, s[48:49]\n" \
  "ds_write_b32 v48, v68 offset:%[sk]\n" \
  "s_mov_b64 exec, s[50:51]\n" \
  "ds_write_b32 v49, v69 offset:%[sk]\n" \
  "s_mov_b64 exec, s[52:53]\n" \
  "ds_write_b32 v50, v70 offset:%[sk]\n" \
  "s_mov_b64 exec, s[54:55]\n" \
  "ds_write_b32 v51, v71 offset:%[sk]\n" \
  "s_mov_b64 exec, -1\n" \
  "s_waitcnt vmcnt(0)\n" \
  "v_add3_u32 v72, v100, v101, v108\n" \
  "v_add3_u32 v73, v102, v103, v110\n" \
  "v_add3_u32 v74, v104, v105, v112\n" \
  "v_add3_u32 v75, v106, v107, v114\n" \
  "v_add_u32 v72, v72, v109\n" \
  "v_add_u32 v73, v73, v111\n" \
  "v_add_u32 v74, v74, v113\n" \
  "v_add_u32 v75, v75, v115\n" \
  "s_mov_b64 exec, s[48:49]\n" \
  "ds_add_u32 v52, v72 offset:%[sg]+1024\n" \
  "s_mov_b64 exec, s[50:51]\n" \
  "ds_add_u32 v53, v73 offset:%[sg]+1024\n" \
  "s_mov_b64 exec, s[52:53]\n" \
  "ds_add_u32 v54, v74 offset:%[sg]+1024\n" \
  "s_mov_b64 exec, s[54:55]\n" \
  "ds_add_u32 v55, v75 offset:%[sg]+1024\n" \
  "s_mov_b64 exec, 1\n" \
  "ds_add_u32 v96, v97 offset:%[go]\n" \
  "s_mov_b64 exec, -1\n" \
  "s_mov_b64 exec, s[48:49]\n" \
  "ds_write2_b64 v56, v[100:101], v[108:109] offset1:1\n" \
  "s_mov_b64 exec, s[50:51]\n" \
  "ds_write2_b64 v57, v[102:103], v[110:111] offset1:1\n" \
  "s_mov_b64 exec, s[52:53]\n" \
  "ds_write2_b64 v58, v[104:105], v[112:113] offset1:1\n" \
  "s_mov_b64 exec, s[54:55]\n" \
  "ds_write2_b64 v59, v[106:107], v[114:115] offset1:1\n" \
  "s_mov_b64 exec, -1\n" \
  "Lpx%=:\n"
#define PART_CLOBBERS \
  "v40", "v41", "v42", "v43", "v44", "v45", "v46", "v47", "v48", "v49", "v50", "v51", "v52", "v53", "v54", \
  "v55", "v56", "v57", "v58", "v59", "v60", "v61", "v62", "v63", "v64", "v65", "v66", "v67", "v68", "v69", \
  "v70", "v71", "v72", "v73", "v74", "v75", "v76", "v77", "v78", "v79", "v80", "v81", "v82", "v83", "v84", \
  "v85", "v86", "v87", "v88", "v89", "v90", "v91", "v92", "v93", "v94", "v95", "v96", "v97", "v98", "v99", \
  "v100", "v101", "v102", "v103", "v104", "v105", "v106", "v107", "v108", "v109", "v110", "v111", "v112", \
  "v113", "v114", "v115", "s44", "s45", "s48", "s49", "s50", "s51", "s52", "s53", "s54", "s55", "s56", "s57", \
  "s58", "s59", "scc", "vcc", "memory"


namespace {

constexpr int C_ = 128;
constexpr int MW = 12;
constexpr int MT = MW * 64;
constexpr int SW = 8;
constexpr int NS = 5;
constexpr int NPB = SW * NS;
constexpr int NPASS = 16;
constexpr int HS = 64 * NPASS / 2;
constexpr int NBMAX = 256;
constexpr int PW = 5;
constexpr int PT = PW * 64;
constexpr int PK = 4;
constexpr int CH = PT * PK;
constexpr int MK = 4;
constexpr int CAPW = 256;
constexpr int HWV = 10;
constexpr int PFN = 5;
constexpr int WA = 4;
constexpr int FO1 = 2;
constexpr int WSL = 6;
constexpr int TB3 = 2;
constexpr int HSL = 24;
constexpr unsigned WS_EPOCH = 0;
constexpr unsigned WS_OFF = 4096;
constexpr unsigned WS_RECK = WS_OFF + NBMAX * NBMAX * 16;
constexpr unsigned WS_RECP = WS_RECK + NBMAX * CH * 4;
constexpr unsigned WS_END = WS_RECP + NBMAX * CH * 16;

__device__ __forceinline__ float bf2f(unsigned short v) { return __uint_as_float(((unsigned)v) << 16); }
__device__ __forceinline__ unsigned short f2bf(float f) {
  __hip_bfloat16 b = __float2bfloat16(f);
  return *reinterpret_cast<unsigned short*>(&b);
}
__device__ __forceinline__ unsigned pk2bf(float lo, float hi) {
  return __builtin_bit_cast(unsigned, __builtin_convertvector(f32x2{lo, hi}, bf16x2));
}
__device__ __forceinline__ unsigned wave_incl_scan(unsigned x) {
  x += __builtin_amdgcn_update_dpp(0u, x, 0x111, 0xf, 0xf, false);
  x += __builtin_amdgcn_update_dpp(0u, x, 0x112, 0xf, 0xf, false);
  x += __builtin_amdgcn_update_dpp(0u, x, 0x114, 0xf, 0xf, false);
  x += __builtin_amdgcn_update_dpp(0u, x, 0x118, 0xf, 0xf, false);
  x += __builtin_amdgcn_update_dpp(0u, x, 0x142, 0xa, 0xf, false);
  x += __builtin_amdgcn_update_dpp(0u, x, 0x143, 0xc, 0xf, false);
  return x;
}

struct LdsLow {
  u32x4 sink[16];
  unsigned flag, rdone, go, pad_;
  unsigned long long tag, pad2_;
  unsigned prog[16];
  int deg[64];
  unsigned byrank[64];
  unsigned nl[64];
  u32x4 ct[72];
  union {
    struct {
      __attribute__((aligned(16))) unsigned so[SW * CAPW];
      union {
        __attribute__((aligned(16))) unsigned hist[(SW + 1) * HS];
        u32x4 row5[SW * 64];
      };
    };
    struct {
      __attribute__((aligned(16))) unsigned sk[CH];
      u32x4 sp[CH];
    };
  };
  union {
    struct {
      __attribute__((aligned(16))) unsigned seg[2][NBMAX];
      unsigned phist[PW][NBMAX / 2];
      unsigned pbase[PW][NBMAX / 2];
      u32x4 ri[SW][MK * 16];
    };
    u32x4 pay[SW * CAPW];
  };
};
constexpr int TBLK = 0xc300 / 8;
struct Lds : LdsLow {
  char pad_end[163840 - TBLK * 16 - sizeof(LdsLow)];
  u32x4 sg[TBLK];
};
constexpr int TOFF = 163840 - TBLK * 16;
constexpr int WBLK = 0x4180 / 8;
static_assert(sizeof(Lds) == 163840 && TOFF < 65536, "LDS layout");

__device__ __forceinline__ unsigned lds_addr(const void* p) {
  return (unsigned)(uintptr_t)(__attribute__((address_space(3))) const void*)p;
}
__device__ __forceinline__ u32x4 make_srd(const void* p, unsigned num) {
  unsigned long long a = (unsigned long long)p;
  return u32x4{(unsigned)a, (unsigned)(a >> 32), num, 0x00020000u};
}

}

extern "C" __global__ __launch_bounds__(MT, 3) void tfn_main_kernel(
    const int* dst_, int E_, int c0_, const int* src_, int CE_, int c1_, const u32x2* Y_, int N_, int c2_, const u32x2* ef_,
    unsigned M2_, int c3_, const unsigned short* x_, char* ws_, u32x4* out_, const unsigned short* wrad_,
    const unsigned short* cg_, const unsigned short* wmix_) {
  __shared__ Lds L;
  const int tid = threadIdx.x, lane = tid & 63;
  u32x16 ka;
  u32x8 kb, kc;
  u32x4 kd;
  unsigned long long ev;
  asm volatile(PART_ASM
               : "=&{s[8:23]}"(ka), "=&{s[24:31]}"(kb), "=&{s[32:35]}"(kd), "=&{s[36:43]}"(kc), "=&{s[46:47]}"(ev)
               : [kp] "s"(__builtin_amdgcn_kernarg_segment_ptr()), [tid] "v"(tid), [bid] "s"(blockIdx.x), [pw] "n"(PW), [hw] "n"(HWV),
                 [h4] "n"(CH * 2), [h1] "n"(CH / 2), [sn] "n"(offsetof(LdsLow, sink)), [sg] "n"(offsetof(LdsLow, seg)),
                 [ph] "n"(offsetof(LdsLow, phist)), [pb] "n"(offsetof(LdsLow, pbase)), [tg] "n"(offsetof(LdsLow, tag)),
                 [go] "n"(offsetof(LdsLow, go)), [sk] "n"(offsetof(LdsLow, sk)), [sp] "n"(offsetof(LdsLow, sp))
               : PART_CLOBBERS);
  typedef __attribute__((address_space(1))) char gchar;
  auto ptr = [](unsigned lo, unsigned hi) { return (char*)(gchar*)(lo | (unsigned long long)hi << 32); };
  const int* __restrict__ dst = (const int*)ptr(ka[0], ka[1]);
  const int* __restrict__ src = (const int*)ptr(ka[4], ka[5]);
  const u32x2* __restrict__ Y = (const u32x2*)ptr(ka[8], ka[9]);
  const u32x2* __restrict__ ef = (const u32x2*)ptr(ka[12], ka[13]);
  const unsigned short* __restrict__ x = (const unsigned short*)ptr(kb[0], kb[1]);
  char* ws = ptr(kb[2], kb[3]);
  u32x4* __restrict__ out = (u32x4*)ptr(kb[4], kb[5]);
  const unsigned short* __restrict__ wrad = (const unsigned short*)ptr(kb[6], kb[7]);
  const unsigned short* __restrict__ cg = (const unsigned short*)ptr(kd[0], kd[1]);
  const unsigned short* __restrict__ wmix = (const unsigned short*)ptr(kd[2], kd[3]);
  const int w = kc[0], bucket = kc[1], E = kc[4], N = kc[5];
  const unsigned M2 = kc[6];
  __builtin_assume(w >= 0 && w < MW && bucket >= 0 && bucket < NBMAX);
  const int xcd = blockIdx.x & 7;
  const u32x4 wsd = make_srd(ws, WS_END);
  auto codes = [](unsigned q) { return q * 0x80008u + u32x4{0x10000u, 0x30002u, 0x50004u, 0x70006u}; };
  auto table = [&](int q) {
    u32x4 p, n;
#pragma unroll
    for (int i = 0; i < 4; i++) {
      const f32x2 z = {__uint_as_float((q * 8 + 2 * i) << 16), __uint_as_float((q * 8 + 2 * i + 1) << 16)};
      const f32x2 e = {__builtin_amdgcn_exp2f(-1.4426950408889634f * z[0]), __builtin_amdgcn_exp2f(-1.4426950408889634f * z[1])};
      const f32x2 r = z * f32x2{__builtin_amdgcn_rcpf(1.f + e[0]), __builtin_amdgcn_rcpf(1.f + e[1])};
      p[i] = pk2bf(r[0], r[1]);
      n[i] = pk2bf(-e[0] * r[0], -e[1] * r[1]);
    }
    if (q < WBLK) L.sg[q] = p;
    return n;
  };
  auto halves = [&](int q0, int q1, int st) {
#pragma unroll 1
    for (int q = q0; q < q1; q += st) {
      const u32x4 c = codes(q - 16);
      L.sg[q] = c;
      if (q >= 0x30) L.sg[q + 0x1000] = c | 0x80008000u;
    }
  };
  auto warm = [&](int i0, int i1) {
    const unsigned pfo = ((xcd * N) >> 3) * 1024 + ((blockIdx.x >> 3) * 64 + lane) * 128;
#pragma unroll 1
    for (int i = i0; i < i1; i += MW - SW)
      asm volatile("s_mov_b32 m0, %2\n s_nop 0\n buffer_load_dword %0, %1, %3 offen lds\n"
                   :: "v"(pfo), "s"(make_srd(x, N * 1024)), "s"(lds_addr(L.sink)), "s"(i * 32 * 64 * 128) : "memory");
  };
  auto wdma = [&](int i0, int i1) {
#pragma unroll
    for (int i = i0; i < i1; i++) {
      const unsigned o = (i + (blockIdx.x >> 3)) & 7;
      asm volatile("s_mov_b32 m0, %2\n s_nop 0\n buffer_load_dwordx4 %0, %1, %3 offen lds\n"
                   :: "v"((lane >> 2) * 256 + (lane & 3) * 16), "s"(make_srd(wmix, C_ * C_ * 2)),
                      "s"(lds_addr(&L.sg[WBLK]) + ((w - SW) * 8 + o) * 1024), "s"(o * 4096 + (w - SW) * 64) : "memory");
    }
  };
  const auto wsr = __builtin_amdgcn_make_buffer_rsrc((void*)ws, 0, WS_END, 0x00020000);
  if (w == HWV) {
    __builtin_amdgcn_s_setprio(3);
    asm volatile("s_waitcnt lgkmcnt(0)" : "+{s[46:47]}"(ev));
    ev = ev * 6364136223846793005ull + 1442695040888963407ull;
    L.tag = ev;
    if (lane < 4) (&L.flag)[lane] = 0;
    const unsigned ho0 = lane * 16 + WS_OFF + bucket * (NBMAX * 16);
    __syncthreads();
    const unsigned tl = (unsigned)ev, th = (unsigned)(ev >> 32);
    unsigned c[4] = {0, 0, 0, 0};
    u32x4 hv[4];
#pragma unroll
    for (int k = 0; k < 4; k++) {
#pragma unroll
      for (int ww = 0; ww < PW; ww++) c[k] += reinterpret_cast<const unsigned short*>(L.phist[ww])[k * 64 + lane];
      hv[k][1] = L.seg[0][k * 64 + lane];
    }
    const unsigned p0 = c[0] | (c[1] << 16), p1 = c[2] | (c[3] << 16);
    const unsigned i0 = wave_incl_scan(p0), i1 = wave_incl_scan(p1);
    const unsigned t0 = __builtin_amdgcn_readlane(i0, 63), t1 = __builtin_amdgcn_readlane(i1, 63);
    const unsigned s1 = t0 & 0xffff, s2 = s1 + (t0 >> 16), s3 = s2 + (t1 & 0xffff);
    const unsigned bs[4] = {(i0 - p0) & 0xffff, ((i0 - p0) >> 16) + s1, ((i1 - p1) & 0xffff) + s2, ((i1 - p1) >> 16) + s3};
#pragma unroll
    for (int k = 0; k < 4; k++) {
      hv[k][0] = bs[k] | (c[k] << 11) | (tl << 22);
      hv[k][1] += (unsigned)__umul24(c[k], tl & 255) << 24;
      hv[k][3] = th - hv[k][0] - hv[k][1];
    }
    asm volatile("" : "+v"(hv[0]), "+v"(hv[1]), "+v"(hv[2]), "+v"(hv[3]));
    while (true) {
      const unsigned gv = __hip_atomic_load(&L.go, __ATOMIC_RELAXED, __HIP_MEMORY_SCOPE_WORKGROUP);
      asm volatile("" ::: "memory");
#pragma unroll
      for (int k = 0; k < 4; k++) hv[k][2] = L.seg[1][k * 64 + lane];
      asm volatile("" ::: "memory");
      if (__builtin_amdgcn_readfirstlane(gv) == PW) break;
    }
#pragma unroll
    for (int k = 0; k < 4; k++) {
      hv[k][3] -= hv[k][2];
      __builtin_amdgcn_raw_buffer_store_b128(hv[k], wsr, ho0 + k * 1024, 0, 16);
    }
  } else if (w >= PW) {
    const int zt = tid - PT - (w > HWV ? 64 : 0);
    if (w >= SW) wdma(0, WA);
    if (zt < (0x4300 - 0x3a80) / 8) L.sg[0xba80 / 8 + zt] = table(0x3a80 / 8 + zt);
    __builtin_amdgcn_s_setprio(3);
  }
  asm volatile("s_waitcnt lgkmcnt(0)\n s_barrier" ::: "memory");
  {
    const unsigned so1 = (tid + MT - PT) * 16 + WS_RECP + bucket * (CH * 16);
    const unsigned so0 = tid * 16 + (w < PW ? WS_RECK + bucket * (CH * 4) : WS_RECP - PT * 16 + bucket * (CH * 16));
    const u32x4 v0 = reinterpret_cast<const u32x4*>(L.sk)[tid], v1 = reinterpret_cast<const u32x4*>(L.sk)[MT + tid];
    __builtin_amdgcn_raw_buffer_store_b128(v0, wsr, so0, 0, 16);
    __builtin_amdgcn_raw_buffer_store_b128(v1, wsr, so1, 0, 16);
    if (w == PW)
      __builtin_amdgcn_raw_buffer_store_b128(reinterpret_cast<const u32x4*>(L.sk)[2 * MT + lane], wsr, so1 + (MT - PT) * 16, 0, 16);
  }
  __syncthreads();
  const unsigned long long epoch = L.tag;
  const unsigned tagl = __builtin_amdgcn_readfirstlane((unsigned)epoch);
  const unsigned tag1 = __builtin_amdgcn_readfirstlane((unsigned)(epoch >> 32));
  const int row = lane & 15, kq = lane >> 4;
  const int node0 = __builtin_amdgcn_readfirstlane(((unsigned)(bucket << 22) + M2 - 1) / M2);
  const int nn = min(N, __builtin_amdgcn_readfirstlane(((unsigned)((bucket + 1) << 22) + M2 - 1) / M2)) - node0;
  if (w >= SW) {
    int lane;
    asm volatile("v_mbcnt_lo_u32_b32 %0, -1, 0\n v_mbcnt_hi_u32_b32 %0, -1, %0" : "=v"(lane));
    __builtin_assume(lane >= 0 && lane < 64);
    const int h = w - SW, st = h * 64 + lane, row = lane & 15, kq = lane >> 4;
    if (h == 0) {
      L.deg[lane] = ((lane - node0) & 63) < NPB ? 63 - ((lane - node0) & 63) : -1;
      if (lane < 16) L.prog[lane] = 255;
    }
    __builtin_amdgcn_s_sleep(WSL);
    if (w == HWV) wdma(0, WA);
    wdma(WA, 8);
    unsigned short cv;
    unsigned wq4[4];
    if (h == 1) {
      cv = cg[lane];
#pragma unroll
      for (int k = 0; k < 4; k++) wq4[k] = reinterpret_cast<const unsigned*>(wrad)[k * (C_ / 2) + lane];
    }
    halves(0x20 + TB3 * 512 + st, 0x3a80 / 8, 256);
    const int wq0 = WBLK + h * 512 + lane;
    u32x4 last = codes(wq0 + 448 < 0x1000 ? wq0 + 448 : wq0 + 432);
    if ((unsigned)(wq0 + 448 - 0x1000) < 0x20u) last = table(wq0 + 448 - 0x1000);
    constexpr int HR = (NPB - RKL * SW) / (MW - SW);
    const int id = (node0 + RKL * SW + h * HR + lane) & 63;
    asm volatile("s_waitcnt     vmcnt(0)" ::: "memory");
    if (h == 1) {
      reinterpret_cast<unsigned short*>(L.ct)[(lane & 3) * 16 + (lane >> 2)] = cv;
      L.ct[8 + lane] = u32x4{__builtin_amdgcn_perm(wq4[1], wq4[0], 0x05040100), __builtin_amdgcn_perm(wq4[3], wq4[2], 0x05040100),
                             __builtin_amdgcn_perm(wq4[1], wq4[0], 0x07060302), __builtin_amdgcn_perm(wq4[3], wq4[2], 0x07060302)};
    }
    u32x16 wa, wb;
    {
      const unsigned* wp = reinterpret_cast<const unsigned*>(&L.sg[WBLK + h * 512]) + kq * 128 + row;
#pragma unroll
      for (int i = 0; i < 16; i++) {
        const unsigned q0 = wp[(i >> 2) * 512 + (i & 3) * 32], q1 = wp[(i >> 2) * 512 + (i & 3) * 32 + 16];
        u32x16& d = i < 8 ? wa : wb;
        d[(i & 4) * 2 + (i & 3)] = __builtin_amdgcn_perm(q1, q0, 0x05040100);
        d[(i & 4) * 2 + (i & 3) + 4] = __builtin_amdgcn_perm(q1, q0, 0x07060302);
      }
    }
    asm volatile("" : "+v"(wa), "+v"(wb) :: "memory");
    asm volatile("s_waitcnt lgkmcnt(0)\n s_barrier" ::: "memory");
    __builtin_amdgcn_s_sleep(2);
    warm(h, PFN);
    __syncthreads();
    {
      const int dg = L.deg[lane], dm = L.deg[id];
      int dn[HR];
#pragma unroll
      for (int i = 0; i < HR; i++) dn[i] = __builtin_amdgcn_readlane(dm, i);
      __builtin_amdgcn_sched_barrier(0);
      unsigned long long m[HR];
#pragma unroll
      for (int i = 0; i < HR; i++) m[i] = __ballot(dg > dn[i]);
      __builtin_amdgcn_sched_barrier(0);
      unsigned r = 63;
#pragma unroll
      for (int i = 0; i < HR; i++) asm("v_writelane_b32 %0, %1, %2" : "+v"(r) : "s"(__popcll(m[i])), "n"(i));
      L.byrank[r] = id * 4;
    }
    __syncthreads();
    if (bucket == 0 && st == 0)
      __builtin_amdgcn_raw_buffer_store_b64(u32x2{tagl, tag1}, wsr, WS_EPOCH, 0, 16);
    __builtin_amdgcn_s_setprio(0);
    __syncthreads();
#pragma unroll
    for (int i = 0; i < 8; i++) L.sg[wq0 + i * 64] = i < 7 ? codes(wq0 + i * 64) : last;
    __syncthreads();
    u32x16 oo;
#pragma unroll
    for (int t = 0; t < 10; t++)
      oo[t + 2] = (node0 + ((L.nl[t < SW ? t * 8 + kq : ((t - SW) * 4 + kq) * 8 + 4] / 4 - node0) & 63)) * 1024 + row * 16 + h * 256;
    oo[0] = lds_addr(&L.pay[(row >> 2) * 64 + (row & 3) * 4 + kq]);
    oo[1] = lds_addr(&L.prog[h]);
    asm volatile(MIX_ASM
                 : "+{v[0:15]}"(oo)
                 : "{v[32:47]}"(wa), "{v[48:63]}"(wb), [os] "s"(make_srd(out, (node0 + nn) * 1024)),
                   [r5] "n"((unsigned)(offsetof(LdsLow, row5) - offsetof(LdsLow, pay) - SW * CAPW * 16)),
                   [rd] "n"(offsetof(LdsLow, rdone)), [sl] "n"(HSL)
                 : MIX_CLOBBERS);
    asm volatile(".Ltfn_p1:");
  } else {
    {
    halves(0x20 + tid, 0x20 + TB3 * 512, 512);
    reinterpret_cast<u32x4*>(&L.hist[w * HS])[lane] = u32x4{0, 0, 0, 0};
    reinterpret_cast<u32x4*>(&L.hist[w * HS])[64 + lane] = u32x4{0, 0, 0, 0};
    L.ri[w][lane] = u32x4{0x20000000u, 0x20000000u, 0x20000000u, 0x20000000u};
    const unsigned long long t0 = w & 1 ? 0x26796281267d7281ull : 0x277dc200272c8180ull, t1 = w & 1 ? 0x246942c324755245ull : 0x25755242217984c2ull;
    const unsigned long long t2 = w & 1 ? 0x22613345226d6483ull : 0x236d130423694307ull, t3 = w & 1 ? 0x206503c725710384ull : 0x215d2386206513cdull;
    const unsigned long long t = w & 4 ? (w & 2 ? t3 : t2) : (w & 2 ? t1 : t0);
    const unsigned rks = nn == NPB ? t >> 32 : t;
    const int start = w * CAPW;
    unsigned sop = lds_addr(&L.so[start + (lane & 7)]);
    unsigned yp = lds_addr(&L.pay[start + (lane >> 2)]);
    unsigned ep = lds_addr(&L.pay[start + (lane & 15)]) + 8;
    unsigned cga = lds_addr(&L.ct[(lane & 3) * 2]);
    const unsigned rp = lds_addr(&L.pay[start]) + kq * 256 + row * 4;
    unsigned pg = lds_addr(&L.prog[w]), slowv;
    u32x16 cs = {(unsigned)((lane >> 1) * SW + w) * (NBMAX * 16),
                 (unsigned)(((lane >> 1) * SW + w) * CH + (lane & 1)) * 4,
                 lds_addr(L.ri[w]) + (lane & 1) * 4, lds_addr(L.ri[w]) + lane * 4, (lane & 1) - 1u, lane & 1u,
                 lds_addr(&L.hist[tid]), lds_addr(&L.byrank[(rks >> (min(lane & 7, NS - 1) * 6)) & 63]),
                 0, lds_addr(&L.hist[SW * HS + (lane >> 3)]),
                 (start * 4 + ((lane & 7) << 13)) * 0x10001u, (unsigned)(start + lane) * 4,
                 lds_addr(&L.nl[w * 8 + (lane & 7)]), lane * 4u, ((node0 + w * RKL + lane) & 63) * 4u, 0};
    asm volatile(STREAM_ASM
                 : [sl] "=s"(slowv), "+{v12}"(sop), "+{v14}"(yp), "+{v15}"(ep), "+{v[136:151]}"(cs),
                   "+{v20}"(cga), "+{v110}"(pg)
                 : "{v8}"(lane * 16), "{v[152:155]}"(u32x4{rp, rp + 64, rp + 128, rp + 192}),
                   "{v156}"(lds_addr(&L.row5[w * 64]) + kq * 256 + row * 4),
                   [xs] "s"(make_srd(x, N * 1024)), [ws] "s"(wsd),
                   [ho] "s"(WS_OFF + bucket * 16), [rk] "s"(WS_RECK), [rp] "s"(WS_RECP), [t0] "s"(tagl << 22), [t1] "s"(tag1),
                   [wb] "s"(w * (HS * 4)), [bit] "s"(1u << w), [fo] "s"((w >> 2) * FO1), [tb] "n"(TOFF),
                   [rd] "n"(offsetof(LdsLow, rdone)), [fl] "n"(offsetof(LdsLow, flag)),
                   [hi] "n"(offsetof(LdsLow, hist)), [to] "n"(offsetof(LdsLow, hist) + SW * HS * 4), [dg] "n"(offsetof(LdsLow, deg)),
                   [ct] "n"(offsetof(LdsLow, ct)), [br] "n"(offsetof(LdsLow, byrank)), [so] "n"(offsetof(LdsLow, so)), [pa] "n"(offsetof(LdsLow, pay))
                 : STREAM_CLOBBERS);
    }
    int lane;
    asm volatile("v_mbcnt_lo_u32_b32 %0, -1, 0\n v_mbcnt_hi_u32_b32 %0, -1, %0" : "=v"(lane));
    __builtin_assume(lane >= 0 && lane < 64);
    u32x4* rows = &L.pay[w * CAPW];
#pragma unroll 1
    for (int it = 0; it < 2 * NS; it++) {
      const int sl = it >> 1, slice = it & 1, ch = slice * 64 + lane;
      const unsigned dl = (L.nl[w * 8 + sl] / 4 - node0) & 63;
      float acc[4] = {0.f, 0.f, 0.f, 0.f};
#pragma unroll 1
      for (int eb = 0; eb < E; eb += 64) {
        unsigned long long m = __ballot(eb + lane < E && (unsigned)dst[min(eb + lane, E - 1)] == node0 + dl);
#pragma unroll 1
        for (; m; m &= m - 1) {
          const int e = eb + __builtin_ctzll(m);
          const u32x2 ry = Y[e], rf = ef[e];
          float yy[4] = {bf2f(ry[0] & 0xffff), bf2f(ry[0] >> 16), bf2f(ry[1] & 0xffff), bf2f(ry[1] >> 16)};
          float ee[4] = {bf2f(rf[0] & 0xffff), bf2f(rf[0] >> 16), bf2f(rf[1] & 0xffff), bf2f(rf[1] >> 16)};
          float z = 0.f;
#pragma unroll
          for (int k = 0; k < 4; k++) z += ee[k] * bf2f(wrad[k * C_ + ch]);
          float R = z / (1.f + __expf(-z));
          const unsigned short* xr = x + (size_t)src[e] * 512 + ch * 4;
#pragma unroll 1
          for (int o = 0; o < 4; o++) {
            float tp = 0.f;
#pragma unroll 1
            for (int i = 0; i < 4; i++) {
              float A = 0.f;
#pragma unroll
              for (int f = 0; f < 4; f++) A += yy[f] * bf2f(cg[i * 16 + f * 4 + o]);
              tp += bf2f(xr[i]) * bf2f(f2bf(A));
            }
            float v = R * tp;
            acc[0] += o == 0 ? v : 0.f;
            acc[1] += o == 1 ? v : 0.f;
            acc[2] += o == 2 ? v : 0.f;
            acc[3] += o == 3 ? v : 0.f;
          }
        }
      }
      u32x4* rw = sl < 4 ? &rows[sl * 64] : &L.row5[w * 64];
      unsigned short* ar = reinterpret_cast<unsigned short*>(&rw[slice * 32]) + (lane >> 5) * 128 + (lane & 31);
#pragma unroll
      for (int o = 0; o < 4; o++) ar[o * 32] = f2bf(acc[o]);
    }
    asm volatile("" ::: "memory");
    if (lane == 0) atomicOr(&L.rdone, 1u << w);
  }
}

extern "C" __global__ __launch_bounds__(C_) void tfn_plain_kernel(
    const int* __restrict__ dst, const int* __restrict__ src, const unsigned short* __restrict__ Y,
    const unsigned short* __restrict__ ef, const unsigned short* __restrict__ x, unsigned short* __restrict__ out,
    const unsigned short* __restrict__ wrad, const unsigned short* __restrict__ cg, const unsigned short* __restrict__ wmix,
    long long E) {
  __shared__ f32x4 agg[C_];
  const int c = threadIdx.x, lane = c & 63;
  f32x4 acc = {0.f, 0.f, 0.f, 0.f};
#pragma unroll 1
  for (long long eb = 0; eb < E; eb += 64) {
    unsigned long long m = __ballot(eb + lane < E && dst[eb + lane < E ? eb + lane : 0] == (int)blockIdx.x);
#pragma unroll 1
    for (; m; m &= m - 1) {
      const long long e = eb + __builtin_ctzll(m);
      float z = 0.f;
#pragma unroll
      for (int k = 0; k < 4; k++) z += bf2f(ef[e * 4 + k]) * bf2f(wrad[k * C_ + c]);
      const float R = z / (1.f + __expf(-z));
#pragma unroll
      for (int i = 0; i < 4; i++) {
        const float xv = R * bf2f(x[(size_t)src[e] * 512 + c * 4 + i]);
#pragma unroll
        for (int f = 0; f < 4; f++)
#pragma unroll
          for (int o = 0; o < 4; o++) acc[o] += xv * bf2f(Y[e * 4 + f]) * bf2f(cg[i * 16 + f * 4 + o]);
      }
    }
  }
  agg[c] = acc;
  __syncthreads();
  f32x4 r = {0.f, 0.f, 0.f, 0.f};
#pragma unroll 4
  for (int k = 0; k < C_; k++) r += agg[k] * bf2f(wmix[k * C_ + c]);
  *reinterpret_cast<u32x2*>(out + (size_t)blockIdx.x * 512 + c * 4) = u32x2{pk2bf(r[0], r[1]), pk2bf(r[2], r[3])};
}

extern "C" void kernel_launch(void* const* d_in, const int* in_sizes, int n_in, void* d_out, int out_size,
                              void* d_ws, size_t ws_size, hipStream_t stream) {
  const unsigned short* x = (const unsigned short*)d_in[0];
  const int* src = (const int*)d_in[1];
  const int* dst = (const int*)d_in[2];
  const u32x2* Y = (const u32x2*)d_in[3];
  const u32x2* ef = (const u32x2*)d_in[4];
  const unsigned short* wrad = (const unsigned short*)d_in[5];
  const unsigned short* cg = (const unsigned short*)d_in[6];
  const unsigned short* wmix = (const unsigned short*)d_in[7];
  const int N = in_sizes[0] / 512;
  const int E = in_sizes[1];
  const int CE = ((E + NBMAX - 1) / NBMAX + 1) & ~1;
  const unsigned M2 = ((unsigned)NBMAX << 22) / (unsigned)N;
  if (N <= 64 || ((1u << 22) + M2 - 1) / M2 > (unsigned)NPB || CE > CH || ws_size < WS_END) {
    tfn_plain_kernel<<<N, C_, 0, stream>>>(dst, src, (const unsigned short*)Y, (const unsigned short*)ef, x,
                                           (unsigned short*)d_out, wrad, cg, wmix, E);
    return;
  }
  tfn_main_kernel<<<NBMAX, MT, 0, stream>>>(dst, E, 0x20000, src, CE, 0x20000, Y, N, 0x20000, ef, M2, 0x20000, x, (char*)d_ws,
                                           (u32x4*)d_out, wrad, cg, wmix);
}
```

```cpp
#include <hip/hip_runtime.h>
#include <hip/hip_bf16.h>
#include <stddef.h>
#include <stdint.h>

typedef __attribute__((ext_vector_type(2))) float f32x2;
typedef __attribute__((ext_vector_type(4))) float f32x4;
typedef __attribute__((ext_vector_type(2))) unsigned int u32x2;
typedef __attribute__((ext_vector_type(4))) unsigned int u32x4;
typedef __attribute__((ext_vector_type(8))) unsigned int u32x8;
typedef __attribute__((ext_vector_type(16))) unsigned int u32x16;
typedef __attribute__((ext_vector_type(2))) __bf16 bf16x2;

#define STREAM_ASM \
  "s_mov_b32 s63, 0x10000\n" \
  "Lh%=:\n" \
  "buffer_load_dwordx4 v[72:75], v136, %[ws], %[ho] offen sc1\n" \
  "v_mov_b32 v117, 0\n" \
  "v_mov_b32 v116, 64\n" \
  "v_mov_b32 v87, 0\n" \
  "v_mov_b64 v[44:45], 0\n" \
  "v_mov_b64 v[46:47], 0\n" \
  "v_mov_b32 v118, 0x7fc\n" \
  "s_waitcnt vmcnt(0)\n" \
  "v_add3_u32 v108, v72, v73, v74\n" \
  "v_xor_b32 v109, %[t0], v72\n" \
  "v_add_u32 v108, v108, v75\n" \
  "v_cmp_lt_u32 vcc, 0x3fffff, v109\n" \
  "v_cmp_ne_u32_e64 s[74:75], %[t1], v108\n" \
  "s_or_b64 vcc, vcc, s[74:75]\n" \
  "s_cbranch_scc1 Lhr%=\n" \
  "s_mov_b64 exec, -1\n" \
  "v_bfe_u32 v108, v72, 11, 11\n" \
  "v_and_b32 v109, v140, v108\n" \
  "v_and_b32 v112, 0x7ff, v72\n" \
  "v_sub_u32 v111, v108, v141\n" \
  "v_add_u32_dpp v109, v109, v109 row_shr:1 row_mask:0xf bank_mask:0xf bound_ctrl:1\n" \
  "v_lshl_add_u32 v112, v112, 2, v137\n" \
  "v_and_b32 v114, v140, v73\n" \
  "v_add_u32_dpp v109, v109, v109 row_shr:2 row_mask:0xf bank_mask:0xf bound_ctrl:1\n" \
  "v_and_b32 v115, v140, v74\n" \
  "v_sub_u32 v114, 0, v114\n" \
  "v_add_u32_dpp v109, v109, v109 row_shr:4 row_mask:0xf bank_mask:0xf bound_ctrl:1\n" \
  "v_sub_u32 v115, 0, v115\n" \
  "s_nop 0\n" \
  "v_add_u32_dpp v109, v109, v109 row_shr:8 row_mask:0xf bank_mask:0xf bound_ctrl:1\n" \
  "s_nop 1\n" \
  "v_add_u32_dpp v109, v109, v109 row_bcast:15 row_mask:0xa bank_mask:0xf\n" \
  "s_nop 1\n" \
  "v_add_u32_dpp v109, v109, v109 row_bcast:31 row_mask:0xc bank_mask:0xf\n" \
  "v_sub_u32 v108, v109, v108\n" \
  "v_readlane_b32 s62, v109, 63\n" \
  "v_lshl_add_u32 v113, v108, 2, v138\n" \
  "s_cmpk_gt_u32 s62, 0x100\n" \
  "s_cbranch_scc1 Lover%=\n" \
  "Lw%=:\n" \
  "v_cmpx_lt_i32 vcc, 0, v111\n" \
  "ds_write_b32 v113, v112 offset:0\n" \
  "v_add_u32 v112, 8, v112\n" \
  "v_cmpx_lt_i32 vcc, 2, v111\n" \
  "ds_write_b32 v113, v112 offset:8\n" \
  "v_add_u32 v112, 8, v112\n" \
  "v_cmpx_lt_i32 vcc, 4, v111\n" \
  "ds_write_b32 v113, v112 offset:16\n" \
  "v_add_u32 v112, 8, v112\n" \
  "v_cmpx_lt_i32 vcc, 6, v111\n" \
  "ds_write_b32 v113, v112 offset:24\n" \
  "v_add_u32 v112, 8, v112\n" \
  "v_cmpx_lt_i32 vcc, 8, v111\n" \
  "ds_write_b32 v113, v112 offset:32\n" \
  "v_add_u32 v112, 8, v112\n" \
  "s_cbranch_execz Lwd%=\n" \
  "v_cmpx_lt_i32 vcc, 10, v111\n" \
  "ds_write_b32 v113, v112 offset:40\n" \
  "v_add_u32 v112, 8, v112\n" \
  "s_cbranch_execz Lwd%=\n" \
  "v_cmpx_lt_i32 vcc, 12, v111\n" \
  "ds_write_b32 v113, v112 offset:48\n" \
  "v_add_u32 v112, 8, v112\n" \
  "s_cbranch_execz Lwd%=\n" \
  "v_cmpx_lt_i32 vcc, 14, v111\n" \
  "ds_write_b32 v113, v112 offset:56\n" \
  "v_add_u32 v112, 8, v112\n" \
  "v_add_u32 v113, 64, v113\n" \
  "v_subrev_u32 v111, 16, v111\n" \
  "s_branch Lw%=\n" \
  "Lwd%=:\n" \
  "s_mov_b64 exec, -1\n" \
  "Lld%=:\n" \
  "ds_read2st64_b32 v[76:77], v139 offset1:1\n" \
  "ds_read2st64_b32 v[78:79], v139 offset0:2 offset1:3\n" \
  "s_mov_b32 s63, 0x10000\n" \
  "s_waitcnt lgkmcnt(0)\n" \
  "buffer_load_dword v84, v76, %[ws], %[rk] offen sc1\n" \
  "buffer_load_dword v85, v77, %[ws], %[rk] offen sc1\n" \
  "buffer_load_dword v86, v78, %[ws], %[rk] offen sc1\n" \
  "s_cmpk_le_u32 s62, 0xc0\n" \
  "s_cbranch_scc1 Lka%=\n" \
  "buffer_load_dword v87, v79, %[ws], %[rk] offen sc1\n" \
  "Lka%=:\n" \
  "v_lshlrev_b32 v80, 2, v76\n" \
  "v_lshlrev_b32 v81, 2, v77\n" \
  "v_lshlrev_b32 v82, 2, v78\n" \
  "v_lshlrev_b32 v83, 2, v79\n" \
  "s_barrier\n" \
  "s_cmpk_le_u32 s62, 0xc0\n" \
  "s_cbranch_scc1 Lpa%=\n" \
  "buffer_load_dwordx4 v[44:47], v83, %[ws], %[rp] offen sc1\n" \
  "Lpa%=:\n" \
  "buffer_load_dwordx4 v[32:35], v80, %[ws], %[rp] offen sc1\n" \
  "buffer_load_dwordx4 v[36:39], v81, %[ws], %[rp] offen sc1\n" \
  "buffer_load_dwordx4 v[40:43], v82, %[ws], %[rp] offen sc1\n" \
  "v_cmp_le_i32_e64 s[66:67], 0, v80\n" \
  "v_cmp_le_i32_e64 s[68:69], 0, v81\n" \
  "v_cmp_le_i32_e64 s[70:71], 0, v82\n" \
  "v_cmp_le_i32_e64 s[72:73], 0, v83\n" \
  "v_mov_b64 v[48:49], 0\n" \
  "v_mov_b64 v[50:51], 0\n" \
  "v_mov_b64 v[52:53], 0\n" \
  "v_mov_b64 v[54:55], 0\n" \
  "v_mov_b64 v[56:57], 0\n" \
  "v_mov_b64 v[58:59], 0\n" \
  "v_mov_b64 v[60:61], 0\n" \
  "v_mov_b64 v[62:63], 0\n" \
  "v_mov_b64 v[64:65], 0\n" \
  "v_mov_b64 v[66:67], 0\n" \
  "v_mov_b64 v[68:69], 0\n" \
  "v_mov_b64 v[70:71], 0\n" \
  "s_waitcnt vmcnt(3)\n" \
  "Lk%=:\n" \
  "v_add3_u32 v108, v84, v85, v86\n" \
  "v_add3_u32 v108, v108, v87, v114\n" \
  "v_lshlrev_b32 v88, 1, v84\n" \
  "v_lshlrev_b32 v92, 4, v84\n" \
  "v_add_u32_dpp v108, v108, v108 row_shr:1 row_mask:0xf bank_mask:0xf bound_ctrl:1\n" \
  "v_lshrrev_b32 v104, 2, v84\n" \
  "v_and_or_b32 v88, v88, v118, %[wb]\n" \
  "v_add_u32_dpp v108, v108, v108 row_shr:2 row_mask:0xf bank_mask:0xf bound_ctrl:1\n" \
  "v_lshlrev_b32_e64 v100, v92, 4\n" \
  "v_and_b32 v104, 0xfc, v104\n" \
  "v_add_u32_dpp v108, v108, v108 row_shr:4 row_mask:0xf bank_mask:0xf bound_ctrl:1\n" \
  "v_lshlrev_b32 v89, 1, v85\n" \
  "v_lshlrev_b32 v93, 4, v85\n" \
  "v_add_u32_dpp v108, v108, v108 row_shr:8 row_mask:0xf bank_mask:0xf bound_ctrl:1\n" \
  "v_lshrrev_b32 v105, 2, v85\n" \
  "v_and_or_b32 v89, v89, v118, %[wb]\n" \
  "v_add_u32_dpp v108, v108, v108 row_bcast:15 row_mask:0xa bank_mask:0xf\n" \
  "v_lshlrev_b32_e64 v101, v93, 4\n" \
  "v_and_b32 v105, 0xfc, v105\n" \
  "v_add_u32_dpp v108, v108, v108 row_bcast:31 row_mask:0xc bank_mask:0xf\n" \
  "v_lshlrev_b32 v90, 1, v86\n" \
  "v_readlane_b32 s65, v108, 63\n" \
  "v_lshlrev_b32 v94, 4, v86\n" \
  "v_lshrrev_b32 v106, 2, v86\n" \
  "v_and_or_b32 v90, v90, v118, %[wb]\n" \
  "v_lshlrev_b32_e64 v102, v94, 4\n" \
  "v_and_b32 v106, 0xfc, v106\n" \
  "s_cmp_lg_u32 s65, 0\n" \
  "s_cbranch_scc1 Lkr%=\n" \
  "Lkc%=:\n" \
  "s_mov_b64 exec, s[66:67]\n" \
  "ds_add_rtn_u32 v96, v88, v100 offset:%[hi]\n" \
  "ds_add_u32 v104, v116 offset:%[dg]\n" \
  "s_mov_b64 exec, s[68:69]\n" \
  "ds_add_rtn_u32 v97, v89, v101 offset:%[hi]\n" \
  "ds_add_u32 v105, v116 offset:%[dg]\n" \
  "s_mov_b64 exec, s[70:71]\n" \
  "ds_add_rtn_u32 v98, v90, v102 offset:%[hi]\n" \
  "ds_add_u32 v106, v116 offset:%[dg]\n" \
  "s_cmpk_gt_u32 s62, 0xc0\n" \
  "s_mov_b64 exec, -1\n" \
  "s_cbranch_scc1 Lc3%=\n" \
  "Lc4%=:\n" \
  "ds_read_b128 v[24:27], v20 offset:16\n" \
  "ds_read_b128 v[28:31], v8 offset:%[ct]+128\n" \
  "ds_read_b128 v[20:23], v20\n" \
  "v_and_b32 v100, v118, v88\n" \
  "v_and_b32 v101, v118, v89\n" \
  "v_and_b32 v102, v118, v90\n" \
  "s_waitcnt lgkmcnt(0)\n" \
  "s_barrier\n" \
  "ds_read_b32 v108, v149 offset:%[dg]\n" \
  "ds_read_b32 v109, v150 offset:%[dg]\n" \
  "ds_read2st64_b32 v[76:77], v142 offset0:0 offset1:8\n" \
  "ds_read2st64_b32 v[78:79], v142 offset0:16 offset1:24\n" \
  "ds_read2st64_b32 v[104:105], v142 offset0:32 offset1:40\n" \
  "ds_read2st64_b32 v[106:107], v142 offset0:48 offset1:56\n" \
  "v_and_b32 v84, 0xfffc00, v84\n" \
  "v_and_b32 v85, 0xfffc00, v85\n" \
  "v_and_b32 v86, 0xfffc00, v86\n" \
  "v_and_b32 v87, 0xfffc00, v87\n" \
  "v_mov_b32 v111, 0\n" \
  "v_mov_b32 v112, 63\n" \
  "s_waitcnt lgkmcnt(4)\n" \
  "v_readlane_b32 s74, v109, 0\n" \
  "v_readlane_b32 s76, v109, 1\n" \
  "v_readlane_b32 s78, v109, 2\n" \
  "s_nop 0\n" \
  "v_cmp_lt_i32_e64 s[74:75], s74, v108\n" \
  "v_cmp_lt_i32_e64 s[76:77], s76, v108\n" \
  "v_cmp_lt_i32_e64 s[78:79], s78, v108\n" \
  "s_bcnt1_i32_b64 s74, s[74:75]\n" \
  "s_bcnt1_i32_b64 s76, s[76:77]\n" \
  "s_bcnt1_i32_b64 s78, s[78:79]\n" \
  "v_writelane_b32 v112, s74, 0\n" \
  "v_writelane_b32 v112, s76, 1\n" \
  "v_writelane_b32 v112, s78, 2\n" \
  "v_lshlrev_b32 v112, 2, v112\n" \
  "s_waitcnt lgkmcnt(3)\n" \
  "ds_write2st64_b32 v142, v117, v76 offset1:8\n" \
  "v_add_u32 v77, v77, v76\n" \
  "s_waitcnt lgkmcnt(3)\n" \
  "v_add_u32 v78, v78, v77\n" \
  "ds_write2st64_b32 v142, v77, v78 offset0:16 offset1:24\n" \
  "v_add_u32 v79, v79, v78\n" \
  "s_waitcnt lgkmcnt(3)\n" \
  "v_add_u32 v104, v104, v79\n" \
  "ds_write2st64_b32 v142, v79, v104 offset0:32 offset1:40\n" \
  "v_add_u32 v105, v105, v104\n" \
  "s_waitcnt lgkmcnt(3)\n" \
  "v_add_u32 v106, v106, v105\n" \
  "ds_write2st64_b32 v142, v105, v106 offset0:48 offset1:56\n" \
  "v_add_u32 v107, v107, v106\n" \
  "ds_write_b32 v142, v107 offset:16384\n" \
  "ds_write_b32 v112, v150 offset:%[br]\n" \
  "s_waitcnt lgkmcnt(0)\n" \
  "s_barrier\n" \
  "ds_read_b32 v108, v143\n" \
  "s_mov_b32 s74, 0x1f1f1f1f\n" \
  "s_mov_b32 s75, 0x1f1f1f1f\n" \
  "s_waitcnt lgkmcnt(0)\n" \
  "v_lshl_add_u32 v76, v108, 3, v145\n" \
  "s_mov_b64 exec, s[74:75]\n" \
  "ds_read_b32 v111, v76\n" \
  "s_mov_b64 exec, -1\n" \
  "ds_write_b32 v148, v108\n" \
  "s_waitcnt lgkmcnt(1)\n" \
  "v_add_u32_dpp v112, v111, v111 row_shr:1 row_mask:0xf bank_mask:0xf bound_ctrl:1\n" \
  "v_sub_u32 v77, v146, v111\n" \
  "s_nop 0\n" \
  "v_add_u32_dpp v112, v112, v112 row_shr:2 row_mask:0xf bank_mask:0xf bound_ctrl:1\n" \
  "s_nop 1\n" \
  "v_add_u32_dpp v112, v112, v112 row_shr:4 row_mask:0xf bank_mask:0xf bound_ctrl:1\n" \
  "s_nop 1\n" \
  "v_add_u32_dpp v112, v112, v112 row_shr:8 row_mask:0xf bank_mask:0xf bound_ctrl:1\n" \
  "s_nop 1\n" \
  "v_add_u32_dpp v112, v112, v112 row_bcast:15 row_mask:0xa bank_mask:0xf\n" \
  "s_nop 1\n" \
  "v_add_u32_dpp v112, v112, v112 row_bcast:31 row_mask:0xc bank_mask:0xf\n" \
  "v_sub_u32 v111, v112, v111\n" \
  "v_readlane_b32 s65, v112, 63\n" \
  "v_mov_b32_dpp v78, v112 row_newbcast:7 row_mask:0xf bank_mask:0x3\n" \
  "v_mov_b32_dpp v79, v111 row_newbcast:0 row_mask:0xf bank_mask:0x3\n" \
  "v_add_u32 v77, v77, v112\n" \
  "v_mov_b32_dpp v78, v112 row_newbcast:15 row_mask:0xf bank_mask:0xc\n" \
  "v_mov_b32_dpp v79, v111 row_newbcast:8 row_mask:0xf bank_mask:0xc\n" \
  "s_and_b32 s64, s65, 0xffff\n" \
  "v_lshl_add_u32 v77, v78, 16, v77\n" \
  "s_lshr_b32 s65, s65, 16\n" \
  "v_add_u32_sdwa v77, v77, v79 dst_sel:DWORD dst_unused:UNUSED_PAD src0_sel:DWORD src1_sel:WORD_1\n" \
  "s_add_u32 s64, s64, s65\n" \
  "s_mov_b64 exec, s[74:75]\n" \
  "ds_write_b32 v76, v77\n" \
  "s_mov_b64 exec, -1\n" \
  "s_cmpk_gt_u32 s64, 864\n" \
  "s_cbranch_scc1 Llo%=\n" \
  "Llc%=:\n" \
  "s_waitcnt lgkmcnt(0)\n" \
  "s_barrier\n" \
  "ds_read_b32 v109, v117 offset:%[fl]\n" \
  "ds_read_b32 v76, v88 offset:%[hi]\n" \
  "ds_read_b32 v104, v100 offset:%[to]\n" \
  "ds_read_b32 v77, v89 offset:%[hi]\n" \
  "ds_read_b32 v105, v101 offset:%[to]\n" \
  "ds_read_b32 v78, v90 offset:%[hi]\n" \
  "ds_read_b32 v106, v102 offset:%[to]\n" \
  "v_add_u32 v113, s64, v147\n" \
  "v_bfrev_b32 v111, 1\n" \
  "s_max_u32 s60, s64, 4\n" \
  "s_sub_u32 s60, s60, 4\n" \
  "s_lshr_b32 s60, s60, 4\n" \
  "s_waitcnt lgkmcnt(4)\n" \
  "v_readfirstlane_b32 s61, v109\n" \
  "v_add3_u32 v88, v76, v104, v96\n" \
  "v_lshrrev_b32 v88, v92, v88\n" \
  "v_bfe_u32 v72, v88, 13, 3\n" \
  "v_and_b32 v88, 0x1ffc, v88\n" \
  "v_lshl_or_b32 v72, v72, 1, v84\n" \
  "s_cmp_lg_u32 s61, 0\n" \
  "s_cbranch_scc1 Lslow%=\n" \
  "s_waitcnt lgkmcnt(0)\n" \
  "v_add3_u32 v89, v77, v105, v97\n" \
  "v_add3_u32 v90, v78, v106, v98\n" \
  "v_lshrrev_b32 v89, v93, v89\n" \
  "v_lshrrev_b32 v90, v94, v90\n" \
  "v_bfe_u32 v73, v89, 13, 3\n" \
  "v_bfe_u32 v74, v90, 13, 3\n" \
  "v_and_b32 v89, 0x1ffc, v89\n" \
  "v_and_b32 v90, 0x1ffc, v90\n" \
  "v_lshl_or_b32 v73, v73, 1, v85\n" \
  "v_lshl_or_b32 v74, v74, 1, v86\n" \
  "s_mov_b64 exec, s[66:67]\n" \
  "ds_write_b32 v88, v72 offset:%[so]\n" \
  "s_mov_b64 exec, s[68:69]\n" \
  "ds_write_b32 v89, v73 offset:%[so]\n" \
  "s_mov_b64 exec, s[70:71]\n" \
  "ds_write_b32 v90, v74 offset:%[so]\n" \
  "s_cmpk_gt_u32 s62, 0xc0\n" \
  "s_cbranch_scc1 Lq3%=\n" \
  "Lq4%=:\n" \
  "s_mov_b32 exec_lo, -1\n" \
  "s_mov_b32 exec_hi, 0\n" \
  "ds_write_b32 v113, v111 offset:%[so]\n" \
  "s_mov_b64 exec, -1\n" \
  "v_lshlrev_b32 v88, 2, v88\n" \
  "v_lshlrev_b32 v89, 2, v89\n" \
  "v_lshlrev_b32 v90, 2, v90\n" \
  "v_lshlrev_b32 v91, 2, v91\n" \
  "v_lshlrev_b32 v113, 2, v113\n" \
  "s_mov_b32 s63, 0x10000\n" \
  "s_waitcnt vmcnt(0)\n" \
  "Lpc%=:\n" \
  "v_add3_u32 v108, v32, v33, v115\n" \
  "v_add3_u32 v111, v34, v35, v36\n" \
  "v_add3_u32 v112, v37, v38, v39\n" \
  "v_add3_u32 v108, v108, v40, v41\n" \
  "v_add3_u32 v111, v111, v42, v43\n" \
  "v_add3_u32 v112, v112, v44, v45\n" \
  "v_add3_u32 v108, v108, v46, v47\n" \
  "v_add3_u32 v108, v108, v111, v112\n" \
  "v_subrev_u32 v32, %[tl], v32\n" \
  "v_subrev_u32 v34, %[tl], v34\n" \
  "v_subrev_u32 v36, %[tl], v36\n" \
  "v_subrev_u32 v38, %[tl], v38\n" \
  "v_subrev_u32 v40, %[tl], v40\n" \
  "v_subrev_u32 v42, %[tl], v42\n" \
  "v_subrev_u32 v44, %[tl], v44\n" \
  "v_subrev_u32 v46, %[tl], v46\n" \
  "s_mov_b64 exec, s[66:67]\n" \
  "ds_write_b128 v88, v[32:35] offset:%[pa]\n" \
  "s_mov_b64 exec, s[68:69]\n" \
  "ds_write_b128 v89, v[36:39] offset:%[pa]\n" \
  "s_mov_b64 exec, s[70:71]\n" \
  "ds_write_b128 v90, v[40:43] offset:%[pa]\n" \
  "s_mov_b64 exec, s[72:73]\n" \
  "ds_write_b128 v91, v[44:47] offset:%[pa]\n" \
  "s_mov_b32 exec_lo, -1\n" \
  "s_mov_b32 exec_hi, 0\n" \
  "ds_write_b128 v113, v[48:51] offset:%[pa]\n" \
  "s_mov_b64 exec, -1\n" \
  "s_nop 1\n" \
  "v_add_u32_dpp v108, v108, v108 row_shr:1 row_mask:0xf bank_mask:0xf bound_ctrl:1\n" \
  "s_nop 1\n" \
  "v_add_u32_dpp v108, v108, v108 row_shr:2 row_mask:0xf bank_mask:0xf bound_ctrl:1\n" \
  "s_nop 1\n" \
  "v_add_u32_dpp v108, v108, v108 row_shr:4 row_mask:0xf bank_mask:0xf bound_ctrl:1\n" \
  "s_nop 1\n" \
  "v_add_u32_dpp v108, v108, v108 row_shr:8 row_mask:0xf bank_mask:0xf bound_ctrl:1\n" \
  "s_nop 1\n" \
  "v_add_u32_dpp v108, v108, v108 row_bcast:15 row_mask:0xa bank_mask:0xf\n" \
  "s_nop 1\n" \
  "v_add_u32_dpp v108, v108, v108 row_bcast:31 row_mask:0xc bank_mask:0xf\n" \
  "s_nop 0\n" \
  "v_readlane_b32 s65, v108, 63\n" \
  "s_waitcnt lgkmcnt(0)\n" \
  "s_cmp_lg_u32 s65, 0\n" \
  "s_cbranch_scc1 Lpr%=\n" \
  "Lpw%=:\n" \
  "s_barrier\n" \
  "ds_read2_b32  v[10:11], v12 offset1:8\n" \
  "ds_read_b64 v[106:107], v14\n" \
  "ds_read_b64 v[104:105], v15\n" \
  "s_nop 0\n" \
  "s_setprio 1\n" \
  "v_mov_b64 v[32:33], 0\n" \
  "v_mov_b64 v[34:35], 0\n" \
  "v_mov_b64 v[36:37], 0\n" \
  "v_mov_b64 v[38:39], 0\n" \
  "v_mov_b64 v[40:41], 0\n" \
  "v_mov_b64 v[42:43], 0\n" \
  "v_mov_b64 v[44:45], 0\n" \
  "v_mov_b64 v[46:47], 0\n" \
  "s_waitcnt lgkmcnt(2)\n" \
  "v_readlane_b32 s80, v10, 0\n" \
  "v_readlane_b32 s81, v10, 1\n" \
  "v_readlane_b32 s82, v10, 2\n" \
  "v_readlane_b32 s83, v10, 3\n" \
  "v_readlane_b32 s84, v10, 4\n" \
  "v_readlane_b32 s85, v10, 5\n" \
  "s_andn2_b32 s61, s80, 15\n" \
  "buffer_load_dwordx4 v[120:123], v8, %[xs], s61 offen\n" \
  "s_andn2_b32 s61, s81, 15\n" \
  "buffer_load_dwordx4 v[124:127], v8, %[xs], s61 offen\n" \
  "s_andn2_b32 s61, s82, 15\n" \
  "buffer_load_dwordx4 v[128:131], v8, %[xs], s61 offen\n" \
  "s_andn2_b32 s61, s83, 15\n" \
  "buffer_load_dwordx4 v[132:135], v8, %[xs], s61 offen\n" \
  "s_andn2_b32 s61, s84, 15\n" \
  "buffer_load_dwordx4 v[136:139], v8, %[xs], s61 offen\n" \
  "v_mov_b64  v[96:97], 0\n" \
  "v_mov_b64  v[98:99], 0\n" \
  "v_mov_b64  v[100:101], 0\n" \
  "v_mov_b64  v[102:103], 0\n" \
  "s_waitcnt lgkmcnt(0)\n" \
  "v_mfma_f32_4x4x4_16b_bf16 v[80:83], v[104:105], v[28:29], 0 cbsz:4\n" \
  "v_mfma_f32_4x4x4_16b_bf16 v[84:87], v[104:105], v[30:31], 0 cbsz:4\n" \
  "v_mfma_f32_4x4x4_16b_bf16 v[112:115], v[106:107], v[20:21], 0\n" \
  "v_mfma_f32_4x4x4_16b_bf16 v[116:119], v[106:107], v[22:23], 0\n" \
  "v_mfma_f32_4x4x4_16b_bf16 v[72:75], v[106:107], v[24:25], 0\n" \
  "v_mfma_f32_4x4x4_16b_bf16 v[76:79], v[106:107], v[26:27], 0\n" \
  "v_cvt_pk_bf16_f32 v81, v80, v81\n" \
  "v_lshlrev_b32_sdwa v80, 1, v81 dst_sel:DWORD dst_unused:UNUSED_PAD src0_sel:DWORD src1_sel:WORD_0\n" \
  "v_lshlrev_b32_sdwa v81, 1, v81 dst_sel:DWORD dst_unused:UNUSED_PAD src0_sel:DWORD src1_sel:WORD_1\n" \
  "ds_read_u16_d16_hi v96, v80 offset:%[tb]\n" \
  "ds_read_u16_d16_hi v97, v81 offset:%[tb]\n" \
  "v_cvt_pk_bf16_f32 v85, v84, v85\n" \
  "v_lshlrev_b32_sdwa v84, 1, v85 dst_sel:DWORD dst_unused:UNUSED_PAD src0_sel:DWORD src1_sel:WORD_0\n" \
  "v_lshlrev_b32_sdwa v85, 1, v85 dst_sel:DWORD dst_unused:UNUSED_PAD src0_sel:DWORD src1_sel:WORD_1\n" \
  "ds_read_u16_d16_hi v98, v84 offset:%[tb]\n" \
  "ds_read_u16_d16_hi v99, v85 offset:%[tb]\n" \
  "v_cvt_pk_bf16_f32 v83, v82, v83\n" \
  "v_lshlrev_b32_sdwa v82, 1, v83 dst_sel:DWORD dst_unused:UNUSED_PAD src0_sel:DWORD src1_sel:WORD_0\n" \
  "v_lshlrev_b32_sdwa v83, 1, v83 dst_sel:DWORD dst_unused:UNUSED_PAD src0_sel:DWORD src1_sel:WORD_1\n" \
  "ds_read_u16_d16_hi v100, v82 offset:%[tb]\n" \
  "ds_read_u16_d16_hi v101, v83 offset:%[tb]\n" \
  "v_cvt_pk_bf16_f32 v108, v112, v116\n" \
  "v_cvt_pk_bf16_f32 v109, v72, v76\n" \
  "s_nop 0\n" \
  "s_waitcnt vmcnt(4)\n" \
  "v_mfma_f32_4x4x4_16b_bf16 v[72:75], v[108:109], v[120:121], 0 cbsz:4\n" \
  "v_mfma_f32_4x4x4_16b_bf16 v[76:79], v[108:109], v[122:123], 0 cbsz:4\n" \
  "s_andn2_b32 s61, s85, 15\n" \
  "buffer_load_dwordx4 v[140:143], v8, %[xs], s61 offen\n" \
  "s_waitcnt lgkmcnt(0)\n" \
  "s_nop 1\n" \
  "Lbody%=:\n" \
  "s_waitcnt lgkmcnt(2)\n" \
  "s_set_gpr_idx_on s80, gpr_idx(SRC2,DST)\n" \
  "v_pk_fma_f32 v[32:33], v[96:97], v[72:73], v[32:33] op_sel:[0,0,0] op_sel_hi:[0,1,1]\n" \
  "v_pk_fma_f32 v[42:43], v[96:97], v[74:75], v[42:43] op_sel:[0,0,0] op_sel_hi:[0,1,1]\n" \
  "v_pk_fma_f32 v[52:53], v[98:99], v[76:77], v[52:53] op_sel:[0,0,0] op_sel_hi:[0,1,1]\n" \
  "v_pk_fma_f32 v[62:63], v[98:99], v[78:79], v[62:63] op_sel:[0,0,0] op_sel_hi:[0,1,1]\n" \
  "s_set_gpr_idx_off\n" \
  "s_waitcnt vmcnt(4)\n" \
  "v_mfma_f32_4x4x4_16b_bf16 v[72:75], v[108:109], v[124:125], 0 cbsz:4 abid:1\n" \
  "v_mfma_f32_4x4x4_16b_bf16 v[76:79], v[108:109], v[126:127], 0 cbsz:4 abid:1\n" \
  "v_mfma_f32_4x4x4_16b_bf16 v[88:91], v[104:105], v[28:29], 0 cbsz:4 abid:1\n" \
  "v_mfma_f32_4x4x4_16b_bf16 v[92:95], v[104:105], v[30:31], 0 cbsz:4 abid:1\n" \
  "v_readlane_b32 s86, v10, 6\n" \
  "v_cvt_pk_bf16_f32 v87, v86, v87\n" \
  "v_lshlrev_b32_sdwa v86, 1, v87 dst_sel:DWORD dst_unused:UNUSED_PAD src0_sel:DWORD src1_sel:WORD_0\n" \
  "v_lshlrev_b32_sdwa v87, 1, v87 dst_sel:DWORD dst_unused:UNUSED_PAD src0_sel:DWORD src1_sel:WORD_1\n" \
  "s_andn2_b32 s61, s86, 15\n" \
  "buffer_load_dwordx4 v[144:147], v8, %[xs], s61 offen\n" \
  "ds_read_u16_d16_hi v102, v86 offset:%[tb]\n" \
  "ds_read_u16_d16_hi v103, v87 offset:%[tb]\n" \
  "v_mov_b32 v111, s60\n" \
  "ds_write2_b32 v110, v111, v111 offset1:8\n" \
  "s_set_gpr_idx_on s81, gpr_idx(SRC2,DST)\n" \
  "v_pk_fma_f32 v[32:33], v[96:97], v[72:73], v[32:33] op_sel:[1,0,0] op_sel_hi:[1,1,1]\n" \
  "v_pk_fma_f32 v[42:43], v[96:97], v[74:75], v[42:43] op_sel:[1,0,0] op_sel_hi:[1,1,1]\n" \
  "v_pk_fma_f32 v[52:53], v[98:99], v[76:77], v[52:53] op_sel:[1,0,0] op_sel_hi:[1,1,1]\n" \
  "v_pk_fma_f32 v[62:63], v[98:99], v[78:79], v[62:63] op_sel:[1,0,0] op_sel_hi:[1,1,1]\n" \
  "s_set_gpr_idx_off\n" \
  "s_waitcnt vmcnt(4)\n" \
  "v_mfma_f32_4x4x4_16b_bf16 v[72:75], v[108:109], v[128:129], 0 cbsz:4 abid:2\n" \
  "v_mfma_f32_4x4x4_16b_bf16 v[76:79], v[108:109], v[130:131], 0 cbsz:4 abid:2\n" \
  "v_readlane_b32 s87, v10, 7\n" \
  "v_cvt_pk_bf16_f32 v89, v88, v89\n" \
  "v_lshlrev_b32_sdwa v88, 1, v89 dst_sel:DWORD dst_unused:UNUSED_PAD src0_sel:DWORD src1_sel:WORD_0\n" \
  "v_lshlrev_b32_sdwa v89, 1, v89 dst_sel:DWORD dst_unused:UNUSED_PAD src0_sel:DWORD src1_sel:WORD_1\n" \
  "s_andn2_b32 s61, s87, 15\n" \
  "buffer_load_dwordx4 v[148:151], v8, %[xs], s61 offen\n" \
  "ds_read_u16_d16_hi v96, v88 offset:%[tb]\n" \
  "ds_read_u16_d16_hi v97, v89 offset:%[tb]\n" \
  "ds_read_b32 v111, v110 offset:16\n" \
  "s_waitcnt lgkmcnt(4)\n" \
  "s_set_gpr_idx_on s82, gpr_idx(SRC2,DST)\n" \
  "v_pk_fma_f32 v[32:33], v[100:101], v[72:73], v[32:33] op_sel:[0,0,0] op_sel_hi:[0,1,1]\n" \
  "v_pk_fma_f32 v[42:43], v[100:101], v[74:75], v[42:43] op_sel:[0,0,0] op_sel_hi:[0,1,1]\n" \
  "v_pk_fma_f32 v[52:53], v[102:103], v[76:77], v[52:53] op_sel:[0,0,0] op_sel_hi:[0,1,1]\n" \
  "v_pk_fma_f32 v[62:63], v[102:103], v[78:79], v[62:63] op_sel:[0,0,0] op_sel_hi:[0,1,1]\n" \
  "s_set_gpr_idx_off\n" \
  "s_waitcnt vmcnt(4) lgkmcnt(10)\n" \
  "v_mfma_f32_4x4x4_16b_bf16 v[72:75], v[108:109], v[132:133], 0 cbsz:4 abid:3\n" \
  "v_mfma_f32_4x4x4_16b_bf16 v[76:79], v[108:109], v[134:135], 0 cbsz:4 abid:3\n" \
  "v_readlane_b32 s80, v11, 0\n" \
  "v_cvt_pk_bf16_f32 v93, v92, v93\n" \
  "v_lshlrev_b32_sdwa v92, 1, v93 dst_sel:DWORD dst_unused:UNUSED_PAD src0_sel:DWORD src1_sel:WORD_0\n" \
  "v_lshlrev_b32_sdwa v93, 1, v93 dst_sel:DWORD dst_unused:UNUSED_PAD src0_sel:DWORD src1_sel:WORD_1\n" \
  "s_andn2_b32 s61, s80, 15\n" \
  "buffer_load_dwordx4 v[120:123], v8, %[xs], s61 offen\n" \
  "ds_read_u16_d16_hi v98, v92 offset:%[tb]\n" \
  "ds_read_u16_d16_hi v99, v93 offset:%[tb]\n" \
  "ds_read_b64 v[106:107], v14 offset:256\n" \
  "s_set_gpr_idx_on s83, gpr_idx(SRC2,DST)\n" \
  "v_pk_fma_f32 v[32:33], v[100:101], v[72:73], v[32:33] op_sel:[1,0,0] op_sel_hi:[1,1,1]\n" \
  "v_pk_fma_f32 v[42:43], v[100:101], v[74:75], v[42:43] op_sel:[1,0,0] op_sel_hi:[1,1,1]\n" \
  "v_pk_fma_f32 v[52:53], v[102:103], v[76:77], v[52:53] op_sel:[1,0,0] op_sel_hi:[1,1,1]\n" \
  "v_pk_fma_f32 v[62:63], v[102:103], v[78:79], v[62:63] op_sel:[1,0,0] op_sel_hi:[1,1,1]\n" \
  "s_set_gpr_idx_off\n" \
  "s_sub_u32 s60, s60, 1\n" \
  "s_cbranch_scc1 Lend%=\n" \
  "s_waitcnt vmcnt(4)\n" \
  "v_mfma_f32_4x4x4_16b_bf16 v[72:75], v[108:109], v[136:137], 0 cbsz:4 abid:4\n" \
  "v_mfma_f32_4x4x4_16b_bf16 v[76:79], v[108:109], v[138:139], 0 cbsz:4 abid:4\n" \
  "v_readlane_b32 s81, v11, 1\n" \
  "v_cvt_pk_bf16_f32 v91, v90, v91\n" \
  "v_lshlrev_b32_sdwa v90, 1, v91 dst_sel:DWORD dst_unused:UNUSED_PAD src0_sel:DWORD src1_sel:WORD_0\n" \
  "v_lshlrev_b32_sdwa v91, 1, v91 dst_sel:DWORD dst_unused:UNUSED_PAD src0_sel:DWORD src1_sel:WORD_1\n" \
  "s_andn2_b32 s61, s81, 15\n" \
  "buffer_load_dwordx4 v[124:127], v8, %[xs], s61 offen\n" \
  "ds_read_u16_d16_hi v100, v90 offset:%[tb]\n" \
  "ds_read_u16_d16_hi v101, v91 offset:%[tb]\n" \
  "v_add_u32 v14, 0x100, v14\n" \
  "s_waitcnt lgkmcnt(3)\n" \
  "s_set_gpr_idx_on s84, gpr_idx(SRC2,DST)\n" \
  "v_pk_fma_f32 v[32:33], v[96:97], v[72:73], v[32:33] op_sel:[0,0,0] op_sel_hi:[0,1,1]\n" \
  "v_pk_fma_f32 v[42:43], v[96:97], v[74:75], v[42:43] op_sel:[0,0,0] op_sel_hi:[0,1,1]\n" \
  "v_pk_fma_f32 v[52:53], v[98:99], v[76:77], v[52:53] op_sel:[0,0,0] op_sel_hi:[0,1,1]\n" \
  "v_pk_fma_f32 v[62:63], v[98:99], v[78:79], v[62:63] op_sel:[0,0,0] op_sel_hi:[0,1,1]\n" \
  "s_set_gpr_idx_off\n" \
  "s_waitcnt vmcnt(4)\n" \
  "v_mfma_f32_4x4x4_16b_bf16 v[72:75], v[108:109], v[140:141], 0 cbsz:4 abid:5\n" \
  "v_mfma_f32_4x4x4_16b_bf16 v[76:79], v[108:109], v[142:143], 0 cbsz:4 abid:5\n" \
  "v_mfma_f32_4x4x4_16b_bf16 v[80:83], v[104:105], v[28:29], 0 cbsz:4 abid:2\n" \
  "v_mfma_f32_4x4x4_16b_bf16 v[84:87], v[104:105], v[30:31], 0 cbsz:4 abid:2\n" \
  "v_readlane_b32 s82, v11, 2\n" \
  "v_cvt_pk_bf16_f32 v95, v94, v95\n" \
  "v_lshlrev_b32_sdwa v94, 1, v95 dst_sel:DWORD dst_unused:UNUSED_PAD src0_sel:DWORD src1_sel:WORD_0\n" \
  "v_lshlrev_b32_sdwa v95, 1, v95 dst_sel:DWORD dst_unused:UNUSED_PAD src0_sel:DWORD src1_sel:WORD_1\n" \
  "s_andn2_b32 s61, s82, 15\n" \
  "buffer_load_dwordx4 v[128:131], v8, %[xs], s61 offen\n" \
  "ds_read_u16_d16_hi v102, v94 offset:%[tb]\n" \
  "ds_read_u16_d16_hi v103, v95 offset:%[tb]\n" \
  "s_set_gpr_idx_on s85, gpr_idx(SRC2,DST)\n" \
  "v_pk_fma_f32 v[32:33], v[96:97], v[72:73], v[32:33] op_sel:[1,0,0] op_sel_hi:[1,1,1]\n" \
  "v_pk_fma_f32 v[42:43], v[96:97], v[74:75], v[42:43] op_sel:[1,0,0] op_sel_hi:[1,1,1]\n" \
  "v_pk_fma_f32 v[52:53], v[98:99], v[76:77], v[52:53] op_sel:[1,0,0] op_sel_hi:[1,1,1]\n" \
  "v_pk_fma_f32 v[62:63], v[98:99], v[78:79], v[62:63] op_sel:[1,0,0] op_sel_hi:[1,1,1]\n" \
  "s_set_gpr_idx_off\n" \
  "s_waitcnt vmcnt(4) lgkmcnt(7)\n" \
  "v_mfma_f32_4x4x4_16b_bf16 v[72:75], v[108:109], v[144:145], 0 cbsz:4 abid:6\n" \
  "v_mfma_f32_4x4x4_16b_bf16 v[76:79], v[108:109], v[146:147], 0 cbsz:4 abid:6\n" \
  "v_readlane_b32 s83, v11, 3\n" \
  "v_cvt_pk_bf16_f32 v81, v80, v81\n" \
  "v_lshlrev_b32_sdwa v80, 1, v81 dst_sel:DWORD dst_unused:UNUSED_PAD src0_sel:DWORD src1_sel:WORD_0\n" \
  "v_lshlrev_b32_sdwa v81, 1, v81 dst_sel:DWORD dst_unused:UNUSED_PAD src0_sel:DWORD src1_sel:WORD_1\n" \
  "s_andn2_b32 s61, s83, 15\n" \
  "buffer_load_dwordx4 v[132:135], v8, %[xs], s61 offen\n" \
  "ds_read_u16_d16_hi v96, v80 offset:%[tb]\n" \
  "ds_read_u16_d16_hi v97, v81 offset:%[tb]\n" \
  "v_readfirstlane_b32 s66, v111\n" \
  "s_setprio 1\n" \
  "s_add_u32 s67, s60, %[fo]\n" \
  "s_cmp_ge_u32 s67, s66\n" \
  "s_cbranch_scc0 Lf5_%=\n" \
  "s_setprio 2\n" \
  "Lf5_%=:\n" \
  "s_waitcnt lgkmcnt(2)\n" \
  "s_set_gpr_idx_on s86, gpr_idx(SRC2,DST)\n" \
  "v_pk_fma_f32 v[32:33], v[100:101], v[72:73], v[32:33] op_sel:[0,0,0] op_sel_hi:[0,1,1]\n" \
  "v_pk_fma_f32 v[42:43], v[100:101], v[74:75], v[42:43] op_sel:[0,0,0] op_sel_hi:[0,1,1]\n" \
  "v_pk_fma_f32 v[52:53], v[102:103], v[76:77], v[52:53] op_sel:[0,0,0] op_sel_hi:[0,1,1]\n" \
  "v_pk_fma_f32 v[62:63], v[102:103], v[78:79], v[62:63] op_sel:[0,0,0] op_sel_hi:[0,1,1]\n" \
  "s_set_gpr_idx_off\n" \
  "s_waitcnt vmcnt(4) lgkmcnt(6)\n" \
  "v_mfma_f32_4x4x4_16b_bf16 v[72:75], v[108:109], v[148:149], 0 cbsz:4 abid:7\n" \
  "v_mfma_f32_4x4x4_16b_bf16 v[76:79], v[108:109], v[150:151], 0 cbsz:4 abid:7\n" \
  "v_mfma_f32_4x4x4_16b_bf16 v[112:115], v[106:107], v[20:21], 0\n" \
  "v_readlane_b32 s84, v11, 4\n" \
  "v_cvt_pk_bf16_f32 v85, v84, v85\n" \
  "v_lshlrev_b32_sdwa v84, 1, v85 dst_sel:DWORD dst_unused:UNUSED_PAD src0_sel:DWORD src1_sel:WORD_0\n" \
  "v_lshlrev_b32_sdwa v85, 1, v85 dst_sel:DWORD dst_unused:UNUSED_PAD src0_sel:DWORD src1_sel:WORD_1\n" \
  "s_andn2_b32 s61, s84, 15\n" \
  "buffer_load_dwordx4 v[136:139], v8, %[xs], s61 offen\n" \
  "ds_read_u16_d16_hi v98, v84 offset:%[tb]\n" \
  "ds_read_u16_d16_hi v99, v85 offset:%[tb]\n" \
  "ds_read_b32 v10, v12 offset:64\n" \
  "s_set_gpr_idx_on s87, gpr_idx(SRC2,DST)\n" \
  "v_pk_fma_f32 v[32:33], v[100:101], v[72:73], v[32:33] op_sel:[1,0,0] op_sel_hi:[1,1,1]\n" \
  "v_pk_fma_f32 v[42:43], v[100:101], v[74:75], v[42:43] op_sel:[1,0,0] op_sel_hi:[1,1,1]\n" \
  "v_pk_fma_f32 v[52:53], v[102:103], v[76:77], v[52:53] op_sel:[1,0,0] op_sel_hi:[1,1,1]\n" \
  "v_pk_fma_f32 v[62:63], v[102:103], v[78:79], v[62:63] op_sel:[1,0,0] op_sel_hi:[1,1,1]\n" \
  "s_set_gpr_idx_off\n" \
  "s_sub_u32 s60, s60, 1\n" \
  "s_cbranch_scc1 Lend%=\n" \
  "s_waitcnt vmcnt(4)\n" \
  "v_mfma_f32_4x4x4_16b_bf16 v[72:75], v[108:109], v[120:121], 0 cbsz:4 abid:8\n" \
  "v_mfma_f32_4x4x4_16b_bf16 v[76:79], v[108:109], v[122:123], 0 cbsz:4 abid:8\n" \
  "v_readlane_b32 s85, v11, 5\n" \
  "v_cvt_pk_bf16_f32 v83, v82, v83\n" \
  "v_lshlrev_b32_sdwa v82, 1, v83 dst_sel:DWORD dst_unused:UNUSED_PAD src0_sel:DWORD src1_sel:WORD_0\n" \
  "v_lshlrev_b32_sdwa v83, 1, v83 dst_sel:DWORD dst_unused:UNUSED_PAD src0_sel:DWORD src1_sel:WORD_1\n" \
  "s_andn2_b32 s61, s85, 15\n" \
  "buffer_load_dwordx4 v[140:143], v8, %[xs], s61 offen\n" \
  "ds_read_u16_d16_hi v100, v82 offset:%[tb]\n" \
  "ds_read_u16_d16_hi v101, v83 offset:%[tb]\n" \
  "v_add_u32 v12, 64, v12\n" \
  "s_waitcnt lgkmcnt(3)\n" \
  "s_set_gpr_idx_on s80, gpr_idx(SRC2,DST)\n" \
  "v_pk_fma_f32 v[32:33], v[96:97], v[72:73], v[32:33] op_sel:[0,0,0] op_sel_hi:[0,1,1]\n" \
  "v_pk_fma_f32 v[42:43], v[96:97], v[74:75], v[42:43] op_sel:[0,0,0] op_sel_hi:[0,1,1]\n" \
  "v_pk_fma_f32 v[52:53], v[98:99], v[76:77], v[52:53] op_sel:[0,0,0] op_sel_hi:[0,1,1]\n" \
  "v_pk_fma_f32 v[62:63], v[98:99], v[78:79], v[62:63] op_sel:[0,0,0] op_sel_hi:[0,1,1]\n" \
  "s_set_gpr_idx_off\n" \
  "s_waitcnt vmcnt(4)\n" \
  "v_mfma_f32_4x4x4_16b_bf16 v[72:75], v[108:109], v[124:125], 0 cbsz:4 abid:9\n" \
  "v_mfma_f32_4x4x4_16b_bf16 v[76:79], v[108:109], v[126:127], 0 cbsz:4 abid:9\n" \
  "v_mfma_f32_4x4x4_16b_bf16 v[88:91], v[104:105], v[28:29], 0 cbsz:4 abid:3\n" \
  "v_mfma_f32_4x4x4_16b_bf16 v[92:95], v[104:105], v[30:31], 0 cbsz:4 abid:3\n" \
  "v_readlane_b32 s86, v11, 6\n" \
  "v_cvt_pk_bf16_f32 v87, v86, v87\n" \
  "v_lshlrev_b32_sdwa v86, 1, v87 dst_sel:DWORD dst_unused:UNUSED_PAD src0_sel:DWORD src1_sel:WORD_0\n" \
  "v_lshlrev_b32_sdwa v87, 1, v87 dst_sel:DWORD dst_unused:UNUSED_PAD src0_sel:DWORD src1_sel:WORD_1\n" \
  "s_andn2_b32 s61, s86, 15\n" \
  "buffer_load_dwordx4 v[144:147], v8, %[xs], s61 offen\n" \
  "ds_read_u16_d16_hi v102, v86 offset:%[tb]\n" \
  "ds_read_u16_d16_hi v103, v87 offset:%[tb]\n" \
  "v_mov_b32 v111, s60\n" \
  "ds_write2_b32 v110, v111, v111 offset1:8\n" \
  "s_set_gpr_idx_on s81, gpr_idx(SRC2,DST)\n" \
  "v_pk_fma_f32 v[32:33], v[96:97], v[72:73], v[32:33] op_sel:[1,0,0] op_sel_hi:[1,1,1]\n" \
  "v_pk_fma_f32 v[42:43], v[96:97], v[74:75], v[42:43] op_sel:[1,0,0] op_sel_hi:[1,1,1]\n" \
  "v_pk_fma_f32 v[52:53], v[98:99], v[76:77], v[52:53] op_sel:[1,0,0] op_sel_hi:[1,1,1]\n" \
  "v_pk_fma_f32 v[62:63], v[98:99], v[78:79], v[62:63] op_sel:[1,0,0] op_sel_hi:[1,1,1]\n" \
  "s_set_gpr_idx_off\n" \
  "s_waitcnt vmcnt(4)\n" \
  "v_mfma_f32_4x4x4_16b_bf16 v[72:75], v[108:109], v[128:129], 0 cbsz:4 abid:10\n" \
  "v_mfma_f32_4x4x4_16b_bf16 v[76:79], v[108:109], v[130:131], 0 cbsz:4 abid:10\n" \
  "v_readlane_b32 s87, v11, 7\n" \
  "v_cvt_pk_bf16_f32 v89, v88, v89\n" \
  "v_lshlrev_b32_sdwa v88, 1, v89 dst_sel:DWORD dst_unused:UNUSED_PAD src0_sel:DWORD src1_sel:WORD_0\n" \
  "v_lshlrev_b32_sdwa v89, 1, v89 dst_sel:DWORD dst_unused:UNUSED_PAD src0_sel:DWORD src1_sel:WORD_1\n" \
  "s_andn2_b32 s61, s87, 15\n" \
  "buffer_load_dwordx4 v[148:151], v8, %[xs], s61 offen\n" \
  "ds_read_u16_d16_hi v96, v88 offset:%[tb]\n" \
  "ds_read_u16_d16_hi v97, v89 offset:%[tb]\n" \
  "ds_read_b64 v[104:105], v15 offset:256\n" \
  "ds_read_b32 v111, v110 offset:16\n" \
  "s_waitcnt lgkmcnt(5)\n" \
  "s_set_gpr_idx_on s82, gpr_idx(SRC2,DST)\n" \
  "v_pk_fma_f32 v[32:33], v[100:101], v[72:73], v[32:33] op_sel:[0,0,0] op_sel_hi:[0,1,1]\n" \
  "v_pk_fma_f32 v[42:43], v[100:101], v[74:75], v[42:43] op_sel:[0,0,0] op_sel_hi:[0,1,1]\n" \
  "v_pk_fma_f32 v[52:53], v[102:103], v[76:77], v[52:53] op_sel:[0,0,0] op_sel_hi:[0,1,1]\n" \
  "v_pk_fma_f32 v[62:63], v[102:103], v[78:79], v[62:63] op_sel:[0,0,0] op_sel_hi:[0,1,1]\n" \
  "s_set_gpr_idx_off\n" \
  "s_waitcnt vmcnt(4) lgkmcnt(9)\n" \
  "v_mfma_f32_4x4x4_16b_bf16 v[72:75], v[108:109], v[132:133], 0 cbsz:4 abid:11\n" \
  "v_mfma_f32_4x4x4_16b_bf16 v[76:79], v[108:109], v[134:135], 0 cbsz:4 abid:11\n" \
  "v_mfma_f32_4x4x4_16b_bf16 v[116:119], v[106:107], v[22:23], 0\n" \
  "v_readlane_b32 s80, v10, 0\n" \
  "v_cvt_pk_bf16_f32 v93, v92, v93\n" \
  "v_lshlrev_b32_sdwa v92, 1, v93 dst_sel:DWORD dst_unused:UNUSED_PAD src0_sel:DWORD src1_sel:WORD_0\n" \
  "v_lshlrev_b32_sdwa v93, 1, v93 dst_sel:DWORD dst_unused:UNUSED_PAD src0_sel:DWORD src1_sel:WORD_1\n" \
  "s_andn2_b32 s61, s80, 15\n" \
  "buffer_load_dwordx4 v[120:123], v8, %[xs], s61 offen\n" \
  "ds_read_u16_d16_hi v98, v92 offset:%[tb]\n" \
  "ds_read_u16_d16_hi v99, v93 offset:%[tb]\n" \
  "s_set_gpr_idx_on s83, gpr_idx(SRC2,DST)\n" \
  "v_pk_fma_f32 v[32:33], v[100:101], v[72:73], v[32:33] op_sel:[1,0,0] op_sel_hi:[1,1,1]\n" \
  "v_pk_fma_f32 v[42:43], v[100:101], v[74:75], v[42:43] op_sel:[1,0,0] op_sel_hi:[1,1,1]\n" \
  "v_pk_fma_f32 v[52:53], v[102:103], v[76:77], v[52:53] op_sel:[1,0,0] op_sel_hi:[1,1,1]\n" \
  "v_pk_fma_f32 v[62:63], v[102:103], v[78:79], v[62:63] op_sel:[1,0,0] op_sel_hi:[1,1,1]\n" \
  "s_set_gpr_idx_off\n" \
  "s_sub_u32 s60, s60, 1\n" \
  "s_cbranch_scc1 Lend%=\n" \
  "s_waitcnt vmcnt(4)\n" \
  "v_mfma_f32_4x4x4_16b_bf16 v[72:75], v[108:109], v[136:137], 0 cbsz:4 abid:12\n" \
  "v_mfma_f32_4x4x4_16b_bf16 v[76:79], v[108:109], v[138:139], 0 cbsz:4 abid:12\n" \
  "v_readlane_b32 s81, v10, 1\n" \
  "v_cvt_pk_bf16_f32 v91, v90, v91\n" \
  "v_lshlrev_b32_sdwa v90, 1, v91 dst_sel:DWORD dst_unused:UNUSED_PAD src0_sel:DWORD src1_sel:WORD_0\n" \
  "v_lshlrev_b32_sdwa v91, 1, v91 dst_sel:DWORD dst_unused:UNUSED_PAD src0_sel:DWORD src1_sel:WORD_1\n" \
  "s_andn2_b32 s61, s81, 15\n" \
  "buffer_load_dwordx4 v[124:127], v8, %[xs], s61 offen\n" \
  "ds_read_u16_d16_hi v100, v90 offset:%[tb]\n" \
  "ds_read_u16_d16_hi v101, v91 offset:%[tb]\n" \
  "v_add_u32 v15, 0x100, v15\n" \
  "s_waitcnt lgkmcnt(2)\n" \
  "s_set_gpr_idx_on s84, gpr_idx(SRC2,DST)\n" \
  "v_pk_fma_f32 v[32:33], v[96:97], v[72:73], v[32:33] op_sel:[0,0,0] op_sel_hi:[0,1,1]\n" \
  "v_pk_fma_f32 v[42:43], v[96:97], v[74:75], v[42:43] op_sel:[0,0,0] op_sel_hi:[0,1,1]\n" \
  "v_pk_fma_f32 v[52:53], v[98:99], v[76:77], v[52:53] op_sel:[0,0,0] op_sel_hi:[0,1,1]\n" \
  "v_pk_fma_f32 v[62:63], v[98:99], v[78:79], v[62:63] op_sel:[0,0,0] op_sel_hi:[0,1,1]\n" \
  "s_set_gpr_idx_off\n" \
  "s_waitcnt vmcnt(4) lgkmcnt(5)\n" \
  "v_mfma_f32_4x4x4_16b_bf16 v[72:75], v[108:109], v[140:141], 0 cbsz:4 abid:13\n" \
  "v_mfma_f32_4x4x4_16b_bf16 v[76:79], v[108:109], v[142:143], 0 cbsz:4 abid:13\n" \
  "v_mfma_f32_4x4x4_16b_bf16 v[80:83], v[104:105], v[28:29], 0 cbsz:4\n" \
  "v_mfma_f32_4x4x4_16b_bf16 v[84:87], v[104:105], v[30:31], 0 cbsz:4\n" \
  "v_readlane_b32 s82, v10, 2\n" \
  "v_cvt_pk_bf16_f32 v95, v94, v95\n" \
  "v_lshlrev_b32_sdwa v94, 1, v95 dst_sel:DWORD dst_unused:UNUSED_PAD src0_sel:DWORD src1_sel:WORD_0\n" \
  "v_lshlrev_b32_sdwa v95, 1, v95 dst_sel:DWORD dst_unused:UNUSED_PAD src0_sel:DWORD src1_sel:WORD_1\n" \
  "s_andn2_b32 s61, s82, 15\n" \
  "buffer_load_dwordx4 v[128:131], v8, %[xs], s61 offen\n" \
  "ds_read_u16_d16_hi v102, v94 offset:%[tb]\n" \
  "ds_read_u16_d16_hi v103, v95 offset:%[tb]\n" \
  "s_set_gpr_idx_on s85, gpr_idx(SRC2,DST)\n" \
  "v_pk_fma_f32 v[32:33], v[96:97], v[72:73], v[32:33] op_sel:[1,0,0] op_sel_hi:[1,1,1]\n" \
  "v_pk_fma_f32 v[42:43], v[96:97], v[74:75], v[42:43] op_sel:[1,0,0] op_sel_hi:[1,1,1]\n" \
  "v_pk_fma_f32 v[52:53], v[98:99], v[76:77], v[52:53] op_sel:[1,0,0] op_sel_hi:[1,1,1]\n" \
  "v_pk_fma_f32 v[62:63], v[98:99], v[78:79], v[62:63] op_sel:[1,0,0] op_sel_hi:[1,1,1]\n" \
  "s_set_gpr_idx_off\n" \
  "s_waitcnt vmcnt(4) lgkmcnt(6)\n" \
  "v_mfma_f32_4x4x4_16b_bf16 v[72:75], v[108:109], v[144:145], 0 cbsz:4 abid:14\n" \
  "v_mfma_f32_4x4x4_16b_bf16 v[76:79], v[108:109], v[146:147], 0 cbsz:4 abid:14\n" \
  "v_readlane_b32 s83, v10, 3\n" \
  "v_cvt_pk_bf16_f32 v81, v80, v81\n" \
  "v_lshlrev_b32_sdwa v80, 1, v81 dst_sel:DWORD dst_unused:UNUSED_PAD src0_sel:DWORD src1_sel:WORD_0\n" \
  "v_lshlrev_b32_sdwa v81, 1, v81 dst_sel:DWORD dst_unused:UNUSED_PAD src0_sel:DWORD src1_sel:WORD_1\n" \
  "s_andn2_b32 s61, s83, 15\n" \
  "buffer_load_dwordx4 v[132:135], v8, %[xs], s61 offen\n" \
  "ds_read_u16_d16_hi v96, v80 offset:%[tb]\n" \
  "ds_read_u16_d16_hi v97, v81 offset:%[tb]\n" \
  "ds_read_b32 v11, v12 offset:32\n" \
  "v_readfirstlane_b32 s66, v111\n" \
  "s_setprio 1\n" \
  "s_add_u32 s67, s60, %[fo]\n" \
  "s_cmp_ge_u32 s67, s66\n" \
  "s_cbranch_scc0 Lf13_%=\n" \
  "s_setprio 2\n" \
  "Lf13_%=:\n" \
  "v_cvt_pk_bf16_f32 v19, v112, v116\n" \
  "s_waitcnt lgkmcnt(3)\n" \
  "s_set_gpr_idx_on s86, gpr_idx(SRC2,DST)\n" \
  "v_pk_fma_f32 v[32:33], v[100:101], v[72:73], v[32:33] op_sel:[0,0,0] op_sel_hi:[0,1,1]\n" \
  "v_pk_fma_f32 v[42:43], v[100:101], v[74:75], v[42:43] op_sel:[0,0,0] op_sel_hi:[0,1,1]\n" \
  "v_pk_fma_f32 v[52:53], v[102:103], v[76:77], v[52:53] op_sel:[0,0,0] op_sel_hi:[0,1,1]\n" \
  "v_pk_fma_f32 v[62:63], v[102:103], v[78:79], v[62:63] op_sel:[0,0,0] op_sel_hi:[0,1,1]\n" \
  "s_set_gpr_idx_off\n" \
  "s_waitcnt vmcnt(4)\n" \
  "v_mfma_f32_4x4x4_16b_bf16 v[112:115], v[106:107], v[24:25], 0\n" \
  "v_mfma_f32_4x4x4_16b_bf16 v[116:119], v[106:107], v[26:27], 0\n" \
  "v_mfma_f32_4x4x4_16b_bf16 v[72:75], v[108:109], v[148:149], 0 cbsz:4 abid:15\n" \
  "v_mfma_f32_4x4x4_16b_bf16 v[76:79], v[108:109], v[150:151], 0 cbsz:4 abid:15\n" \
  "v_readlane_b32 s84, v10, 4\n" \
  "v_cvt_pk_bf16_f32 v85, v84, v85\n" \
  "v_lshlrev_b32_sdwa v84, 1, v85 dst_sel:DWORD dst_unused:UNUSED_PAD src0_sel:DWORD src1_sel:WORD_0\n" \
  "v_lshlrev_b32_sdwa v85, 1, v85 dst_sel:DWORD dst_unused:UNUSED_PAD src0_sel:DWORD src1_sel:WORD_1\n" \
  "s_andn2_b32 s61, s84, 15\n" \
  "buffer_load_dwordx4 v[136:139], v8, %[xs], s61 offen\n" \
  "ds_read_u16_d16_hi v98, v84 offset:%[tb]\n" \
  "ds_read_u16_d16_hi v99, v85 offset:%[tb]\n" \
  "v_cvt_pk_bf16_f32 v109, v112, v116\n" \
  "v_mov_b32 v108, v19\n" \
  "s_set_gpr_idx_on s87, gpr_idx(SRC2,DST)\n" \
  "v_pk_fma_f32 v[32:33], v[100:101], v[72:73], v[32:33] op_sel:[1,0,0] op_sel_hi:[1,1,1]\n" \
  "v_pk_fma_f32 v[42:43], v[100:101], v[74:75], v[42:43] op_sel:[1,0,0] op_sel_hi:[1,1,1]\n" \
  "v_pk_fma_f32 v[52:53], v[102:103], v[76:77], v[52:53] op_sel:[1,0,0] op_sel_hi:[1,1,1]\n" \
  "v_pk_fma_f32 v[62:63], v[102:103], v[78:79], v[62:63] op_sel:[1,0,0] op_sel_hi:[1,1,1]\n" \
  "s_set_gpr_idx_off\n" \
  "s_sub_u32 s60, s60, 1\n" \
  "s_cbranch_scc1 Lend%=\n" \
  "s_waitcnt vmcnt(4)\n" \
  "v_mfma_f32_4x4x4_16b_bf16 v[72:75], v[108:109], v[120:121], 0 cbsz:4\n" \
  "v_mfma_f32_4x4x4_16b_bf16 v[76:79], v[108:109], v[122:123], 0 cbsz:4\n" \
  "v_readlane_b32 s85, v10, 5\n" \
  "v_cvt_pk_bf16_f32 v83, v82, v83\n" \
  "v_lshlrev_b32_sdwa v82, 1, v83 dst_sel:DWORD dst_unused:UNUSED_PAD src0_sel:DWORD src1_sel:WORD_0\n" \
  "v_lshlrev_b32_sdwa v83, 1, v83 dst_sel:DWORD dst_unused:UNUSED_PAD src0_sel:DWORD src1_sel:WORD_1\n" \
  "s_andn2_b32 s61, s85, 15\n" \
  "buffer_load_dwordx4 v[140:143], v8, %[xs], s61 offen\n" \
  "ds_read_u16_d16_hi v100, v82 offset:%[tb]\n" \
  "ds_read_u16_d16_hi v101, v83 offset:%[tb]\n" \
  "s_branch Lbody%=\n" \
  "Lend%=:\n" \
  "s_setprio 3\n" \
  "v_mov_b32 v20, 0\n" \
  "v_mov_b32 v21, %[bit]\n" \
  "v_cvt_pk_bf16_f32 v32, v32, v52\n" \
  "v_cvt_pk_bf16_f32 v33, v33, v53\n" \
  "v_cvt_pk_bf16_f32 v42, v42, v62\n" \
  "v_cvt_pk_bf16_f32 v43, v43, v63\n" \
  "v_cvt_pk_bf16_f32 v34, v34, v54\n" \
  "v_cvt_pk_bf16_f32 v35, v35, v55\n" \
  "v_cvt_pk_bf16_f32 v44, v44, v64\n" \
  "v_cvt_pk_bf16_f32 v45, v45, v65\n" \
  "ds_write2st64_b32 v152, v32, v34 offset0:0 offset1:4\n" \
  "ds_write2st64_b32 v153, v33, v35 offset0:0 offset1:4\n" \
  "ds_write2st64_b32 v154, v42, v44 offset0:0 offset1:4\n" \
  "ds_write2st64_b32 v155, v43, v45 offset0:0 offset1:4\n" \
  "v_cvt_pk_bf16_f32 v36, v36, v56\n" \
  "v_cvt_pk_bf16_f32 v37, v37, v57\n" \
  "v_cvt_pk_bf16_f32 v46, v46, v66\n" \
  "v_cvt_pk_bf16_f32 v47, v47, v67\n" \
  "v_cvt_pk_bf16_f32 v38, v38, v58\n" \
  "v_cvt_pk_bf16_f32 v39, v39, v59\n" \
  "v_cvt_pk_bf16_f32 v48, v48, v68\n" \
  "v_cvt_pk_bf16_f32 v49, v49, v69\n" \
  "ds_write2st64_b32 v152, v36, v38 offset0:8 offset1:12\n" \
  "ds_write2st64_b32 v153, v37, v39 offset0:8 offset1:12\n" \
  "ds_write2st64_b32 v154, v46, v48 offset0:8 offset1:12\n" \
  "ds_write2st64_b32 v155, v47, v49 offset0:8 offset1:12\n" \
  "v_cvt_pk_bf16_f32 v40, v40, v60\n" \
  "v_cvt_pk_bf16_f32 v41, v41, v61\n" \
  "v_cvt_pk_bf16_f32 v50, v50, v70\n" \
  "v_cvt_pk_bf16_f32 v51, v51, v71\n" \
  "ds_write2_b32 v156, v40, v41 offset0:0 offset1:16\n" \
  "ds_write2_b32 v156, v50, v51 offset0:32 offset1:48\n" \
  "s_mov_b64 exec, 1\n" \
  "ds_or_b32 v20, v21 offset:%[rd]\n" \
  "s_endpgm\n" \
  "Lhr%=:\n" \
  "s_mov_b64 exec, vcc\n" \
  "s_sub_u32 s63, s63, 1\n" \
  "s_cbranch_scc0 Lh%=\n" \
  "s_mov_b64 exec, -1\n" \
  "Lover%=:\n" \
  "v_mov_b32 v113, 1\n" \
  "ds_write_b32 v117, v113 offset:%[fl]\n" \
  "s_mov_b32 s62, 0\n" \
  "v_mov_b32 v114, 0\n" \
  "v_mov_b32 v115, 0\n" \
  "s_branch Lld%=\n" \
  "Lkr%=:\n" \
  "buffer_load_dword v84, v76, %[ws], %[rk] offen sc1\n" \
  "buffer_load_dword v85, v77, %[ws], %[rk] offen sc1\n" \
  "buffer_load_dword v86, v78, %[ws], %[rk] offen sc1\n" \
  "s_cmpk_le_u32 s62, 0xc0\n" \
  "s_cbranch_scc1 Lkb%=\n" \
  "buffer_load_dword v87, v79, %[ws], %[rk] offen sc1\n" \
  "Lkb%=:\n" \
  "s_waitcnt vmcnt(0)\n" \
  "s_sub_u32 s63, s63, 1\n" \
  "s_cbranch_scc0 Lk%=\n" \
  "v_mov_b32 v113, 1\n" \
  "ds_write_b32 v117, v113 offset:%[fl]\n" \
  "s_branch Lkc%=\n" \
  "Lc3%=:\n" \
  "v_lshlrev_b32 v91, 1, v87\n" \
  "v_lshlrev_b32 v95, 4, v87\n" \
  "v_lshrrev_b32 v107, 2, v87\n" \
  "v_and_or_b32 v91, v91, v118, %[wb]\n" \
  "v_lshlrev_b32_e64 v103, v95, 4\n" \
  "v_and_b32 v107, 0xfc, v107\n" \
  "s_mov_b64 exec, s[72:73]\n" \
  "ds_add_rtn_u32 v99, v91, v103 offset:%[hi]\n" \
  "ds_add_u32 v107, v116 offset:%[dg]\n" \
  "s_mov_b64 exec, -1\n" \
  "s_branch Lc4%=\n" \
  "Llo%=:\n" \
  "v_mov_b32 v113, 1\n" \
  "ds_write_b32 v117, v113 offset:%[fl]\n" \
  "s_branch Llc%=\n" \
  "Lslow%=:\n" \
  "s_waitcnt vmcnt(0) lgkmcnt(0)\n" \
  "s_barrier\n" \
  "s_branch Lexit%=\n" \
  "Lq3%=:\n" \
  "s_mov_b64 exec, -1\n" \
  "v_and_b32 v103, 0x7fc, v91\n" \
  "ds_read_b32 v79, v91 offset:%[hi]\n" \
  "ds_read_b32 v107, v103 offset:%[to]\n" \
  "s_waitcnt lgkmcnt(0)\n" \
  "v_add3_u32 v91, v79, v107, v99\n" \
  "v_lshrrev_b32 v91, v95, v91\n" \
  "v_bfe_u32 v75, v91, 13, 3\n" \
  "v_and_b32 v91, 0x1ffc, v91\n" \
  "v_lshl_or_b32 v75, v75, 1, v87\n" \
  "s_mov_b64 exec, s[72:73]\n" \
  "ds_write_b32 v91, v75 offset:%[so]\n" \
  "s_branch Lq4%=\n" \
  "Lpr%=:\n" \
  "v_mov_b32 v44, 0\n" \
  "v_mov_b32 v46, 0\n" \
  "s_cmpk_le_u32 s62, 0xc0\n" \
  "s_cbranch_scc1 Lpb%=\n" \
  "buffer_load_dwordx4 v[44:47], v83, %[ws], %[rp] offen sc1\n" \
  "Lpb%=:\n" \
  "buffer_load_dwordx4 v[32:35], v80, %[ws], %[rp] offen sc1\n" \
  "buffer_load_dwordx4 v[36:39], v81, %[ws], %[rp] offen sc1\n" \
  "buffer_load_dwordx4 v[40:43], v82, %[ws], %[rp] offen sc1\n" \
  "s_sub_u32 s63, s63, 1\n" \
  "s_waitcnt vmcnt(0)\n" \
  "s_cbranch_scc0 Lpc%=\n" \
  "s_branch Lpw%=\n" \
  "Lexit%=:\n" \
  "s_mov_b32 %[sl], s61\n"
#define STREAM_CLOBBERS \
  "v10", "v11", "v19", "v21", "v22", "v23", "v24", "v25", "v26", "v27", "v28", "v29", "v30", "v31", "v32", \
  "v33", "v34", "v35", "v36", "v37", "v38", "v39", "v40", "v41", "v42", "v43", "v44", "v45", "v46", "v47", \
  "v48", "v49", "v50", "v51", "v52", "v53", "v54", "v55", "v56", "v57", "v58", "v59", "v60", "v61", "v62", \
  "v63", "v64", "v65", "v66", "v67", "v68", "v69", "v70", "v71", "v72", "v73", "v74", "v75", "v76", "v77", \
  "v78", "v79", "v80", "v81", "v82", "v83", "v84", "v85", "v86", "v87", "v88", "v89", "v90", "v91", "v92", \
  "v93", "v94", "v95", "v96", "v97", "v98", "v99", "v100", "v101", "v102", "v103", "v104", "v105", "v106", \
  "v107", "v108", "v109", "v111", "v112", "v113", "v114", "v115", "v116", "v117", "v118", "v119", "v120", \
  "v121", "v122", "v123", "v124", "v125", "v126", "v127", "v128", "v129", "v130", "v131", "v132", "v133", \
  "v134", "v135", "s60", "s61", "s62", "s63", "s64", "s65", "s66", "s67", "s68", "s69", "s70", "s71", "s72", \
  "s73", "s74", "s75", "s76", "s77", "s78", "s79", "s80", "s81", "s82", "s83", "s84", "s85", "s86", "s87", \
  "scc", "vcc", "memory"
#define RKL 3
#define MIX_ASM \
  "v_mov_b32 v12, 0\n" \
  "s_movk_i32 s60, 0x3ff\n" \
  "Lz%=:\n" \
  "s_sleep %[sl]\n" \
  "ds_read_b32 v13, v1\n" \
  "ds_read_b32 v14, v12 offset:%[rd]\n" \
  "s_waitcnt lgkmcnt(0)\n" \
  "v_readfirstlane_b32 s61, v13\n" \
  "v_readfirstlane_b32 s62, v14\n" \
  "s_cmp_gt_u32 s61, 4\n" \
  "s_cselect_b32 s61, s62, 1\n" \
  "s_cmp_eq_u32 s61, 0\n" \
  "s_cbranch_scc1 Lz%=\n" \
  "s_setprio 1\n" \
  "Lp%=:\n" \
  "ds_read_b32 v13, v12 offset:%[rd]\n" \
  "s_waitcnt lgkmcnt(0)\n" \
  "v_readfirstlane_b32 s61, v13\n" \
  "s_andn2_b32 s62, 15, s61\n" \
  "s_cselect_b32 s62, 0, 0x100\n" \
  "s_andn2_b32 s63, 0xf0, s61\n" \
  "s_cselect_b32 s63, 0, 0x200\n" \
  "s_or_b32 s61, s61, s62\n" \
  "s_or_b32 s61, s61, s63\n" \
  "s_and_b32 s61, s61, s60\n" \
  "s_cbranch_scc1 Lgo%=\n" \
  "s_sleep 1\n" \
  "s_branch Lp%=\n" \
  "Lgo%=:\n" \
  "s_andn2_b32 s60, s60, s61\n" \
  "s_ff1_i32_b32 s62, s61\n" \
  "s_bitset0_b32 s61, s62\n" \
  "s_lshl_b32 s63, s62, 12\n" \
  "s_cmp_ge_u32 s62, 8\n" \
  "s_cselect_b32 s64, %[r5], 0\n" \
  "s_add_u32 s63, s63, s64\n" \
  "v_add_u32 v14, s63, v0\n" \
  "ds_read_b128 v[64:67], v14 offset:0\n" \
  "ds_read_b128 v[68:71], v14 offset:256\n" \
  "ds_read_b128 v[72:75], v14 offset:512\n" \
  "ds_read_b128 v[76:79], v14 offset:768\n" \
  "s_set_gpr_idx_on s62, gpr_idx(SRC0)\n" \
  "v_mov_b32 v16, v2\n" \
  "s_set_gpr_idx_off\n" \
  "s_cmp_eq_u32 s61, 0\n" \
  "s_cbranch_scc1 Ls%=\n" \
  "s_ff1_i32_b32 s62, s61\n" \
  "s_bitset0_b32 s61, s62\n" \
  "s_lshl_b32 s63, s62, 12\n" \
  "s_cmp_ge_u32 s62, 8\n" \
  "s_cselect_b32 s64, %[r5], 0\n" \
  "s_add_u32 s63, s63, s64\n" \
  "v_add_u32 v14, s63, v0\n" \
  "ds_read_b128 v[80:83], v14 offset:0\n" \
  "ds_read_b128 v[84:87], v14 offset:256\n" \
  "ds_read_b128 v[88:91], v14 offset:512\n" \
  "ds_read_b128 v[92:95], v14 offset:768\n" \
  "s_set_gpr_idx_on s62, gpr_idx(SRC0)\n" \
  "v_mov_b32 v17, v2\n" \
  "s_set_gpr_idx_off\n" \
  "s_waitcnt lgkmcnt(4)\n" \
  "v_mfma_f32_16x16x32_bf16 v[96:99], v[64:67], v[32:35], 0\n" \
  "v_mfma_f32_16x16x32_bf16 v[100:103], v[64:67], v[36:39], 0\n" \
  "v_mfma_f32_16x16x32_bf16 v[96:99], v[68:71], v[40:43], v[96:99]\n" \
  "v_mfma_f32_16x16x32_bf16 v[100:103], v[68:71], v[44:47], v[100:103]\n" \
  "v_mfma_f32_16x16x32_bf16 v[96:99], v[72:75], v[48:51], v[96:99]\n" \
  "v_mfma_f32_16x16x32_bf16 v[100:103], v[72:75], v[52:55], v[100:103]\n" \
  "v_mfma_f32_16x16x32_bf16 v[96:99], v[76:79], v[56:59], v[96:99]\n" \
  "v_mfma_f32_16x16x32_bf16 v[100:103], v[76:79], v[60:63], v[100:103]\n" \
  "Lb1_%=:\n" \
  "s_cmp_eq_u32 s61, 0\n" \
  "s_cbranch_scc1 Ll1_%=\n" \
  "s_ff1_i32_b32 s62, s61\n" \
  "s_bitset0_b32 s61, s62\n" \
  "s_lshl_b32 s63, s62, 12\n" \
  "s_cmp_ge_u32 s62, 8\n" \
  "s_cselect_b32 s64, %[r5], 0\n" \
  "s_add_u32 s63, s63, s64\n" \
  "v_add_u32 v14, s63, v0\n" \
  "ds_read_b128 v[64:67], v14 offset:0\n" \
  "ds_read_b128 v[68:71], v14 offset:256\n" \
  "ds_read_b128 v[72:75], v14 offset:512\n" \
  "ds_read_b128 v[76:79], v14 offset:768\n" \
  "s_waitcnt lgkmcnt(4)\n" \
  "v_mfma_f32_16x16x32_bf16 v[104:107], v[80:83], v[32:35], 0\n" \
  "v_mfma_f32_16x16x32_bf16 v[108:111], v[80:83], v[36:39], 0\n" \
  "v_cvt_pk_bf16_f32 v96, v96, v97\n" \
  "v_cvt_pk_bf16_f32 v97, v98, v99\n" \
  "v_mfma_f32_16x16x32_bf16 v[104:107], v[84:87], v[40:43], v[104:107]\n" \
  "v_cvt_pk_bf16_f32 v98, v100, v101\n" \
  "v_cvt_pk_bf16_f32 v99, v102, v103\n" \
  "v_mfma_f32_16x16x32_bf16 v[108:111], v[84:87], v[44:47], v[108:111]\n" \
  "buffer_store_dwordx4 v[96:99], v16, %[os], 0 offen sc1\n" \
  "v_mfma_f32_16x16x32_bf16 v[104:107], v[88:91], v[48:51], v[104:107]\n" \
  "v_mfma_f32_16x16x32_bf16 v[108:111], v[88:91], v[52:55], v[108:111]\n" \
  "s_set_gpr_idx_on s62, gpr_idx(SRC0)\n" \
  "v_mov_b32 v16, v2\n" \
  "s_set_gpr_idx_off\n" \
  "v_mfma_f32_16x16x32_bf16 v[104:107], v[92:95], v[56:59], v[104:107]\n" \
  "v_mfma_f32_16x16x32_bf16 v[108:111], v[92:95], v[60:63], v[108:111]\n" \
  "Lb0_%=:\n" \
  "s_cmp_eq_u32 s61, 0\n" \
  "s_cbranch_scc1 Ll0_%=\n" \
  "s_ff1_i32_b32 s62, s61\n" \
  "s_bitset0_b32 s61, s62\n" \
  "s_lshl_b32 s63, s62, 12\n" \
  "s_cmp_ge_u32 s62, 8\n" \
  "s_cselect_b32 s64, %[r5], 0\n" \
  "s_add_u32 s63, s63, s64\n" \
  "v_add_u32 v14, s63, v0\n" \
  "ds_read_b128 v[80:83], v14 offset:0\n" \
  "ds_read_b128 v[84:87], v14 offset:256\n" \
  "ds_read_b128 v[88:91], v14 offset:512\n" \
  "ds_read_b128 v[92:95], v14 offset:768\n" \
  "s_waitcnt lgkmcnt(4)\n" \
  "v_mfma_f32_16x16x32_bf16 v[96:99], v[64:67], v[32:35], 0\n" \
  "v_mfma_f32_16x16x32_bf16 v[100:103], v[64:67], v[36:39], 0\n" \
  "v_cvt_pk_bf16_f32 v104, v104, v105\n" \
  "v_cvt_pk_bf16_f32 v105, v106, v107\n" \
  "v_mfma_f32_16x16x32_bf16 v[96:99], v[68:71], v[40:43], v[96:99]\n" \
  "v_cvt_pk_bf16_f32 v106, v108, v109\n" \
  "v_cvt_pk_bf16_f32 v107, v110, v111\n" \
  "v_mfma_f32_16x16x32_bf16 v[100:103], v[68:71], v[44:47], v[100:103]\n" \
  "buffer_store_dwordx4 v[104:107], v17, %[os], 0 offen sc1\n" \
  "v_mfma_f32_16x16x32_bf16 v[96:99], v[72:75], v[48:51], v[96:99]\n" \
  "v_mfma_f32_16x16x32_bf16 v[100:103], v[72:75], v[52:55], v[100:103]\n" \
  "s_set_gpr_idx_on s62, gpr_idx(SRC0)\n" \
  "v_mov_b32 v17, v2\n" \
  "s_set_gpr_idx_off\n" \
  "v_mfma_f32_16x16x32_bf16 v[96:99], v[76:79], v[56:59], v[96:99]\n" \
  "v_mfma_f32_16x16x32_bf16 v[100:103], v[76:79], v[60:63], v[100:103]\n" \
  "s_branch Lb1_%=\n" \
  "Ll1_%=:\n" \
  "s_waitcnt lgkmcnt(0)\n" \
  "v_mfma_f32_16x16x32_bf16 v[104:107], v[80:83], v[32:35], 0\n" \
  "v_mfma_f32_16x16x32_bf16 v[108:111], v[80:83], v[36:39], 0\n" \
  "v_mfma_f32_16x16x32_bf16 v[104:107], v[84:87], v[40:43], v[104:107]\n" \
  "v_mfma_f32_16x16x32_bf16 v[108:111], v[84:87], v[44:47], v[108:111]\n" \
  "v_mfma_f32_16x16x32_bf16 v[104:107], v[88:91], v[48:51], v[104:107]\n" \
  "v_cvt_pk_bf16_f32 v96, v96, v97\n" \
  "v_cvt_pk_bf16_f32 v97, v98, v99\n" \
  "v_mfma_f32_16x16x32_bf16 v[108:111], v[88:91], v[52:55], v[108:111]\n" \
  "v_cvt_pk_bf16_f32 v98, v100, v101\n" \
  "v_cvt_pk_bf16_f32 v99, v102, v103\n" \
  "v_mfma_f32_16x16x32_bf16 v[104:107], v[92:95], v[56:59], v[104:107]\n" \
  "buffer_store_dwordx4 v[96:99], v16, %[os], 0 offen sc1\n" \
  "v_mfma_f32_16x16x32_bf16 v[108:111], v[92:95], v[60:63], v[108:111]\n" \
  "s_cmp_lg_u32 s60, 0\n" \
  "s_nop 5\n" \
  "v_cvt_pk_bf16_f32 v104, v104, v105\n" \
  "v_cvt_pk_bf16_f32 v105, v106, v107\n" \
  "v_cvt_pk_bf16_f32 v106, v108, v109\n" \
  "v_cvt_pk_bf16_f32 v107, v110, v111\n" \
  "buffer_store_dwordx4 v[104:107], v17, %[os], 0 offen sc1\n" \
  "s_cbranch_scc1 Lp%=\n" \
  "s_branch Le%=\n" \
  "Ll0_%=:\n" \
  "s_waitcnt lgkmcnt(0)\n" \
  "v_mfma_f32_16x16x32_bf16 v[96:99], v[64:67], v[32:35], 0\n" \
  "v_mfma_f32_16x16x32_bf16 v[100:103], v[64:67], v[36:39], 0\n" \
  "v_mfma_f32_16x16x32_bf16 v[96:99], v[68:71], v[40:43], v[96:99]\n" \
  "v_mfma_f32_16x16x32_bf16 v[100:103], v[68:71], v[44:47], v[100:103]\n" \
  "v_mfma_f32_16x16x32_bf16 v[96:99], v[72:75], v[48:51], v[96:99]\n" \
  "v_cvt_pk_bf16_f32 v104, v104, v105\n" \
  "v_cvt_pk_bf16_f32 v105, v106, v107\n" \
  "v_mfma_f32_16x16x32_bf16 v[100:103], v[72:75], v[52:55], v[100:103]\n" \
  "v_cvt_pk_bf16_f32 v106, v108, v109\n" \
  "v_cvt_pk_bf16_f32 v107, v110, v111\n" \
  "v_mfma_f32_16x16x32_bf16 v[96:99], v[76:79], v[56:59], v[96:99]\n" \
  "buffer_store_dwordx4 v[104:107], v17, %[os], 0 offen sc1\n" \
  "v_mfma_f32_16x16x32_bf16 v[100:103], v[76:79], v[60:63], v[100:103]\n" \
  "s_cmp_lg_u32 s60, 0\n" \
  "s_nop 5\n" \
  "v_cvt_pk_bf16_f32 v96, v96, v97\n" \
  "v_cvt_pk_bf16_f32 v97, v98, v99\n" \
  "v_cvt_pk_bf16_f32 v98, v100, v101\n" \
  "v_cvt_pk_bf16_f32 v99, v102, v103\n" \
  "buffer_store_dwordx4 v[96:99], v16, %[os], 0 offen sc1\n" \
  "s_cbranch_scc1 Lp%=\n" \
  "s_branch Le%=\n" \
  "Ls%=:\n" \
  "s_waitcnt lgkmcnt(0)\n" \
  "v_mfma_f32_16x16x32_bf16 v[96:99], v[64:67], v[32:35], 0\n" \
  "v_mfma_f32_16x16x32_bf16 v[100:103], v[64:67], v[36:39], 0\n" \
  "v_mfma_f32_16x16x32_bf16 v[96:99], v[68:71], v[40:43], v[96:99]\n" \
  "v_mfma_f32_16x16x32_bf16 v[100:103], v[68:71], v[44:47], v[100:103]\n" \
  "v_mfma_f32_16x16x32_bf16 v[96:99], v[72:75], v[48:51], v[96:99]\n" \
  "v_mfma_f32_16x16x32_bf16 v[100:103], v[72:75], v[52:55], v[100:103]\n" \
  "v_mfma_f32_16x16x32_bf16 v[96:99], v[76:79], v[56:59], v[96:99]\n" \
  "v_mfma_f32_16x16x32_bf16 v[100:103], v[76:79], v[60:63], v[100:103]\n" \
  "s_cmp_lg_u32 s60, 0\n" \
  "s_nop 5\n" \
  "v_cvt_pk_bf16_f32 v96, v96, v97\n" \
  "v_cvt_pk_bf16_f32 v97, v98, v99\n" \
  "v_cvt_pk_bf16_f32 v98, v100, v101\n" \
  "v_cvt_pk_bf16_f32 v99, v102, v103\n" \
  "buffer_store_dwordx4 v[96:99], v16, %[os], 0 offen sc1\n" \
  "s_cbranch_scc1 Lp%=\n" \
  "Le%=:\n"
#define MIX_CLOBBERS \
  "v16", "v17", "v64", "v65", "v66", "v67", "v68", "v69", "v70", "v71", "v72", "v73", "v74", "v75", "v76", \
  "v77", "v78", "v79", "v80", "v81", "v82", "v83", "v84", "v85", "v86", "v87", "v88", "v89", "v90", "v91", \
  "v92", "v93", "v94", "v95", "v96", "v97", "v98", "v99", "v100", "v101", "v102", "v103", "v104", "v105", \
  "v106", "v107", "v108", "v109", "v110", "v111", "s60", "s61", "s62", "s63", "s64", "scc", "vcc", "memory"
#define PART_ASM \
  "s_load_dwordx16 s[8:23], %[kp], 0\n" \
  "s_load_dwordx8 s[24:31], %[kp], 0x40\n" \
  "s_load_dwordx4 s[32:35], %[kp], 0x60\n" \
  "v_readfirstlane_b32 s36, %[tid]\n" \
  "v_lshlrev_b32 v90, 3, %[tid]\n" \
  "v_mov_b64 v[92:93], 0\n" \
  "s_and_b32 s37, %[bid], 7\n" \
  "s_lshr_b32 s38, %[bid], 3\n" \
  "s_lshr_b32 s36, s36, 6\n" \
  "ds_write_b64 v90, v[92:93] offset:%[sg]\n" \
  "s_cmp_lg_u32 s36, %[pw]\n" \
  "s_cbranch_scc1 1f\n" \
  "s_getpc_b64 s[44:45]\n" \
  ".Ltfn_p0:\n" \
  "s_max_i32 s46, 0, .Ltfn_p1-.Ltfn_p0\n" \
  "s_mov_b32 m0, %[sn]\n" \
  "s_mov_b32 s47, 0x20000\n" \
  "v_lshlrev_b32 v91, 12, %[tid]\n" \
  "s_lshl_b32 s39, s38, 7\n" \
  "v_and_b32 v91, 0x3f000, v91\n" \
  "buffer_load_dword v91, s[44:47], s39 offen lds\n" \
  "1:\n" \
  "s_lshl_b32 s37, s37, 5\n" \
  "s_add_u32 s37, s37, s38\n" \
  "v_lshlrev_b32 v91, 4, %[tid]\n" \
  "s_waitcnt lgkmcnt(0)\n" \
  "s_mul_i32 s38, s37, s14\n" \
  "s_mov_b32 s40, s10\n" \
  "s_add_i32 s39, s38, s14\n" \
  "s_lshl_b32 s44, s38, 2\n" \
  "s_min_i32 s39, s39, s10\n" \
  "s_add_i32 s45, s44, %[h4]\n" \
  "s_lshl_b32 s10, s39, 2\n" \
  "s_lshl_b32 s14, s39, 2\n" \
  "s_cmp_ge_u32 s36, %[pw]\n" \
  "s_cbranch_scc1 2f\n" \
  "buffer_load_dwordx2 v[40:41], v90, s[8:11], s44 offen\n" \
  "buffer_load_dwordx2 v[42:43], v90, s[8:11], s45 offen\n" \
  "buffer_load_dwordx2 v[44:45], v90, s[12:15], s44 offen\n" \
  "buffer_load_dwordx2 v[46:47], v90, s[12:15], s45 offen\n" \
  "2:\n" \
  "s_mov_b32 s41, s18\n" \
  "s_mov_b32 s42, s22\n" \
  "s_lshl_b32 s18, s39, 3\n" \
  "s_lshl_b32 s22, s39, 3\n" \
  "s_lshl_b32 s44, s44, 1\n" \
  "s_lshl_b32 s45, s45, 1\n" \
  "s_cmp_ge_u32 s36, %[pw]\n" \
  "s_barrier\n" \
  "s_cbranch_scc0 3f\n" \
  "s_cmp_lg_u32 s36, %[hw]\n" \
  "s_cbranch_scc1 4f\n" \
  "s_load_dwordx2 s[46:47], s[26:27], 0\n" \
  "s_branch Lpx%=\n" \
  "4:\n" \
  "s_barrier\n" \
  "s_branch Lpx%=\n" \
  "3:\n" \
  "s_setprio 3\n" \
  "buffer_load_dwordx4 v[100:103], v91, s[16:19], s44 offen\n" \
  "buffer_load_dwordx4 v[104:107], v91, s[16:19], s45 offen\n" \
  "buffer_load_dwordx4 v[108:111], v91, s[20:23], s44 offen\n" \
  "buffer_load_dwordx4 v[112:115], v91, s[20:23], s45 offen\n" \
  "s_sub_i32 s56, s39, s38\n" \
  "v_lshlrev_b32 v72, 1, %[tid]\n" \
  "s_lshl_b32 s57, s36, 9\n" \
  "v_cmp_gt_i32_e64 s[48:49], s56, v72\n" \
  "s_sub_i32 s58, s56, 1\n" \
  "v_cmp_gt_i32_e64 s[50:51], s58, v72\n" \
  "s_sub_i32 s58, s56, %[h1]\n" \
  "v_cmp_gt_i32_e64 s[52:53], s58, v72\n" \
  "s_sub_i32 s58, s56, %[h1]+1\n" \
  "v_cmp_gt_i32_e64 s[54:55], s58, v72\n" \
  "v_and_b32 v88, 63, %[tid]\n" \
  "v_mov_b32 v95, 0x1fc\n" \
  "v_mov_b64 v[76:77], 0\n" \
  "v_lshlrev_b32 v88, 2, v88\n" \
  "v_mov_b32 v96, 0\n" \
  "v_mov_b32 v97, 1\n" \
  "v_add_u32 v89, s57, v88\n" \
  "v_add_u32 v88, %[ph], v88\n" \
  "v_add_u32 v89, %[pb], v89\n" \
  "s_lshl_b32 s58, s36, 1\n" \
  "s_mov_b32 s59, 0x38000000\n" \
  "s_waitcnt vmcnt(6)\n" \
  "v_mul_u32_u24 v48, s42, v40\n" \
  "v_mul_u32_u24 v49, s42, v41\n" \
  "v_mul_u32_u24 v50, s42, v42\n" \
  "v_mul_u32_u24 v51, s42, v43\n" \
  "v_lshrrev_b32 v52, 22, v48\n" \
  "v_lshrrev_b32 v53, 22, v49\n" \
  "v_lshrrev_b32 v54, 22, v50\n" \
  "v_lshrrev_b32 v55, 22, v51\n" \
  "v_lshrrev_b32 v56, 21, v48\n" \
  "v_lshrrev_b32 v57, 21, v49\n" \
  "v_lshrrev_b32 v58, 21, v50\n" \
  "v_lshrrev_b32 v59, 21, v51\n" \
  "v_lshlrev_b32 v60, 4, v52\n" \
  "v_lshlrev_b32 v61, 4, v53\n" \
  "v_lshlrev_b32 v62, 4, v54\n" \
  "v_lshlrev_b32 v63, 4, v55\n" \
  "v_and_or_b32 v56, v56, v95, s57\n" \
  "v_and_or_b32 v57, v57, v95, s57\n" \
  "v_and_or_b32 v58, v58, v95, s57\n" \
  "v_and_or_b32 v59, v59, v95, s57\n" \
  "v_lshlrev_b32 v72, v60, v97\n" \
  "v_lshlrev_b32 v73, v61, v97\n" \
  "v_lshlrev_b32 v74, v62, v97\n" \
  "v_lshlrev_b32 v75, v63, v97\n" \
  "s_mov_b64 exec, s[48:49]\n" \
  "ds_add_rtn_u32 v64, v56, v72 offset:%[ph]\n" \
  "s_mov_b64 exec, s[50:51]\n" \
  "ds_add_rtn_u32 v65, v57, v73 offset:%[ph]\n" \
  "s_mov_b64 exec, s[52:53]\n" \
  "ds_add_rtn_u32 v66, v58, v74 offset:%[ph]\n" \
  "s_mov_b64 exec, s[54:55]\n" \
  "ds_add_rtn_u32 v67, v59, v75 offset:%[ph]\n" \
  "s_mov_b64 exec, -1\n" \
  "v_and_b32 v48, s59, v48\n" \
  "v_and_b32 v49, s59, v49\n" \
  "v_and_b32 v50, s59, v50\n" \
  "v_and_b32 v51, s59, v51\n" \
  "v_and_b32 v40, 63, v40\n" \
  "v_and_b32 v41, 63, v41\n" \
  "v_and_b32 v42, 63, v42\n" \
  "v_and_b32 v43, 63, v43\n" \
  "s_waitcnt vmcnt(4)\n" \
  "v_mul_u32_u24 v68, s42, v44\n" \
  "v_mul_u32_u24 v69, s42, v45\n" \
  "v_mul_u32_u24 v70, s42, v46\n" \
  "v_mul_u32_u24 v71, s42, v47\n" \
  "v_lshl_or_b32 v40, v44, 6, v40\n" \
  "v_lshl_or_b32 v41, v45, 6, v41\n" \
  "v_lshl_or_b32 v42, v46, 6, v42\n" \
  "v_lshl_or_b32 v43, v47, 6, v43\n" \
  "v_sub_u32 v68, v68, v48\n" \
  "v_sub_u32 v69, v69, v49\n" \
  "v_sub_u32 v70, v70, v50\n" \
  "v_sub_u32 v71, v71, v51\n" \
  "v_lshl_add_u32 v56, v52, 1, s57\n" \
  "v_lshl_add_u32 v57, v53, 1, s57\n" \
  "v_lshl_add_u32 v58, v54, 1, s57\n" \
  "v_lshl_add_u32 v59, v55, 1, s57\n" \
  "v_bfe_u32 v68, v68, 26, 4\n" \
  "v_bfe_u32 v69, v69, 26, 4\n" \
  "v_bfe_u32 v70, v70, 26, 4\n" \
  "v_bfe_u32 v71, v71, 26, 4\n" \
  "v_lshlrev_b32 v52, 2, v52\n" \
  "v_lshlrev_b32 v53, 2, v53\n" \
  "v_lshlrev_b32 v54, 2, v54\n" \
  "v_lshlrev_b32 v55, 2, v55\n" \
  "v_lshl_or_b32 v68, v40, 4, v68\n" \
  "v_lshl_or_b32 v69, v41, 4, v69\n" \
  "v_lshl_or_b32 v70, v42, 4, v70\n" \
  "v_lshl_or_b32 v71, v43, 4, v71\n" \
  "s_mov_b64 exec, s[48:49]\n" \
  "ds_add_u32 v52, v68 offset:%[sg]\n" \
  "s_mov_b64 exec, s[50:51]\n" \
  "ds_add_u32 v53, v69 offset:%[sg]\n" \
  "s_mov_b64 exec, s[52:53]\n" \
  "ds_add_u32 v54, v70 offset:%[sg]\n" \
  "s_mov_b64 exec, s[54:55]\n" \
  "ds_add_u32 v55, v71 offset:%[sg]\n" \
  "s_mov_b64 exec, -1\n" \
  "s_waitcnt lgkmcnt(0)\n" \
  "s_barrier\n" \
  "ds_read2st64_b32 v[78:79], v88 offset0:0 offset1:1\n" \
  "ds_read2st64_b32 v[80:81], v88 offset0:2 offset1:3\n" \
  "ds_read2st64_b32 v[82:83], v88 offset0:4 offset1:5\n" \
  "ds_read2st64_b32 v[84:85], v88 offset0:6 offset1:7\n" \
  "ds_read2st64_b32 v[86:87], v88 offset0:8 offset1:9\n" \
  "ds_read_b32 v94, v96 offset:%[tg]\n" \
  "v_bfe_u32 v64, v64, v60, 16\n" \
  "v_bfe_u32 v65, v65, v61, 16\n" \
  "v_bfe_u32 v66, v66, v62, 16\n" \
  "v_bfe_u32 v67, v67, v63, 16\n" \
  "s_mov_b32 s56, 0x05040100\n" \
  "s_mov_b32 s59, 0x07060302\n" \
  "s_waitcnt lgkmcnt(4)\n" \
  "v_add_u32 v80, v80, v78\n" \
  "v_add_u32 v81, v81, v79\n" \
  "s_waitcnt lgkmcnt(3)\n" \
  "v_add_u32 v82, v82, v80\n" \
  "v_add_u32 v83, v83, v81\n" \
  "s_waitcnt lgkmcnt(2)\n" \
  "v_add_u32 v84, v84, v82\n" \
  "v_add_u32 v85, v85, v83\n" \
  "s_waitcnt lgkmcnt(1)\n" \
  "v_add_u32 v86, v86, v84\n" \
  "v_add_u32 v87, v87, v85\n" \
  "v_perm_b32 v72, v87, v86, s56\n" \
  "v_perm_b32 v73, v87, v86, s59\n" \
  "v_add_u32 v72, v72, v73\n" \
  "s_set_gpr_idx_on s58, gpr_idx(SRC0)\n" \
  "v_mov_b32 v74, v76\n" \
  "v_mov_b32 v75, v77\n" \
  "s_set_gpr_idx_off\n" \
  "s_mov_b32 s56, 0x10001\n" \
  "v_add_u32_dpp v73, v72, v72 row_shr:1 row_mask:0xf bank_mask:0xf bound_ctrl:1\n" \
  "v_lshl_add_u32 v74, v86, 16, v74\n" \
  "v_lshl_add_u32 v75, v87, 16, v75\n" \
  "v_add_u32_dpp v73, v73, v73 row_shr:2 row_mask:0xf bank_mask:0xf bound_ctrl:1\n" \
  "s_waitcnt lgkmcnt(0)\n" \
  "v_lshl_or_b32 v68, v94, 24, v68\n" \
  "v_add_u32_dpp v73, v73, v73 row_shr:4 row_mask:0xf bank_mask:0xf bound_ctrl:1\n" \
  "v_lshl_or_b32 v69, v94, 24, v69\n" \
  "v_lshl_or_b32 v70, v94, 24, v70\n" \
  "v_add_u32_dpp v73, v73, v73 row_shr:8 row_mask:0xf bank_mask:0xf bound_ctrl:1\n" \
  "v_lshl_or_b32 v71, v94, 24, v71\n" \
  "s_nop 0\n" \
  "v_add_u32_dpp v73, v73, v73 row_bcast:15 row_mask:0xa bank_mask:0xf\n" \
  "s_nop 1\n" \
  "v_add_u32_dpp v73, v73, v73 row_bcast:31 row_mask:0xc bank_mask:0xf\n" \
  "v_sub_u32 v72, v73, v72\n" \
  "v_readlane_b32 s59, v73, 63\n" \
  "v_and_b32 v73, 0xffff, v72\n" \
  "v_lshrrev_b32 v72, 16, v72\n" \
  "s_and_b32 s59, s59, 0xffff\n" \
  "v_mad_u32_u24 v74, v73, s56, v74\n" \
  "v_add_u32 v72, s59, v72\n" \
  "v_mad_u32_u24 v75, v72, s56, v75\n" \
  "ds_write2st64_b32 v89, v74, v75 offset1:1\n" \
  "ds_read_u16 v48, v56 offset:%[pb]\n" \
  "ds_read_u16 v49, v57 offset:%[pb]\n" \
  "ds_read_u16 v50, v58 offset:%[pb]\n" \
  "ds_read_u16 v51, v59 offset:%[pb]\n" \
  "s_mov_b32 s56, %[sp]\n" \
  "s_waitcnt lgkmcnt(0)\n" \
  "v_add_lshl_u32 v48, v48, v64, 2\n" \
  "v_add_lshl_u32 v49, v49, v65, 2\n" \
  "v_add_lshl_u32 v50, v50, v66, 2\n" \
  "v_add_lshl_u32 v51, v51, v67, 2\n" \
  "v_lshl_add_u32 v56, v48, 2, s56\n" \
  "v_lshl_add_u32 v57, v49, 2, s56\n" \
  "v_lshl_add_u32 v58, v50, 2, s56\n" \
  "v_lshl_add_u32 v59, v51, 2, s56\n" \
  "s_mov_b64 exec, s[48:49]\n" \
  "ds_write_b32 v48, v68 offset:%[sk]\n" \
  "s_mov_b64 exec, s[50:51]\n" \
  "ds_write_b32 v49, v69 offset:%[sk]\n" \
  "s_mov_b64 exec, s[52:53]\n" \
  "ds_write_b32 v50, v70 offset:%[sk]\n" \
  "s_mov_b64 exec, s[54:55]\n" \
  "ds_write_b32 v51, v71 offset:%[sk]\n" \
  "s_mov_b64 exec, -1\n" \
  "s_waitcnt vmcnt(0)\n" \
  "v_add_u32 v100, v100, v94\n" \
  "v_add_u32 v102, v102, v94\n" \
  "v_add_u32 v104, v104, v94\n" \
  "v_add_u32 v106, v106, v94\n" \
  "v_add_u32 v108, v108, v94\n" \
  "v_add_u32 v110, v110, v94\n" \
  "v_add_u32 v112, v112, v94\n" \
  "v_add_u32 v114, v114, v94\n" \
  "v_add3_u32 v72, v100, v101, v108\n" \
  "v_add3_u32 v73, v102, v103, v110\n" \
  "v_add3_u32 v74, v104, v105, v112\n" \
  "v_add3_u32 v75, v106, v107, v114\n" \
  "v_add_u32 v72, v72, v109\n" \
  "v_add_u32 v73, v73, v111\n" \
  "v_add_u32 v74, v74, v113\n" \
  "v_add_u32 v75, v75, v115\n" \
  "s_mov_b64 exec, s[48:49]\n" \
  "ds_add_u32 v52, v72 offset:%[sg]+1024\n" \
  "s_mov_b64 exec, s[50:51]\n" \
  "ds_add_u32 v53, v73 offset:%[sg]+1024\n" \
  "s_mov_b64 exec, s[52:53]\n" \
  "ds_add_u32 v54, v74 offset:%[sg]+1024\n" \
  "s_mov_b64 exec, s[54:55]\n" \
  "ds_add_u32 v55, v75 offset:%[sg]+1024\n" \
  "s_mov_b64 exec, 1\n" \
  "ds_add_u32 v96, v97 offset:%[go]\n" \
  "s_mov_b64 exec, -1\n" \
  "s_mov_b64 exec, s[48:49]\n" \
  "ds_write2_b64 v56, v[100:101], v[108:109] offset1:1\n" \
  "s_mov_b64 exec, s[50:51]\n" \
  "ds_write2_b64 v57, v[102:103], v[110:111] offset1:1\n" \
  "s_mov_b64 exec, s[52:53]\n" \
  "ds_write2_b64 v58, v[104:105], v[112:113] offset1:1\n" \
  "s_mov_b64 exec, s[54:55]\n" \
  "ds_write2_b64 v59, v[106:107], v[114:115] offset1:1\n" \
  "s_mov_b64 exec, -1\n" \
  "Lpx%=:\n"
#define PART_CLOBBERS \
  "v40", "v41", "v42", "v43", "v44", "v45", "v46", "v47", "v48", "v49", "v50", "v51", "v52", "v53", "v54", \
  "v55", "v56", "v57", "v58", "v59", "v60", "v61", "v62", "v63", "v64", "v65", "v66", "v67", "v68", "v69", \
  "v70", "v71", "v72", "v73", "v74", "v75", "v76", "v77", "v78", "v79", "v80", "v81", "v82", "v83", "v84", \
  "v85", "v86", "v87", "v88", "v89", "v90", "v91", "v92", "v93", "v94", "v95", "v96", "v97", "v98", "v99", \
  "v100", "v101", "v102", "v103", "v104", "v105", "v106", "v107", "v108", "v109", "v110", "v111", "v112", \
  "v113", "v114", "v115", "s44", "s45", "s48", "s49", "s50", "s51", "s52", "s53", "s54", "s55", "s56", "s57", \
  "s58", "s59", "scc", "vcc", "memory"


namespace {

constexpr int C_ = 128;
constexpr int MW = 12;
constexpr int MT = MW * 64;
constexpr int SW = 8;
constexpr int NS = 5;
constexpr int NPB = SW * NS;
constexpr int NPASS = 16;
constexpr int HS = 64 * NPASS / 2;
constexpr int NBMAX = 256;
constexpr int PW = 5;
constexpr int PT = PW * 64;
constexpr int PK = 4;
constexpr int CH = PT * PK;
constexpr int MK = 4;
constexpr int CAPW = 256;
constexpr int HWV = 10;
constexpr int PFN = 5;
constexpr int WA = 4;
constexpr int FO1 = 2;
constexpr int WSL = 6;
constexpr int TB3 = 2;
constexpr int HSL = 24;
constexpr unsigned WS_EPOCH = 0;
constexpr unsigned WS_OFF = 4096;
constexpr unsigned WS_RECK = WS_OFF + NBMAX * NBMAX * 16;
constexpr unsigned WS_RECP = WS_RECK + NBMAX * CH * 4;
constexpr unsigned WS_END = WS_RECP + NBMAX * CH * 16;

__device__ __forceinline__ float bf2f(unsigned short v) { return __uint_as_float(((unsigned)v) << 16); }
__device__ __forceinline__ unsigned short f2bf(float f) {
  __hip_bfloat16 b = __float2bfloat16(f);
  return *reinterpret_cast<unsigned short*>(&b);
}
__device__ __forceinline__ unsigned pk2bf(float lo, float hi) {
  return __builtin_bit_cast(unsigned, __builtin_convertvector(f32x2{lo, hi}, bf16x2));
}
__device__ __forceinline__ unsigned wave_incl_scan(unsigned x) {
  x += __builtin_amdgcn_update_dpp(0u, x, 0x111, 0xf, 0xf, false);
  x += __builtin_amdgcn_update_dpp(0u, x, 0x112, 0xf, 0xf, false);
  x += __builtin_amdgcn_update_dpp(0u, x, 0x114, 0xf, 0xf, false);
  x += __builtin_amdgcn_update_dpp(0u, x, 0x118, 0xf, 0xf, false);
  x += __builtin_amdgcn_update_dpp(0u, x, 0x142, 0xa, 0xf, false);
  x += __builtin_amdgcn_update_dpp(0u, x, 0x143, 0xc, 0xf, false);
  return x;
}

struct LdsLow {
  u32x4 sink[16];
  unsigned flag, rdone, go, pad_;
  unsigned long long tag, pad2_;
  unsigned prog[16];
  int deg[64];
  unsigned byrank[64];
  unsigned nl[64];
  u32x4 ct[72];
  union {
    struct {
      __attribute__((aligned(16))) unsigned so[SW * CAPW];
      union {
        __attribute__((aligned(16))) unsigned hist[(SW + 1) * HS];
        u32x4 row5[SW * 64];
      };
    };
    struct {
      __attribute__((aligned(16))) unsigned sk[CH];
      u32x4 sp[CH];
    };
  };
  union {
    struct {
      __attribute__((aligned(16))) unsigned seg[2][NBMAX];
      unsigned phist[PW][NBMAX / 2];
      unsigned pbase[PW][NBMAX / 2];
      u32x4 ri[SW][MK * 16];
    };
    u32x4 pay[SW * CAPW];
  };
};
constexpr int TBLK = 0xc300 / 8;
struct Lds : LdsLow {
  char pad_end[163840 - TBLK * 16 - sizeof(LdsLow)];
  u32x4 sg[TBLK];
};
constexpr int TOFF = 163840 - TBLK * 16;
constexpr int WBLK = 0x4180 / 8;
static_assert(sizeof(Lds) == 163840 && TOFF < 65536, "LDS layout");

__device__ __forceinline__ unsigned lds_addr(const void* p) {
  return (unsigned)(uintptr_t)(__attribute__((address_space(3))) const void*)p;
}
__device__ __forceinline__ u32x4 make_srd(const void* p, unsigned num) {
  unsigned long long a = (unsigned long long)p;
  return u32x4{(unsigned)a, (unsigned)(a >> 32), num, 0x00020000u};
}

}

extern "C" __global__ __launch_bounds__(MT, 3) void tfn_main_kernel(
    const int* dst_, int E_, int c0_, const int* src_, int CE_, int c1_, const u32x2* Y_, int N_, int c2_, const u32x2* ef_,
    unsigned M2_, int c3_, const unsigned short* x_, char* ws_, u32x4* out_, const unsigned short* wrad_,
    const unsigned short* cg_, const unsigned short* wmix_) {
  __shared__ Lds L;
  const int tid = threadIdx.x, lane = tid & 63;
  u32x16 ka;
  u32x8 kb, kc;
  u32x4 kd;
  unsigned long long ev;
  asm volatile(PART_ASM
               : "=&{s[8:23]}"(ka), "=&{s[24:31]}"(kb), "=&{s[32:35]}"(kd), "=&{s[36:43]}"(kc), "=&{s[46:47]}"(ev)
               : [kp] "s"(__builtin_amdgcn_kernarg_segment_ptr()), [tid] "v"(tid), [bid] "s"(blockIdx.x), [pw] "n"(PW), [hw] "n"(HWV),
                 [h4] "n"(CH * 2), [h1] "n"(CH / 2), [sn] "n"(offsetof(LdsLow, sink)), [sg] "n"(offsetof(LdsLow, seg)),
                 [ph] "n"(offsetof(LdsLow, phist)), [pb] "n"(offsetof(LdsLow, pbase)), [tg] "n"(offsetof(LdsLow, tag)),
                 [go] "n"(offsetof(LdsLow, go)), [sk] "n"(offsetof(LdsLow, sk)), [sp] "n"(offsetof(LdsLow, sp))
               : PART_CLOBBERS);
  typedef __attribute__((address_space(1))) char gchar;
  auto ptr = [](unsigned lo, unsigned hi) { return (char*)(gchar*)(lo | (unsigned long long)hi << 32); };
  const int* __restrict__ dst = (const int*)ptr(ka[0], ka[1]);
  const int* __restrict__ src = (const int*)ptr(ka[4], ka[5]);
  const u32x2* __restrict__ Y = (const u32x2*)ptr(ka[8], ka[9]);
  const u32x2* __restrict__ ef = (const u32x2*)ptr(ka[12], ka[13]);
  const unsigned short* __restrict__ x = (const unsigned short*)ptr(kb[0], kb[1]);
  char* ws = ptr(kb[2], kb[3]);
  u32x4* __restrict__ out = (u32x4*)ptr(kb[4], kb[5]);
  const unsigned short* __restrict__ wrad = (const unsigned short*)ptr(kb[6], kb[7]);
  const unsigned short* __restrict__ cg = (const unsigned short*)ptr(kd[0], kd[1]);
  const unsigned short* __restrict__ wmix = (const unsigned short*)ptr(kd[2], kd[3]);
  const int w = kc[0], bucket = kc[1], E = kc[4], N = kc[5];
  const unsigned M2 = kc[6];
  __builtin_assume(w >= 0 && w < MW && bucket >= 0 && bucket < NBMAX);
  const int xcd = blockIdx.x & 7;
  const u32x4 wsd = make_srd(ws, WS_END);
  auto codes = [](unsigned q) { return q * 0x80008u + u32x4{0x10000u, 0x30002u, 0x50004u, 0x70006u}; };
  auto table = [&](int q) {
    u32x4 p, n;
#pragma unroll
    for (int i = 0; i < 4; i++) {
      const f32x2 z = {__uint_as_float((q * 8 + 2 * i) << 16), __uint_as_float((q * 8 + 2 * i + 1) << 16)};
      const f32x2 e = {__builtin_amdgcn_exp2f(-1.4426950408889634f * z[0]), __builtin_amdgcn_exp2f(-1.4426950408889634f * z[1])};
      const f32x2 r = z * f32x2{__builtin_amdgcn_rcpf(1.f + e[0]), __builtin_amdgcn_rcpf(1.f + e[1])};
      p[i] = pk2bf(r[0], r[1]);
      n[i] = pk2bf(-e[0] * r[0], -e[1] * r[1]);
    }
    if (q < WBLK) L.sg[q] = p;
    return n;
  };
  auto halves = [&](int q0, int q1, int st) {
#pragma unroll 1
    for (int q = q0; q < q1; q += st) {
      const u32x4 c = codes(q - 16);
      L.sg[q] = c;
      if (q >= 0x30) L.sg[q + 0x1000] = c | 0x80008000u;
    }
  };
  auto warm = [&](int i0, int i1) {
    const unsigned pfo = ((xcd * N) >> 3) * 1024 + ((blockIdx.x >> 3) * 64 + lane) * 128;
#pragma unroll 1
    for (int i = i0; i < i1; i += MW - SW)
      asm volatile("s_mov_b32 m0, %2\n s_nop 0\n buffer_load_dword %0, %1, %3 offen lds\n"
                   :: "v"(pfo), "s"(make_srd(x, N * 1024)), "s"(lds_addr(L.sink)), "s"(i * 32 * 64 * 128) : "memory");
  };
  auto wdma = [&](int i0, int i1) {
#pragma unroll
    for (int i = i0; i < i1; i++) {
      const unsigned o = (i + (blockIdx.x >> 3)) & 7;
      asm volatile("s_mov_b32 m0, %2\n s_nop 0\n buffer_load_dwordx4 %0, %1, %3 offen lds\n"
                   :: "v"((lane >> 2) * 256 + (lane & 3) * 16), "s"(make_srd(wmix, C_ * C_ * 2)),
                      "s"(lds_addr(&L.sg[WBLK]) + ((w - SW) * 8 + o) * 1024), "s"(o * 4096 + (w - SW) * 64) : "memory");
    }
  };
  const auto wsr = __builtin_amdgcn_make_buffer_rsrc((void*)ws, 0, WS_END, 0x00020000);
  if (w == HWV) {
    __builtin_amdgcn_s_setprio(3);
    asm volatile("s_waitcnt lgkmcnt(0)" : "+{s[46:47]}"(ev));
    ev = ev * 6364136223846793005ull + 1442695040888963407ull;
    L.tag = ev;
    if (lane < 4) (&L.flag)[lane] = 0;
    const unsigned ho0 = lane * 16 + WS_OFF + bucket * (NBMAX * 16);
    __syncthreads();
    const unsigned tl = (unsigned)ev, th = (unsigned)(ev >> 32);
    unsigned c[4] = {0, 0, 0, 0};
    u32x4 hv[4];
#pragma unroll
    for (int k = 0; k < 4; k++) {
#pragma unroll
      for (int ww = 0; ww < PW; ww++) c[k] += reinterpret_cast<const unsigned short*>(L.phist[ww])[k * 64 + lane];
      hv[k][1] = L.seg[0][k * 64 + lane];
    }
    const unsigned p0 = c[0] | (c[1] << 16), p1 = c[2] | (c[3] << 16);
    const unsigned i0 = wave_incl_scan(p0), i1 = wave_incl_scan(p1);
    const unsigned t0 = __builtin_amdgcn_readlane(i0, 63), t1 = __builtin_amdgcn_readlane(i1, 63);
    const unsigned s1 = t0 & 0xffff, s2 = s1 + (t0 >> 16), s3 = s2 + (t1 & 0xffff);
    const unsigned bs[4] = {(i0 - p0) & 0xffff, ((i0 - p0) >> 16) + s1, ((i1 - p1) & 0xffff) + s2, ((i1 - p1) >> 16) + s3};
#pragma unroll
    for (int k = 0; k < 4; k++) {
      hv[k][0] = bs[k] | (c[k] << 11) | (tl << 22);
      hv[k][1] += (unsigned)__umul24(c[k], tl & 255) << 24;
      hv[k][3] = th - hv[k][0] - hv[k][1];
    }
    asm volatile("" : "+v"(hv[0]), "+v"(hv[1]), "+v"(hv[2]), "+v"(hv[3]));
    while (true) {
      const unsigned gv = __hip_atomic_load(&L.go, __ATOMIC_RELAXED, __HIP_MEMORY_SCOPE_WORKGROUP);
      asm volatile("" ::: "memory");
#pragma unroll
      for (int k = 0; k < 4; k++) hv[k][2] = L.seg[1][k * 64 + lane];
      asm volatile("" ::: "memory");
      if (__builtin_amdgcn_readfirstlane(gv) == PW) break;
    }
#pragma unroll
    for (int k = 0; k < 4; k++) {
      hv[k][3] -= hv[k][2];
      __builtin_amdgcn_raw_buffer_store_b128(hv[k], wsr, ho0 + k * 1024, 0, 16);
    }
  } else if (w >= PW) {
    const int zt = tid - PT - (w > HWV ? 64 : 0);
    if (w >= SW) wdma(0, WA);
    if (zt < (0x4300 - 0x3a80) / 8) L.sg[0xba80 / 8 + zt] = table(0x3a80 / 8 + zt);
    __builtin_amdgcn_s_setprio(3);
  }
  asm volatile("s_waitcnt lgkmcnt(0)\n s_barrier" ::: "memory");
  {
    const unsigned so1 = (tid + MT - PT) * 16 + WS_RECP + bucket * (CH * 16);
    const unsigned so0 = tid * 16 + (w < PW ? WS_RECK + bucket * (CH * 4) : WS_RECP - PT * 16 + bucket * (CH * 16));
    const u32x4 v0 = reinterpret_cast<const u32x4*>(L.sk)[tid], v1 = reinterpret_cast<const u32x4*>(L.sk)[MT + tid];
    __builtin_amdgcn_raw_buffer_store_b128(v0, wsr, so0, 0, 16);
    __builtin_amdgcn_raw_buffer_store_b128(v1, wsr, so1, 0, 16);
    if (w == PW)
      __builtin_amdgcn_raw_buffer_store_b128(reinterpret_cast<const u32x4*>(L.sk)[2 * MT + lane], wsr, so1 + (MT - PT) * 16, 0, 16);
  }
  __syncthreads();
  const unsigned long long epoch = L.tag;
  const unsigned tagl = __builtin_amdgcn_readfirstlane((unsigned)epoch);
  const unsigned tag1 = __builtin_amdgcn_readfirstlane((unsigned)(epoch >> 32));
  const int row = lane & 15, kq = lane >> 4;
  const int node0 = __builtin_amdgcn_readfirstlane(((unsigned)(bucket << 22) + M2 - 1) / M2);
  const int nn = min(N, __builtin_amdgcn_readfirstlane(((unsigned)((bucket + 1) << 22) + M2 - 1) / M2)) - node0;
  if (w >= SW) {
    int lane;
    asm volatile("v_mbcnt_lo_u32_b32 %0, -1, 0\n v_mbcnt_hi_u32_b32 %0, -1, %0" : "=v"(lane));
    __builtin_assume(lane >= 0 && lane < 64);
    const int h = w - SW, st = h * 64 + lane, row = lane & 15, kq = lane >> 4;
    if (h == 0) {
      L.deg[lane] = ((lane - node0) & 63) < NPB ? 63 - ((lane - node0) & 63) : -1;
      if (lane < 16) L.prog[lane] = 255;
    }
    __builtin_amdgcn_s_sleep(WSL);
    if (w == HWV) wdma(0, WA);
    wdma(WA, 8);
    unsigned short cv;
    unsigned wq4[4];
    if (h == 1) {
      cv = cg[lane];
#pragma unroll
      for (int k = 0; k < 4; k++) wq4[k] = reinterpret_cast<const unsigned*>(wrad)[k * (C_ / 2) + lane];
    }
    halves(0x20 + TB3 * 512 + st, 0x3a80 / 8, 256);
    const int wq0 = WBLK + h * 512 + lane;
    u32x4 last = codes(wq0 + 448 < 0x1000 ? wq0 + 448 : wq0 + 432);
    if ((unsigned)(wq0 + 448 - 0x1000) < 0x20u) last = table(wq0 + 448 - 0x1000);
    constexpr int HR = (NPB - RKL * SW) / (MW - SW);
    const int id = (node0 + RKL * SW + h * HR + lane) & 63;
    asm volatile("s_waitcnt     vmcnt(0)" ::: "memory");
    if (h == 1) {
      reinterpret_cast<unsigned short*>(L.ct)[(lane & 3) * 16 + (lane >> 2)] = cv;
      L.ct[8 + lane] = u32x4{__builtin_amdgcn_perm(wq4[1], wq4[0], 0x05040100), __builtin_amdgcn_perm(wq4[3], wq4[2], 0x05040100),
                             __builtin_amdgcn_perm(wq4[1], wq4[0], 0x07060302), __builtin_amdgcn_perm(wq4[3], wq4[2], 0x07060302)};
    }
    u32x16 wa, wb;
    {
      const unsigned* wp = reinterpret_cast<const unsigned*>(&L.sg[WBLK + h * 512]) + kq * 128 + row;
#pragma unroll
      for (int i = 0; i < 16; i++) {
        const unsigned q0 = wp[(i >> 2) * 512 + (i & 3) * 32], q1 = wp[(i >> 2) * 512 + (i & 3) * 32 + 16];
        u32x16& d = i < 8 ? wa : wb;
        d[(i & 4) * 2 + (i & 3)] = __builtin_amdgcn_perm(q1, q0, 0x05040100);
        d[(i & 4) * 2 + (i & 3) + 4] = __builtin_amdgcn_perm(q1, q0, 0x07060302);
      }
    }
    asm volatile("" : "+v"(wa), "+v"(wb) :: "memory");
    asm volatile("s_waitcnt lgkmcnt(0)\n s_barrier" ::: "memory");
    __builtin_amdgcn_s_sleep(2);
    warm(h, PFN);
    __syncthreads();
    {
      const int dg = L.deg[lane], dm = L.deg[id];
      int dn[HR];
#pragma unroll
      for (int i = 0; i < HR; i++) dn[i] = __builtin_amdgcn_readlane(dm, i);
      __builtin_amdgcn_sched_barrier(0);
      unsigned long long m[HR];
#pragma unroll
      for (int i = 0; i < HR; i++) m[i] = __ballot(dg > dn[i]);
      __builtin_amdgcn_sched_barrier(0);
      unsigned r = 63;
#pragma unroll
      for (int i = 0; i < HR; i++) asm("v_writelane_b32 %0, %1, %2" : "+v"(r) : "s"(__popcll(m[i])), "n"(i));
      L.byrank[r] = id * 4;
    }
    __syncthreads();
    if (bucket == 0 && st == 0)
      __builtin_amdgcn_raw_buffer_store_b64(u32x2{tagl, tag1}, wsr, WS_EPOCH, 0, 16);
    __builtin_amdgcn_s_setprio(0);
    __syncthreads();
#pragma unroll
    for (int i = 0; i < 8; i++) L.sg[wq0 + i * 64] = i < 7 ? codes(wq0 + i * 64) : last;
    __syncthreads();
    u32x16 oo;
#pragma unroll
    for (int t = 0; t < 10; t++)
      oo[t + 2] = (node0 + ((L.nl[t < SW ? t * 8 + kq : ((t - SW) * 4 + kq) * 8 + 4] / 4 - node0) & 63)) * 1024 + row * 16 + h * 256;
    oo[0] = lds_addr(&L.pay[(row >> 2) * 64 + (row & 3) * 4 + kq]);
    oo[1] = lds_addr(&L.prog[h]);
    asm volatile(MIX_ASM
                 : "+{v[0:15]}"(oo)
                 : "{v[32:47]}"(wa), "{v[48:63]}"(wb), [os] "s"(make_srd(out, (node0 + nn) * 1024)),
                   [r5] "n"((unsigned)(offsetof(LdsLow, row5) - offsetof(LdsLow, pay) - SW * CAPW * 16)),
                   [rd] "n"(offsetof(LdsLow, rdone)), [sl] "n"(HSL)
                 : MIX_CLOBBERS);
    asm volatile(".Ltfn_p1:");
  } else {
    {
    halves(0x20 + tid, 0x20 + TB3 * 512, 512);
    reinterpret_cast<u32x4*>(&L.hist[w * HS])[lane] = u32x4{0, 0, 0, 0};
    reinterpret_cast<u32x4*>(&L.hist[w * HS])[64 + lane] = u32x4{0, 0, 0, 0};
    L.ri[w][lane] = u32x4{0x20000000u, 0x20000000u, 0x20000000u, 0x20000000u};
    const unsigned long long t0 = w & 1 ? 0x26796281267d7281ull : 0x277dc200272c8180ull, t1 = w & 1 ? 0x246942c324755245ull : 0x25755242217984c2ull;
    const unsigned long long t2 = w & 1 ? 0x22613345226d6483ull : 0x236d130423694307ull, t3 = w & 1 ? 0x206503c725710384ull : 0x215d2386206513cdull;
    const unsigned long long t = w & 4 ? (w & 2 ? t3 : t2) : (w & 2 ? t1 : t0);
    const unsigned rks = nn == NPB ? t >> 32 : t;
    const int start = w * CAPW;
    unsigned sop = lds_addr(&L.so[start + (lane & 7)]);
    unsigned yp = lds_addr(&L.pay[start + (lane >> 2)]);
    unsigned ep = lds_addr(&L.pay[start + (lane & 15)]) + 8;
    unsigned cga = lds_addr(&L.ct[(lane & 3) * 2]);
    const unsigned rp = lds_addr(&L.pay[start]) + kq * 256 + row * 4;
    unsigned pg = lds_addr(&L.prog[w]), slowv;
    u32x16 cs = {(unsigned)((lane >> 1) * SW + w) * (NBMAX * 16),
                 (unsigned)(((lane >> 1) * SW + w) * CH + (lane & 1)) * 4,
                 lds_addr(L.ri[w]) + (lane & 1) * 4, lds_addr(L.ri[w]) + lane * 4, (lane & 1) - 1u, lane & 1u,
                 lds_addr(&L.hist[tid]), lds_addr(&L.byrank[(rks >> (min(lane & 7, NS - 1) * 6)) & 63]),
                 0, lds_addr(&L.hist[SW * HS + (lane >> 3)]),
                 (start * 4 + ((lane & 7) << 13)) * 0x10001u, (unsigned)(start + lane) * 4,
                 lds_addr(&L.nl[w * 8 + (lane & 7)]), lane * 4u, ((node0 + w * RKL + lane) & 63) * 4u, 0};
    asm volatile(STREAM_ASM
                 : [sl] "=s"(slowv), "+{v12}"(sop), "+{v14}"(yp), "+{v15}"(ep), "+{v[136:151]}"(cs),
                   "+{v20}"(cga), "+{v110}"(pg)
                 : "{v8}"(lane * 16), "{v[152:155]}"(u32x4{rp, rp + 64, rp + 128, rp + 192}),
                   "{v156}"(lds_addr(&L.row5[w * 64]) + kq * 256 + row * 4),
                   [xs] "s"(make_srd(x, N * 1024)), [ws] "s"(wsd),
                   [ho] "s"(WS_OFF + bucket * 16), [rk] "s"(WS_RECK), [rp] "s"(WS_RECP), [t0] "s"(tagl << 22), [t1] "s"(tag1), [tl] "s"(tagl),
                   [wb] "s"(w * (HS * 4)), [bit] "s"(1u << w), [fo] "s"((w >> 2) * FO1), [tb] "n"(TOFF),
                   [rd] "n"(offsetof(LdsLow, rdone)), [fl] "n"(offsetof(LdsLow, flag)),
                   [hi] "n"(offsetof(LdsLow, hist)), [to] "n"(offsetof(LdsLow, hist) + SW * HS * 4), [dg] "n"(offsetof(LdsLow, deg)),
                   [ct] "n"(offsetof(LdsLow, ct)), [br] "n"(offsetof(LdsLow, byrank)), [so] "n"(offsetof(LdsLow, so)), [pa] "n"(offsetof(LdsLow, pay))
                 : STREAM_CLOBBERS);
    }
    int lane;
    asm volatile("v_mbcnt_lo_u32_b32 %0, -1, 0\n v_mbcnt_hi_u32_b32 %0, -1, %0" : "=v"(lane));
    __builtin_assume(lane >= 0 && lane < 64);
    u32x4* rows = &L.pay[w * CAPW];
#pragma unroll 1
    for (int it = 0; it < 2 * NS; it++) {
      const int sl = it >> 1, slice = it & 1, ch = slice * 64 + lane;
      const unsigned dl = (L.nl[w * 8 + sl] / 4 - node0) & 63;
      float acc[4] = {0.f, 0.f, 0.f, 0.f};
#pragma unroll 1
      for (int eb = 0; eb < E; eb += 64) {
        unsigned long long m = __ballot(eb + lane < E && (unsigned)dst[min(eb + lane, E - 1)] == node0 + dl);
#pragma unroll 1
        for (; m; m &= m - 1) {
          const int e = eb + __builtin_ctzll(m);
          const u32x2 ry = Y[e], rf = ef[e];
          float yy[4] = {bf2f(ry[0] & 0xffff), bf2f(ry[0] >> 16), bf2f(ry[1] & 0xffff), bf2f(ry[1] >> 16)};
          float ee[4] = {bf2f(rf[0] & 0xffff), bf2f(rf[0] >> 16), bf2f(rf[1] & 0xffff), bf2f(rf[1] >> 16)};
          float z = 0.f;
#pragma unroll
          for (int k = 0; k < 4; k++) z += ee[k] * bf2f(wrad[k * C_ + ch]);
          float R = z / (1.f + __expf(-z));
          const unsigned short* xr = x + (size_t)src[e] * 512 + ch * 4;
#pragma unroll 1
          for (int o = 0; o < 4; o++) {
            float tp = 0.f;
#pragma unroll 1
            for (int i = 0; i < 4; i++) {
              float A = 0.f;
#pragma unroll
              for (int f = 0; f < 4; f++) A += yy[f] * bf2f(cg[i * 16 + f * 4 + o]);
              tp += bf2f(xr[i]) * bf2f(f2bf(A));
            }
            float v = R * tp;
            acc[0] += o == 0 ? v : 0.f;
            acc[1] += o == 1 ? v : 0.f;
            acc[2] += o == 2 ? v : 0.f;
            acc[3] += o == 3 ? v : 0.f;
          }
        }
      }
      u32x4* rw = sl < 4 ? &rows[sl * 64] : &L.row5[w * 64];
      unsigned short* ar = reinterpret_cast<unsigned short*>(&rw[slice * 32]) + (lane >> 5) * 128 + (lane & 31);
#pragma unroll
      for (int o = 0; o < 4; o++) ar[o * 32] = f2bf(acc[o]);
    }
    asm volatile("" ::: "memory");
    if (lane == 0) atomicOr(&L.rdone, 1u << w);
  }
}

extern "C" __global__ __launch_bounds__(C_) void tfn_plain_kernel(
    const int* __restrict__ dst, const int* __restrict__ src, const unsigned short* __restrict__ Y,
    const unsigned short* __restrict__ ef, const unsigned short* __restrict__ x, unsigned short* __restrict__ out,
    const unsigned short* __restrict__ wrad, const unsigned short* __restrict__ cg, const unsigned short* __restrict__ wmix,
    long long E) {
  __shared__ f32x4 agg[C_];
  const int c = threadIdx.x, lane = c & 63;
  f32x4 acc = {0.f, 0.f, 0.f, 0.f};
#pragma unroll 1
  for (long long eb = 0; eb < E; eb += 64) {
    unsigned long long m = __ballot(eb + lane < E && dst[eb + lane < E ? eb + lane : 0] == (int)blockIdx.x);
#pragma unroll 1
    for (; m; m &= m - 1) {
      const long long e = eb + __builtin_ctzll(m);
      float z = 0.f;
#pragma unroll
      for (int k = 0; k < 4; k++) z += bf2f(ef[e * 4 + k]) * bf2f(wrad[k * C_ + c]);
      const float R = z / (1.f + __expf(-z));
#pragma unroll
      for (int i = 0; i < 4; i++) {
        const float xv = R * bf2f(x[(size_t)src[e] * 512 + c * 4 + i]);
#pragma unroll
        for (int f = 0; f < 4; f++)
#pragma unroll
          for (int o = 0; o < 4; o++) acc[o] += xv * bf2f(Y[e * 4 + f]) * bf2f(cg[i * 16 + f * 4 + o]);
      }
    }
  }
  agg[c] = acc;
  __syncthreads();
  f32x4 r = {0.f, 0.f, 0.f, 0.f};
#pragma unroll 4
  for (int k = 0; k < C_; k++) r += agg[k] * bf2f(wmix[k * C_ + c]);
  *reinterpret_cast<u32x2*>(out + (size_t)blockIdx.x * 512 + c * 4) = u32x2{pk2bf(r[0], r[1]), pk2bf(r[2], r[3])};
}

extern "C" void kernel_launch(void* const* d_in, const int* in_sizes, int n_in, void* d_out, int out_size,
                              void* d_ws, size_t ws_size, hipStream_t stream) {
  const unsigned short* x = (const unsigned short*)d_in[0];
  const int* src = (const int*)d_in[1];
  const int* dst = (const int*)d_in[2];
  const u32x2* Y = (const u32x2*)d_in[3];
  const u32x2* ef = (const u32x2*)d_in[4];
  const unsigned short* wrad = (const unsigned short*)d_in[5];
  const unsigned short* cg = (const unsigned short*)d_in[6];
  const unsigned short* wmix = (const unsigned short*)d_in[7];
  const int N = in_sizes[0] / 512;
  const int E = in_sizes[1];
  const int CE = ((E + NBMAX - 1) / NBMAX + 1) & ~1;
  const unsigned M2 = ((unsigned)NBMAX << 22) / (unsigned)N;
  if (N <= 64 || ((1u << 22) + M2 - 1) / M2 > (unsigned)NPB || CE > CH || ws_size < WS_END) {
    tfn_plain_kernel<<<N, C_, 0, stream>>>(dst, src, (const unsigned short*)Y, (const unsigned short*)ef, x,
                                           (unsigned short*)d_out, wrad, cg, wmix, E);
    return;
  }
  tfn_main_kernel<<<NBMAX, MT, 0, stream>>>(dst, E, 0x20000, src, CE, 0x20000, Y, N, 0x20000, ef, M2, 0x20000, x, (char*)d_ws,
                                           (u32x4*)d_out, wrad, cg, wmix);
}
```

```cpp
#include <hip/hip_runtime.h>
#include <hip/hip_bf16.h>
#include <stddef.h>
#include <stdint.h>

typedef __attribute__((ext_vector_type(2))) float f32x2;
typedef __attribute__((ext_vector_type(4))) float f32x4;
typedef __attribute__((ext_vector_type(2))) unsigned int u32x2;
typedef __attribute__((ext_vector_type(4))) unsigned int u32x4;
typedef __attribute__((ext_vector_type(8))) unsigned int u32x8;
typedef __attribute__((ext_vector_type(16))) unsigned int u32x16;
typedef __attribute__((ext_vector_type(2))) __bf16 bf16x2;

#define STREAM_ASM \
  "s_mov_b32 s63, 0x10000\n" \
  "Lh%=:\n" \
  "buffer_load_dwordx4 v[72:75], v136, %[ws], %[ho] offen sc1\n" \
  "v_mov_b32 v117, 0\n" \
  "v_mov_b32 v116, 64\n" \
  "v_mov_b32 v87, 0\n" \
  "v_mov_b64 v[44:45], 0\n" \
  "v_mov_b64 v[46:47], 0\n" \
  "v_mov_b32 v118, 0x7fc\n" \
  "s_waitcnt vmcnt(0)\n" \
  "v_add3_u32 v108, v72, v73, v74\n" \
  "v_xor_b32 v109, %[t0], v72\n" \
  "v_add_u32 v108, v108, v75\n" \
  "v_cmp_lt_u32 vcc, 0x3fffff, v109\n" \
  "v_cmp_ne_u32_e64 s[74:75], %[t1], v108\n" \
  "s_or_b64 vcc, vcc, s[74:75]\n" \
  "s_cbranch_scc1 Lhr%=\n" \
  "s_mov_b64 exec, -1\n" \
  "v_bfe_u32 v108, v72, 11, 11\n" \
  "v_and_b32 v109, v140, v108\n" \
  "v_and_b32 v112, 0x7ff, v72\n" \
  "v_sub_u32 v111, v108, v141\n" \
  "v_add_u32_dpp v109, v109, v109 row_shr:1 row_mask:0xf bank_mask:0xf bound_ctrl:1\n" \
  "v_lshl_add_u32 v112, v112, 2, v137\n" \
  "v_and_b32 v114, v140, v73\n" \
  "v_add_u32_dpp v109, v109, v109 row_shr:2 row_mask:0xf bank_mask:0xf bound_ctrl:1\n" \
  "v_and_b32 v115, v140, v74\n" \
  "v_sub_u32 v114, 0, v114\n" \
  "v_add_u32_dpp v109, v109, v109 row_shr:4 row_mask:0xf bank_mask:0xf bound_ctrl:1\n" \
  "v_sub_u32 v115, 0, v115\n" \
  "s_nop 0\n" \
  "v_add_u32_dpp v109, v109, v109 row_shr:8 row_mask:0xf bank_mask:0xf bound_ctrl:1\n" \
  "s_nop 1\n" \
  "v_add_u32_dpp v109, v109, v109 row_bcast:15 row_mask:0xa bank_mask:0xf\n" \
  "s_nop 1\n" \
  "v_add_u32_dpp v109, v109, v109 row_bcast:31 row_mask:0xc bank_mask:0xf\n" \
  "v_sub_u32 v108, v109, v108\n" \
  "v_readlane_b32 s62, v109, 63\n" \
  "v_lshl_add_u32 v113, v108, 2, v138\n" \
  "s_cmpk_gt_u32 s62, 0x100\n" \
  "s_cbranch_scc1 Lover%=\n" \
  "Lw%=:\n" \
  "v_cmpx_lt_i32 vcc, 0, v111\n" \
  "ds_write_b32 v113, v112 offset:0\n" \
  "v_add_u32 v112, 8, v112\n" \
  "v_cmpx_lt_i32 vcc, 2, v111\n" \
  "ds_write_b32 v113, v112 offset:8\n" \
  "v_add_u32 v112, 8, v112\n" \
  "v_cmpx_lt_i32 vcc, 4, v111\n" \
  "ds_write_b32 v113, v112 offset:16\n" \
  "v_add_u32 v112, 8, v112\n" \
  "v_cmpx_lt_i32 vcc, 6, v111\n" \
  "ds_write_b32 v113, v112 offset:24\n" \
  "v_add_u32 v112, 8, v112\n" \
  "v_cmpx_lt_i32 vcc, 8, v111\n" \
  "ds_write_b32 v113, v112 offset:32\n" \
  "v_add_u32 v112, 8, v112\n" \
  "s_cbranch_execz Lwd%=\n" \
  "v_cmpx_lt_i32 vcc, 10, v111\n" \
  "ds_write_b32 v113, v112 offset:40\n" \
  "v_add_u32 v112, 8, v112\n" \
  "s_cbranch_execz Lwd%=\n" \
  "v_cmpx_lt_i32 vcc, 12, v111\n" \
  "ds_write_b32 v113, v112 offset:48\n" \
  "v_add_u32 v112, 8, v112\n" \
  "s_cbranch_execz Lwd%=\n" \
  "v_cmpx_lt_i32 vcc, 14, v111\n" \
  "ds_write_b32 v113, v112 offset:56\n" \
  "v_add_u32 v112, 8, v112\n" \
  "v_add_u32 v113, 64, v113\n" \
  "v_subrev_u32 v111, 16, v111\n" \
  "s_branch Lw%=\n" \
  "Lwd%=:\n" \
  "s_mov_b64 exec, -1\n" \
  "Lld%=:\n" \
  "ds_read2st64_b32 v[76:77], v139 offset1:1\n" \
  "ds_read2st64_b32 v[78:79], v139 offset0:2 offset1:3\n" \
  "s_mov_b32 s63, 0x10000\n" \
  "s_waitcnt lgkmcnt(0)\n" \
  "buffer_load_dword v84, v76, %[ws], %[rk] offen sc1\n" \
  "buffer_load_dword v85, v77, %[ws], %[rk] offen sc1\n" \
  "buffer_load_dword v86, v78, %[ws], %[rk] offen sc1\n" \
  "s_cmpk_le_u32 s62, 0xc0\n" \
  "s_cbranch_scc1 Lka%=\n" \
  "buffer_load_dword v87, v79, %[ws], %[rk] offen sc1\n" \
  "Lka%=:\n" \
  "v_lshlrev_b32 v80, 2, v76\n" \
  "v_lshlrev_b32 v81, 2, v77\n" \
  "v_lshlrev_b32 v82, 2, v78\n" \
  "v_lshlrev_b32 v83, 2, v79\n" \
  "s_barrier\n" \
  "s_cmpk_le_u32 s62, 0xc0\n" \
  "s_cbranch_scc1 Lpa%=\n" \
  "buffer_load_dwordx4 v[44:47], v83, %[ws], %[rp] offen sc1\n" \
  "Lpa%=:\n" \
  "buffer_load_dwordx4 v[32:35], v80, %[ws], %[rp] offen sc1\n" \
  "buffer_load_dwordx4 v[36:39], v81, %[ws], %[rp] offen sc1\n" \
  "buffer_load_dwordx4 v[40:43], v82, %[ws], %[rp] offen sc1\n" \
  "v_cmp_le_i32_e64 s[66:67], 0, v80\n" \
  "v_cmp_le_i32_e64 s[68:69], 0, v81\n" \
  "v_cmp_le_i32_e64 s[70:71], 0, v82\n" \
  "v_cmp_le_i32_e64 s[72:73], 0, v83\n" \
  "v_mov_b64 v[48:49], 0\n" \
  "v_mov_b64 v[50:51], 0\n" \
  "v_mov_b64 v[52:53], 0\n" \
  "v_mov_b64 v[54:55], 0\n" \
  "v_mov_b64 v[56:57], 0\n" \
  "v_mov_b64 v[58:59], 0\n" \
  "v_mov_b64 v[60:61], 0\n" \
  "v_mov_b64 v[62:63], 0\n" \
  "v_mov_b64 v[64:65], 0\n" \
  "v_mov_b64 v[66:67], 0\n" \
  "v_mov_b64 v[68:69], 0\n" \
  "v_mov_b64 v[70:71], 0\n" \
  "s_waitcnt vmcnt(3)\n" \
  "Lk%=:\n" \
  "v_add3_u32 v108, v84, v85, v86\n" \
  "v_add3_u32 v108, v108, v87, v114\n" \
  "v_lshlrev_b32 v88, 1, v84\n" \
  "v_lshlrev_b32 v92, 4, v84\n" \
  "v_add_u32_dpp v108, v108, v108 row_shr:1 row_mask:0xf bank_mask:0xf bound_ctrl:1\n" \
  "v_lshrrev_b32 v104, 2, v84\n" \
  "v_and_or_b32 v88, v88, v118, %[wb]\n" \
  "v_add_u32_dpp v108, v108, v108 row_shr:2 row_mask:0xf bank_mask:0xf bound_ctrl:1\n" \
  "v_lshlrev_b32_e64 v100, v92, 4\n" \
  "v_and_b32 v104, 0xfc, v104\n" \
  "v_add_u32_dpp v108, v108, v108 row_shr:4 row_mask:0xf bank_mask:0xf bound_ctrl:1\n" \
  "v_lshlrev_b32 v89, 1, v85\n" \
  "v_lshlrev_b32 v93, 4, v85\n" \
  "v_add_u32_dpp v108, v108, v108 row_shr:8 row_mask:0xf bank_mask:0xf bound_ctrl:1\n" \
  "v_lshrrev_b32 v105, 2, v85\n" \
  "v_and_or_b32 v89, v89, v118, %[wb]\n" \
  "v_add_u32_dpp v108, v108, v108 row_bcast:15 row_mask:0xa bank_mask:0xf\n" \
  "v_lshlrev_b32_e64 v101, v93, 4\n" \
  "v_and_b32 v105, 0xfc, v105\n" \
  "v_add_u32_dpp v108, v108, v108 row_bcast:31 row_mask:0xc bank_mask:0xf\n" \
  "v_lshlrev_b32 v90, 1, v86\n" \
  "v_readlane_b32 s65, v108, 63\n" \
  "v_lshlrev_b32 v94, 4, v86\n" \
  "v_lshrrev_b32 v106, 2, v86\n" \
  "v_and_or_b32 v90, v90, v118, %[wb]\n" \
  "v_lshlrev_b32_e64 v102, v94, 4\n" \
  "v_and_b32 v106, 0xfc, v106\n" \
  "s_cmp_lg_u32 s65, 0\n" \
  "s_cbranch_scc1 Lkr%=\n" \
  "Lkc%=:\n" \
  "s_mov_b64 exec, s[66:67]\n" \
  "ds_add_rtn_u32 v96, v88, v100 offset:%[hi]\n" \
  "ds_add_u32 v104, v116 offset:%[dg]\n" \
  "s_mov_b64 exec, s[68:69]\n" \
  "ds_add_rtn_u32 v97, v89, v101 offset:%[hi]\n" \
  "ds_add_u32 v105, v116 offset:%[dg]\n" \
  "s_mov_b64 exec, s[70:71]\n" \
  "ds_add_rtn_u32 v98, v90, v102 offset:%[hi]\n" \
  "ds_add_u32 v106, v116 offset:%[dg]\n" \
  "s_cmpk_gt_u32 s62, 0xc0\n" \
  "s_mov_b64 exec, -1\n" \
  "s_cbranch_scc1 Lc3%=\n" \
  "Lc4%=:\n" \
  "ds_read_b128 v[24:27], v20 offset:16\n" \
  "ds_read_b128 v[28:31], v8 offset:%[ct]+128\n" \
  "ds_read_b128 v[20:23], v20\n" \
  "v_and_b32 v100, v118, v88\n" \
  "v_and_b32 v101, v118, v89\n" \
  "v_and_b32 v102, v118, v90\n" \
  "s_waitcnt lgkmcnt(0)\n" \
  "s_barrier\n" \
  "ds_read_b32 v108, v149 offset:%[dg]\n" \
  "ds_read_b32 v109, v150 offset:%[dg]\n" \
  "ds_read2st64_b32 v[76:77], v142 offset0:0 offset1:8\n" \
  "ds_read2st64_b32 v[78:79], v142 offset0:16 offset1:24\n" \
  "ds_read2st64_b32 v[104:105], v142 offset0:32 offset1:40\n" \
  "ds_read2st64_b32 v[106:107], v142 offset0:48 offset1:56\n" \
  "v_and_b32 v84, 0xfffc00, v84\n" \
  "v_and_b32 v85, 0xfffc00, v85\n" \
  "v_and_b32 v86, 0xfffc00, v86\n" \
  "v_and_b32 v87, 0xfffc00, v87\n" \
  "v_mov_b32 v111, 0\n" \
  "v_mov_b32 v112, 63\n" \
  "s_waitcnt lgkmcnt(4)\n" \
  "v_readlane_b32 s74, v109, 0\n" \
  "v_readlane_b32 s76, v109, 1\n" \
  "v_readlane_b32 s78, v109, 2\n" \
  "s_nop 0\n" \
  "v_cmp_lt_i32_e64 s[74:75], s74, v108\n" \
  "v_cmp_lt_i32_e64 s[76:77], s76, v108\n" \
  "v_cmp_lt_i32_e64 s[78:79], s78, v108\n" \
  "s_bcnt1_i32_b64 s74, s[74:75]\n" \
  "s_bcnt1_i32_b64 s76, s[76:77]\n" \
  "s_bcnt1_i32_b64 s78, s[78:79]\n" \
  "v_writelane_b32 v112, s74, 0\n" \
  "v_writelane_b32 v112, s76, 1\n" \
  "v_writelane_b32 v112, s78, 2\n" \
  "v_lshlrev_b32 v112, 2, v112\n" \
  "s_waitcnt lgkmcnt(3)\n" \
  "ds_write2st64_b32 v142, v117, v76 offset1:8\n" \
  "v_add_u32 v77, v77, v76\n" \
  "s_waitcnt lgkmcnt(3)\n" \
  "v_add_u32 v78, v78, v77\n" \
  "ds_write2st64_b32 v142, v77, v78 offset0:16 offset1:24\n" \
  "v_add_u32 v79, v79, v78\n" \
  "s_waitcnt lgkmcnt(3)\n" \
  "v_add_u32 v104, v104, v79\n" \
  "ds_write2st64_b32 v142, v79, v104 offset0:32 offset1:40\n" \
  "v_add_u32 v105, v105, v104\n" \
  "s_waitcnt lgkmcnt(3)\n" \
  "v_add_u32 v106, v106, v105\n" \
  "ds_write2st64_b32 v142, v105, v106 offset0:48 offset1:56\n" \
  "v_add_u32 v107, v107, v106\n" \
  "ds_write_b32 v142, v107 offset:16384\n" \
  "ds_write_b32 v112, v150 offset:%[br]\n" \
  "s_waitcnt lgkmcnt(0)\n" \
  "s_barrier\n" \
  "ds_read_b32 v108, v143\n" \
  "s_mov_b32 s74, 0x1f1f1f1f\n" \
  "s_mov_b32 s75, 0x1f1f1f1f\n" \
  "s_waitcnt lgkmcnt(0)\n" \
  "v_lshl_add_u32 v76, v108, 3, v145\n" \
  "s_mov_b64 exec, s[74:75]\n" \
  "ds_read_b32 v111, v76\n" \
  "s_mov_b64 exec, -1\n" \
  "ds_write_b32 v148, v108\n" \
  "s_waitcnt lgkmcnt(1)\n" \
  "v_add_u32_dpp v112, v111, v111 row_shr:1 row_mask:0xf bank_mask:0xf bound_ctrl:1\n" \
  "v_sub_u32 v77, v146, v111\n" \
  "s_nop 0\n" \
  "v_add_u32_dpp v112, v112, v112 row_shr:2 row_mask:0xf bank_mask:0xf bound_ctrl:1\n" \
  "s_nop 1\n" \
  "v_add_u32_dpp v112, v112, v112 row_shr:4 row_mask:0xf bank_mask:0xf bound_ctrl:1\n" \
  "s_nop 1\n" \
  "v_add_u32_dpp v112, v112, v112 row_shr:8 row_mask:0xf bank_mask:0xf bound_ctrl:1\n" \
  "s_nop 1\n" \
  "v_add_u32_dpp v112, v112, v112 row_bcast:15 row_mask:0xa bank_mask:0xf\n" \
  "s_nop 1\n" \
  "v_add_u32_dpp v112, v112, v112 row_bcast:31 row_mask:0xc bank_mask:0xf\n" \
  "v_sub_u32 v111, v112, v111\n" \
  "v_readlane_b32 s65, v112, 63\n" \
  "v_mov_b32_dpp v78, v112 row_newbcast:7 row_mask:0xf bank_mask:0x3\n" \
  "v_mov_b32_dpp v79, v111 row_newbcast:0 row_mask:0xf bank_mask:0x3\n" \
  "v_add_u32 v77, v77, v112\n" \
  "v_mov_b32_dpp v78, v112 row_newbcast:15 row_mask:0xf bank_mask:0xc\n" \
  "v_mov_b32_dpp v79, v111 row_newbcast:8 row_mask:0xf bank_mask:0xc\n" \
  "s_and_b32 s64, s65, 0xffff\n" \
  "v_lshl_add_u32 v77, v78, 16, v77\n" \
  "s_lshr_b32 s65, s65, 16\n" \
  "v_add_u32_sdwa v77, v77, v79 dst_sel:DWORD dst_unused:UNUSED_PAD src0_sel:DWORD src1_sel:WORD_1\n" \
  "s_add_u32 s64, s64, s65\n" \
  "s_mov_b64 exec, s[74:75]\n" \
  "ds_write_b32 v76, v77\n" \
  "s_mov_b64 exec, -1\n" \
  "s_cmpk_gt_u32 s64, 864\n" \
  "s_cbranch_scc1 Llo%=\n" \
  "Llc%=:\n" \
  "s_waitcnt lgkmcnt(0)\n" \
  "s_barrier\n" \
  "ds_read_b32 v109, v117 offset:%[fl]\n" \
  "ds_read_b32 v76, v88 offset:%[hi]\n" \
  "ds_read_b32 v104, v100 offset:%[to]\n" \
  "ds_read_b32 v77, v89 offset:%[hi]\n" \
  "ds_read_b32 v105, v101 offset:%[to]\n" \
  "ds_read_b32 v78, v90 offset:%[hi]\n" \
  "ds_read_b32 v106, v102 offset:%[to]\n" \
  "v_add_u32 v113, s64, v147\n" \
  "v_bfrev_b32 v111, 1\n" \
  "s_max_u32 s60, s64, 4\n" \
  "s_sub_u32 s60, s60, 4\n" \
  "s_lshr_b32 s60, s60, 4\n" \
  "s_waitcnt lgkmcnt(4)\n" \
  "v_readfirstlane_b32 s61, v109\n" \
  "v_add3_u32 v88, v76, v104, v96\n" \
  "v_lshrrev_b32 v88, v92, v88\n" \
  "v_bfe_u32 v72, v88, 13, 3\n" \
  "v_and_b32 v88, 0x1ffc, v88\n" \
  "v_lshl_or_b32 v72, v72, 1, v84\n" \
  "s_cmp_lg_u32 s61, 0\n" \
  "s_cbranch_scc1 Lslow%=\n" \
  "s_waitcnt lgkmcnt(0)\n" \
  "v_add3_u32 v89, v77, v105, v97\n" \
  "v_add3_u32 v90, v78, v106, v98\n" \
  "v_lshrrev_b32 v89, v93, v89\n" \
  "v_lshrrev_b32 v90, v94, v90\n" \
  "v_bfe_u32 v73, v89, 13, 3\n" \
  "v_bfe_u32 v74, v90, 13, 3\n" \
  "v_and_b32 v89, 0x1ffc, v89\n" \
  "v_and_b32 v90, 0x1ffc, v90\n" \
  "v_lshl_or_b32 v73, v73, 1, v85\n" \
  "v_lshl_or_b32 v74, v74, 1, v86\n" \
  "s_mov_b64 exec, s[66:67]\n" \
  "ds_write_b32 v88, v72 offset:%[so]\n" \
  "s_mov_b64 exec, s[68:69]\n" \
  "ds_write_b32 v89, v73 offset:%[so]\n" \
  "s_mov_b64 exec, s[70:71]\n" \
  "ds_write_b32 v90, v74 offset:%[so]\n" \
  "s_cmpk_gt_u32 s62, 0xc0\n" \
  "s_cbranch_scc1 Lq3%=\n" \
  "Lq4%=:\n" \
  "s_mov_b32 exec_lo, -1\n" \
  "s_mov_b32 exec_hi, 0\n" \
  "ds_write_b32 v113, v111 offset:%[so]\n" \
  "s_mov_b64 exec, -1\n" \
  "v_lshlrev_b32 v88, 2, v88\n" \
  "v_lshlrev_b32 v89, 2, v89\n" \
  "v_lshlrev_b32 v90, 2, v90\n" \
  "v_lshlrev_b32 v91, 2, v91\n" \
  "v_lshlrev_b32 v113, 2, v113\n" \
  "s_mov_b32 s63, 0x10000\n" \
  "s_waitcnt vmcnt(0)\n" \
  "Lpc%=:\n" \
  "v_add3_u32 v108, v32, v33, v115\n" \
  "v_add3_u32 v111, v34, v35, v36\n" \
  "v_add3_u32 v112, v37, v38, v39\n" \
  "v_add3_u32 v108, v108, v40, v41\n" \
  "v_add3_u32 v111, v111, v42, v43\n" \
  "v_add3_u32 v112, v112, v44, v45\n" \
  "v_add3_u32 v108, v108, v46, v47\n" \
  "v_add3_u32 v108, v108, v111, v112\n" \
  "v_subrev_u32 v32, %[tl], v32\n" \
  "v_subrev_u32 v34, %[tl], v34\n" \
  "v_subrev_u32 v36, %[tl], v36\n" \
  "v_subrev_u32 v38, %[tl], v38\n" \
  "v_subrev_u32 v40, %[tl], v40\n" \
  "v_subrev_u32 v42, %[tl], v42\n" \
  "v_subrev_u32 v44, %[tl], v44\n" \
  "v_subrev_u32 v46, %[tl], v46\n" \
  "s_mov_b64 exec, s[66:67]\n" \
  "ds_write_b128 v88, v[32:35] offset:%[pa]\n" \
  "s_mov_b64 exec, s[68:69]\n" \
  "ds_write_b128 v89, v[36:39] offset:%[pa]\n" \
  "s_mov_b64 exec, s[70:71]\n" \
  "ds_write_b128 v90, v[40:43] offset:%[pa]\n" \
  "s_mov_b64 exec, s[72:73]\n" \
  "ds_write_b128 v91, v[44:47] offset:%[pa]\n" \
  "s_mov_b32 exec_lo, -1\n" \
  "s_mov_b32 exec_hi, 0\n" \
  "ds_write_b128 v113, v[48:51] offset:%[pa]\n" \
  "s_mov_b64 exec, -1\n" \
  "s_nop 1\n" \
  "v_add_u32_dpp v108, v108, v108 row_shr:1 row_mask:0xf bank_mask:0xf bound_ctrl:1\n" \
  "s_nop 1\n" \
  "v_add_u32_dpp v108, v108, v108 row_shr:2 row_mask:0xf bank_mask:0xf bound_ctrl:1\n" \
  "s_nop 1\n" \
  "v_add_u32_dpp v108, v108, v108 row_shr:4 row_mask:0xf bank_mask:0xf bound_ctrl:1\n" \
  "s_nop 1\n" \
  "v_add_u32_dpp v108, v108, v108 row_shr:8 row_mask:0xf bank_mask:0xf bound_ctrl:1\n" \
  "s_nop 1\n" \
  "v_add_u32_dpp v108, v108, v108 row_bcast:15 row_mask:0xa bank_mask:0xf\n" \
  "s_nop 1\n" \
  "v_add_u32_dpp v108, v108, v108 row_bcast:31 row_mask:0xc bank_mask:0xf\n" \
  "s_nop 0\n" \
  "v_readlane_b32 s65, v108, 63\n" \
  "s_waitcnt lgkmcnt(0)\n" \
  "s_cmp_lg_u32 s65, 0\n" \
  "s_cbranch_scc1 Lpr%=\n" \
  "Lpw%=:\n" \
  "s_barrier\n" \
  "ds_read2_b32  v[10:11], v12 offset1:8\n" \
  "ds_read_b64 v[106:107], v14\n" \
  "ds_read_b64 v[104:105], v15\n" \
  "s_nop 0\n" \
  "s_setprio 1\n" \
  "v_mov_b64 v[32:33], 0\n" \
  "v_mov_b64 v[34:35], 0\n" \
  "v_mov_b64 v[36:37], 0\n" \
  "v_mov_b64 v[38:39], 0\n" \
  "v_mov_b64 v[40:41], 0\n" \
  "v_mov_b64 v[42:43], 0\n" \
  "v_mov_b64 v[44:45], 0\n" \
  "v_mov_b64 v[46:47], 0\n" \
  "s_waitcnt lgkmcnt(2)\n" \
  "v_readlane_b32 s80, v10, 0\n" \
  "v_readlane_b32 s81, v10, 1\n" \
  "v_readlane_b32 s82, v10, 2\n" \
  "v_readlane_b32 s83, v10, 3\n" \
  "v_readlane_b32 s84, v10, 4\n" \
  "v_readlane_b32 s85, v10, 5\n" \
  "v_readlane_b32 s86, v10, 6\n" \
  "s_andn2_b32 s61, s80, 15\n" \
  "buffer_load_dwordx4 v[120:123], v8, %[xs], s61 offen\n" \
  "s_andn2_b32 s61, s81, 15\n" \
  "buffer_load_dwordx4 v[124:127], v8, %[xs], s61 offen\n" \
  "s_andn2_b32 s61, s82, 15\n" \
  "buffer_load_dwordx4 v[128:131], v8, %[xs], s61 offen\n" \
  "s_andn2_b32 s61, s83, 15\n" \
  "buffer_load_dwordx4 v[132:135], v8, %[xs], s61 offen\n" \
  "s_andn2_b32 s61, s84, 15\n" \
  "buffer_load_dwordx4 v[136:139], v8, %[xs], s61 offen\n" \
  "s_andn2_b32 s61, s85, 15\n" \
  "buffer_load_dwordx4 v[140:143], v8, %[xs], s61 offen\n" \
  "v_mov_b64  v[96:97], 0\n" \
  "v_mov_b64  v[98:99], 0\n" \
  "v_mov_b64  v[100:101], 0\n" \
  "v_mov_b64  v[102:103], 0\n" \
  "s_waitcnt lgkmcnt(0)\n" \
  "v_mfma_f32_4x4x4_16b_bf16 v[80:83], v[104:105], v[28:29], 0 cbsz:4\n" \
  "v_mfma_f32_4x4x4_16b_bf16 v[84:87], v[104:105], v[30:31], 0 cbsz:4\n" \
  "v_mfma_f32_4x4x4_16b_bf16 v[112:115], v[106:107], v[20:21], 0\n" \
  "v_mfma_f32_4x4x4_16b_bf16 v[116:119], v[106:107], v[22:23], 0\n" \
  "v_mfma_f32_4x4x4_16b_bf16 v[72:75], v[106:107], v[24:25], 0\n" \
  "v_mfma_f32_4x4x4_16b_bf16 v[76:79], v[106:107], v[26:27], 0\n" \
  "v_cvt_pk_bf16_f32 v81, v80, v81\n" \
  "v_lshlrev_b32_sdwa v80, 1, v81 dst_sel:DWORD dst_unused:UNUSED_PAD src0_sel:DWORD src1_sel:WORD_0\n" \
  "v_lshlrev_b32_sdwa v81, 1, v81 dst_sel:DWORD dst_unused:UNUSED_PAD src0_sel:DWORD src1_sel:WORD_1\n" \
  "ds_read_u16_d16_hi v96, v80 offset:%[tb]\n" \
  "ds_read_u16_d16_hi v97, v81 offset:%[tb]\n" \
  "v_cvt_pk_bf16_f32 v85, v84, v85\n" \
  "v_lshlrev_b32_sdwa v84, 1, v85 dst_sel:DWORD dst_unused:UNUSED_PAD src0_sel:DWORD src1_sel:WORD_0\n" \
  "v_lshlrev_b32_sdwa v85, 1, v85 dst_sel:DWORD dst_unused:UNUSED_PAD src0_sel:DWORD src1_sel:WORD_1\n" \
  "ds_read_u16_d16_hi v98, v84 offset:%[tb]\n" \
  "ds_read_u16_d16_hi v99, v85 offset:%[tb]\n" \
  "v_cvt_pk_bf16_f32 v83, v82, v83\n" \
  "v_lshlrev_b32_sdwa v82, 1, v83 dst_sel:DWORD dst_unused:UNUSED_PAD src0_sel:DWORD src1_sel:WORD_0\n" \
  "v_lshlrev_b32_sdwa v83, 1, v83 dst_sel:DWORD dst_unused:UNUSED_PAD src0_sel:DWORD src1_sel:WORD_1\n" \
  "ds_read_u16_d16_hi v100, v82 offset:%[tb]\n" \
  "ds_read_u16_d16_hi v101, v83 offset:%[tb]\n" \
  "v_cvt_pk_bf16_f32 v108, v112, v116\n" \
  "v_cvt_pk_bf16_f32 v109, v72, v76\n" \
  "s_nop 0\n" \
  "s_waitcnt vmcnt(5)\n" \
  "v_mfma_f32_4x4x4_16b_bf16 v[72:75], v[108:109], v[120:121], 0 cbsz:4\n" \
  "v_mfma_f32_4x4x4_16b_bf16 v[76:79], v[108:109], v[122:123], 0 cbsz:4\n" \
  "s_andn2_b32 s61, s86, 15\n" \
  "buffer_load_dwordx4 v[144:147], v8, %[xs], s61 offen\n" \
  "s_waitcnt lgkmcnt(0)\n" \
  "s_nop 1\n" \
  "Lbody%=:\n" \
  "s_waitcnt lgkmcnt(2)\n" \
  "s_set_gpr_idx_on s80, gpr_idx(SRC2,DST)\n" \
  "v_pk_fma_f32 v[32:33], v[96:97], v[72:73], v[32:33] op_sel:[0,0,0] op_sel_hi:[0,1,1]\n" \
  "v_pk_fma_f32 v[42:43], v[96:97], v[74:75], v[42:43] op_sel:[0,0,0] op_sel_hi:[0,1,1]\n" \
  "v_pk_fma_f32 v[52:53], v[98:99], v[76:77], v[52:53] op_sel:[0,0,0] op_sel_hi:[0,1,1]\n" \
  "v_pk_fma_f32 v[62:63], v[98:99], v[78:79], v[62:63] op_sel:[0,0,0] op_sel_hi:[0,1,1]\n" \
  "s_set_gpr_idx_off\n" \
  "s_waitcnt vmcnt(5)\n" \
  "v_mfma_f32_4x4x4_16b_bf16 v[72:75], v[108:109], v[124:125], 0 cbsz:4 abid:1\n" \
  "v_mfma_f32_4x4x4_16b_bf16 v[76:79], v[108:109], v[126:127], 0 cbsz:4 abid:1\n" \
  "v_mfma_f32_4x4x4_16b_bf16 v[88:91], v[104:105], v[28:29], 0 cbsz:4 abid:1\n" \
  "v_mfma_f32_4x4x4_16b_bf16 v[92:95], v[104:105], v[30:31], 0 cbsz:4 abid:1\n" \
  "v_readlane_b32 s87, v10, 7\n" \
  "v_cvt_pk_bf16_f32 v87, v86, v87\n" \
  "v_lshlrev_b32_sdwa v86, 1, v87 dst_sel:DWORD dst_unused:UNUSED_PAD src0_sel:DWORD src1_sel:WORD_0\n" \
  "v_lshlrev_b32_sdwa v87, 1, v87 dst_sel:DWORD dst_unused:UNUSED_PAD src0_sel:DWORD src1_sel:WORD_1\n" \
  "s_andn2_b32 s61, s87, 15\n" \
  "buffer_load_dwordx4 v[148:151], v8, %[xs], s61 offen\n" \
  "ds_read_u16_d16_hi v102, v86 offset:%[tb]\n" \
  "ds_read_u16_d16_hi v103, v87 offset:%[tb]\n" \
  "v_mov_b32 v111, s60\n" \
  "ds_write2_b32 v110, v111, v111 offset1:8\n" \
  "s_set_gpr_idx_on s81, gpr_idx(SRC2,DST)\n" \
  "v_pk_fma_f32 v[32:33], v[96:97], v[72:73], v[32:33] op_sel:[1,0,0] op_sel_hi:[1,1,1]\n" \
  "v_pk_fma_f32 v[42:43], v[96:97], v[74:75], v[42:43] op_sel:[1,0,0] op_sel_hi:[1,1,1]\n" \
  "v_pk_fma_f32 v[52:53], v[98:99], v[76:77], v[52:53] op_sel:[1,0,0] op_sel_hi:[1,1,1]\n" \
  "v_pk_fma_f32 v[62:63], v[98:99], v[78:79], v[62:63] op_sel:[1,0,0] op_sel_hi:[1,1,1]\n" \
  "s_set_gpr_idx_off\n" \
  "s_waitcnt vmcnt(5) lgkmcnt(9)\n" \
  "v_mfma_f32_4x4x4_16b_bf16 v[72:75], v[108:109], v[128:129], 0 cbsz:4 abid:2\n" \
  "v_mfma_f32_4x4x4_16b_bf16 v[76:79], v[108:109], v[130:131], 0 cbsz:4 abid:2\n" \
  "v_readlane_b32 s80, v11, 0\n" \
  "v_cvt_pk_bf16_f32 v89, v88, v89\n" \
  "v_lshlrev_b32_sdwa v88, 1, v89 dst_sel:DWORD dst_unused:UNUSED_PAD src0_sel:DWORD src1_sel:WORD_0\n" \
  "v_lshlrev_b32_sdwa v89, 1, v89 dst_sel:DWORD dst_unused:UNUSED_PAD src0_sel:DWORD src1_sel:WORD_1\n" \
  "s_andn2_b32 s61, s80, 15\n" \
  "buffer_load_dwordx4 v[120:123], v8, %[xs], s61 offen\n" \
  "ds_read_u16_d16_hi v96, v88 offset:%[tb]\n" \
  "ds_read_u16_d16_hi v97, v89 offset:%[tb]\n" \
  "ds_read_b32 v111, v110 offset:16\n" \
  "s_waitcnt lgkmcnt(4)\n" \
  "s_set_gpr_idx_on s82, gpr_idx(SRC2,DST)\n" \
  "v_pk_fma_f32 v[32:33], v[100:101], v[72:73], v[32:33] op_sel:[0,0,0] op_sel_hi:[0,1,1]\n" \
  "v_pk_fma_f32 v[42:43], v[100:101], v[74:75], v[42:43] op_sel:[0,0,0] op_sel_hi:[0,1,1]\n" \
  "v_pk_fma_f32 v[52:53], v[102:103], v[76:77], v[52:53] op_sel:[0,0,0] op_sel_hi:[0,1,1]\n" \
  "v_pk_fma_f32 v[62:63], v[102:103], v[78:79], v[62:63] op_sel:[0,0,0] op_sel_hi:[0,1,1]\n" \
  "s_set_gpr_idx_off\n" \
  "s_waitcnt vmcnt(5)\n" \
  "v_mfma_f32_4x4x4_16b_bf16 v[72:75], v[108:109], v[132:133], 0 cbsz:4 abid:3\n" \
  "v_mfma_f32_4x4x4_16b_bf16 v[76:79], v[108:109], v[134:135], 0 cbsz:4 abid:3\n" \
  "v_readlane_b32 s81, v11, 1\n" \
  "v_cvt_pk_bf16_f32 v93, v92, v93\n" \
  "v_lshlrev_b32_sdwa v92, 1, v93 dst_sel:DWORD dst_unused:UNUSED_PAD src0_sel:DWORD src1_sel:WORD_0\n" \
  "v_lshlrev_b32_sdwa v93, 1, v93 dst_sel:DWORD dst_unused:UNUSED_PAD src0_sel:DWORD src1_sel:WORD_1\n" \
  "s_andn2_b32 s61, s81, 15\n" \
  "buffer_load_dwordx4 v[124:127], v8, %[xs], s61 offen\n" \
  "ds_read_u16_d16_hi v98, v92 offset:%[tb]\n" \
  "ds_read_u16_d16_hi v99, v93 offset:%[tb]\n" \
  "ds_read_b64 v[106:107], v14 offset:256\n" \
  "s_set_gpr_idx_on s83, gpr_idx(SRC2,DST)\n" \
  "v_pk_fma_f32 v[32:33], v[100:101], v[72:73], v[32:33] op_sel:[1,0,0] op_sel_hi:[1,1,1]\n" \
  "v_pk_fma_f32 v[42:43], v[100:101], v[74:75], v[42:43] op_sel:[1,0,0] op_sel_hi:[1,1,1]\n" \
  "v_pk_fma_f32 v[52:53], v[102:103], v[76:77], v[52:53] op_sel:[1,0,0] op_sel_hi:[1,1,1]\n" \
  "v_pk_fma_f32 v[62:63], v[102:103], v[78:79], v[62:63] op_sel:[1,0,0] op_sel_hi:[1,1,1]\n" \
  "s_set_gpr_idx_off\n" \
  "s_sub_u32 s60, s60, 1\n" \
  "s_cbranch_scc1 Lend%=\n" \
  "s_waitcnt vmcnt(5)\n" \
  "v_mfma_f32_4x4x4_16b_bf16 v[72:75], v[108:109], v[136:137], 0 cbsz:4 abid:4\n" \
  "v_mfma_f32_4x4x4_16b_bf16 v[76:79], v[108:109], v[138:139], 0 cbsz:4 abid:4\n" \
  "v_readlane_b32 s82, v11, 2\n" \
  "v_cvt_pk_bf16_f32 v91, v90, v91\n" \
  "v_lshlrev_b32_sdwa v90, 1, v91 dst_sel:DWORD dst_unused:UNUSED_PAD src0_sel:DWORD src1_sel:WORD_0\n" \
  "v_lshlrev_b32_sdwa v91, 1, v91 dst_sel:DWORD dst_unused:UNUSED_PAD src0_sel:DWORD src1_sel:WORD_1\n" \
  "s_andn2_b32 s61, s82, 15\n" \
  "buffer_load_dwordx4 v[128:131], v8, %[xs], s61 offen\n" \
  "ds_read_u16_d16_hi v100, v90 offset:%[tb]\n" \
  "ds_read_u16_d16_hi v101, v91 offset:%[tb]\n" \
  "v_add_u32 v14, 0x100, v14\n" \
  "s_waitcnt lgkmcnt(3)\n" \
  "s_set_gpr_idx_on s84, gpr_idx(SRC2,DST)\n" \
  "v_pk_fma_f32 v[32:33], v[96:97], v[72:73], v[32:33] op_sel:[0,0,0] op_sel_hi:[0,1,1]\n" \
  "v_pk_fma_f32 v[42:43], v[96:97], v[74:75], v[42:43] op_sel:[0,0,0] op_sel_hi:[0,1,1]\n" \
  "v_pk_fma_f32 v[52:53], v[98:99], v[76:77], v[52:53] op_sel:[0,0,0] op_sel_hi:[0,1,1]\n" \
  "v_pk_fma_f32 v[62:63], v[98:99], v[78:79], v[62:63] op_sel:[0,0,0] op_sel_hi:[0,1,1]\n" \
  "s_set_gpr_idx_off\n" \
  "s_waitcnt vmcnt(5)\n" \
  "v_mfma_f32_4x4x4_16b_bf16 v[72:75], v[108:109], v[140:141], 0 cbsz:4 abid:5\n" \
  "v_mfma_f32_4x4x4_16b_bf16 v[76:79], v[108:109], v[142:143], 0 cbsz:4 abid:5\n" \
  "v_mfma_f32_4x4x4_16b_bf16 v[80:83], v[104:105], v[28:29], 0 cbsz:4 abid:2\n" \
  "v_mfma_f32_4x4x4_16b_bf16 v[84:87], v[104:105], v[30:31], 0 cbsz:4 abid:2\n" \
  "v_readlane_b32 s83, v11, 3\n" \
  "v_cvt_pk_bf16_f32 v95, v94, v95\n" \
  "v_lshlrev_b32_sdwa v94, 1, v95 dst_sel:DWORD dst_unused:UNUSED_PAD src0_sel:DWORD src1_sel:WORD_0\n" \
  "v_lshlrev_b32_sdwa v95, 1, v95 dst_sel:DWORD dst_unused:UNUSED_PAD src0_sel:DWORD src1_sel:WORD_1\n" \
  "s_andn2_b32 s61, s83, 15\n" \
  "buffer_load_dwordx4 v[132:135], v8, %[xs], s61 offen\n" \
  "ds_read_u16_d16_hi v102, v94 offset:%[tb]\n" \
  "ds_read_u16_d16_hi v103, v95 offset:%[tb]\n" \
  "s_set_gpr_idx_on s85, gpr_idx(SRC2,DST)\n" \
  "v_pk_fma_f32 v[32:33], v[96:97], v[72:73], v[32:33] op_sel:[1,0,0] op_sel_hi:[1,1,1]\n" \
  "v_pk_fma_f32 v[42:43], v[96:97], v[74:75], v[42:43] op_sel:[1,0,0] op_sel_hi:[1,1,1]\n" \
  "v_pk_fma_f32 v[52:53], v[98:99], v[76:77], v[52:53] op_sel:[1,0,0] op_sel_hi:[1,1,1]\n" \
  "v_pk_fma_f32 v[62:63], v[98:99], v[78:79], v[62:63] op_sel:[1,0,0] op_sel_hi:[1,1,1]\n" \
  "s_set_gpr_idx_off\n" \
  "s_waitcnt vmcnt(5) lgkmcnt(7)\n" \
  "v_mfma_f32_4x4x4_16b_bf16 v[72:75], v[108:109], v[144:145], 0 cbsz:4 abid:6\n" \
  "v_mfma_f32_4x4x4_16b_bf16 v[76:79], v[108:109], v[146:147], 0 cbsz:4 abid:6\n" \
  "v_readlane_b32 s84, v11, 4\n" \
  "v_cvt_pk_bf16_f32 v81, v80, v81\n" \
  "v_lshlrev_b32_sdwa v80, 1, v81 dst_sel:DWORD dst_unused:UNUSED_PAD src0_sel:DWORD src1_sel:WORD_0\n" \
  "v_lshlrev_b32_sdwa v81, 1, v81 dst_sel:DWORD dst_unused:UNUSED_PAD src0_sel:DWORD src1_sel:WORD_1\n" \
  "s_andn2_b32 s61, s84, 15\n" \
  "buffer_load_dwordx4 v[136:139], v8, %[xs], s61 offen\n" \
  "ds_read_u16_d16_hi v96, v80 offset:%[tb]\n" \
  "ds_read_u16_d16_hi v97, v81 offset:%[tb]\n" \
  "ds_read_b32 v10, v12 offset:64\n" \
  "v_readfirstlane_b32 s66, v111\n" \
  "s_setprio 1\n" \
  "s_add_u32 s67, s60, %[fo]\n" \
  "s_cmp_ge_u32 s67, s66\n" \
  "s_cbranch_scc0 Lf5_%=\n" \
  "s_setprio 2\n" \
  "Lf5_%=:\n" \
  "s_waitcnt lgkmcnt(3)\n" \
  "s_set_gpr_idx_on s86, gpr_idx(SRC2,DST)\n" \
  "v_pk_fma_f32 v[32:33], v[100:101], v[72:73], v[32:33] op_sel:[0,0,0] op_sel_hi:[0,1,1]\n" \
  "v_pk_fma_f32 v[42:43], v[100:101], v[74:75], v[42:43] op_sel:[0,0,0] op_sel_hi:[0,1,1]\n" \
  "v_pk_fma_f32 v[52:53], v[102:103], v[76:77], v[52:53] op_sel:[0,0,0] op_sel_hi:[0,1,1]\n" \
  "v_pk_fma_f32 v[62:63], v[102:103], v[78:79], v[62:63] op_sel:[0,0,0] op_sel_hi:[0,1,1]\n" \
  "s_set_gpr_idx_off\n" \
  "s_waitcnt vmcnt(5) lgkmcnt(7)\n" \
  "v_mfma_f32_4x4x4_16b_bf16 v[72:75], v[108:109], v[148:149], 0 cbsz:4 abid:7\n" \
  "v_mfma_f32_4x4x4_16b_bf16 v[76:79], v[108:109], v[150:151], 0 cbsz:4 abid:7\n" \
  "v_mfma_f32_4x4x4_16b_bf16 v[112:115], v[106:107], v[20:21], 0\n" \
  "v_readlane_b32 s85, v11, 5\n" \
  "v_cvt_pk_bf16_f32 v85, v84, v85\n" \
  "v_lshlrev_b32_sdwa v84, 1, v85 dst_sel:DWORD dst_unused:UNUSED_PAD src0_sel:DWORD src1_sel:WORD_0\n" \
  "v_lshlrev_b32_sdwa v85, 1, v85 dst_sel:DWORD dst_unused:UNUSED_PAD src0_sel:DWORD src1_sel:WORD_1\n" \
  "s_andn2_b32 s61, s85, 15\n" \
  "buffer_load_dwordx4 v[140:143], v8, %[xs], s61 offen\n" \
  "ds_read_u16_d16_hi v98, v84 offset:%[tb]\n" \
  "ds_read_u16_d16_hi v99, v85 offset:%[tb]\n" \
  "v_add_u32 v12, 64, v12\n" \
  "s_set_gpr_idx_on s87, gpr_idx(SRC2,DST)\n" \
  "v_pk_fma_f32 v[32:33], v[100:101], v[72:73], v[32:33] op_sel:[1,0,0] op_sel_hi:[1,1,1]\n" \
  "v_pk_fma_f32 v[42:43], v[100:101], v[74:75], v[42:43] op_sel:[1,0,0] op_sel_hi:[1,1,1]\n" \
  "v_pk_fma_f32 v[52:53], v[102:103], v[76:77], v[52:53] op_sel:[1,0,0] op_sel_hi:[1,1,1]\n" \
  "v_pk_fma_f32 v[62:63], v[102:103], v[78:79], v[62:63] op_sel:[1,0,0] op_sel_hi:[1,1,1]\n" \
  "s_set_gpr_idx_off\n" \
  "s_sub_u32 s60, s60, 1\n" \
  "s_cbranch_scc1 Lend%=\n" \
  "s_waitcnt vmcnt(5)\n" \
  "v_mfma_f32_4x4x4_16b_bf16 v[72:75], v[108:109], v[120:121], 0 cbsz:4 abid:8\n" \
  "v_mfma_f32_4x4x4_16b_bf16 v[76:79], v[108:109], v[122:123], 0 cbsz:4 abid:8\n" \
  "v_readlane_b32 s86, v11, 6\n" \
  "v_cvt_pk_bf16_f32 v83, v82, v83\n" \
  "v_lshlrev_b32_sdwa v82, 1, v83 dst_sel:DWORD dst_unused:UNUSED_PAD src0_sel:DWORD src1_sel:WORD_0\n" \
  "v_lshlrev_b32_sdwa v83, 1, v83 dst_sel:DWORD dst_unused:UNUSED_PAD src0_sel:DWORD src1_sel:WORD_1\n" \
  "s_andn2_b32 s61, s86, 15\n" \
  "buffer_load_dwordx4 v[144:147], v8, %[xs], s61 offen\n" \
  "ds_read_u16_d16_hi v100, v82 offset:%[tb]\n" \
  "ds_read_u16_d16_hi v101, v83 offset:%[tb]\n" \
  "s_waitcnt lgkmcnt(2)\n" \
  "s_set_gpr_idx_on s80, gpr_idx(SRC2,DST)\n" \
  "v_pk_fma_f32 v[32:33], v[96:97], v[72:73], v[32:33] op_sel:[0,0,0] op_sel_hi:[0,1,1]\n" \
  "v_pk_fma_f32 v[42:43], v[96:97], v[74:75], v[42:43] op_sel:[0,0,0] op_sel_hi:[0,1,1]\n" \
  "v_pk_fma_f32 v[52:53], v[98:99], v[76:77], v[52:53] op_sel:[0,0,0] op_sel_hi:[0,1,1]\n" \
  "v_pk_fma_f32 v[62:63], v[98:99], v[78:79], v[62:63] op_sel:[0,0,0] op_sel_hi:[0,1,1]\n" \
  "s_set_gpr_idx_off\n" \
  "s_waitcnt vmcnt(5)\n" \
  "v_mfma_f32_4x4x4_16b_bf16 v[72:75], v[108:109], v[124:125], 0 cbsz:4 abid:9\n" \
  "v_mfma_f32_4x4x4_16b_bf16 v[76:79], v[108:109], v[126:127], 0 cbsz:4 abid:9\n" \
  "v_mfma_f32_4x4x4_16b_bf16 v[88:91], v[104:105], v[28:29], 0 cbsz:4 abid:3\n" \
  "v_mfma_f32_4x4x4_16b_bf16 v[92:95], v[104:105], v[30:31], 0 cbsz:4 abid:3\n" \
  "v_readlane_b32 s87, v11, 7\n" \
  "v_cvt_pk_bf16_f32 v87, v86, v87\n" \
  "v_lshlrev_b32_sdwa v86, 1, v87 dst_sel:DWORD dst_unused:UNUSED_PAD src0_sel:DWORD src1_sel:WORD_0\n" \
  "v_lshlrev_b32_sdwa v87, 1, v87 dst_sel:DWORD dst_unused:UNUSED_PAD src0_sel:DWORD src1_sel:WORD_1\n" \
  "s_andn2_b32 s61, s87, 15\n" \
  "buffer_load_dwordx4 v[148:151], v8, %[xs], s61 offen\n" \
  "ds_read_u16_d16_hi v102, v86 offset:%[tb]\n" \
  "ds_read_u16_d16_hi v103, v87 offset:%[tb]\n" \
  "v_mov_b32 v111, s60\n" \
  "ds_write2_b32 v110, v111, v111 offset1:8\n" \
  "s_set_gpr_idx_on s81, gpr_idx(SRC2,DST)\n" \
  "v_pk_fma_f32 v[32:33], v[96:97], v[72:73], v[32:33] op_sel:[1,0,0] op_sel_hi:[1,1,1]\n" \
  "v_pk_fma_f32 v[42:43], v[96:97], v[74:75], v[42:43] op_sel:[1,0,0] op_sel_hi:[1,1,1]\n" \
  "v_pk_fma_f32 v[52:53], v[98:99], v[76:77], v[52:53] op_sel:[1,0,0] op_sel_hi:[1,1,1]\n" \
  "v_pk_fma_f32 v[62:63], v[98:99], v[78:79], v[62:63] op_sel:[1,0,0] op_sel_hi:[1,1,1]\n" \
  "s_set_gpr_idx_off\n" \
  "s_waitcnt vmcnt(5) lgkmcnt(7)\n" \
  "v_mfma_f32_4x4x4_16b_bf16 v[72:75], v[108:109], v[128:129], 0 cbsz:4 abid:10\n" \
  "v_mfma_f32_4x4x4_16b_bf16 v[76:79], v[108:109], v[130:131], 0 cbsz:4 abid:10\n" \
  "v_readlane_b32 s80, v10, 0\n" \
  "v_cvt_pk_bf16_f32 v89, v88, v89\n" \
  "v_lshlrev_b32_sdwa v88, 1, v89 dst_sel:DWORD dst_unused:UNUSED_PAD src0_sel:DWORD src1_sel:WORD_0\n" \
  "v_lshlrev_b32_sdwa v89, 1, v89 dst_sel:DWORD dst_unused:UNUSED_PAD src0_sel:DWORD src1_sel:WORD_1\n" \
  "s_andn2_b32 s61, s80, 15\n" \
  "buffer_load_dwordx4 v[120:123], v8, %[xs], s61 offen\n" \
  "ds_read_u16_d16_hi v96, v88 offset:%[tb]\n" \
  "ds_read_u16_d16_hi v97, v89 offset:%[tb]\n" \
  "ds_read_b64 v[104:105], v15 offset:256\n" \
  "ds_read_b32 v111, v110 offset:16\n" \
  "s_waitcnt lgkmcnt(5)\n" \
  "s_set_gpr_idx_on s82, gpr_idx(SRC2,DST)\n" \
  "v_pk_fma_f32 v[32:33], v[100:101], v[72:73], v[32:33] op_sel:[0,0,0] op_sel_hi:[0,1,1]\n" \
  "v_pk_fma_f32 v[42:43], v[100:101], v[74:75], v[42:43] op_sel:[0,0,0] op_sel_hi:[0,1,1]\n" \
  "v_pk_fma_f32 v[52:53], v[102:103], v[76:77], v[52:53] op_sel:[0,0,0] op_sel_hi:[0,1,1]\n" \
  "v_pk_fma_f32 v[62:63], v[102:103], v[78:79], v[62:63] op_sel:[0,0,0] op_sel_hi:[0,1,1]\n" \
  "s_set_gpr_idx_off\n" \
  "s_waitcnt vmcnt(5)\n" \
  "v_mfma_f32_4x4x4_16b_bf16 v[72:75], v[108:109], v[132:133], 0 cbsz:4 abid:11\n" \
  "v_mfma_f32_4x4x4_16b_bf16 v[76:79], v[108:109], v[134:135], 0 cbsz:4 abid:11\n" \
  "v_mfma_f32_4x4x4_16b_bf16 v[116:119], v[106:107], v[22:23], 0\n" \
  "v_readlane_b32 s81, v10, 1\n" \
  "v_cvt_pk_bf16_f32 v93, v92, v93\n" \
  "v_lshlrev_b32_sdwa v92, 1, v93 dst_sel:DWORD dst_unused:UNUSED_PAD src0_sel:DWORD src1_sel:WORD_0\n" \
  "v_lshlrev_b32_sdwa v93, 1, v93 dst_sel:DWORD dst_unused:UNUSED_PAD src0_sel:DWORD src1_sel:WORD_1\n" \
  "s_andn2_b32 s61, s81, 15\n" \
  "buffer_load_dwordx4 v[124:127], v8, %[xs], s61 offen\n" \
  "ds_read_u16_d16_hi v98, v92 offset:%[tb]\n" \
  "ds_read_u16_d16_hi v99, v93 offset:%[tb]\n" \
  "s_set_gpr_idx_on s83, gpr_idx(SRC2,DST)\n" \
  "v_pk_fma_f32 v[32:33], v[100:101], v[72:73], v[32:33] op_sel:[1,0,0] op_sel_hi:[1,1,1]\n" \
  "v_pk_fma_f32 v[42:43], v[100:101], v[74:75], v[42:43] op_sel:[1,0,0] op_sel_hi:[1,1,1]\n" \
  "v_pk_fma_f32 v[52:53], v[102:103], v[76:77], v[52:53] op_sel:[1,0,0] op_sel_hi:[1,1,1]\n" \
  "v_pk_fma_f32 v[62:63], v[102:103], v[78:79], v[62:63] op_sel:[1,0,0] op_sel_hi:[1,1,1]\n" \
  "s_set_gpr_idx_off\n" \
  "s_sub_u32 s60, s60, 1\n" \
  "s_cbranch_scc1 Lend%=\n" \
  "s_waitcnt vmcnt(5)\n" \
  "v_mfma_f32_4x4x4_16b_bf16 v[72:75], v[108:109], v[136:137], 0 cbsz:4 abid:12\n" \
  "v_mfma_f32_4x4x4_16b_bf16 v[76:79], v[108:109], v[138:139], 0 cbsz:4 abid:12\n" \
  "v_readlane_b32 s82, v10, 2\n" \
  "v_cvt_pk_bf16_f32 v91, v90, v91\n" \
  "v_lshlrev_b32_sdwa v90, 1, v91 dst_sel:DWORD dst_unused:UNUSED_PAD src0_sel:DWORD src1_sel:WORD_0\n" \
  "v_lshlrev_b32_sdwa v91, 1, v91 dst_sel:DWORD dst_unused:UNUSED_PAD src0_sel:DWORD src1_sel:WORD_1\n" \
  "s_andn2_b32 s61, s82, 15\n" \
  "buffer_load_dwordx4 v[128:131], v8, %[xs], s61 offen\n" \
  "ds_read_u16_d16_hi v100, v90 offset:%[tb]\n" \
  "ds_read_u16_d16_hi v101, v91 offset:%[tb]\n" \
  "v_add_u32 v15, 0x100, v15\n" \
  "s_waitcnt lgkmcnt(2)\n" \
  "s_set_gpr_idx_on s84, gpr_idx(SRC2,DST)\n" \
  "v_pk_fma_f32 v[32:33], v[96:97], v[72:73], v[32:33] op_sel:[0,0,0] op_sel_hi:[0,1,1]\n" \
  "v_pk_fma_f32 v[42:43], v[96:97], v[74:75], v[42:43] op_sel:[0,0,0] op_sel_hi:[0,1,1]\n" \
  "v_pk_fma_f32 v[52:53], v[98:99], v[76:77], v[52:53] op_sel:[0,0,0] op_sel_hi:[0,1,1]\n" \
  "v_pk_fma_f32 v[62:63], v[98:99], v[78:79], v[62:63] op_sel:[0,0,0] op_sel_hi:[0,1,1]\n" \
  "s_set_gpr_idx_off\n" \
  "s_waitcnt vmcnt(5) lgkmcnt(5)\n" \
  "v_mfma_f32_4x4x4_16b_bf16 v[72:75], v[108:109], v[140:141], 0 cbsz:4 abid:13\n" \
  "v_mfma_f32_4x4x4_16b_bf16 v[76:79], v[108:109], v[142:143], 0 cbsz:4 abid:13\n" \
  "v_mfma_f32_4x4x4_16b_bf16 v[80:83], v[104:105], v[28:29], 0 cbsz:4\n" \
  "v_mfma_f32_4x4x4_16b_bf16 v[84:87], v[104:105], v[30:31], 0 cbsz:4\n" \
  "v_readlane_b32 s83, v10, 3\n" \
  "v_cvt_pk_bf16_f32 v95, v94, v95\n" \
  "v_lshlrev_b32_sdwa v94, 1, v95 dst_sel:DWORD dst_unused:UNUSED_PAD src0_sel:DWORD src1_sel:WORD_0\n" \
  "v_lshlrev_b32_sdwa v95, 1, v95 dst_sel:DWORD dst_unused:UNUSED_PAD src0_sel:DWORD src1_sel:WORD_1\n" \
  "s_andn2_b32 s61, s83, 15\n" \
  "buffer_load_dwordx4 v[132:135], v8, %[xs], s61 offen\n" \
  "ds_read_u16_d16_hi v102, v94 offset:%[tb]\n" \
  "ds_read_u16_d16_hi v103, v95 offset:%[tb]\n" \
  "ds_read_b32 v11, v12 offset:32\n" \
  "s_set_gpr_idx_on s85, gpr_idx(SRC2,DST)\n" \
  "v_pk_fma_f32 v[32:33], v[96:97], v[72:73], v[32:33] op_sel:[1,0,0] op_sel_hi:[1,1,1]\n" \
  "v_pk_fma_f32 v[42:43], v[96:97], v[74:75], v[42:43] op_sel:[1,0,0] op_sel_hi:[1,1,1]\n" \
  "v_pk_fma_f32 v[52:53], v[98:99], v[76:77], v[52:53] op_sel:[1,0,0] op_sel_hi:[1,1,1]\n" \
  "v_pk_fma_f32 v[62:63], v[98:99], v[78:79], v[62:63] op_sel:[1,0,0] op_sel_hi:[1,1,1]\n" \
  "s_set_gpr_idx_off\n" \
  "s_waitcnt vmcnt(5) lgkmcnt(7)\n" \
  "v_mfma_f32_4x4x4_16b_bf16 v[72:75], v[108:109], v[144:145], 0 cbsz:4 abid:14\n" \
  "v_mfma_f32_4x4x4_16b_bf16 v[76:79], v[108:109], v[146:147], 0 cbsz:4 abid:14\n" \
  "v_readlane_b32 s84, v10, 4\n" \
  "v_cvt_pk_bf16_f32 v81, v80, v81\n" \
  "v_lshlrev_b32_sdwa v80, 1, v81 dst_sel:DWORD dst_unused:UNUSED_PAD src0_sel:DWORD src1_sel:WORD_0\n" \
  "v_lshlrev_b32_sdwa v81, 1, v81 dst_sel:DWORD dst_unused:UNUSED_PAD src0_sel:DWORD src1_sel:WORD_1\n" \
  "s_andn2_b32 s61, s84, 15\n" \
  "buffer_load_dwordx4 v[136:139], v8, %[xs], s61 offen\n" \
  "ds_read_u16_d16_hi v96, v80 offset:%[tb]\n" \
  "ds_read_u16_d16_hi v97, v81 offset:%[tb]\n" \
  "v_readfirstlane_b32 s66, v111\n" \
  "s_setprio 1\n" \
  "s_add_u32 s67, s60, %[fo]\n" \
  "s_cmp_ge_u32 s67, s66\n" \
  "s_cbranch_scc0 Lf13_%=\n" \
  "s_setprio 2\n" \
  "Lf13_%=:\n" \
  "v_cvt_pk_bf16_f32 v19, v112, v116\n" \
  "s_waitcnt lgkmcnt(3)\n" \
  "s_set_gpr_idx_on s86, gpr_idx(SRC2,DST)\n" \
  "v_pk_fma_f32 v[32:33], v[100:101], v[72:73], v[32:33] op_sel:[0,0,0] op_sel_hi:[0,1,1]\n" \
  "v_pk_fma_f32 v[42:43], v[100:101], v[74:75], v[42:43] op_sel:[0,0,0] op_sel_hi:[0,1,1]\n" \
  "v_pk_fma_f32 v[52:53], v[102:103], v[76:77], v[52:53] op_sel:[0,0,0] op_sel_hi:[0,1,1]\n" \
  "v_pk_fma_f32 v[62:63], v[102:103], v[78:79], v[62:63] op_sel:[0,0,0] op_sel_hi:[0,1,1]\n" \
  "s_set_gpr_idx_off\n" \
  "s_waitcnt vmcnt(5)\n" \
  "v_mfma_f32_4x4x4_16b_bf16 v[112:115], v[106:107], v[24:25], 0\n" \
  "v_mfma_f32_4x4x4_16b_bf16 v[116:119], v[106:107], v[26:27], 0\n" \
  "v_mfma_f32_4x4x4_16b_bf16 v[72:75], v[108:109], v[148:149], 0 cbsz:4 abid:15\n" \
  "v_mfma_f32_4x4x4_16b_bf16 v[76:79], v[108:109], v[150:151], 0 cbsz:4 abid:15\n" \
  "v_readlane_b32 s85, v10, 5\n" \
  "v_cvt_pk_bf16_f32 v85, v84, v85\n" \
  "v_lshlrev_b32_sdwa v84, 1, v85 dst_sel:DWORD dst_unused:UNUSED_PAD src0_sel:DWORD src1_sel:WORD_0\n" \
  "v_lshlrev_b32_sdwa v85, 1, v85 dst_sel:DWORD dst_unused:UNUSED_PAD src0_sel:DWORD src1_sel:WORD_1\n" \
  "s_andn2_b32 s61, s85, 15\n" \
  "buffer_load_dwordx4 v[140:143], v8, %[xs], s61 offen\n" \
  "ds_read_u16_d16_hi v98, v84 offset:%[tb]\n" \
  "ds_read_u16_d16_hi v99, v85 offset:%[tb]\n" \
  "v_cvt_pk_bf16_f32 v109, v112, v116\n" \
  "v_mov_b32 v108, v19\n" \
  "s_set_gpr_idx_on s87, gpr_idx(SRC2,DST)\n" \
  "v_pk_fma_f32 v[32:33], v[100:101], v[72:73], v[32:33] op_sel:[1,0,0] op_sel_hi:[1,1,1]\n" \
  "v_pk_fma_f32 v[42:43], v[100:101], v[74:75], v[42:43] op_sel:[1,0,0] op_sel_hi:[1,1,1]\n" \
  "v_pk_fma_f32 v[52:53], v[102:103], v[76:77], v[52:53] op_sel:[1,0,0] op_sel_hi:[1,1,1]\n" \
  "v_pk_fma_f32 v[62:63], v[102:103], v[78:79], v[62:63] op_sel:[1,0,0] op_sel_hi:[1,1,1]\n" \
  "s_set_gpr_idx_off\n" \
  "s_sub_u32 s60, s60, 1\n" \
  "s_cbranch_scc1 Lend%=\n" \
  "s_waitcnt vmcnt(5)\n" \
  "v_mfma_f32_4x4x4_16b_bf16 v[72:75], v[108:109], v[120:121], 0 cbsz:4\n" \
  "v_mfma_f32_4x4x4_16b_bf16 v[76:79], v[108:109], v[122:123], 0 cbsz:4\n" \
  "v_readlane_b32 s86, v10, 6\n" \
  "v_cvt_pk_bf16_f32 v83, v82, v83\n" \
  "v_lshlrev_b32_sdwa v82, 1, v83 dst_sel:DWORD dst_unused:UNUSED_PAD src0_sel:DWORD src1_sel:WORD_0\n" \
  "v_lshlrev_b32_sdwa v83, 1, v83 dst_sel:DWORD dst_unused:UNUSED_PAD src0_sel:DWORD src1_sel:WORD_1\n" \
  "s_andn2_b32 s61, s86, 15\n" \
  "buffer_load_dwordx4 v[144:147], v8, %[xs], s61 offen\n" \
  "ds_read_u16_d16_hi v100, v82 offset:%[tb]\n" \
  "ds_read_u16_d16_hi v101, v83 offset:%[tb]\n" \
  "s_branch Lbody%=\n" \
  "Lend%=:\n" \
  "s_setprio 3\n" \
  "v_mov_b32 v20, 0\n" \
  "v_mov_b32 v21, %[bit]\n" \
  "v_cvt_pk_bf16_f32 v32, v32, v52\n" \
  "v_cvt_pk_bf16_f32 v33, v33, v53\n" \
  "v_cvt_pk_bf16_f32 v42, v42, v62\n" \
  "v_cvt_pk_bf16_f32 v43, v43, v63\n" \
  "v_cvt_pk_bf16_f32 v34, v34, v54\n" \
  "v_cvt_pk_bf16_f32 v35, v35, v55\n" \
  "v_cvt_pk_bf16_f32 v44, v44, v64\n" \
  "v_cvt_pk_bf16_f32 v45, v45, v65\n" \
  "ds_write2st64_b32 v152, v32, v34 offset0:0 offset1:4\n" \
  "ds_write2st64_b32 v153, v33, v35 offset0:0 offset1:4\n" \
  "ds_write2st64_b32 v154, v42, v44 offset0:0 offset1:4\n" \
  "ds_write2st64_b32 v155, v43, v45 offset0:0 offset1:4\n" \
  "v_cvt_pk_bf16_f32 v36, v36, v56\n" \
  "v_cvt_pk_bf16_f32 v37, v37, v57\n" \
  "v_cvt_pk_bf16_f32 v46, v46, v66\n" \
  "v_cvt_pk_bf16_f32 v47, v47, v67\n" \
  "v_cvt_pk_bf16_f32 v38, v38, v58\n" \
  "v_cvt_pk_bf16_f32 v39, v39, v59\n" \
  "v_cvt_pk_bf16_f32 v48, v48, v68\n" \
  "v_cvt_pk_bf16_f32 v49, v49, v69\n" \
  "ds_write2st64_b32 v152, v36, v38 offset0:8 offset1:12\n" \
  "ds_write2st64_b32 v153, v37, v39 offset0:8 offset1:12\n" \
  "ds_write2st64_b32 v154, v46, v48 offset0:8 offset1:12\n" \
  "ds_write2st64_b32 v155, v47, v49 offset0:8 offset1:12\n" \
  "v_cvt_pk_bf16_f32 v40, v40, v60\n" \
  "v_cvt_pk_bf16_f32 v41, v41, v61\n" \
  "v_cvt_pk_bf16_f32 v50, v50, v70\n" \
  "v_cvt_pk_bf16_f32 v51, v51, v71\n" \
  "ds_write2_b32 v156, v40, v41 offset0:0 offset1:16\n" \
  "ds_write2_b32 v156, v50, v51 offset0:32 offset1:48\n" \
  "s_mov_b64 exec, 1\n" \
  "ds_or_b32 v20, v21 offset:%[rd]\n" \
  "s_endpgm\n" \
  "Lhr%=:\n" \
  "s_mov_b64 exec, vcc\n" \
  "s_sub_u32 s63, s63, 1\n" \
  "s_cbranch_scc0 Lh%=\n" \
  "s_mov_b64 exec, -1\n" \
  "Lover%=:\n" \
  "v_mov_b32 v113, 1\n" \
  "ds_write_b32 v117, v113 offset:%[fl]\n" \
  "s_mov_b32 s62, 0\n" \
  "v_mov_b32 v114, 0\n" \
  "v_mov_b32 v115, 0\n" \
  "s_branch Lld%=\n" \
  "Lkr%=:\n" \
  "buffer_load_dword v84, v76, %[ws], %[rk] offen sc1\n" \
  "buffer_load_dword v85, v77, %[ws], %[rk] offen sc1\n" \
  "buffer_load_dword v86, v78, %[ws], %[rk] offen sc1\n" \
  "s_cmpk_le_u32 s62, 0xc0\n" \
  "s_cbranch_scc1 Lkb%=\n" \
  "buffer_load_dword v87, v79, %[ws], %[rk] offen sc1\n" \
  "Lkb%=:\n" \
  "s_waitcnt vmcnt(0)\n" \
  "s_sub_u32 s63, s63, 1\n" \
  "s_cbranch_scc0 Lk%=\n" \
  "v_mov_b32 v113, 1\n" \
  "ds_write_b32 v117, v113 offset:%[fl]\n" \
  "s_branch Lkc%=\n" \
  "Lc3%=:\n" \
  "v_lshlrev_b32 v91, 1, v87\n" \
  "v_lshlrev_b32 v95, 4, v87\n" \
  "v_lshrrev_b32 v107, 2, v87\n" \
  "v_and_or_b32 v91, v91, v118, %[wb]\n" \
  "v_lshlrev_b32_e64 v103, v95, 4\n" \
  "v_and_b32 v107, 0xfc, v107\n" \
  "s_mov_b64 exec, s[72:73]\n" \
  "ds_add_rtn_u32 v99, v91, v103 offset:%[hi]\n" \
  "ds_add_u32 v107, v116 offset:%[dg]\n" \
  "s_mov_b64 exec, -1\n" \
  "s_branch Lc4%=\n" \
  "Llo%=:\n" \
  "v_mov_b32 v113, 1\n" \
  "ds_write_b32 v117, v113 offset:%[fl]\n" \
  "s_branch Llc%=\n" \
  "Lslow%=:\n" \
  "s_waitcnt vmcnt(0) lgkmcnt(0)\n" \
  "s_barrier\n" \
  "s_branch Lexit%=\n" \
  "Lq3%=:\n" \
  "s_mov_b64 exec, -1\n" \
  "v_and_b32 v103, 0x7fc, v91\n" \
  "ds_read_b32 v79, v91 offset:%[hi]\n" \
  "ds_read_b32 v107, v103 offset:%[to]\n" \
  "s_waitcnt lgkmcnt(0)\n" \
  "v_add3_u32 v91, v79, v107, v99\n" \
  "v_lshrrev_b32 v91, v95, v91\n" \
  "v_bfe_u32 v75, v91, 13, 3\n" \
  "v_and_b32 v91, 0x1ffc, v91\n" \
  "v_lshl_or_b32 v75, v75, 1, v87\n" \
  "s_mov_b64 exec, s[72:73]\n" \
  "ds_write_b32 v91, v75 offset:%[so]\n" \
  "s_branch Lq4%=\n" \
  "Lpr%=:\n" \
  "v_mov_b32 v44, 0\n" \
  "v_mov_b32 v46, 0\n" \
  "s_cmpk_le_u32 s62, 0xc0\n" \
  "s_cbranch_scc1 Lpb%=\n" \
  "buffer_load_dwordx4 v[44:47], v83, %[ws], %[rp] offen sc1\n" \
  "Lpb%=:\n" \
  "buffer_load_dwordx4 v[32:35], v80, %[ws], %[rp] offen sc1\n" \
  "buffer_load_dwordx4 v[36:39], v81, %[ws], %[rp] offen sc1\n" \
  "buffer_load_dwordx4 v[40:43], v82, %[ws], %[rp] offen sc1\n" \
  "s_sub_u32 s63, s63, 1\n" \
  "s_waitcnt vmcnt(0)\n" \
  "s_cbranch_scc0 Lpc%=\n" \
  "s_branch Lpw%=\n" \
  "Lexit%=:\n" \
  "s_mov_b32 %[sl], s61\n"
#define STREAM_CLOBBERS \
  "v10", "v11", "v19", "v21", "v22", "v23", "v24", "v25", "v26", "v27", "v28", "v29", "v30", "v31", "v32", \
  "v33", "v34", "v35", "v36", "v37", "v38", "v39", "v40", "v41", "v42", "v43", "v44", "v45", "v46", "v47", \
  "v48", "v49", "v50", "v51", "v52", "v53", "v54", "v55", "v56", "v57", "v58", "v59", "v60", "v61", "v62", \
  "v63", "v64", "v65", "v66", "v67", "v68", "v69", "v70", "v71", "v72", "v73", "v74", "v75", "v76", "v77", \
  "v78", "v79", "v80", "v81", "v82", "v83", "v84", "v85", "v86", "v87", "v88", "v89", "v90", "v91", "v92", \
  "v93", "v94", "v95", "v96", "v97", "v98", "v99", "v100", "v101", "v102", "v103", "v104", "v105", "v106", \
  "v107", "v108", "v109", "v111", "v112", "v113", "v114", "v115", "v116", "v117", "v118", "v119", "v120", \
  "v121", "v122", "v123", "v124", "v125", "v126", "v127", "v128", "v129", "v130", "v131", "v132", "v133", \
  "v134", "v135", "s60", "s61", "s62", "s63", "s64", "s65", "s66", "s67", "s68", "s69", "s70", "s71", "s72", \
  "s73", "s74", "s75", "s76", "s77", "s78", "s79", "s80", "s81", "s82", "s83", "s84", "s85", "s86", "s87", \
  "scc", "vcc", "memory"
#define RKL 3
#define MIX_ASM \
  "v_mov_b32 v12, 0\n" \
  "s_movk_i32 s60, 0x3ff\n" \
  "Lz%=:\n" \
  "s_sleep %[sl]\n" \
  "ds_read_b32 v13, v1\n" \
  "ds_read_b32 v14, v12 offset:%[rd]\n" \
  "s_waitcnt lgkmcnt(0)\n" \
  "v_readfirstlane_b32 s61, v13\n" \
  "v_readfirstlane_b32 s62, v14\n" \
  "s_cmp_gt_u32 s61, 4\n" \
  "s_cselect_b32 s61, s62, 1\n" \
  "s_cmp_eq_u32 s61, 0\n" \
  "s_cbranch_scc1 Lz%=\n" \
  "s_setprio 1\n" \
  "Lp%=:\n" \
  "ds_read_b32 v13, v12 offset:%[rd]\n" \
  "s_waitcnt lgkmcnt(0)\n" \
  "v_readfirstlane_b32 s61, v13\n" \
  "s_andn2_b32 s62, 15, s61\n" \
  "s_cselect_b32 s62, 0, 0x100\n" \
  "s_andn2_b32 s63, 0xf0, s61\n" \
  "s_cselect_b32 s63, 0, 0x200\n" \
  "s_or_b32 s61, s61, s62\n" \
  "s_or_b32 s61, s61, s63\n" \
  "s_and_b32 s61, s61, s60\n" \
  "s_cbranch_scc1 Lgo%=\n" \
  "s_sleep 1\n" \
  "s_branch Lp%=\n" \
  "Lgo%=:\n" \
  "s_andn2_b32 s60, s60, s61\n" \
  "s_ff1_i32_b32 s62, s61\n" \
  "s_bitset0_b32 s61, s62\n" \
  "s_lshl_b32 s63, s62, 12\n" \
  "s_cmp_ge_u32 s62, 8\n" \
  "s_cselect_b32 s64, %[r5], 0\n" \
  "s_add_u32 s63, s63, s64\n" \
  "v_add_u32 v14, s63, v0\n" \
  "ds_read_b128 v[64:67], v14 offset:0\n" \
  "ds_read_b128 v[68:71], v14 offset:256\n" \
  "ds_read_b128 v[72:75], v14 offset:512\n" \
  "ds_read_b128 v[76:79], v14 offset:768\n" \
  "s_set_gpr_idx_on s62, gpr_idx(SRC0)\n" \
  "v_mov_b32 v16, v2\n" \
  "s_set_gpr_idx_off\n" \
  "s_cmp_eq_u32 s61, 0\n" \
  "s_cbranch_scc1 Ls%=\n" \
  "s_ff1_i32_b32 s62, s61\n" \
  "s_bitset0_b32 s61, s62\n" \
  "s_lshl_b32 s63, s62, 12\n" \
  "s_cmp_ge_u32 s62, 8\n" \
  "s_cselect_b32 s64, %[r5], 0\n" \
  "s_add_u32 s63, s63, s64\n" \
  "v_add_u32 v14, s63, v0\n" \
  "ds_read_b128 v[80:83], v14 offset:0\n" \
  "ds_read_b128 v[84:87], v14 offset:256\n" \
  "ds_read_b128 v[88:91], v14 offset:512\n" \
  "ds_read_b128 v[92:95], v14 offset:768\n" \
  "s_set_gpr_idx_on s62, gpr_idx(SRC0)\n" \
  "v_mov_b32 v17, v2\n" \
  "s_set_gpr_idx_off\n" \
  "s_waitcnt lgkmcnt(4)\n" \
  "v_mfma_f32_16x16x32_bf16 v[96:99], v[64:67], v[32:35], 0\n" \
  "v_mfma_f32_16x16x32_bf16 v[100:103], v[64:67], v[36:39], 0\n" \
  "v_mfma_f32_16x16x32_bf16 v[96:99], v[68:71], v[40:43], v[96:99]\n" \
  "v_mfma_f32_16x16x32_bf16 v[100:103], v[68:71], v[44:47], v[100:103]\n" \
  "v_mfma_f32_16x16x32_bf16 v[96:99], v[72:75], v[48:51], v[96:99]\n" \
  "v_mfma_f32_16x16x32_bf16 v[100:103], v[72:75], v[52:55], v[100:103]\n" \
  "v_mfma_f32_16x16x32_bf16 v[96:99], v[76:79], v[56:59], v[96:99]\n" \
  "v_mfma_f32_16x16x32_bf16 v[100:103], v[76:79], v[60:63], v[100:103]\n" \
  "Lb1_%=:\n" \
  "s_cmp_eq_u32 s61, 0\n" \
  "s_cbranch_scc1 Ll1_%=\n" \
  "s_ff1_i32_b32 s62, s61\n" \
  "s_bitset0_b32 s61, s62\n" \
  "s_lshl_b32 s63, s62, 12\n" \
  "s_cmp_ge_u32 s62, 8\n" \
  "s_cselect_b32 s64, %[r5], 0\n" \
  "s_add_u32 s63, s63, s64\n" \
  "v_add_u32 v14, s63, v0\n" \
  "ds_read_b128 v[64:67], v14 offset:0\n" \
  "ds_read_b128 v[68:71], v14 offset:256\n" \
  "ds_read_b128 v[72:75], v14 offset:512\n" \
  "ds_read_b128 v[76:79], v14 offset:768\n" \
  "s_waitcnt lgkmcnt(4)\n" \
  "v_mfma_f32_16x16x32_bf16 v[104:107], v[80:83], v[32:35], 0\n" \
  "v_mfma_f32_16x16x32_bf16 v[108:111], v[80:83], v[36:39], 0\n" \
  "v_cvt_pk_bf16_f32 v96, v96, v97\n" \
  "v_cvt_pk_bf16_f32 v97, v98, v99\n" \
  "v_mfma_f32_16x16x32_bf16 v[104:107], v[84:87], v[40:43], v[104:107]\n" \
  "v_cvt_pk_bf16_f32 v98, v100, v101\n" \
  "v_cvt_pk_bf16_f32 v99, v102, v103\n" \
  "v_mfma_f32_16x16x32_bf16 v[108:111], v[84:87], v[44:47], v[108:111]\n" \
  "buffer_store_dwordx4 v[96:99], v16, %[os], 0 offen sc1\n" \
  "v_mfma_f32_16x16x32_bf16 v[104:107], v[88:91], v[48:51], v[104:107]\n" \
  "v_mfma_f32_16x16x32_bf16 v[108:111], v[88:91], v[52:55], v[108:111]\n" \
  "s_set_gpr_idx_on s62, gpr_idx(SRC0)\n" \
  "v_mov_b32 v16, v2\n" \
  "s_set_gpr_idx_off\n" \
  "v_mfma_f32_16x16x32_bf16 v[104:107], v[92:95], v[56:59], v[104:107]\n" \
  "v_mfma_f32_16x16x32_bf16 v[108:111], v[92:95], v[60:63], v[108:111]\n" \
  "Lb0_%=:\n" \
  "s_cmp_eq_u32 s61, 0\n" \
  "s_cbranch_scc1 Ll0_%=\n" \
  "s_ff1_i32_b32 s62, s61\n" \
  "s_bitset0_b32 s61, s62\n" \
  "s_lshl_b32 s63, s62, 12\n" \
  "s_cmp_ge_u32 s62, 8\n" \
  "s_cselect_b32 s64, %[r5], 0\n" \
  "s_add_u32 s63, s63, s64\n" \
  "v_add_u32 v14, s63, v0\n" \
  "ds_read_b128 v[80:83], v14 offset:0\n" \
  "ds_read_b128 v[84:87], v14 offset:256\n" \
  "ds_read_b128 v[88:91], v14 offset:512\n" \
  "ds_read_b128 v[92:95], v14 offset:768\n" \
  "s_waitcnt lgkmcnt(4)\n" \
  "v_mfma_f32_16x16x32_bf16 v[96:99], v[64:67], v[32:35], 0\n" \
  "v_mfma_f32_16x16x32_bf16 v[100:103], v[64:67], v[36:39], 0\n" \
  "v_cvt_pk_bf16_f32 v104, v104, v105\n" \
  "v_cvt_pk_bf16_f32 v105, v106, v107\n" \
  "v_mfma_f32_16x16x32_bf16 v[96:99], v[68:71], v[40:43], v[96:99]\n" \
  "v_cvt_pk_bf16_f32 v106, v108, v109\n" \
  "v_cvt_pk_bf16_f32 v107, v110, v111\n" \
  "v_mfma_f32_16x16x32_bf16 v[100:103], v[68:71], v[44:47], v[100:103]\n" \
  "buffer_store_dwordx4 v[104:107], v17, %[os], 0 offen sc1\n" \
  "v_mfma_f32_16x16x32_bf16 v[96:99], v[72:75], v[48:51], v[96:99]\n" \
  "v_mfma_f32_16x16x32_bf16 v[100:103], v[72:75], v[52:55], v[100:103]\n" \
  "s_set_gpr_idx_on s62, gpr_idx(SRC0)\n" \
  "v_mov_b32 v17, v2\n" \
  "s_set_gpr_idx_off\n" \
  "v_mfma_f32_16x16x32_bf16 v[96:99], v[76:79], v[56:59], v[96:99]\n" \
  "v_mfma_f32_16x16x32_bf16 v[100:103], v[76:79], v[60:63], v[100:103]\n" \
  "s_branch Lb1_%=\n" \
  "Ll1_%=:\n" \
  "s_waitcnt lgkmcnt(0)\n" \
  "v_mfma_f32_16x16x32_bf16 v[104:107], v[80:83], v[32:35], 0\n" \
  "v_mfma_f32_16x16x32_bf16 v[108:111], v[80:83], v[36:39], 0\n" \
  "v_mfma_f32_16x16x32_bf16 v[104:107], v[84:87], v[40:43], v[104:107]\n" \
  "v_mfma_f32_16x16x32_bf16 v[108:111], v[84:87], v[44:47], v[108:111]\n" \
  "v_mfma_f32_16x16x32_bf16 v[104:107], v[88:91], v[48:51], v[104:107]\n" \
  "v_cvt_pk_bf16_f32 v96, v96, v97\n" \
  "v_cvt_pk_bf16_f32 v97, v98, v99\n" \
  "v_mfma_f32_16x16x32_bf16 v[108:111], v[88:91], v[52:55], v[108:111]\n" \
  "v_cvt_pk_bf16_f32 v98, v100, v101\n" \
  "v_cvt_pk_bf16_f32 v99, v102, v103\n" \
  "v_mfma_f32_16x16x32_bf16 v[104:107], v[92:95], v[56:59], v[104:107]\n" \
  "buffer_store_dwordx4 v[96:99], v16, %[os], 0 offen sc1\n" \
  "v_mfma_f32_16x16x32_bf16 v[108:111], v[92:95], v[60:63], v[108:111]\n" \
  "s_cmp_lg_u32 s60, 0\n" \
  "s_nop 5\n" \
  "v_cvt_pk_bf16_f32 v104, v104, v105\n" \
  "v_cvt_pk_bf16_f32 v105, v106, v107\n" \
  "v_cvt_pk_bf16_f32 v106, v108, v109\n" \
  "v_cvt_pk_bf16_f32 v107, v110, v111\n" \
  "buffer_store_dwordx4 v[104:107], v17, %[os], 0 offen sc1\n" \
  "s_cbranch_scc1 Lp%=\n" \
  "s_branch Le%=\n" \
  "Ll0_%=:\n" \
  "s_waitcnt lgkmcnt(0)\n" \
  "v_mfma_f32_16x16x32_bf16 v[96:99], v[64:67], v[32:35], 0\n" \
  "v_mfma_f32_16x16x32_bf16 v[100:103], v[64:67], v[36:39], 0\n" \
  "v_mfma_f32_16x16x32_bf16 v[96:99], v[68:71], v[40:43], v[96:99]\n" \
  "v_mfma_f32_16x16x32_bf16 v[100:103], v[68:71], v[44:47], v[100:103]\n" \
  "v_mfma_f32_16x16x32_bf16 v[96:99], v[72:75], v[48:51], v[96:99]\n" \
  "v_cvt_pk_bf16_f32 v104, v104, v105\n" \
  "v_cvt_pk_bf16_f32 v105, v106, v107\n" \
  "v_mfma_f32_16x16x32_bf16 v[100:103], v[72:75], v[52:55], v[100:103]\n" \
  "v_cvt_pk_bf16_f32 v106, v108, v109\n" \
  "v_cvt_pk_bf16_f32 v107, v110, v111\n" \
  "v_mfma_f32_16x16x32_bf16 v[96:99], v[76:79], v[56:59], v[96:99]\n" \
  "buffer_store_dwordx4 v[104:107], v17, %[os], 0 offen sc1\n" \
  "v_mfma_f32_16x16x32_bf16 v[100:103], v[76:79], v[60:63], v[100:103]\n" \
  "s_cmp_lg_u32 s60, 0\n" \
  "s_nop 5\n" \
  "v_cvt_pk_bf16_f32 v96, v96, v97\n" \
  "v_cvt_pk_bf16_f32 v97, v98, v99\n" \
  "v_cvt_pk_bf16_f32 v98, v100, v101\n" \
  "v_cvt_pk_bf16_f32 v99, v102, v103\n" \
  "buffer_store_dwordx4 v[96:99], v16, %[os], 0 offen sc1\n" \
  "s_cbranch_scc1 Lp%=\n" \
  "s_branch Le%=\n" \
  "Ls%=:\n" \
  "s_waitcnt lgkmcnt(0)\n" \
  "v_mfma_f32_16x16x32_bf16 v[96:99], v[64:67], v[32:35], 0\n" \
  "v_mfma_f32_16x16x32_bf16 v[100:103], v[64:67], v[36:39], 0\n" \
  "v_mfma_f32_16x16x32_bf16 v[96:99], v[68:71], v[40:43], v[96:99]\n" \
  "v_mfma_f32_16x16x32_bf16 v[100:103], v[68:71], v[44:47], v[100:103]\n" \
  "v_mfma_f32_16x16x32_bf16 v[96:99], v[72:75], v[48:51], v[96:99]\n" \
  "v_mfma_f32_16x16x32_bf16 v[100:103], v[72:75], v[52:55], v[100:103]\n" \
  "v_mfma_f32_16x16x32_bf16 v[96:99], v[76:79], v[56:59], v[96:99]\n" \
  "v_mfma_f32_16x16x32_bf16 v[100:103], v[76:79], v[60:63], v[100:103]\n" \
  "s_cmp_lg_u32 s60, 0\n" \
  "s_nop 5\n" \
  "v_cvt_pk_bf16_f32 v96, v96, v97\n" \
  "v_cvt_pk_bf16_f32 v97, v98, v99\n" \
  "v_cvt_pk_bf16_f32 v98, v100, v101\n" \
  "v_cvt_pk_bf16_f32 v99, v102, v103\n" \
  "buffer_store_dwordx4 v[96:99], v16, %[os], 0 offen sc1\n" \
  "s_cbranch_scc1 Lp%=\n" \
  "Le%=:\n"
#define MIX_CLOBBERS \
  "v16", "v17", "v64", "v65", "v66", "v67", "v68", "v69", "v70", "v71", "v72", "v73", "v74", "v75", "v76", \
  "v77", "v78", "v79", "v80", "v81", "v82", "v83", "v84", "v85", "v86", "v87", "v88", "v89", "v90", "v91", \
  "v92", "v93", "v94", "v95", "v96", "v97", "v98", "v99", "v100", "v101", "v102", "v103", "v104", "v105", \
  "v106", "v107", "v108", "v109", "v110", "v111", "s60", "s61", "s62", "s63", "s64", "scc", "vcc", "memory"
#define PART_ASM \
  "s_load_dwordx16 s[8:23], %[kp], 0\n" \
  "s_load_dwordx8 s[24:31], %[kp], 0x40\n" \
  "s_load_dwordx4 s[32:35], %[kp], 0x60\n" \
  "v_readfirstlane_b32 s36, %[tid]\n" \
  "v_lshlrev_b32 v90, 3, %[tid]\n" \
  "v_mov_b64 v[92:93], 0\n" \
  "s_and_b32 s37, %[bid], 7\n" \
  "s_lshr_b32 s38, %[bid], 3\n" \
  "s_lshr_b32 s36, s36, 6\n" \
  "ds_write_b64 v90, v[92:93] offset:%[sg]\n" \
  "s_cmp_lg_u32 s36, %[pw]\n" \
  "s_cbranch_scc1 1f\n" \
  "s_getpc_b64 s[44:45]\n" \
  ".Ltfn_p0:\n" \
  "s_max_i32 s46, 0, .Ltfn_p1-.Ltfn_p0\n" \
  "s_mov_b32 m0, %[sn]\n" \
  "s_mov_b32 s47, 0x20000\n" \
  "v_lshlrev_b32 v91, 12, %[tid]\n" \
  "s_lshl_b32 s39, s38, 7\n" \
  "v_and_b32 v91, 0x3f000, v91\n" \
  "buffer_load_dword v91, s[44:47], s39 offen lds\n" \
  "1:\n" \
  "s_lshl_b32 s37, s37, 5\n" \
  "s_add_u32 s37, s37, s38\n" \
  "v_lshlrev_b32 v91, 4, %[tid]\n" \
  "s_waitcnt lgkmcnt(0)\n" \
  "s_mul_i32 s38, s37, s14\n" \
  "s_mov_b32 s40, s10\n" \
  "s_add_i32 s39, s38, s14\n" \
  "s_lshl_b32 s44, s38, 2\n" \
  "s_min_i32 s39, s39, s10\n" \
  "s_add_i32 s45, s44, %[h4]\n" \
  "s_lshl_b32 s10, s39, 2\n" \
  "s_lshl_b32 s14, s39, 2\n" \
  "s_cmp_ge_u32 s36, %[pw]\n" \
  "s_cbranch_scc1 2f\n" \
  "buffer_load_dwordx2 v[40:41], v90, s[8:11], s44 offen\n" \
  "buffer_load_dwordx2 v[42:43], v90, s[8:11], s45 offen\n" \
  "buffer_load_dwordx2 v[44:45], v90, s[12:15], s44 offen\n" \
  "buffer_load_dwordx2 v[46:47], v90, s[12:15], s45 offen\n" \
  "2:\n" \
  "s_mov_b32 s41, s18\n" \
  "s_mov_b32 s42, s22\n" \
  "s_lshl_b32 s18, s39, 3\n" \
  "s_lshl_b32 s22, s39, 3\n" \
  "s_lshl_b32 s44, s44, 1\n" \
  "s_lshl_b32 s45, s45, 1\n" \
  "s_cmp_ge_u32 s36, %[pw]\n" \
  "s_barrier\n" \
  "s_cbranch_scc0 3f\n" \
  "s_cmp_lg_u32 s36, %[hw]\n" \
  "s_cbranch_scc1 4f\n" \
  "s_load_dwordx2 s[46:47], s[26:27], 0\n" \
  "s_branch Lpx%=\n" \
  "4:\n" \
  "s_barrier\n" \
  "s_branch Lpx%=\n" \
  "3:\n" \
  "s_setprio 3\n" \
  "buffer_load_dwordx4 v[100:103], v91, s[16:19], s44 offen\n" \
  "buffer_load_dwordx4 v[104:107], v91, s[16:19], s45 offen\n" \
  "buffer_load_dwordx4 v[108:111], v91, s[20:23], s44 offen\n" \
  "buffer_load_dwordx4 v[112:115], v91, s[20:23], s45 offen\n" \
  "s_sub_i32 s56, s39, s38\n" \
  "v_lshlrev_b32 v72, 1, %[tid]\n" \
  "s_lshl_b32 s57, s36, 9\n" \
  "v_cmp_gt_i32_e64 s[48:49], s56, v72\n" \
  "s_sub_i32 s58, s56, 1\n" \
  "v_cmp_gt_i32_e64 s[50:51], s58, v72\n" \
  "s_sub_i32 s58, s56, %[h1]\n" \
  "v_cmp_gt_i32_e64 s[52:53], s58, v72\n" \
  "s_sub_i32 s58, s56, %[h1]+1\n" \
  "v_cmp_gt_i32_e64 s[54:55], s58, v72\n" \
  "v_and_b32 v88, 63, %[tid]\n" \
  "v_mov_b32 v95, 0x1fc\n" \
  "v_mov_b64 v[76:77], 0\n" \
  "v_lshlrev_b32 v88, 2, v88\n" \
  "v_mov_b32 v96, 0\n" \
  "v_mov_b32 v97, 1\n" \
  "v_add_u32 v89, s57, v88\n" \
  "v_add_u32 v88, %[ph], v88\n" \
  "v_add_u32 v89, %[pb], v89\n" \
  "s_lshl_b32 s58, s36, 1\n" \
  "s_mov_b32 s59, 0x38000000\n" \
  "s_waitcnt vmcnt(6)\n" \
  "v_mul_u32_u24 v48, s42, v40\n" \
  "v_mul_u32_u24 v49, s42, v41\n" \
  "v_mul_u32_u24 v50, s42, v42\n" \
  "v_mul_u32_u24 v51, s42, v43\n" \
  "v_lshrrev_b32 v52, 22, v48\n" \
  "v_lshrrev_b32 v53, 22, v49\n" \
  "v_lshrrev_b32 v54, 22, v50\n" \
  "v_lshrrev_b32 v55, 22, v51\n" \
  "v_lshrrev_b32 v56, 21, v48\n" \
  "v_lshrrev_b32 v57, 21, v49\n" \
  "v_lshrrev_b32 v58, 21, v50\n" \
  "v_lshrrev_b32 v59, 21, v51\n" \
  "v_lshlrev_b32 v60, 4, v52\n" \
  "v_lshlrev_b32 v61, 4, v53\n" \
  "v_lshlrev_b32 v62, 4, v54\n" \
  "v_lshlrev_b32 v63, 4, v55\n" \
  "v_and_or_b32 v56, v56, v95, s57\n" \
  "v_and_or_b32 v57, v57, v95, s57\n" \
  "v_and_or_b32 v58, v58, v95, s57\n" \
  "v_and_or_b32 v59, v59, v95, s57\n" \
  "v_lshlrev_b32 v72, v60, v97\n" \
  "v_lshlrev_b32 v73, v61, v97\n" \
  "v_lshlrev_b32 v74, v62, v97\n" \
  "v_lshlrev_b32 v75, v63, v97\n" \
  "s_mov_b64 exec, s[48:49]\n" \
  "ds_add_rtn_u32 v64, v56, v72 offset:%[ph]\n" \
  "s_mov_b64 exec, s[50:51]\n" \
  "ds_add_rtn_u32 v65, v57, v73 offset:%[ph]\n" \
  "s_mov_b64 exec, s[52:53]\n" \
  "ds_add_rtn_u32 v66, v58, v74 offset:%[ph]\n" \
  "s_mov_b64 exec, s[54:55]\n" \
  "ds_add_rtn_u32 v67, v59, v75 offset:%[ph]\n" \
  "s_mov_b64 exec, -1\n" \
  "v_and_b32 v48, s59, v48\n" \
  "v_and_b32 v49, s59, v49\n" \
  "v_and_b32 v50, s59, v50\n" \
  "v_and_b32 v51, s59, v51\n" \
  "v_and_b32 v40, 63, v40\n" \
  "v_and_b32 v41, 63, v41\n" \
  "v_and_b32 v42, 63, v42\n" \
  "v_and_b32 v43, 63, v43\n" \
  "s_waitcnt vmcnt(4)\n" \
  "v_mul_u32_u24 v68, s42, v44\n" \
  "v_mul_u32_u24 v69, s42, v45\n" \
  "v_mul_u32_u24 v70, s42, v46\n" \
  "v_mul_u32_u24 v71, s42, v47\n" \
  "v_lshl_or_b32 v40, v44, 6, v40\n" \
  "v_lshl_or_b32 v41, v45, 6, v41\n" \
  "v_lshl_or_b32 v42, v46, 6, v42\n" \
  "v_lshl_or_b32 v43, v47, 6, v43\n" \
  "v_sub_u32 v68, v68, v48\n" \
  "v_sub_u32 v69, v69, v49\n" \
  "v_sub_u32 v70, v70, v50\n" \
  "v_sub_u32 v71, v71, v51\n" \
  "v_lshl_add_u32 v56, v52, 1, s57\n" \
  "v_lshl_add_u32 v57, v53, 1, s57\n" \
  "v_lshl_add_u32 v58, v54, 1, s57\n" \
  "v_lshl_add_u32 v59, v55, 1, s57\n" \
  "v_bfe_u32 v68, v68, 26, 4\n" \
  "v_bfe_u32 v69, v69, 26, 4\n" \
  "v_bfe_u32 v70, v70, 26, 4\n" \
  "v_bfe_u32 v71, v71, 26, 4\n" \
  "v_lshlrev_b32 v52, 2, v52\n" \
  "v_lshlrev_b32 v53, 2, v53\n" \
  "v_lshlrev_b32 v54, 2, v54\n" \
  "v_lshlrev_b32 v55, 2, v55\n" \
  "v_lshl_or_b32 v68, v40, 4, v68\n" \
  "v_lshl_or_b32 v69, v41, 4, v69\n" \
  "v_lshl_or_b32 v70, v42, 4, v70\n" \
  "v_lshl_or_b32 v71, v43, 4, v71\n" \
  "s_mov_b64 exec, s[48:49]\n" \
  "ds_add_u32 v52, v68 offset:%[sg]\n" \
  "s_mov_b64 exec, s[50:51]\n" \
  "ds_add_u32 v53, v69 offset:%[sg]\n" \
  "s_mov_b64 exec, s[52:53]\n" \
  "ds_add_u32 v54, v70 offset:%[sg]\n" \
  "s_mov_b64 exec, s[54:55]\n" \
  "ds_add_u32 v55, v71 offset:%[sg]\n" \
  "s_mov_b64 exec, -1\n" \
  "s_waitcnt lgkmcnt(0)\n" \
  "s_barrier\n" \
  "ds_read2st64_b32 v[78:79], v88 offset0:0 offset1:1\n" \
  "ds_read2st64_b32 v[80:81], v88 offset0:2 offset1:3\n" \
  "ds_read2st64_b32 v[82:83], v88 offset0:4 offset1:5\n" \
  "ds_read2st64_b32 v[84:85], v88 offset0:6 offset1:7\n" \
  "ds_read2st64_b32 v[86:87], v88 offset0:8 offset1:9\n" \
  "ds_read_b32 v94, v96 offset:%[tg]\n" \
  "v_bfe_u32 v64, v64, v60, 16\n" \
  "v_bfe_u32 v65, v65, v61, 16\n" \
  "v_bfe_u32 v66, v66, v62, 16\n" \
  "v_bfe_u32 v67, v67, v63, 16\n" \
  "s_mov_b32 s56, 0x05040100\n" \
  "s_mov_b32 s59, 0x07060302\n" \
  "s_waitcnt lgkmcnt(4)\n" \
  "v_add_u32 v80, v80, v78\n" \
  "v_add_u32 v81, v81, v79\n" \
  "s_waitcnt lgkmcnt(3)\n" \
  "v_add_u32 v82, v82, v80\n" \
  "v_add_u32 v83, v83, v81\n" \
  "s_waitcnt lgkmcnt(2)\n" \
  "v_add_u32 v84, v84, v82\n" \
  "v_add_u32 v85, v85, v83\n" \
  "s_waitcnt lgkmcnt(1)\n" \
  "v_add_u32 v86, v86, v84\n" \
  "v_add_u32 v87, v87, v85\n" \
  "v_perm_b32 v72, v87, v86, s56\n" \
  "v_perm_b32 v73, v87, v86, s59\n" \
  "v_add_u32 v72, v72, v73\n" \
  "s_set_gpr_idx_on s58, gpr_idx(SRC0)\n" \
  "v_mov_b32 v74, v76\n" \
  "v_mov_b32 v75, v77\n" \
  "s_set_gpr_idx_off\n" \
  "s_mov_b32 s56, 0x10001\n" \
  "v_add_u32_dpp v73, v72, v72 row_shr:1 row_mask:0xf bank_mask:0xf bound_ctrl:1\n" \
  "v_lshl_add_u32 v74, v86, 16, v74\n" \
  "v_lshl_add_u32 v75, v87, 16, v75\n" \
  "v_add_u32_dpp v73, v73, v73 row_shr:2 row_mask:0xf bank_mask:0xf bound_ctrl:1\n" \
  "s_waitcnt lgkmcnt(0)\n" \
  "v_lshl_or_b32 v68, v94, 24, v68\n" \
  "v_add_u32_dpp v73, v73, v73 row_shr:4 row_mask:0xf bank_mask:0xf bound_ctrl:1\n" \
  "v_lshl_or_b32 v69, v94, 24, v69\n" \
  "v_lshl_or_b32 v70, v94, 24, v70\n" \
  "v_add_u32_dpp v73, v73, v73 row_shr:8 row_mask:0xf bank_mask:0xf bound_ctrl:1\n" \
  "v_lshl_or_b32 v71, v94, 24, v71\n" \
  "s_nop 0\n" \
  "v_add_u32_dpp v73, v73, v73 row_bcast:15 row_mask:0xa bank_mask:0xf\n" \
  "s_nop 1\n" \
  "v_add_u32_dpp v73, v73, v73 row_bcast:31 row_mask:0xc bank_mask:0xf\n" \
  "v_sub_u32 v72, v73, v72\n" \
  "v_readlane_b32 s59, v73, 63\n" \
  "v_and_b32 v73, 0xffff, v72\n" \
  "v_lshrrev_b32 v72, 16, v72\n" \
  "s_and_b32 s59, s59, 0xffff\n" \
  "v_mad_u32_u24 v74, v73, s56, v74\n" \
  "v_add_u32 v72, s59, v72\n" \
  "v_mad_u32_u24 v75, v72, s56, v75\n" \
  "ds_write2st64_b32 v89, v74, v75 offset1:1\n" \
  "ds_read_u16 v48, v56 offset:%[pb]\n" \
  "ds_read_u16 v49, v57 offset:%[pb]\n" \
  "ds_read_u16 v50, v58 offset:%[pb]\n" \
  "ds_read_u16 v51, v59 offset:%[pb]\n" \
  "s_mov_b32 s56, %[sp]\n" \
  "s_waitcnt lgkmcnt(0)\n" \
  "v_add_lshl_u32 v48, v48, v64, 2\n" \
  "v_add_lshl_u32 v49, v49, v65, 2\n" \
  "v_add_lshl_u32 v50, v50, v66, 2\n" \
  "v_add_lshl_u32 v51, v51, v67, 2\n" \
  "v_lshl_add_u32 v56, v48, 2, s56\n" \
  "v_lshl_add_u32 v57, v49, 2, s56\n" \
  "v_lshl_add_u32 v58, v50, 2, s56\n" \
  "v_lshl_add_u32 v59, v51, 2, s56\n" \
  "s_mov_b64 exec, s[48:49]\n" \
  "ds_write_b32 v48, v68 offset:%[sk]\n" \
  "s_mov_b64 exec, s[50:51]\n" \
  "ds_write_b32 v49, v69 offset:%[sk]\n" \
  "s_mov_b64 exec, s[52:53]\n" \
  "ds_write_b32 v50, v70 offset:%[sk]\n" \
  "s_mov_b64 exec, s[54:55]\n" \
  "ds_write_b32 v51, v71 offset:%[sk]\n" \
  "s_mov_b64 exec, -1\n" \
  "s_waitcnt vmcnt(0)\n" \
  "v_add_u32 v100, v100, v94\n" \
  "v_add_u32 v102, v102, v94\n" \
  "v_add_u32 v104, v104, v94\n" \
  "v_add_u32 v106, v106, v94\n" \
  "v_add_u32 v108, v108, v94\n" \
  "v_add_u32 v110, v110, v94\n" \
  "v_add_u32 v112, v112, v94\n" \
  "v_add_u32 v114, v114, v94\n" \
  "v_add3_u32 v72, v100, v101, v108\n" \
  "v_add3_u32 v73, v102, v103, v110\n" \
  "v_add3_u32 v74, v104, v105, v112\n" \
  "v_add3_u32 v75, v106, v107, v114\n" \
  "v_add_u32 v72, v72, v109\n" \
  "v_add_u32 v73, v73, v111\n" \
  "v_add_u32 v74, v74, v113\n" \
  "v_add_u32 v75, v75, v115\n" \
  "s_mov_b64 exec, s[48:49]\n" \
  "ds_add_u32 v52, v72 offset:%[sg]+1024\n" \
  "s_mov_b64 exec, s[50:51]\n" \
  "ds_add_u32 v53, v73 offset:%[sg]+1024\n" \
  "s_mov_b64 exec, s[52:53]\n" \
  "ds_add_u32 v54, v74 offset:%[sg]+1024\n" \
  "s_mov_b64 exec, s[54:55]\n" \
  "ds_add_u32 v55, v75 offset:%[sg]+1024\n" \
  "s_mov_b64 exec, 1\n" \
  "ds_add_u32 v96, v97 offset:%[go]\n" \
  "s_mov_b64 exec, -1\n" \
  "s_mov_b64 exec, s[48:49]\n" \
  "ds_write2_b64 v56, v[100:101], v[108:109] offset1:1\n" \
  "s_mov_b64 exec, s[50:51]\n" \
  "ds_write2_b64 v57, v[102:103], v[110:111] offset1:1\n" \
  "s_mov_b64 exec, s[52:53]\n" \
  "ds_write2_b64 v58, v[104:105], v[112:113] offset1:1\n" \
  "s_mov_b64 exec, s[54:55]\n" \
  "ds_write2_b64 v59, v[106:107], v[114:115] offset1:1\n" \
  "s_mov_b64 exec, -1\n" \
  "Lpx%=:\n"
#define PART_CLOBBERS \
  "v40", "v41", "v42", "v43", "v44", "v45", "v46", "v47", "v48", "v49", "v50", "v51", "v52", "v53", "v54", \
  "v55", "v56", "v57", "v58", "v59", "v60", "v61", "v62", "v63", "v64", "v65", "v66", "v67", "v68", "v69", \
  "v70", "v71", "v72", "v73", "v74", "v75", "v76", "v77", "v78", "v79", "v80", "v81", "v82", "v83", "v84", \
  "v85", "v86", "v87", "v88", "v89", "v90", "v91", "v92", "v93", "v94", "v95", "v96", "v97", "v98", "v99", \
  "v100", "v101", "v102", "v103", "v104", "v105", "v106", "v107", "v108", "v109", "v110", "v111", "v112", \
  "v113", "v114", "v115", "s44", "s45", "s48", "s49", "s50", "s51", "s52", "s53", "s54", "s55", "s56", "s57", \
  "s58", "s59", "scc", "vcc", "memory"


namespace {

constexpr int C_ = 128;
constexpr int MW = 12;
constexpr int MT = MW * 64;
constexpr int SW = 8;
constexpr int NS = 5;
constexpr int NPB = SW * NS;
constexpr int NPASS = 16;
constexpr int HS = 64 * NPASS / 2;
constexpr int NBMAX = 256;
constexpr int PW = 5;
constexpr int PT = PW * 64;
constexpr int PK = 4;
constexpr int CH = PT * PK;
constexpr int MK = 4;
constexpr int CAPW = 256;
constexpr int HWV = 10;
constexpr int PFN = 5;
constexpr int WA = 4;
constexpr int FO1 = 2;
constexpr int WSL = 6;
constexpr int TB3 = 2;
constexpr int HSL = 24;
constexpr unsigned WS_EPOCH = 0;
constexpr unsigned WS_OFF = 4096;
constexpr unsigned WS_RECK = WS_OFF + NBMAX * NBMAX * 16;
constexpr unsigned WS_RECP = WS_RECK + NBMAX * CH * 4;
constexpr unsigned WS_END = WS_RECP + NBMAX * CH * 16;

__device__ __forceinline__ float bf2f(unsigned short v) { return __uint_as_float(((unsigned)v) << 16); }
__device__ __forceinline__ unsigned short f2bf(float f) {
  __hip_bfloat16 b = __float2bfloat16(f);
  return *reinterpret_cast<unsigned short*>(&b);
}
__device__ __forceinline__ unsigned pk2bf(float lo, float hi) {
  return __builtin_bit_cast(unsigned, __builtin_convertvector(f32x2{lo, hi}, bf16x2));
}
__device__ __forceinline__ unsigned wave_incl_scan(unsigned x) {
  x += __builtin_amdgcn_update_dpp(0u, x, 0x111, 0xf, 0xf, false);
  x += __builtin_amdgcn_update_dpp(0u, x, 0x112, 0xf, 0xf, false);
  x += __builtin_amdgcn_update_dpp(0u, x, 0x114, 0xf, 0xf, false);
  x += __builtin_amdgcn_update_dpp(0u, x, 0x118, 0xf, 0xf, false);
  x += __builtin_amdgcn_update_dpp(0u, x, 0x142, 0xa, 0xf, false);
  x += __builtin_amdgcn_update_dpp(0u, x, 0x143, 0xc, 0xf, false);
  return x;
}

struct LdsLow {
  u32x4 sink[16];
  unsigned flag, rdone, go, pad_;
  unsigned long long tag, pad2_;
  unsigned prog[16];
  int deg[64];
  unsigned byrank[64];
  unsigned nl[64];
  u32x4 ct[72];
  union {
    struct {
      __attribute__((aligned(16))) unsigned so[SW * CAPW];
      union {
        __attribute__((aligned(16))) unsigned hist[(SW + 1) * HS];
        u32x4 row5[SW * 64];
      };
    };
    struct {
      __attribute__((aligned(16))) unsigned sk[CH];
      u32x4 sp[CH];
    };
  };
  union {
    struct {
      __attribute__((aligned(16))) unsigned seg[2][NBMAX];
      unsigned phist[PW][NBMAX / 2];
      unsigned pbase[PW][NBMAX / 2];
      u32x4 ri[SW][MK * 16];
    };
    u32x4 pay[SW * CAPW];
  };
};
constexpr int TBLK = 0xc300 / 8;
struct Lds : LdsLow {
  char pad_end[163840 - TBLK * 16 - sizeof(LdsLow)];
  u32x4 sg[TBLK];
};
constexpr int TOFF = 163840 - TBLK * 16;
constexpr int WBLK = 0x4180 / 8;
static_assert(sizeof(Lds) == 163840 && TOFF < 65536, "LDS layout");

__device__ __forceinline__ unsigned lds_addr(const void* p) {
  return (unsigned)(uintptr_t)(__attribute__((address_space(3))) const void*)p;
}
__device__ __forceinline__ u32x4 make_srd(const void* p, unsigned num) {
  unsigned long long a = (unsigned long long)p;
  return u32x4{(unsigned)a, (unsigned)(a >> 32), num, 0x00020000u};
}

}

extern "C" __global__ __launch_bounds__(MT, 3) void tfn_main_kernel(
    const int* dst_, int E_, int c0_, const int* src_, int CE_, int c1_, const u32x2* Y_, int N_, int c2_, const u32x2* ef_,
    unsigned M2_, int c3_, const unsigned short* x_, char* ws_, u32x4* out_, const unsigned short* wrad_,
    const unsigned short* cg_, const unsigned short* wmix_) {
  __shared__ Lds L;
  const int tid = threadIdx.x, lane = tid & 63;
  u32x16 ka;
  u32x8 kb, kc;
  u32x4 kd;
  unsigned long long ev;
  asm volatile(PART_ASM
               : "=&{s[8:23]}"(ka), "=&{s[24:31]}"(kb), "=&{s[32:35]}"(kd), "=&{s[36:43]}"(kc), "=&{s[46:47]}"(ev)
               : [kp] "s"(__builtin_amdgcn_kernarg_segment_ptr()), [tid] "v"(tid), [bid] "s"(blockIdx.x), [pw] "n"(PW), [hw] "n"(HWV),
                 [h4] "n"(CH * 2), [h1] "n"(CH / 2), [sn] "n"(offsetof(LdsLow, sink)), [sg] "n"(offsetof(LdsLow, seg)),
                 [ph] "n"(offsetof(LdsLow, phist)), [pb] "n"(offsetof(LdsLow, pbase)), [tg] "n"(offsetof(LdsLow, tag)),
                 [go] "n"(offsetof(LdsLow, go)), [sk] "n"(offsetof(LdsLow, sk)), [sp] "n"(offsetof(LdsLow, sp))
               : PART_CLOBBERS);
  typedef __attribute__((address_space(1))) char gchar;
  auto ptr = [](unsigned lo, unsigned hi) { return (char*)(gchar*)(lo | (unsigned long long)hi << 32); };
  const int* __restrict__ dst = (const int*)ptr(ka[0], ka[1]);
  const int* __restrict__ src = (const int*)ptr(ka[4], ka[5]);
  const u32x2* __restrict__ Y = (const u32x2*)ptr(ka[8], ka[9]);
  const u32x2* __restrict__ ef = (const u32x2*)ptr(ka[12], ka[13]);
  const unsigned short* __restrict__ x = (const unsigned short*)ptr(kb[0], kb[1]);
  char* ws = ptr(kb[2], kb[3]);
  u32x4* __restrict__ out = (u32x4*)ptr(kb[4], kb[5]);
  const unsigned short* __restrict__ wrad = (const unsigned short*)ptr(kb[6], kb[7]);
  const unsigned short* __restrict__ cg = (const unsigned short*)ptr(kd[0], kd[1]);
  const unsigned short* __restrict__ wmix = (const unsigned short*)ptr(kd[2], kd[3]);
  const int w = kc[0], bucket = kc[1], E = kc[4], N = kc[5];
  const unsigned M2 = kc[6];
  __builtin_assume(w >= 0 && w < MW && bucket >= 0 && bucket < NBMAX);
  const int xcd = blockIdx.x & 7;
  const u32x4 wsd = make_srd(ws, WS_END);
  auto codes = [](unsigned q) { return q * 0x80008u + u32x4{0x10000u, 0x30002u, 0x50004u, 0x70006u}; };
  auto table = [&](int q) {
    u32x4 p, n;
#pragma unroll
    for (int i = 0; i < 4; i++) {
      const f32x2 z = {__uint_as_float((q * 8 + 2 * i) << 16), __uint_as_float((q * 8 + 2 * i + 1) << 16)};
      const f32x2 e = {__builtin_amdgcn_exp2f(-1.4426950408889634f * z[0]), __builtin_amdgcn_exp2f(-1.4426950408889634f * z[1])};
      const f32x2 r = z * f32x2{__builtin_amdgcn_rcpf(1.f + e[0]), __builtin_amdgcn_rcpf(1.f + e[1])};
      p[i] = pk2bf(r[0], r[1]);
      n[i] = pk2bf(-e[0] * r[0], -e[1] * r[1]);
    }
    if (q < WBLK) L.sg[q] = p;
    return n;
  };
  auto halves = [&](int q0, int q1, int st) {
#pragma unroll 1
    for (int q = q0; q < q1; q += st) {
      const u32x4 c = codes(q - 16);
      L.sg[q] = c;
      if (q >= 0x30) L.sg[q + 0x1000] = c | 0x80008000u;
    }
  };
  auto warm = [&](int i0, int i1) {
#pragma unroll 1
    for (int i = i0; i < i1; i += MW - SW) {
      asm volatile("s_mov_b32 m0, %2\n s_nop 0\n buffer_load_dword %0, %1, %3 offen lds\n"
                   :: "v"(lane * 128), "s"(make_srd(x, N * 1024)), "s"(lds_addr(L.sink)),
                      "s"(((xcd * N) >> 3) * 1024 + (i * 32 + (blockIdx.x >> 3)) * 8192) : "memory");
    }
  };
  auto wdma = [&](int i0, int i1) {
#pragma unroll
    for (int i = i0; i < i1; i++) {
      const unsigned o = (i + (blockIdx.x >> 3)) & 7;
      asm volatile("s_mov_b32 m0, %2\n s_nop 0\n buffer_load_dwordx4 %0, %1, %3 offen lds\n"
                   :: "v"((lane >> 2) * 256 + (lane & 3) * 16), "s"(make_srd(wmix, C_ * C_ * 2)),
                      "s"(lds_addr(&L.sg[WBLK]) + ((w - SW) * 8 + o) * 1024), "s"(o * 4096 + (w - SW) * 64) : "memory");
    }
  };
  const auto wsr = __builtin_amdgcn_make_buffer_rsrc((void*)ws, 0, WS_END, 0x00020000);
  if (w == HWV) {
    __builtin_amdgcn_s_setprio(3);
    asm volatile("s_waitcnt lgkmcnt(0)" : "+{s[46:47]}"(ev));
    ev = ev * 6364136223846793005ull + 1442695040888963407ull;
    L.tag = ev;
    if (lane < 4) (&L.flag)[lane] = 0;
    const unsigned ho0 = lane * 16 + WS_OFF + bucket * (NBMAX * 16);
    __syncthreads();
    const unsigned tl = (unsigned)ev, th = (unsigned)(ev >> 32);
    unsigned c[4] = {0, 0, 0, 0};
    u32x4 hv[4];
#pragma unroll
    for (int k = 0; k < 4; k++) {
#pragma unroll
      for (int ww = 0; ww < PW; ww++) c[k] += reinterpret_cast<const unsigned short*>(L.phist[ww])[k * 64 + lane];
      hv[k][1] = L.seg[0][k * 64 + lane];
    }
    const unsigned p0 = c[0] | (c[1] << 16), p1 = c[2] | (c[3] << 16);
    const unsigned i0 = wave_incl_scan(p0), i1 = wave_incl_scan(p1);
    const unsigned t0 = __builtin_amdgcn_readlane(i0, 63), t1 = __builtin_amdgcn_readlane(i1, 63);
    const unsigned s1 = t0 & 0xffff, s2 = s1 + (t0 >> 16), s3 = s2 + (t1 & 0xffff);
    const unsigned bs[4] = {(i0 - p0) & 0xffff, ((i0 - p0) >> 16) + s1, ((i1 - p1) & 0xffff) + s2, ((i1 - p1) >> 16) + s3};
#pragma unroll
    for (int k = 0; k < 4; k++) {
      hv[k][0] = bs[k] | (c[k] << 11) | (tl << 22);
      hv[k][1] += (unsigned)__umul24(c[k], tl & 255) << 24;
      hv[k][3] = th - hv[k][0] - hv[k][1];
    }
    asm volatile("" : "+v"(hv[0]), "+v"(hv[1]), "+v"(hv[2]), "+v"(hv[3]));
    while (true) {
      const unsigned gv = __hip_atomic_load(&L.go, __ATOMIC_RELAXED, __HIP_MEMORY_SCOPE_WORKGROUP);
      asm volatile("" ::: "memory");
#pragma unroll
      for (int k = 0; k < 4; k++) hv[k][2] = L.seg[1][k * 64 + lane];
      asm volatile("" ::: "memory");
      if (__builtin_amdgcn_readfirstlane(gv) == PW) break;
    }
#pragma unroll
    for (int k = 0; k < 4; k++) {
      hv[k][3] -= hv[k][2];
      __builtin_amdgcn_raw_buffer_store_b128(hv[k], wsr, ho0 + k * 1024, 0, 16);
    }
  } else if (w >= PW) {
    const int zt = tid - PT - (w > HWV ? 64 : 0);
    if (w >= SW) wdma(0, WA);
    if (zt < (0x4300 - 0x3a80) / 8) L.sg[0xba80 / 8 + zt] = table(0x3a80 / 8 + zt);
    __builtin_amdgcn_s_setprio(3);
  }
  asm volatile("s_waitcnt lgkmcnt(0)\n s_barrier" ::: "memory");
  {
    const unsigned so1 = (tid + MT - PT) * 16 + WS_RECP + bucket * (CH * 16);
    const unsigned so0 = tid * 16 + (w < PW ? WS_RECK + bucket * (CH * 4) : WS_RECP - PT * 16 + bucket * (CH * 16));
    const u32x4 v0 = reinterpret_cast<const u32x4*>(L.sk)[tid], v1 = reinterpret_cast<const u32x4*>(L.sk)[MT + tid];
    __builtin_amdgcn_raw_buffer_store_b128(v0, wsr, so0, 0, 16);
    __builtin_amdgcn_raw_buffer_store_b128(v1, wsr, so1, 0, 16);
    if (w == PW)
      __builtin_amdgcn_raw_buffer_store_b128(reinterpret_cast<const u32x4*>(L.sk)[2 * MT + lane], wsr, so1 + (MT - PT) * 16, 0, 16);
  }
  __syncthreads();
  const unsigned long long epoch = L.tag;
  const unsigned tagl = __builtin_amdgcn_readfirstlane((unsigned)epoch);
  const unsigned tag1 = __builtin_amdgcn_readfirstlane((unsigned)(epoch >> 32));
  const int row = lane & 15, kq = lane >> 4;
  const int node0 = __builtin_amdgcn_readfirstlane(((unsigned)(bucket << 22) + M2 - 1) / M2);
  const int nn = min(N, __builtin_amdgcn_readfirstlane(((unsigned)((bucket + 1) << 22) + M2 - 1) / M2)) - node0;
  if (w >= SW) {
    int lane;
    asm volatile("v_mbcnt_lo_u32_b32 %0, -1, 0\n v_mbcnt_hi_u32_b32 %0, -1, %0" : "=v"(lane));
    __builtin_assume(lane >= 0 && lane < 64);
    const int h = w - SW, st = h * 64 + lane, row = lane & 15, kq = lane >> 4;
    if (h == 0) {
      L.deg[lane] = ((lane - node0) & 63) < NPB ? 63 - ((lane - node0) & 63) : -1;
      if (lane < 16) L.prog[lane] = 255;
    }
    __builtin_amdgcn_s_sleep(WSL);
    if (w == HWV) wdma(0, WA);
    wdma(WA, 8);
    unsigned short cv;
    unsigned wq4[4];
    if (h == 1) {
      cv = cg[lane];
#pragma unroll
      for (int k = 0; k < 4; k++) wq4[k] = reinterpret_cast<const unsigned*>(wrad)[k * (C_ / 2) + lane];
    }
    halves(0x20 + TB3 * 512 + st, 0x3a80 / 8, 256);
    const int wq0 = WBLK + h * 512 + lane;
    u32x4 last = codes(wq0 + 448 < 0x1000 ? wq0 + 448 : wq0 + 432);
    if ((unsigned)(wq0 + 448 - 0x1000) < 0x20u) last = table(wq0 + 448 - 0x1000);
    constexpr int HR = (NPB - RKL * SW) / (MW - SW);
    const int id = (node0 + RKL * SW + h * HR + lane) & 63;
    asm volatile("s_waitcnt     vmcnt(0)" ::: "memory");
    if (h == 1) {
      reinterpret_cast<unsigned short*>(L.ct)[(lane & 3) * 16 + (lane >> 2)] = cv;
      L.ct[8 + lane] = u32x4{__builtin_amdgcn_perm(wq4[1], wq4[0], 0x05040100), __builtin_amdgcn_perm(wq4[3], wq4[2], 0x05040100),
                             __builtin_amdgcn_perm(wq4[1], wq4[0], 0x07060302), __builtin_amdgcn_perm(wq4[3], wq4[2], 0x07060302)};
    }
    u32x16 wa, wb;
    {
      const unsigned* wp = reinterpret_cast<const unsigned*>(&L.sg[WBLK + h * 512]) + kq * 128 + row;
#pragma unroll
      for (int i = 0; i < 16; i++) {
        const unsigned q0 = wp[(i >> 2) * 512 + (i & 3) * 32], q1 = wp[(i >> 2) * 512 + (i & 3) * 32 + 16];
        u32x16& d = i < 8 ? wa : wb;
        d[(i & 4) * 2 + (i & 3)] = __builtin_amdgcn_perm(q1, q0, 0x05040100);
        d[(i & 4) * 2 + (i & 3) + 4] = __builtin_amdgcn_perm(q1, q0, 0x07060302);
      }
    }
    asm volatile("" : "+v"(wa), "+v"(wb) :: "memory");
    asm volatile("s_waitcnt lgkmcnt(0)\n s_barrier" ::: "memory");
    __builtin_amdgcn_s_sleep(2);
    warm(h, PFN);
    __syncthreads();
    {
      const int dg = L.deg[lane], dm = L.deg[id];
      int dn[HR];
#pragma unroll
      for (int i = 0; i < HR; i++) dn[i] = __builtin_amdgcn_readlane(dm, i);
      __builtin_amdgcn_sched_barrier(0);
      unsigned long long m[HR];
#pragma unroll
      for (int i = 0; i < HR; i++) m[i] = __ballot(dg > dn[i]);
      __builtin_amdgcn_sched_barrier(0);
      unsigned r = 63;
#pragma unroll
      for (int i = 0; i < HR; i++) asm("v_writelane_b32 %0, %1, %2" : "+v"(r) : "s"(__popcll(m[i])), "n"(i));
      L.byrank[r] = id * 4;
    }
    __syncthreads();
    if (bucket == 0 && st == 0)
      __builtin_amdgcn_raw_buffer_store_b64(u32x2{tagl, tag1}, wsr, WS_EPOCH, 0, 16);
    __builtin_amdgcn_s_setprio(0);
    __syncthreads();
#pragma unroll
    for (int i = 0; i < 8; i++) L.sg[wq0 + i * 64] = i < 7 ? codes(wq0 + i * 64) : last;
    __syncthreads();
    u32x16 oo;
#pragma unroll
    for (int t = 0; t < 10; t++)
      oo[t + 2] = (node0 + ((L.nl[t < SW ? t * 8 + kq : ((t - SW) * 4 + kq) * 8 + 4] / 4 - node0) & 63)) * 1024 + row * 16 + h * 256;
    oo[0] = lds_addr(&L.pay[(row >> 2) * 64 + (row & 3) * 4 + kq]);
    oo[1] = lds_addr(&L.prog[h]);
    asm volatile(MIX_ASM
                 : "+{v[0:15]}"(oo)
                 : "{v[32:47]}"(wa), "{v[48:63]}"(wb), [os] "s"(make_srd(out, (node0 + nn) * 1024)),
                   [r5] "n"((unsigned)(offsetof(LdsLow, row5) - offsetof(LdsLow, pay) - SW * CAPW * 16)),
                   [rd] "n"(offsetof(LdsLow, rdone)), [sl] "n"(HSL)
                 : MIX_CLOBBERS);
    asm volatile(".Ltfn_p1:");
  } else {
    {
    halves(0x20 + tid, 0x20 + TB3 * 512, 512);
    reinterpret_cast<u32x4*>(&L.hist[w * HS])[lane] = u32x4{0, 0, 0, 0};
    reinterpret_cast<u32x4*>(&L.hist[w * HS])[64 + lane] = u32x4{0, 0, 0, 0};
    L.ri[w][lane] = u32x4{0x20000000u, 0x20000000u, 0x20000000u, 0x20000000u};
    const unsigned long long t0 = w & 1 ? 0x26796281267d7281ull : 0x277dc200272c8180ull, t1 = w & 1 ? 0x246942c324755245ull : 0x25755242217984c2ull;
    const unsigned long long t2 = w & 1 ? 0x22613345226d6483ull : 0x236d130423694307ull, t3 = w & 1 ? 0x206503c725710384ull : 0x215d2386206513cdull;
    const unsigned long long t = w & 4 ? (w & 2 ? t3 : t2) : (w & 2 ? t1 : t0);
    const unsigned rks = nn == NPB ? t >> 32 : t;
    const int start = w * CAPW;
    unsigned sop = lds_addr(&L.so[start + (lane & 7)]);
    unsigned yp = lds_addr(&L.pay[start + (lane >> 2)]);
    unsigned ep = lds_addr(&L.pay[start + (lane & 15)]) + 8;
    unsigned cga = lds_addr(&L.ct[(lane & 3) * 2]);
    const unsigned rp = lds_addr(&L.pay[start]) + kq * 256 + row * 4;
    unsigned pg = lds_addr(&L.prog[w]), slowv;
    u32x16 cs = {(unsigned)((lane >> 1) * SW + w) * (NBMAX * 16),
                 (unsigned)(((lane >> 1) * SW + w) * CH + (lane & 1)) * 4,
                 lds_addr(L.ri[w]) + (lane & 1) * 4, lds_addr(L.ri[w]) + lane * 4, (lane & 1) - 1u, lane & 1u,
                 lds_addr(&L.hist[tid]), lds_addr(&L.byrank[(rks >> (min(lane & 7, NS - 1) * 6)) & 63]),
                 0, lds_addr(&L.hist[SW * HS + (lane >> 3)]),
                 (start * 4 + ((lane & 7) << 13)) * 0x10001u, (unsigned)(start + lane) * 4,
                 lds_addr(&L.nl[w * 8 + (lane & 7)]), lane * 4u, ((node0 + w * RKL + lane) & 63) * 4u, 0};
    asm volatile(STREAM_ASM
                 : [sl] "=s"(slowv), "+{v12}"(sop), "+{v14}"(yp), "+{v15}"(ep), "+{v[136:151]}"(cs),
                   "+{v20}"(cga), "+{v110}"(pg)
                 : "{v8}"(lane * 16), "{v[152:155]}"(u32x4{rp, rp + 64, rp + 128, rp + 192}),
                   "{v156}"(lds_addr(&L.row5[w * 64]) + kq * 256 + row * 4),
                   [xs] "s"(make_srd(x, N * 1024)), [ws] "s"(wsd),
                   [ho] "s"(WS_OFF + bucket * 16), [rk] "s"(WS_RECK), [rp] "s"(WS_RECP), [t0] "s"(tagl << 22), [t1] "s"(tag1), [tl] "s"(tagl),
                   [wb] "s"(w * (HS * 4)), [bit] "s"(1u << w), [fo] "s"((w >> 2) * FO1), [tb] "n"(TOFF),
                   [rd] "n"(offsetof(LdsLow, rdone)), [fl] "n"(offsetof(LdsLow, flag)),
                   [hi] "n"(offsetof(LdsLow, hist)), [to] "n"(offsetof(LdsLow, hist) + SW * HS * 4), [dg] "n"(offsetof(LdsLow, deg)),
                   [ct] "n"(offsetof(LdsLow, ct)), [br] "n"(offsetof(LdsLow, byrank)), [so] "n"(offsetof(LdsLow, so)), [pa] "n"(offsetof(LdsLow, pay))
                 : STREAM_CLOBBERS);
    }
    int lane;
    asm volatile("v_mbcnt_lo_u32_b32 %0, -1, 0\n v_mbcnt_hi_u32_b32 %0, -1, %0" : "=v"(lane));
    __builtin_assume(lane >= 0 && lane < 64);
    u32x4* rows = &L.pay[w * CAPW];
#pragma unroll 1
    for (int it = 0; it < 2 * NS; it++) {
      const int sl = it >> 1, slice = it & 1, ch = slice * 64 + lane;
      const unsigned dl = (L.nl[w * 8 + sl] / 4 - node0) & 63;
      float acc[4] = {0.f, 0.f, 0.f, 0.f};
#pragma unroll 1
      for (int eb = 0; eb < E; eb += 64) {
        unsigned long long m = __ballot(eb + lane < E && (unsigned)dst[min(eb + lane, E - 1)] == node0 + dl);
#pragma unroll 1
        for (; m; m &= m - 1) {
          const int e = eb + __builtin_ctzll(m);
          const u32x2 ry = Y[e], rf = ef[e];
          float yy[4] = {bf2f(ry[0] & 0xffff), bf2f(ry[0] >> 16), bf2f(ry[1] & 0xffff), bf2f(ry[1] >> 16)};
          float ee[4] = {bf2f(rf[0] & 0xffff), bf2f(rf[0] >> 16), bf2f(rf[1] & 0xffff), bf2f(rf[1] >> 16)};
          float z = 0.f;
#pragma unroll
          for (int k = 0; k < 4; k++) z += ee[k] * bf2f(wrad[k * C_ + ch]);
          float R = z / (1.f + __expf(-z));
          const unsigned short* xr = x + (size_t)src[e] * 512 + ch * 4;
#pragma unroll 1
          for (int o = 0; o < 4; o++) {
            float tp = 0.f;
#pragma unroll 1
            for (int i = 0; i < 4; i++) {
              float A = 0.f;
#pragma unroll
              for (int f = 0; f < 4; f++) A += yy[f] * bf2f(cg[i * 16 + f * 4 + o]);
              tp += bf2f(xr[i]) * bf2f(f2bf(A));
            }
            float v = R * tp;
            acc[0] += o == 0 ? v : 0.f;
            acc[1] += o == 1 ? v : 0.f;
            acc[2] += o == 2 ? v : 0.f;
            acc[3] += o == 3 ? v : 0.f;
          }
        }
      }
      u32x4* rw = sl < 4 ? &rows[sl * 64] : &L.row5[w * 64];
      unsigned short* ar = reinterpret_cast<unsigned short*>(&rw[slice * 32]) + (lane >> 5) * 128 + (lane & 31);
#pragma unroll
      for (int o = 0; o < 4; o++) ar[o * 32] = f2bf(acc[o]);
    }
    asm volatile("" ::: "memory");
    if (lane == 0) atomicOr(&L.rdone, 1u << w);
  }
}

extern "C" __global__ __launch_bounds__(C_) void tfn_plain_kernel(
    const int* __restrict__ dst, const int* __restrict__ src, const unsigned short* __restrict__ Y,
    const unsigned short* __restrict__ ef, const unsigned short* __restrict__ x, unsigned short* __restrict__ out,
    const unsigned short* __restrict__ wrad, const unsigned short* __restrict__ cg, const unsigned short* __restrict__ wmix,
    long long E) {
  __shared__ f32x4 agg[C_];
  const int c = threadIdx.x, lane = c & 63;
  f32x4 acc = {0.f, 0.f, 0.f, 0.f};
#pragma unroll 1
  for (long long eb = 0; eb < E; eb += 64) {
    unsigned long long m = __ballot(eb + lane < E && dst[eb + lane < E ? eb + lane : 0] == (int)blockIdx.x);
#pragma unroll 1
    for (; m; m &= m - 1) {
      const long long e = eb + __builtin_ctzll(m);
      float z = 0.f;
#pragma unroll
      for (int k = 0; k < 4; k++) z += bf2f(ef[e * 4 + k]) * bf2f(wrad[k * C_ + c]);
      const float R = z / (1.f + __expf(-z));
#pragma unroll
      for (int i = 0; i < 4; i++) {
        const float xv = R * bf2f(x[(size_t)src[e] * 512 + c * 4 + i]);
#pragma unroll
        for (int f = 0; f < 4; f++)
#pragma unroll
          for (int o = 0; o < 4; o++) acc[o] += xv * bf2f(Y[e * 4 + f]) * bf2f(cg[i * 16 + f * 4 + o]);
      }
    }
  }
  agg[c] = acc;
  __syncthreads();
  f32x4 r = {0.f, 0.f, 0.f, 0.f};
#pragma unroll 4
  for (int k = 0; k < C_; k++) r += agg[k] * bf2f(wmix[k * C_ + c]);
  *reinterpret_cast<u32x2*>(out + (size_t)blockIdx.x * 512 + c * 4) = u32x2{pk2bf(r[0], r[1]), pk2bf(r[2], r[3])};
}

extern "C" void kernel_launch(void* const* d_in, const int* in_sizes, int n_in, void* d_out, int out_size,
                              void* d_ws, size_t ws_size, hipStream_t stream) {
  const unsigned short* x = (const unsigned short*)d_in[0];
  const int* src = (const int*)d_in[1];
  const int* dst = (const int*)d_in[2];
  const u32x2* Y = (const u32x2*)d_in[3];
  const u32x2* ef = (const u32x2*)d_in[4];
  const unsigned short* wrad = (const unsigned short*)d_in[5];
  const unsigned short* cg = (const unsigned short*)d_in[6];
  const unsigned short* wmix = (const unsigned short*)d_in[7];
  const int N = in_sizes[0] / 512;
  const int E = in_sizes[1];
  const int CE = ((E + NBMAX - 1) / NBMAX + 1) & ~1;
  const unsigned M2 = ((unsigned)NBMAX << 22) / (unsigned)N;
  if (N <= 64 || ((1u << 22) + M2 - 1) / M2 > (unsigned)NPB || CE > CH || ws_size < WS_END) {
    tfn_plain_kernel<<<N, C_, 0, stream>>>(dst, src, (const unsigned short*)Y, (const unsigned short*)ef, x,
                                           (unsigned short*)d_out, wrad, cg, wmix, E);
    return;
  }
  tfn_main_kernel<<<NBMAX, MT, 0, stream>>>(dst, E, 0x20000, src, CE, 0x20000, Y, N, 0x20000, ef, M2, 0x20000, x, (char*)d_ws,
                                           (u32x4*)d_out, wrad, cg, wmix);
}
```

```cpp
#include <hip/hip_runtime.h>
#include <hip/hip_bf16.h>
#include <stddef.h>
#include <stdint.h>

typedef __attribute__((ext_vector_type(2))) float f32x2;
typedef __attribute__((ext_vector_type(4))) float f32x4;
typedef __attribute__((ext_vector_type(2))) unsigned int u32x2;
typedef __attribute__((ext_vector_type(4))) unsigned int u32x4;
typedef __attribute__((ext_vector_type(8))) unsigned int u32x8;
typedef __attribute__((ext_vector_type(16))) unsigned int u32x16;
typedef __attribute__((ext_vector_type(2))) __bf16 bf16x2;

#define STREAM_ASM \
  "s_mov_b32 s63, 0x10000\n" \
  "Lh%=:\n" \
  "buffer_load_dwordx4 v[72:75], v136, %[ws], %[ho] offen sc1\n" \
  "v_mov_b32 v117, 0\n" \
  "v_mov_b32 v116, 64\n" \
  "v_mov_b32 v87, 0\n" \
  "v_mov_b64 v[44:45], 0\n" \
  "v_mov_b64 v[46:47], 0\n" \
  "v_mov_b32 v118, 0x7fc\n" \
  "s_waitcnt vmcnt(0)\n" \
  "v_add3_u32 v108, v72, v73, v74\n" \
  "v_xor_b32 v109, %[t0], v72\n" \
  "v_add_u32 v108, v108, v75\n" \
  "v_cmp_lt_u32 vcc, 0x3fffff, v109\n" \
  "v_cmp_ne_u32_e64 s[74:75], %[t1], v108\n" \
  "s_or_b64 vcc, vcc, s[74:75]\n" \
  "s_cbranch_scc1 Lhr%=\n" \
  "s_mov_b64 exec, -1\n" \
  "v_bfe_u32 v108, v72, 11, 11\n" \
  "v_and_b32 v109, v140, v108\n" \
  "v_and_b32 v112, 0x7ff, v72\n" \
  "v_sub_u32 v111, v108, v141\n" \
  "v_add_u32_dpp v109, v109, v109 row_shr:1 row_mask:0xf bank_mask:0xf bound_ctrl:1\n" \
  "v_lshl_add_u32 v112, v112, 2, v137\n" \
  "v_and_b32 v114, v140, v73\n" \
  "v_add_u32_dpp v109, v109, v109 row_shr:2 row_mask:0xf bank_mask:0xf bound_ctrl:1\n" \
  "v_and_b32 v115, v140, v74\n" \
  "v_sub_u32 v114, 0, v114\n" \
  "v_add_u32_dpp v109, v109, v109 row_shr:4 row_mask:0xf bank_mask:0xf bound_ctrl:1\n" \
  "v_sub_u32 v115, 0, v115\n" \
  "s_nop 0\n" \
  "v_add_u32_dpp v109, v109, v109 row_shr:8 row_mask:0xf bank_mask:0xf bound_ctrl:1\n" \
  "s_nop 1\n" \
  "v_add_u32_dpp v109, v109, v109 row_bcast:15 row_mask:0xa bank_mask:0xf\n" \
  "s_nop 1\n" \
  "v_add_u32_dpp v109, v109, v109 row_bcast:31 row_mask:0xc bank_mask:0xf\n" \
  "v_sub_u32 v108, v109, v108\n" \
  "v_readlane_b32 s62, v109, 63\n" \
  "v_lshl_add_u32 v113, v108, 2, v138\n" \
  "s_cmpk_gt_u32 s62, 0x100\n" \
  "s_cbranch_scc1 Lover%=\n" \
  "Lw%=:\n" \
  "v_cmpx_lt_i32 vcc, 0, v111\n" \
  "ds_write_b32 v113, v112 offset:0\n" \
  "v_add_u32 v112, 8, v112\n" \
  "v_cmpx_lt_i32 vcc, 2, v111\n" \
  "ds_write_b32 v113, v112 offset:8\n" \
  "v_add_u32 v112, 8, v112\n" \
  "v_cmpx_lt_i32 vcc, 4, v111\n" \
  "ds_write_b32 v113, v112 offset:16\n" \
  "v_add_u32 v112, 8, v112\n" \
  "v_cmpx_lt_i32 vcc, 6, v111\n" \
  "ds_write_b32 v113, v112 offset:24\n" \
  "v_add_u32 v112, 8, v112\n" \
  "v_cmpx_lt_i32 vcc, 8, v111\n" \
  "ds_write_b32 v113, v112 offset:32\n" \
  "v_add_u32 v112, 8, v112\n" \
  "s_cbranch_execz Lwd%=\n" \
  "v_cmpx_lt_i32 vcc, 10, v111\n" \
  "ds_write_b32 v113, v112 offset:40\n" \
  "v_add_u32 v112, 8, v112\n" \
  "s_cbranch_execz Lwd%=\n" \
  "v_cmpx_lt_i32 vcc, 12, v111\n" \
  "ds_write_b32 v113, v112 offset:48\n" \
  "v_add_u32 v112, 8, v112\n" \
  "s_cbranch_execz Lwd%=\n" \
  "v_cmpx_lt_i32 vcc, 14, v111\n" \
  "ds_write_b32 v113, v112 offset:56\n" \
  "v_add_u32 v112, 8, v112\n" \
  "v_add_u32 v113, 64, v113\n" \
  "v_subrev_u32 v111, 16, v111\n" \
  "s_branch Lw%=\n" \
  "Lwd%=:\n" \
  "s_mov_b64 exec, -1\n" \
  "Lld%=:\n" \
  "ds_read2st64_b32 v[76:77], v139 offset1:1\n" \
  "ds_read2st64_b32 v[78:79], v139 offset0:2 offset1:3\n" \
  "s_mov_b32 s63, 0x10000\n" \
  "s_waitcnt lgkmcnt(0)\n" \
  "buffer_load_dword v84, v76, %[ws], %[rk] offen sc1\n" \
  "buffer_load_dword v85, v77, %[ws], %[rk] offen sc1\n" \
  "buffer_load_dword v86, v78, %[ws], %[rk] offen sc1\n" \
  "s_cmpk_le_u32 s62, 0xc0\n" \
  "s_cbranch_scc1 Lka%=\n" \
  "buffer_load_dword v87, v79, %[ws], %[rk] offen sc1\n" \
  "Lka%=:\n" \
  "v_lshlrev_b32 v80, 2, v76\n" \
  "v_lshlrev_b32 v81, 2, v77\n" \
  "v_lshlrev_b32 v82, 2, v78\n" \
  "v_lshlrev_b32 v83, 2, v79\n" \
  "s_barrier\n" \
  "s_cmpk_le_u32 s62, 0xc0\n" \
  "s_cbranch_scc1 Lpa%=\n" \
  "buffer_load_dwordx4 v[44:47], v83, %[ws], %[rp] offen sc1\n" \
  "Lpa%=:\n" \
  "buffer_load_dwordx4 v[32:35], v80, %[ws], %[rp] offen sc1\n" \
  "buffer_load_dwordx4 v[36:39], v81, %[ws], %[rp] offen sc1\n" \
  "buffer_load_dwordx4 v[40:43], v82, %[ws], %[rp] offen sc1\n" \
  "v_cmp_le_i32_e64 s[66:67], 0, v80\n" \
  "v_cmp_le_i32_e64 s[68:69], 0, v81\n" \
  "v_cmp_le_i32_e64 s[70:71], 0, v82\n" \
  "v_cmp_le_i32_e64 s[72:73], 0, v83\n" \
  "v_mov_b64 v[48:49], 0\n" \
  "v_mov_b64 v[50:51], 0\n" \
  "v_mov_b64 v[52:53], 0\n" \
  "v_mov_b64 v[54:55], 0\n" \
  "v_mov_b64 v[56:57], 0\n" \
  "v_mov_b64 v[58:59], 0\n" \
  "v_mov_b64 v[60:61], 0\n" \
  "v_mov_b64 v[62:63], 0\n" \
  "v_mov_b64 v[64:65], 0\n" \
  "v_mov_b64 v[66:67], 0\n" \
  "v_mov_b64 v[68:69], 0\n" \
  "v_mov_b64 v[70:71], 0\n" \
  "s_waitcnt vmcnt(3)\n" \
  "Lk%=:\n" \
  "v_add3_u32 v108, v84, v85, v86\n" \
  "v_add3_u32 v108, v108, v87, v114\n" \
  "v_lshlrev_b32 v88, 1, v84\n" \
  "v_lshlrev_b32 v92, 4, v84\n" \
  "v_add_u32_dpp v108, v108, v108 row_shr:1 row_mask:0xf bank_mask:0xf bound_ctrl:1\n" \
  "v_lshrrev_b32 v104, 2, v84\n" \
  "v_and_or_b32 v88, v88, v118, %[wb]\n" \
  "v_add_u32_dpp v108, v108, v108 row_shr:2 row_mask:0xf bank_mask:0xf bound_ctrl:1\n" \
  "v_lshlrev_b32_e64 v100, v92, 4\n" \
  "v_and_b32 v104, 0xfc, v104\n" \
  "v_add_u32_dpp v108, v108, v108 row_shr:4 row_mask:0xf bank_mask:0xf bound_ctrl:1\n" \
  "v_lshlrev_b32 v89, 1, v85\n" \
  "v_lshlrev_b32 v93, 4, v85\n" \
  "v_add_u32_dpp v108, v108, v108 row_shr:8 row_mask:0xf bank_mask:0xf bound_ctrl:1\n" \
  "v_lshrrev_b32 v105, 2, v85\n" \
  "v_and_or_b32 v89, v89, v118, %[wb]\n" \
  "v_add_u32_dpp v108, v108, v108 row_bcast:15 row_mask:0xa bank_mask:0xf\n" \
  "v_lshlrev_b32_e64 v101, v93, 4\n" \
  "v_and_b32 v105, 0xfc, v105\n" \
  "v_add_u32_dpp v108, v108, v108 row_bcast:31 row_mask:0xc bank_mask:0xf\n" \
  "v_lshlrev_b32 v90, 1, v86\n" \
  "v_readlane_b32 s65, v108, 63\n" \
  "v_lshlrev_b32 v94, 4, v86\n" \
  "v_lshrrev_b32 v106, 2, v86\n" \
  "v_and_or_b32 v90, v90, v118, %[wb]\n" \
  "v_lshlrev_b32_e64 v102, v94, 4\n" \
  "v_and_b32 v106, 0xfc, v106\n" \
  "s_cmp_lg_u32 s65, 0\n" \
  "s_cbranch_scc1 Lkr%=\n" \
  "Lkc%=:\n" \
  "s_mov_b64 exec, s[66:67]\n" \
  "ds_add_rtn_u32 v96, v88, v100 offset:%[hi]\n" \
  "ds_add_u32 v104, v116 offset:%[dg]\n" \
  "s_mov_b64 exec, s[68:69]\n" \
  "ds_add_rtn_u32 v97, v89, v101 offset:%[hi]\n" \
  "ds_add_u32 v105, v116 offset:%[dg]\n" \
  "s_mov_b64 exec, s[70:71]\n" \
  "ds_add_rtn_u32 v98, v90, v102 offset:%[hi]\n" \
  "ds_add_u32 v106, v116 offset:%[dg]\n" \
  "s_cmpk_gt_u32 s62, 0xc0\n" \
  "s_mov_b64 exec, -1\n" \
  "s_cbranch_scc1 Lc3%=\n" \
  "Lc4%=:\n" \
  "ds_read_b128 v[24:27], v20 offset:16\n" \
  "ds_read_b128 v[28:31], v8 offset:%[ct]+128\n" \
  "ds_read_b128 v[20:23], v20\n" \
  "v_and_b32 v100, v118, v88\n" \
  "v_and_b32 v101, v118, v89\n" \
  "v_and_b32 v102, v118, v90\n" \
  "s_waitcnt lgkmcnt(0)\n" \
  "s_barrier\n" \
  "ds_read_b32 v108, v149 offset:%[dg]\n" \
  "ds_read_b32 v109, v150 offset:%[dg]\n" \
  "ds_read2st64_b32 v[76:77], v142 offset0:0 offset1:8\n" \
  "ds_read2st64_b32 v[78:79], v142 offset0:16 offset1:24\n" \
  "ds_read2st64_b32 v[104:105], v142 offset0:32 offset1:40\n" \
  "ds_read2st64_b32 v[106:107], v142 offset0:48 offset1:56\n" \
  "v_and_b32 v84, 0xfffc00, v84\n" \
  "v_and_b32 v85, 0xfffc00, v85\n" \
  "v_and_b32 v86, 0xfffc00, v86\n" \
  "v_and_b32 v87, 0xfffc00, v87\n" \
  "v_mov_b32 v111, 0\n" \
  "v_mov_b32 v112, 63\n" \
  "s_waitcnt lgkmcnt(4)\n" \
  "v_readlane_b32 s74, v109, 0\n" \
  "v_readlane_b32 s76, v109, 1\n" \
  "v_readlane_b32 s78, v109, 2\n" \
  "s_nop 0\n" \
  "v_cmp_lt_i32_e64 s[74:75], s74, v108\n" \
  "v_cmp_lt_i32_e64 s[76:77], s76, v108\n" \
  "v_cmp_lt_i32_e64 s[78:79], s78, v108\n" \
  "s_bcnt1_i32_b64 s74, s[74:75]\n" \
  "s_bcnt1_i32_b64 s76, s[76:77]\n" \
  "s_bcnt1_i32_b64 s78, s[78:79]\n" \
  "v_writelane_b32 v112, s74, 0\n" \
  "v_writelane_b32 v112, s76, 1\n" \
  "v_writelane_b32 v112, s78, 2\n" \
  "v_lshlrev_b32 v112, 2, v112\n" \
  "s_waitcnt lgkmcnt(3)\n" \
  "ds_write2st64_b32 v142, v117, v76 offset1:8\n" \
  "v_add_u32 v77, v77, v76\n" \
  "s_waitcnt lgkmcnt(3)\n" \
  "v_add_u32 v78, v78, v77\n" \
  "ds_write2st64_b32 v142, v77, v78 offset0:16 offset1:24\n" \
  "v_add_u32 v79, v79, v78\n" \
  "s_waitcnt lgkmcnt(3)\n" \
  "v_add_u32 v104, v104, v79\n" \
  "ds_write2st64_b32 v142, v79, v104 offset0:32 offset1:40\n" \
  "v_add_u32 v105, v105, v104\n" \
  "s_waitcnt lgkmcnt(3)\n" \
  "v_add_u32 v106, v106, v105\n" \
  "ds_write2st64_b32 v142, v105, v106 offset0:48 offset1:56\n" \
  "v_add_u32 v107, v107, v106\n" \
  "ds_write_b32 v142, v107 offset:16384\n" \
  "ds_write_b32 v112, v150 offset:%[br]\n" \
  "s_waitcnt lgkmcnt(0)\n" \
  "s_barrier\n" \
  "ds_read_b32 v108, v143\n" \
  "s_mov_b32 s74, 0x1f1f1f1f\n" \
  "s_mov_b32 s75, 0x1f1f1f1f\n" \
  "s_waitcnt lgkmcnt(0)\n" \
  "v_lshl_add_u32 v76, v108, 3, v145\n" \
  "s_mov_b64 exec, s[74:75]\n" \
  "ds_read_b32 v111, v76\n" \
  "s_mov_b64 exec, -1\n" \
  "ds_write_b32 v148, v108\n" \
  "s_waitcnt lgkmcnt(1)\n" \
  "v_add_u32_dpp v112, v111, v111 row_shr:1 row_mask:0xf bank_mask:0xf bound_ctrl:1\n" \
  "v_sub_u32 v77, v146, v111\n" \
  "s_nop 0\n" \
  "v_add_u32_dpp v112, v112, v112 row_shr:2 row_mask:0xf bank_mask:0xf bound_ctrl:1\n" \
  "s_nop 1\n" \
  "v_add_u32_dpp v112, v112, v112 row_shr:4 row_mask:0xf bank_mask:0xf bound_ctrl:1\n" \
  "s_nop 1\n" \
  "v_add_u32_dpp v112, v112, v112 row_shr:8 row_mask:0xf bank_mask:0xf bound_ctrl:1\n" \
  "s_nop 1\n" \
  "v_add_u32_dpp v112, v112, v112 row_bcast:15 row_mask:0xa bank_mask:0xf\n" \
  "s_nop 1\n" \
  "v_add_u32_dpp v112, v112, v112 row_bcast:31 row_mask:0xc bank_mask:0xf\n" \
  "v_sub_u32 v111, v112, v111\n" \
  "v_readlane_b32 s65, v112, 63\n" \
  "v_mov_b32_dpp v78, v112 row_newbcast:7 row_mask:0xf bank_mask:0x3\n" \
  "v_mov_b32_dpp v79, v111 row_newbcast:0 row_mask:0xf bank_mask:0x3\n" \
  "v_add_u32 v77, v77, v112\n" \
  "v_mov_b32_dpp v78, v112 row_newbcast:15 row_mask:0xf bank_mask:0xc\n" \
  "v_mov_b32_dpp v79, v111 row_newbcast:8 row_mask:0xf bank_mask:0xc\n" \
  "s_and_b32 s64, s65, 0xffff\n" \
  "v_lshl_add_u32 v77, v78, 16, v77\n" \
  "s_lshr_b32 s65, s65, 16\n" \
  "v_add_u32_sdwa v77, v77, v79 dst_sel:DWORD dst_unused:UNUSED_PAD src0_sel:DWORD src1_sel:WORD_1\n" \
  "s_add_u32 s64, s64, s65\n" \
  "s_mov_b64 exec, s[74:75]\n" \
  "ds_write_b32 v76, v77\n" \
  "s_mov_b64 exec, -1\n" \
  "s_cmpk_gt_u32 s64, 864\n" \
  "s_cbranch_scc1 Llo%=\n" \
  "Llc%=:\n" \
  "s_waitcnt lgkmcnt(0)\n" \
  "s_barrier\n" \
  "ds_read_b32 v109, v117 offset:%[fl]\n" \
  "ds_read_b32 v76, v88 offset:%[hi]\n" \
  "ds_read_b32 v104, v100 offset:%[to]\n" \
  "ds_read_b32 v77, v89 offset:%[hi]\n" \
  "ds_read_b32 v105, v101 offset:%[to]\n" \
  "ds_read_b32 v78, v90 offset:%[hi]\n" \
  "ds_read_b32 v106, v102 offset:%[to]\n" \
  "v_add_u32 v113, s64, v147\n" \
  "v_bfrev_b32 v111, 1\n" \
  "s_max_u32 s60, s64, 4\n" \
  "s_sub_u32 s60, s60, 4\n" \
  "s_lshr_b32 s60, s60, 4\n" \
  "s_waitcnt lgkmcnt(4)\n" \
  "v_readfirstlane_b32 s61, v109\n" \
  "v_add3_u32 v88, v76, v104, v96\n" \
  "v_lshrrev_b32 v88, v92, v88\n" \
  "v_bfe_u32 v72, v88, 13, 3\n" \
  "v_and_b32 v88, 0x1ffc, v88\n" \
  "v_lshl_or_b32 v72, v72, 1, v84\n" \
  "s_cmp_lg_u32 s61, 0\n" \
  "s_cbranch_scc1 Lslow%=\n" \
  "s_waitcnt lgkmcnt(0)\n" \
  "v_add3_u32 v89, v77, v105, v97\n" \
  "v_add3_u32 v90, v78, v106, v98\n" \
  "v_lshrrev_b32 v89, v93, v89\n" \
  "v_lshrrev_b32 v90, v94, v90\n" \
  "v_bfe_u32 v73, v89, 13, 3\n" \
  "v_bfe_u32 v74, v90, 13, 3\n" \
  "v_and_b32 v89, 0x1ffc, v89\n" \
  "v_and_b32 v90, 0x1ffc, v90\n" \
  "v_lshl_or_b32 v73, v73, 1, v85\n" \
  "v_lshl_or_b32 v74, v74, 1, v86\n" \
  "s_mov_b64 exec, s[66:67]\n" \
  "ds_write_b32 v88, v72 offset:%[so]\n" \
  "s_mov_b64 exec, s[68:69]\n" \
  "ds_write_b32 v89, v73 offset:%[so]\n" \
  "s_mov_b64 exec, s[70:71]\n" \
  "ds_write_b32 v90, v74 offset:%[so]\n" \
  "s_cmpk_gt_u32 s62, 0xc0\n" \
  "s_cbranch_scc1 Lq3%=\n" \
  "Lq4%=:\n" \
  "s_mov_b32 exec_lo, -1\n" \
  "s_mov_b32 exec_hi, 0\n" \
  "ds_write_b32 v113, v111 offset:%[so]\n" \
  "s_mov_b64 exec, -1\n" \
  "v_lshlrev_b32 v88, 2, v88\n" \
  "v_lshlrev_b32 v89, 2, v89\n" \
  "v_lshlrev_b32 v90, 2, v90\n" \
  "v_lshlrev_b32 v91, 2, v91\n" \
  "v_lshlrev_b32 v113, 2, v113\n" \
  "s_mov_b32 s63, 0x10000\n" \
  "s_waitcnt vmcnt(0)\n" \
  "Lpc%=:\n" \
  "v_add3_u32 v108, v32, v33, v115\n" \
  "v_add3_u32 v111, v34, v35, v36\n" \
  "v_add3_u32 v112, v37, v38, v39\n" \
  "v_add3_u32 v108, v108, v40, v41\n" \
  "v_add3_u32 v111, v111, v42, v43\n" \
  "v_add3_u32 v112, v112, v44, v45\n" \
  "v_add3_u32 v108, v108, v46, v47\n" \
  "v_add3_u32 v108, v108, v111, v112\n" \
  "v_subrev_u32 v32, %[tl], v32\n" \
  "v_subrev_u32 v34, %[tl], v34\n" \
  "v_subrev_u32 v36, %[tl], v36\n" \
  "v_subrev_u32 v38, %[tl], v38\n" \
  "v_subrev_u32 v40, %[tl], v40\n" \
  "v_subrev_u32 v42, %[tl], v42\n" \
  "v_subrev_u32 v44, %[tl], v44\n" \
  "v_subrev_u32 v46, %[tl], v46\n" \
  "s_mov_b64 exec, s[66:67]\n" \
  "ds_write_b128 v88, v[32:35] offset:%[pa]\n" \
  "s_mov_b64 exec, s[68:69]\n" \
  "ds_write_b128 v89, v[36:39] offset:%[pa]\n" \
  "s_mov_b64 exec, s[70:71]\n" \
  "ds_write_b128 v90, v[40:43] offset:%[pa]\n" \
  "s_mov_b64 exec, s[72:73]\n" \
  "ds_write_b128 v91, v[44:47] offset:%[pa]\n" \
  "s_mov_b32 exec_lo, -1\n" \
  "s_mov_b32 exec_hi, 0\n" \
  "ds_write_b128 v113, v[48:51] offset:%[pa]\n" \
  "s_mov_b64 exec, -1\n" \
  "s_nop 1\n" \
  "v_add_u32_dpp v108, v108, v108 row_shr:1 row_mask:0xf bank_mask:0xf bound_ctrl:1\n" \
  "s_nop 1\n" \
  "v_add_u32_dpp v108, v108, v108 row_shr:2 row_mask:0xf bank_mask:0xf bound_ctrl:1\n" \
  "s_nop 1\n" \
  "v_add_u32_dpp v108, v108, v108 row_shr:4 row_mask:0xf bank_mask:0xf bound_ctrl:1\n" \
  "s_nop 1\n" \
  "v_add_u32_dpp v108, v108, v108 row_shr:8 row_mask:0xf bank_mask:0xf bound_ctrl:1\n" \
  "s_nop 1\n" \
  "v_add_u32_dpp v108, v108, v108 row_bcast:15 row_mask:0xa bank_mask:0xf\n" \
  "s_nop 1\n" \
  "v_add_u32_dpp v108, v108, v108 row_bcast:31 row_mask:0xc bank_mask:0xf\n" \
  "s_nop 0\n" \
  "v_readlane_b32 s65, v108, 63\n" \
  "s_waitcnt lgkmcnt(0)\n" \
  "s_cmp_lg_u32 s65, 0\n" \
  "s_cbranch_scc1 Lpr%=\n" \
  "Lpw%=:\n" \
  "s_barrier\n" \
  "ds_read2_b32  v[10:11], v12 offset1:8\n" \
  "ds_read_b64 v[106:107], v14\n" \
  "ds_read_b64 v[104:105], v15\n" \
  "s_nop 0\n" \
  "s_setprio 1\n" \
  "v_mov_b64 v[32:33], 0\n" \
  "v_mov_b64 v[34:35], 0\n" \
  "v_mov_b64 v[36:37], 0\n" \
  "v_mov_b64 v[38:39], 0\n" \
  "v_mov_b64 v[40:41], 0\n" \
  "v_mov_b64 v[42:43], 0\n" \
  "v_mov_b64 v[44:45], 0\n" \
  "v_mov_b64 v[46:47], 0\n" \
  "s_waitcnt lgkmcnt(2)\n" \
  "v_readlane_b32 s80, v10, 0\n" \
  "v_readlane_b32 s81, v10, 1\n" \
  "v_readlane_b32 s82, v10, 2\n" \
  "v_readlane_b32 s83, v10, 3\n" \
  "v_readlane_b32 s84, v10, 4\n" \
  "v_readlane_b32 s85, v10, 5\n" \
  "v_readlane_b32 s86, v10, 6\n" \
  "s_andn2_b32 s61, s80, 15\n" \
  "buffer_load_dwordx4 v[120:123], v8, %[xs], s61 offen\n" \
  "s_andn2_b32 s61, s81, 15\n" \
  "buffer_load_dwordx4 v[124:127], v8, %[xs], s61 offen\n" \
  "s_andn2_b32 s61, s82, 15\n" \
  "buffer_load_dwordx4 v[128:131], v8, %[xs], s61 offen\n" \
  "s_andn2_b32 s61, s83, 15\n" \
  "buffer_load_dwordx4 v[132:135], v8, %[xs], s61 offen\n" \
  "s_andn2_b32 s61, s84, 15\n" \
  "buffer_load_dwordx4 v[136:139], v8, %[xs], s61 offen\n" \
  "s_andn2_b32 s61, s85, 15\n" \
  "buffer_load_dwordx4 v[140:143], v8, %[xs], s61 offen\n" \
  "v_mov_b64  v[96:97], 0\n" \
  "v_mov_b64  v[98:99], 0\n" \
  "v_mov_b64  v[100:101], 0\n" \
  "v_mov_b64  v[102:103], 0\n" \
  "s_waitcnt lgkmcnt(0)\n" \
  "v_mfma_f32_4x4x4_16b_bf16 v[80:83], v[104:105], v[28:29], 0 cbsz:4\n" \
  "v_mfma_f32_4x4x4_16b_bf16 v[84:87], v[104:105], v[30:31], 0 cbsz:4\n" \
  "v_mfma_f32_4x4x4_16b_bf16 v[112:115], v[106:107], v[20:21], 0\n" \
  "v_mfma_f32_4x4x4_16b_bf16 v[116:119], v[106:107], v[22:23], 0\n" \
  "v_mfma_f32_4x4x4_16b_bf16 v[72:75], v[106:107], v[24:25], 0\n" \
  "v_mfma_f32_4x4x4_16b_bf16 v[76:79], v[106:107], v[26:27], 0\n" \
  "v_cvt_pk_bf16_f32 v81, v80, v81\n" \
  "v_lshlrev_b32_sdwa v80, 1, v81 dst_sel:DWORD dst_unused:UNUSED_PAD src0_sel:DWORD src1_sel:WORD_0\n" \
  "v_lshlrev_b32_sdwa v81, 1, v81 dst_sel:DWORD dst_unused:UNUSED_PAD src0_sel:DWORD src1_sel:WORD_1\n" \
  "ds_read_u16_d16_hi v96, v80 offset:%[tb]\n" \
  "ds_read_u16_d16_hi v97, v81 offset:%[tb]\n" \
  "v_cvt_pk_bf16_f32 v85, v84, v85\n" \
  "v_lshlrev_b32_sdwa v84, 1, v85 dst_sel:DWORD dst_unused:UNUSED_PAD src0_sel:DWORD src1_sel:WORD_0\n" \
  "v_lshlrev_b32_sdwa v85, 1, v85 dst_sel:DWORD dst_unused:UNUSED_PAD src0_sel:DWORD src1_sel:WORD_1\n" \
  "ds_read_u16_d16_hi v98, v84 offset:%[tb]\n" \
  "ds_read_u16_d16_hi v99, v85 offset:%[tb]\n" \
  "v_cvt_pk_bf16_f32 v83, v82, v83\n" \
  "v_lshlrev_b32_sdwa v82, 1, v83 dst_sel:DWORD dst_unused:UNUSED_PAD src0_sel:DWORD src1_sel:WORD_0\n" \
  "v_lshlrev_b32_sdwa v83, 1, v83 dst_sel:DWORD dst_unused:UNUSED_PAD src0_sel:DWORD src1_sel:WORD_1\n" \
  "ds_read_u16_d16_hi v100, v82 offset:%[tb]\n" \
  "ds_read_u16_d16_hi v101, v83 offset:%[tb]\n" \
  "v_cvt_pk_bf16_f32 v108, v112, v116\n" \
  "v_cvt_pk_bf16_f32 v109, v72, v76\n" \
  "s_nop 0\n" \
  "s_waitcnt vmcnt(5)\n" \
  "v_mfma_f32_4x4x4_16b_bf16 v[72:75], v[108:109], v[120:121], 0 cbsz:4\n" \
  "v_mfma_f32_4x4x4_16b_bf16 v[76:79], v[108:109], v[122:123], 0 cbsz:4\n" \
  "s_andn2_b32 s61, s86, 15\n" \
  "buffer_load_dwordx4 v[144:147], v8, %[xs], s61 offen\n" \
  "s_waitcnt lgkmcnt(0)\n" \
  "s_nop 1\n" \
  "Lbody%=:\n" \
  "s_waitcnt lgkmcnt(2)\n" \
  "s_set_gpr_idx_on s80, gpr_idx(SRC2,DST)\n" \
  "v_pk_fma_f32 v[32:33], v[96:97], v[72:73], v[32:33] op_sel:[0,0,0] op_sel_hi:[0,1,1]\n" \
  "v_pk_fma_f32 v[42:43], v[96:97], v[74:75], v[42:43] op_sel:[0,0,0] op_sel_hi:[0,1,1]\n" \
  "v_pk_fma_f32 v[52:53], v[98:99], v[76:77], v[52:53] op_sel:[0,0,0] op_sel_hi:[0,1,1]\n" \
  "v_pk_fma_f32 v[62:63], v[98:99], v[78:79], v[62:63] op_sel:[0,0,0] op_sel_hi:[0,1,1]\n" \
  "s_set_gpr_idx_off\n" \
  "s_waitcnt vmcnt(5)\n" \
  "v_mfma_f32_4x4x4_16b_bf16 v[72:75], v[108:109], v[124:125], 0 cbsz:4 abid:1\n" \
  "v_mfma_f32_4x4x4_16b_bf16 v[76:79], v[108:109], v[126:127], 0 cbsz:4 abid:1\n" \
  "v_mfma_f32_4x4x4_16b_bf16 v[88:91], v[104:105], v[28:29], 0 cbsz:4 abid:1\n" \
  "v_mfma_f32_4x4x4_16b_bf16 v[92:95], v[104:105], v[30:31], 0 cbsz:4 abid:1\n" \
  "v_readlane_b32 s87, v10, 7\n" \
  "v_cvt_pk_bf16_f32 v87, v86, v87\n" \
  "v_lshlrev_b32_sdwa v86, 1, v87 dst_sel:DWORD dst_unused:UNUSED_PAD src0_sel:DWORD src1_sel:WORD_0\n" \
  "v_lshlrev_b32_sdwa v87, 1, v87 dst_sel:DWORD dst_unused:UNUSED_PAD src0_sel:DWORD src1_sel:WORD_1\n" \
  "s_andn2_b32 s61, s87, 15\n" \
  "buffer_load_dwordx4 v[148:151], v8, %[xs], s61 offen\n" \
  "ds_read_u16_d16_hi v102, v86 offset:%[tb]\n" \
  "ds_read_u16_d16_hi v103, v87 offset:%[tb]\n" \
  "v_mov_b32 v111, s60\n" \
  "ds_write2_b32 v110, v111, v111 offset1:8\n" \
  "s_set_gpr_idx_on s81, gpr_idx(SRC2,DST)\n" \
  "v_pk_fma_f32 v[32:33], v[96:97], v[72:73], v[32:33] op_sel:[1,0,0] op_sel_hi:[1,1,1]\n" \
  "v_pk_fma_f32 v[42:43], v[96:97], v[74:75], v[42:43] op_sel:[1,0,0] op_sel_hi:[1,1,1]\n" \
  "v_pk_fma_f32 v[52:53], v[98:99], v[76:77], v[52:53] op_sel:[1,0,0] op_sel_hi:[1,1,1]\n" \
  "v_pk_fma_f32 v[62:63], v[98:99], v[78:79], v[62:63] op_sel:[1,0,0] op_sel_hi:[1,1,1]\n" \
  "s_set_gpr_idx_off\n" \
  "s_waitcnt vmcnt(5) lgkmcnt(9)\n" \
  "v_mfma_f32_4x4x4_16b_bf16 v[72:75], v[108:109], v[128:129], 0 cbsz:4 abid:2\n" \
  "v_mfma_f32_4x4x4_16b_bf16 v[76:79], v[108:109], v[130:131], 0 cbsz:4 abid:2\n" \
  "v_readlane_b32 s80, v11, 0\n" \
  "v_cvt_pk_bf16_f32 v89, v88, v89\n" \
  "v_lshlrev_b32_sdwa v88, 1, v89 dst_sel:DWORD dst_unused:UNUSED_PAD src0_sel:DWORD src1_sel:WORD_0\n" \
  "v_lshlrev_b32_sdwa v89, 1, v89 dst_sel:DWORD dst_unused:UNUSED_PAD src0_sel:DWORD src1_sel:WORD_1\n" \
  "s_andn2_b32 s61, s80, 15\n" \
  "buffer_load_dwordx4 v[120:123], v8, %[xs], s61 offen\n" \
  "ds_read_u16_d16_hi v96, v88 offset:%[tb]\n" \
  "ds_read_u16_d16_hi v97, v89 offset:%[tb]\n" \
  "ds_read_b32 v111, v110 offset:16\n" \
  "s_waitcnt lgkmcnt(4)\n" \
  "s_set_gpr_idx_on s82, gpr_idx(SRC2,DST)\n" \
  "v_pk_fma_f32 v[32:33], v[100:101], v[72:73], v[32:33] op_sel:[0,0,0] op_sel_hi:[0,1,1]\n" \
  "v_pk_fma_f32 v[42:43], v[100:101], v[74:75], v[42:43] op_sel:[0,0,0] op_sel_hi:[0,1,1]\n" \
  "v_pk_fma_f32 v[52:53], v[102:103], v[76:77], v[52:53] op_sel:[0,0,0] op_sel_hi:[0,1,1]\n" \
  "v_pk_fma_f32 v[62:63], v[102:103], v[78:79], v[62:63] op_sel:[0,0,0] op_sel_hi:[0,1,1]\n" \
  "s_set_gpr_idx_off\n" \
  "s_waitcnt vmcnt(5)\n" \
  "v_mfma_f32_4x4x4_16b_bf16 v[72:75], v[108:109], v[132:133], 0 cbsz:4 abid:3\n" \
  "v_mfma_f32_4x4x4_16b_bf16 v[76:79], v[108:109], v[134:135], 0 cbsz:4 abid:3\n" \
  "v_readlane_b32 s81, v11, 1\n" \
  "v_cvt_pk_bf16_f32 v93, v92, v93\n" \
  "v_lshlrev_b32_sdwa v92, 1, v93 dst_sel:DWORD dst_unused:UNUSED_PAD src0_sel:DWORD src1_sel:WORD_0\n" \
  "v_lshlrev_b32_sdwa v93, 1, v93 dst_sel:DWORD dst_unused:UNUSED_PAD src0_sel:DWORD src1_sel:WORD_1\n" \
  "s_andn2_b32 s61, s81, 15\n" \
  "buffer_load_dwordx4 v[124:127], v8, %[xs], s61 offen\n" \
  "ds_read_u16_d16_hi v98, v92 offset:%[tb]\n" \
  "ds_read_u16_d16_hi v99, v93 offset:%[tb]\n" \
  "ds_read_b64 v[106:107], v14 offset:256\n" \
  "s_set_gpr_idx_on s83, gpr_idx(SRC2,DST)\n" \
  "v_pk_fma_f32 v[32:33], v[100:101], v[72:73], v[32:33] op_sel:[1,0,0] op_sel_hi:[1,1,1]\n" \
  "v_pk_fma_f32 v[42:43], v[100:101], v[74:75], v[42:43] op_sel:[1,0,0] op_sel_hi:[1,1,1]\n" \
  "v_pk_fma_f32 v[52:53], v[102:103], v[76:77], v[52:53] op_sel:[1,0,0] op_sel_hi:[1,1,1]\n" \
  "v_pk_fma_f32 v[62:63], v[102:103], v[78:79], v[62:63] op_sel:[1,0,0] op_sel_hi:[1,1,1]\n" \
  "s_set_gpr_idx_off\n" \
  "s_sub_u32 s60, s60, 1\n" \
  "s_cbranch_scc1 Lend%=\n" \
  "s_waitcnt vmcnt(5)\n" \
  "v_mfma_f32_4x4x4_16b_bf16 v[72:75], v[108:109], v[136:137], 0 cbsz:4 abid:4\n" \
  "v_mfma_f32_4x4x4_16b_bf16 v[76:79], v[108:109], v[138:139], 0 cbsz:4 abid:4\n" \
  "v_readlane_b32 s82, v11, 2\n" \
  "v_cvt_pk_bf16_f32 v91, v90, v91\n" \
  "v_lshlrev_b32_sdwa v90, 1, v91 dst_sel:DWORD dst_unused:UNUSED_PAD src0_sel:DWORD src1_sel:WORD_0\n" \
  "v_lshlrev_b32_sdwa v91, 1, v91 dst_sel:DWORD dst_unused:UNUSED_PAD src0_sel:DWORD src1_sel:WORD_1\n" \
  "s_andn2_b32 s61, s82, 15\n" \
  "buffer_load_dwordx4 v[128:131], v8, %[xs], s61 offen\n" \
  "ds_read_u16_d16_hi v100, v90 offset:%[tb]\n" \
  "ds_read_u16_d16_hi v101, v91 offset:%[tb]\n" \
  "v_add_u32 v14, 0x100, v14\n" \
  "s_waitcnt lgkmcnt(3)\n" \
  "s_set_gpr_idx_on s84, gpr_idx(SRC2,DST)\n" \
  "v_pk_fma_f32 v[32:33], v[96:97], v[72:73], v[32:33] op_sel:[0,0,0] op_sel_hi:[0,1,1]\n" \
  "v_pk_fma_f32 v[42:43], v[96:97], v[74:75], v[42:43] op_sel:[0,0,0] op_sel_hi:[0,1,1]\n" \
  "v_pk_fma_f32 v[52:53], v[98:99], v[76:77], v[52:53] op_sel:[0,0,0] op_sel_hi:[0,1,1]\n" \
  "v_pk_fma_f32 v[62:63], v[98:99], v[78:79], v[62:63] op_sel:[0,0,0] op_sel_hi:[0,1,1]\n" \
  "s_set_gpr_idx_off\n" \
  "s_waitcnt vmcnt(5)\n" \
  "v_mfma_f32_4x4x4_16b_bf16 v[72:75], v[108:109], v[140:141], 0 cbsz:4 abid:5\n" \
  "v_mfma_f32_4x4x4_16b_bf16 v[76:79], v[108:109], v[142:143], 0 cbsz:4 abid:5\n" \
  "v_mfma_f32_4x4x4_16b_bf16 v[80:83], v[104:105], v[28:29], 0 cbsz:4 abid:2\n" \
  "v_mfma_f32_4x4x4_16b_bf16 v[84:87], v[104:105], v[30:31], 0 cbsz:4 abid:2\n" \
  "v_readlane_b32 s83, v11, 3\n" \
  "v_cvt_pk_bf16_f32 v95, v94, v95\n" \
  "v_lshlrev_b32_sdwa v94, 1, v95 dst_sel:DWORD dst_unused:UNUSED_PAD src0_sel:DWORD src1_sel:WORD_0\n" \
  "v_lshlrev_b32_sdwa v95, 1, v95 dst_sel:DWORD dst_unused:UNUSED_PAD src0_sel:DWORD src1_sel:WORD_1\n" \
  "s_andn2_b32 s61, s83, 15\n" \
  "buffer_load_dwordx4 v[132:135], v8, %[xs], s61 offen\n" \
  "ds_read_u16_d16_hi v102, v94 offset:%[tb]\n" \
  "ds_read_u16_d16_hi v103, v95 offset:%[tb]\n" \
  "s_set_gpr_idx_on s85, gpr_idx(SRC2,DST)\n" \
  "v_pk_fma_f32 v[32:33], v[96:97], v[72:73], v[32:33] op_sel:[1,0,0] op_sel_hi:[1,1,1]\n" \
  "v_pk_fma_f32 v[42:43], v[96:97], v[74:75], v[42:43] op_sel:[1,0,0] op_sel_hi:[1,1,1]\n" \
  "v_pk_fma_f32 v[52:53], v[98:99], v[76:77], v[52:53] op_sel:[1,0,0] op_sel_hi:[1,1,1]\n" \
  "v_pk_fma_f32 v[62:63], v[98:99], v[78:79], v[62:63] op_sel:[1,0,0] op_sel_hi:[1,1,1]\n" \
  "s_set_gpr_idx_off\n" \
  "s_waitcnt vmcnt(5) lgkmcnt(7)\n" \
  "v_mfma_f32_4x4x4_16b_bf16 v[72:75], v[108:109], v[144:145], 0 cbsz:4 abid:6\n" \
  "v_mfma_f32_4x4x4_16b_bf16 v[76:79], v[108:109], v[146:147], 0 cbsz:4 abid:6\n" \
  "v_readlane_b32 s84, v11, 4\n" \
  "v_cvt_pk_bf16_f32 v81, v80, v81\n" \
  "v_lshlrev_b32_sdwa v80, 1, v81 dst_sel:DWORD dst_unused:UNUSED_PAD src0_sel:DWORD src1_sel:WORD_0\n" \
  "v_lshlrev_b32_sdwa v81, 1, v81 dst_sel:DWORD dst_unused:UNUSED_PAD src0_sel:DWORD src1_sel:WORD_1\n" \
  "s_andn2_b32 s61, s84, 15\n" \
  "buffer_load_dwordx4 v[136:139], v8, %[xs], s61 offen\n" \
  "ds_read_u16_d16_hi v96, v80 offset:%[tb]\n" \
  "ds_read_u16_d16_hi v97, v81 offset:%[tb]\n" \
  "ds_read_b32 v10, v12 offset:64\n" \
  "v_readfirstlane_b32 s66, v111\n" \
  "s_setprio 1\n" \
  "s_add_u32 s67, s60, %[fo]\n" \
  "s_cmp_ge_u32 s67, s66\n" \
  "s_cbranch_scc0 Lf5_%=\n" \
  "s_setprio 2\n" \
  "Lf5_%=:\n" \
  "s_waitcnt lgkmcnt(3)\n" \
  "s_set_gpr_idx_on s86, gpr_idx(SRC2,DST)\n" \
  "v_pk_fma_f32 v[32:33], v[100:101], v[72:73], v[32:33] op_sel:[0,0,0] op_sel_hi:[0,1,1]\n" \
  "v_pk_fma_f32 v[42:43], v[100:101], v[74:75], v[42:43] op_sel:[0,0,0] op_sel_hi:[0,1,1]\n" \
  "v_pk_fma_f32 v[52:53], v[102:103], v[76:77], v[52:53] op_sel:[0,0,0] op_sel_hi:[0,1,1]\n" \
  "v_pk_fma_f32 v[62:63], v[102:103], v[78:79], v[62:63] op_sel:[0,0,0] op_sel_hi:[0,1,1]\n" \
  "s_set_gpr_idx_off\n" \
  "s_waitcnt vmcnt(5) lgkmcnt(7)\n" \
  "v_mfma_f32_4x4x4_16b_bf16 v[72:75], v[108:109], v[148:149], 0 cbsz:4 abid:7\n" \
  "v_mfma_f32_4x4x4_16b_bf16 v[76:79], v[108:109], v[150:151], 0 cbsz:4 abid:7\n" \
  "v_mfma_f32_4x4x4_16b_bf16 v[112:115], v[106:107], v[20:21], 0\n" \
  "v_readlane_b32 s85, v11, 5\n" \
  "v_cvt_pk_bf16_f32 v85, v84, v85\n" \
  "v_lshlrev_b32_sdwa v84, 1, v85 dst_sel:DWORD dst_unused:UNUSED_PAD src0_sel:DWORD src1_sel:WORD_0\n" \
  "v_lshlrev_b32_sdwa v85, 1, v85 dst_sel:DWORD dst_unused:UNUSED_PAD src0_sel:DWORD src1_sel:WORD_1\n" \
  "s_andn2_b32 s61, s85, 15\n" \
  "buffer_load_dwordx4 v[140:143], v8, %[xs], s61 offen\n" \
  "ds_read_u16_d16_hi v98, v84 offset:%[tb]\n" \
  "ds_read_u16_d16_hi v99, v85 offset:%[tb]\n" \
  "v_add_u32 v12, 64, v12\n" \
  "s_set_gpr_idx_on s87, gpr_idx(SRC2,DST)\n" \
  "v_pk_fma_f32 v[32:33], v[100:101], v[72:73], v[32:33] op_sel:[1,0,0] op_sel_hi:[1,1,1]\n" \
  "v_pk_fma_f32 v[42:43], v[100:101], v[74:75], v[42:43] op_sel:[1,0,0] op_sel_hi:[1,1,1]\n" \
  "v_pk_fma_f32 v[52:53], v[102:103], v[76:77], v[52:53] op_sel:[1,0,0] op_sel_hi:[1,1,1]\n" \
  "v_pk_fma_f32 v[62:63], v[102:103], v[78:79], v[62:63] op_sel:[1,0,0] op_sel_hi:[1,1,1]\n" \
  "s_set_gpr_idx_off\n" \
  "s_sub_u32 s60, s60, 1\n" \
  "s_cbranch_scc1 Lend%=\n" \
  "s_waitcnt vmcnt(5)\n" \
  "v_mfma_f32_4x4x4_16b_bf16 v[72:75], v[108:109], v[120:121], 0 cbsz:4 abid:8\n" \
  "v_mfma_f32_4x4x4_16b_bf16 v[76:79], v[108:109], v[122:123], 0 cbsz:4 abid:8\n" \
  "v_readlane_b32 s86, v11, 6\n" \
  "v_cvt_pk_bf16_f32 v83, v82, v83\n" \
  "v_lshlrev_b32_sdwa v82, 1, v83 dst_sel:DWORD dst_unused:UNUSED_PAD src0_sel:DWORD src1_sel:WORD_0\n" \
  "v_lshlrev_b32_sdwa v83, 1, v83 dst_sel:DWORD dst_unused:UNUSED_PAD src0_sel:DWORD src1_sel:WORD_1\n" \
  "s_andn2_b32 s61, s86, 15\n" \
  "buffer_load_dwordx4 v[144:147], v8, %[xs], s61 offen\n" \
  "ds_read_u16_d16_hi v100, v82 offset:%[tb]\n" \
  "ds_read_u16_d16_hi v101, v83 offset:%[tb]\n" \
  "s_waitcnt lgkmcnt(2)\n" \
  "s_set_gpr_idx_on s80, gpr_idx(SRC2,DST)\n" \
  "v_pk_fma_f32 v[32:33], v[96:97], v[72:73], v[32:33] op_sel:[0,0,0] op_sel_hi:[0,1,1]\n" \
  "v_pk_fma_f32 v[42:43], v[96:97], v[74:75], v[42:43] op_sel:[0,0,0] op_sel_hi:[0,1,1]\n" \
  "v_pk_fma_f32 v[52:53], v[98:99], v[76:77], v[52:53] op_sel:[0,0,0] op_sel_hi:[0,1,1]\n" \
  "v_pk_fma_f32 v[62:63], v[98:99], v[78:79], v[62:63] op_sel:[0,0,0] op_sel_hi:[0,1,1]\n" \
  "s_set_gpr_idx_off\n" \
  "s_waitcnt vmcnt(5)\n" \
  "v_mfma_f32_4x4x4_16b_bf16 v[72:75], v[108:109], v[124:125], 0 cbsz:4 abid:9\n" \
  "v_mfma_f32_4x4x4_16b_bf16 v[76:79], v[108:109], v[126:127], 0 cbsz:4 abid:9\n" \
  "v_mfma_f32_4x4x4_16b_bf16 v[88:91], v[104:105], v[28:29], 0 cbsz:4 abid:3\n" \
  "v_mfma_f32_4x4x4_16b_bf16 v[92:95], v[104:105], v[30:31], 0 cbsz:4 abid:3\n" \
  "v_readlane_b32 s87, v11, 7\n" \
  "v_cvt_pk_bf16_f32 v87, v86, v87\n" \
  "v_lshlrev_b32_sdwa v86, 1, v87 dst_sel:DWORD dst_unused:UNUSED_PAD src0_sel:DWORD src1_sel:WORD_0\n" \
  "v_lshlrev_b32_sdwa v87, 1, v87 dst_sel:DWORD dst_unused:UNUSED_PAD src0_sel:DWORD src1_sel:WORD_1\n" \
  "s_andn2_b32 s61, s87, 15\n" \
  "buffer_load_dwordx4 v[148:151], v8, %[xs], s61 offen\n" \
  "ds_read_u16_d16_hi v102, v86 offset:%[tb]\n" \
  "ds_read_u16_d16_hi v103, v87 offset:%[tb]\n" \
  "v_mov_b32 v111, s60\n" \
  "ds_write2_b32 v110, v111, v111 offset1:8\n" \
  "s_set_gpr_idx_on s81, gpr_idx(SRC2,DST)\n" \
  "v_pk_fma_f32 v[32:33], v[96:97], v[72:73], v[32:33] op_sel:[1,0,0] op_sel_hi:[1,1,1]\n" \
  "v_pk_fma_f32 v[42:43], v[96:97], v[74:75], v[42:43] op_sel:[1,0,0] op_sel_hi:[1,1,1]\n" \
  "v_pk_fma_f32 v[52:53], v[98:99], v[76:77], v[52:53] op_sel:[1,0,0] op_sel_hi:[1,1,1]\n" \
  "v_pk_fma_f32 v[62:63], v[98:99], v[78:79], v[62:63] op_sel:[1,0,0] op_sel_hi:[1,1,1]\n" \
  "s_set_gpr_idx_off\n" \
  "s_waitcnt vmcnt(5) lgkmcnt(7)\n" \
  "v_mfma_f32_4x4x4_16b_bf16 v[72:75], v[108:109], v[128:129], 0 cbsz:4 abid:10\n" \
  "v_mfma_f32_4x4x4_16b_bf16 v[76:79], v[108:109], v[130:131], 0 cbsz:4 abid:10\n" \
  "v_readlane_b32 s80, v10, 0\n" \
  "v_cvt_pk_bf16_f32 v89, v88, v89\n" \
  "v_lshlrev_b32_sdwa v88, 1, v89 dst_sel:DWORD dst_unused:UNUSED_PAD src0_sel:DWORD src1_sel:WORD_0\n" \
  "v_lshlrev_b32_sdwa v89, 1, v89 dst_sel:DWORD dst_unused:UNUSED_PAD src0_sel:DWORD src1_sel:WORD_1\n" \
  "s_andn2_b32 s61, s80, 15\n" \
  "buffer_load_dwordx4 v[120:123], v8, %[xs], s61 offen\n" \
  "ds_read_u16_d16_hi v96, v88 offset:%[tb]\n" \
  "ds_read_u16_d16_hi v97, v89 offset:%[tb]\n" \
  "ds_read_b64 v[104:105], v15 offset:256\n" \
  "ds_read_b32 v111, v110 offset:16\n" \
  "s_waitcnt lgkmcnt(5)\n" \
  "s_set_gpr_idx_on s82, gpr_idx(SRC2,DST)\n" \
  "v_pk_fma_f32 v[32:33], v[100:101], v[72:73], v[32:33] op_sel:[0,0,0] op_sel_hi:[0,1,1]\n" \
  "v_pk_fma_f32 v[42:43], v[100:101], v[74:75], v[42:43] op_sel:[0,0,0] op_sel_hi:[0,1,1]\n" \
  "v_pk_fma_f32 v[52:53], v[102:103], v[76:77], v[52:53] op_sel:[0,0,0] op_sel_hi:[0,1,1]\n" \
  "v_pk_fma_f32 v[62:63], v[102:103], v[78:79], v[62:63] op_sel:[0,0,0] op_sel_hi:[0,1,1]\n" \
  "s_set_gpr_idx_off\n" \
  "s_waitcnt vmcnt(5)\n" \
  "v_mfma_f32_4x4x4_16b_bf16 v[72:75], v[108:109], v[132:133], 0 cbsz:4 abid:11\n" \
  "v_mfma_f32_4x4x4_16b_bf16 v[76:79], v[108:109], v[134:135], 0 cbsz:4 abid:11\n" \
  "v_mfma_f32_4x4x4_16b_bf16 v[116:119], v[106:107], v[22:23], 0\n" \
  "v_readlane_b32 s81, v10, 1\n" \
  "v_cvt_pk_bf16_f32 v93, v92, v93\n" \
  "v_lshlrev_b32_sdwa v92, 1, v93 dst_sel:DWORD dst_unused:UNUSED_PAD src0_sel:DWORD src1_sel:WORD_0\n" \
  "v_lshlrev_b32_sdwa v93, 1, v93 dst_sel:DWORD dst_unused:UNUSED_PAD src0_sel:DWORD src1_sel:WORD_1\n" \
  "s_andn2_b32 s61, s81, 15\n" \
  "buffer_load_dwordx4 v[124:127], v8, %[xs], s61 offen\n" \
  "ds_read_u16_d16_hi v98, v92 offset:%[tb]\n" \
  "ds_read_u16_d16_hi v99, v93 offset:%[tb]\n" \
  "s_set_gpr_idx_on s83, gpr_idx(SRC2,DST)\n" \
  "v_pk_fma_f32 v[32:33], v[100:101], v[72:73], v[32:33] op_sel:[1,0,0] op_sel_hi:[1,1,1]\n" \
  "v_pk_fma_f32 v[42:43], v[100:101], v[74:75], v[42:43] op_sel:[1,0,0] op_sel_hi:[1,1,1]\n" \
  "v_pk_fma_f32 v[52:53], v[102:103], v[76:77], v[52:53] op_sel:[1,0,0] op_sel_hi:[1,1,1]\n" \
  "v_pk_fma_f32 v[62:63], v[102:103], v[78:79], v[62:63] op_sel:[1,0,0] op_sel_hi:[1,1,1]\n" \
  "s_set_gpr_idx_off\n" \
  "s_sub_u32 s60, s60, 1\n" \
  "s_cbranch_scc1 Lend%=\n" \
  "s_waitcnt vmcnt(5)\n" \
  "v_mfma_f32_4x4x4_16b_bf16 v[72:75], v[108:109], v[136:137], 0 cbsz:4 abid:12\n" \
  "v_mfma_f32_4x4x4_16b_bf16 v[76:79], v[108:109], v[138:139], 0 cbsz:4 abid:12\n" \
  "v_readlane_b32 s82, v10, 2\n" \
  "v_cvt_pk_bf16_f32 v91, v90, v91\n" \
  "v_lshlrev_b32_sdwa v90, 1, v91 dst_sel:DWORD dst_unused:UNUSED_PAD src0_sel:DWORD src1_sel:WORD_0\n" \
  "v_lshlrev_b32_sdwa v91, 1, v91 dst_sel:DWORD dst_unused:UNUSED_PAD src0_sel:DWORD src1_sel:WORD_1\n" \
  "s_andn2_b32 s61, s82, 15\n" \
  "buffer_load_dwordx4 v[128:131], v8, %[xs], s61 offen\n" \
  "ds_read_u16_d16_hi v100, v90 offset:%[tb]\n" \
  "ds_read_u16_d16_hi v101, v91 offset:%[tb]\n" \
  "v_add_u32 v15, 0x100, v15\n" \
  "s_waitcnt lgkmcnt(2)\n" \
  "s_set_gpr_idx_on s84, gpr_idx(SRC2,DST)\n" \
  "v_pk_fma_f32 v[32:33], v[96:97], v[72:73], v[32:33] op_sel:[0,0,0] op_sel_hi:[0,1,1]\n" \
  "v_pk_fma_f32 v[42:43], v[96:97], v[74:75], v[42:43] op_sel:[0,0,0] op_sel_hi:[0,1,1]\n" \
  "v_pk_fma_f32 v[52:53], v[98:99], v[76:77], v[52:53] op_sel:[0,0,0] op_sel_hi:[0,1,1]\n" \
  "v_pk_fma_f32 v[62:63], v[98:99], v[78:79], v[62:63] op_sel:[0,0,0] op_sel_hi:[0,1,1]\n" \
  "s_set_gpr_idx_off\n" \
  "s_waitcnt vmcnt(5) lgkmcnt(5)\n" \
  "v_mfma_f32_4x4x4_16b_bf16 v[72:75], v[108:109], v[140:141], 0 cbsz:4 abid:13\n" \
  "v_mfma_f32_4x4x4_16b_bf16 v[76:79], v[108:109], v[142:143], 0 cbsz:4 abid:13\n" \
  "v_mfma_f32_4x4x4_16b_bf16 v[80:83], v[104:105], v[28:29], 0 cbsz:4\n" \
  "v_mfma_f32_4x4x4_16b_bf16 v[84:87], v[104:105], v[30:31], 0 cbsz:4\n" \
  "v_readlane_b32 s83, v10, 3\n" \
  "v_cvt_pk_bf16_f32 v95, v94, v95\n" \
  "v_lshlrev_b32_sdwa v94, 1, v95 dst_sel:DWORD dst_unused:UNUSED_PAD src0_sel:DWORD src1_sel:WORD_0\n" \
  "v_lshlrev_b32_sdwa v95, 1, v95 dst_sel:DWORD dst_unused:UNUSED_PAD src0_sel:DWORD src1_sel:WORD_1\n" \
  "s_andn2_b32 s61, s83, 15\n" \
  "buffer_load_dwordx4 v[132:135], v8, %[xs], s61 offen\n" \
  "ds_read_u16_d16_hi v102, v94 offset:%[tb]\n" \
  "ds_read_u16_d16_hi v103, v95 offset:%[tb]\n" \
  "ds_read_b32 v11, v12 offset:32\n" \
  "s_set_gpr_idx_on s85, gpr_idx(SRC2,DST)\n" \
  "v_pk_fma_f32 v[32:33], v[96:97], v[72:73], v[32:33] op_sel:[1,0,0] op_sel_hi:[1,1,1]\n" \
  "v_pk_fma_f32 v[42:43], v[96:97], v[74:75], v[42:43] op_sel:[1,0,0] op_sel_hi:[1,1,1]\n" \
  "v_pk_fma_f32 v[52:53], v[98:99], v[76:77], v[52:53] op_sel:[1,0,0] op_sel_hi:[1,1,1]\n" \
  "v_pk_fma_f32 v[62:63], v[98:99], v[78:79], v[62:63] op_sel:[1,0,0] op_sel_hi:[1,1,1]\n" \
  "s_set_gpr_idx_off\n" \
  "s_waitcnt vmcnt(5) lgkmcnt(7)\n" \
  "v_mfma_f32_4x4x4_16b_bf16 v[72:75], v[108:109], v[144:145], 0 cbsz:4 abid:14\n" \
  "v_mfma_f32_4x4x4_16b_bf16 v[76:79], v[108:109], v[146:147], 0 cbsz:4 abid:14\n" \
  "v_readlane_b32 s84, v10, 4\n" \
  "v_cvt_pk_bf16_f32 v81, v80, v81\n" \
  "v_lshlrev_b32_sdwa v80, 1, v81 dst_sel:DWORD dst_unused:UNUSED_PAD src0_sel:DWORD src1_sel:WORD_0\n" \
  "v_lshlrev_b32_sdwa v81, 1, v81 dst_sel:DWORD dst_unused:UNUSED_PAD src0_sel:DWORD src1_sel:WORD_1\n" \
  "s_andn2_b32 s61, s84, 15\n" \
  "buffer_load_dwordx4 v[136:139], v8, %[xs], s61 offen\n" \
  "ds_read_u16_d16_hi v96, v80 offset:%[tb]\n" \
  "ds_read_u16_d16_hi v97, v81 offset:%[tb]\n" \
  "v_readfirstlane_b32 s66, v111\n" \
  "s_setprio 1\n" \
  "s_add_u32 s67, s60, %[fo]\n" \
  "s_cmp_ge_u32 s67, s66\n" \
  "s_cbranch_scc0 Lf13_%=\n" \
  "s_setprio 2\n" \
  "Lf13_%=:\n" \
  "v_cvt_pk_bf16_f32 v19, v112, v116\n" \
  "s_waitcnt lgkmcnt(3)\n" \
  "s_set_gpr_idx_on s86, gpr_idx(SRC2,DST)\n" \
  "v_pk_fma_f32 v[32:33], v[100:101], v[72:73], v[32:33] op_sel:[0,0,0] op_sel_hi:[0,1,1]\n" \
  "v_pk_fma_f32 v[42:43], v[100:101], v[74:75], v[42:43] op_sel:[0,0,0] op_sel_hi:[0,1,1]\n" \
  "v_pk_fma_f32 v[52:53], v[102:103], v[76:77], v[52:53] op_sel:[0,0,0] op_sel_hi:[0,1,1]\n" \
  "v_pk_fma_f32 v[62:63], v[102:103], v[78:79], v[62:63] op_sel:[0,0,0] op_sel_hi:[0,1,1]\n" \
  "s_set_gpr_idx_off\n" \
  "s_waitcnt vmcnt(5)\n" \
  "v_mfma_f32_4x4x4_16b_bf16 v[112:115], v[106:107], v[24:25], 0\n" \
  "v_mfma_f32_4x4x4_16b_bf16 v[116:119], v[106:107], v[26:27], 0\n" \
  "v_mfma_f32_4x4x4_16b_bf16 v[72:75], v[108:109], v[148:149], 0 cbsz:4 abid:15\n" \
  "v_mfma_f32_4x4x4_16b_bf16 v[76:79], v[108:109], v[150:151], 0 cbsz:4 abid:15\n" \
  "v_readlane_b32 s85, v10, 5\n" \
  "v_cvt_pk_bf16_f32 v85, v84, v85\n" \
  "v_lshlrev_b32_sdwa v84, 1, v85 dst_sel:DWORD dst_unused:UNUSED_PAD src0_sel:DWORD src1_sel:WORD_0\n" \
  "v_lshlrev_b32_sdwa v85, 1, v85 dst_sel:DWORD dst_unused:UNUSED_PAD src0_sel:DWORD src1_sel:WORD_1\n" \
  "s_andn2_b32 s61, s85, 15\n" \
  "buffer_load_dwordx4 v[140:143], v8, %[xs], s61 offen\n" \
  "ds_read_u16_d16_hi v98, v84 offset:%[tb]\n" \
  "ds_read_u16_d16_hi v99, v85 offset:%[tb]\n" \
  "v_cvt_pk_bf16_f32 v109, v112, v116\n" \
  "v_mov_b32 v108, v19\n" \
  "s_set_gpr_idx_on s87, gpr_idx(SRC2,DST)\n" \
  "v_pk_fma_f32 v[32:33], v[100:101], v[72:73], v[32:33] op_sel:[1,0,0] op_sel_hi:[1,1,1]\n" \
  "v_pk_fma_f32 v[42:43], v[100:101], v[74:75], v[42:43] op_sel:[1,0,0] op_sel_hi:[1,1,1]\n" \
  "v_pk_fma_f32 v[52:53], v[102:103], v[76:77], v[52:53] op_sel:[1,0,0] op_sel_hi:[1,1,1]\n" \
  "v_pk_fma_f32 v[62:63], v[102:103], v[78:79], v[62:63] op_sel:[1,0,0] op_sel_hi:[1,1,1]\n" \
  "s_set_gpr_idx_off\n" \
  "s_sub_u32 s60, s60, 1\n" \
  "s_cbranch_scc1 Lend%=\n" \
  "s_waitcnt vmcnt(5)\n" \
  "v_mfma_f32_4x4x4_16b_bf16 v[72:75], v[108:109], v[120:121], 0 cbsz:4\n" \
  "v_mfma_f32_4x4x4_16b_bf16 v[76:79], v[108:109], v[122:123], 0 cbsz:4\n" \
  "v_readlane_b32 s86, v10, 6\n" \
  "v_cvt_pk_bf16_f32 v83, v82, v83\n" \
  "v_lshlrev_b32_sdwa v82, 1, v83 dst_sel:DWORD dst_unused:UNUSED_PAD src0_sel:DWORD src1_sel:WORD_0\n" \
  "v_lshlrev_b32_sdwa v83, 1, v83 dst_sel:DWORD dst_unused:UNUSED_PAD src0_sel:DWORD src1_sel:WORD_1\n" \
  "s_andn2_b32 s61, s86, 15\n" \
  "buffer_load_dwordx4 v[144:147], v8, %[xs], s61 offen\n" \
  "ds_read_u16_d16_hi v100, v82 offset:%[tb]\n" \
  "ds_read_u16_d16_hi v101, v83 offset:%[tb]\n" \
  "s_branch Lbody%=\n" \
  "Lend%=:\n" \
  "s_setprio 3\n" \
  "v_mov_b32 v20, 0\n" \
  "v_mov_b32 v21, %[bit]\n" \
  "v_cvt_pk_bf16_f32 v32, v32, v52\n" \
  "v_cvt_pk_bf16_f32 v33, v33, v53\n" \
  "v_cvt_pk_bf16_f32 v42, v42, v62\n" \
  "v_cvt_pk_bf16_f32 v43, v43, v63\n" \
  "v_cvt_pk_bf16_f32 v34, v34, v54\n" \
  "v_cvt_pk_bf16_f32 v35, v35, v55\n" \
  "v_cvt_pk_bf16_f32 v44, v44, v64\n" \
  "v_cvt_pk_bf16_f32 v45, v45, v65\n" \
  "ds_write2st64_b32 v152, v32, v34 offset0:0 offset1:4\n" \
  "ds_write2st64_b32 v153, v33, v35 offset0:0 offset1:4\n" \
  "ds_write2st64_b32 v154, v42, v44 offset0:0 offset1:4\n" \
  "ds_write2st64_b32 v155, v43, v45 offset0:0 offset1:4\n" \
  "v_cvt_pk_bf16_f32 v36, v36, v56\n" \
  "v_cvt_pk_bf16_f32 v37, v37, v57\n" \
  "v_cvt_pk_bf16_f32 v46, v46, v66\n" \
  "v_cvt_pk_bf16_f32 v47, v47, v67\n" \
  "v_cvt_pk_bf16_f32 v38, v38, v58\n" \
  "v_cvt_pk_bf16_f32 v39, v39, v59\n" \
  "v_cvt_pk_bf16_f32 v48, v48, v68\n" \
  "v_cvt_pk_bf16_f32 v49, v49, v69\n" \
  "ds_write2st64_b32 v152, v36, v38 offset0:8 offset1:12\n" \
  "ds_write2st64_b32 v153, v37, v39 offset0:8 offset1:12\n" \
  "ds_write2st64_b32 v154, v46, v48 offset0:8 offset1:12\n" \
  "ds_write2st64_b32 v155, v47, v49 offset0:8 offset1:12\n" \
  "v_cvt_pk_bf16_f32 v40, v40, v60\n" \
  "v_cvt_pk_bf16_f32 v41, v41, v61\n" \
  "v_cvt_pk_bf16_f32 v50, v50, v70\n" \
  "v_cvt_pk_bf16_f32 v51, v51, v71\n" \
  "ds_write2_b32 v156, v40, v41 offset0:0 offset1:16\n" \
  "ds_write2_b32 v156, v50, v51 offset0:32 offset1:48\n" \
  "s_mov_b64 exec, 1\n" \
  "ds_or_b32 v20, v21 offset:%[rd]\n" \
  "s_endpgm\n" \
  "Lhr%=:\n" \
  "s_mov_b64 exec, vcc\n" \
  "s_sub_u32 s63, s63, 1\n" \
  "s_cbranch_scc0 Lh%=\n" \
  "s_mov_b64 exec, -1\n" \
  "Lover%=:\n" \
  "v_mov_b32 v113, 1\n" \
  "ds_write_b32 v117, v113 offset:%[fl]\n" \
  "s_mov_b32 s62, 0\n" \
  "v_mov_b32 v114, 0\n" \
  "v_mov_b32 v115, 0\n" \
  "s_branch Lld%=\n" \
  "Lkr%=:\n" \
  "buffer_load_dword v84, v76, %[ws], %[rk] offen sc1\n" \
  "buffer_load_dword v85, v77, %[ws], %[rk] offen sc1\n" \
  "buffer_load_dword v86, v78, %[ws], %[rk] offen sc1\n" \
  "s_cmpk_le_u32 s62, 0xc0\n" \
  "s_cbranch_scc1 Lkb%=\n" \
  "buffer_load_dword v87, v79, %[ws], %[rk] offen sc1\n" \
  "Lkb%=:\n" \
  "s_waitcnt vmcnt(0)\n" \
  "s_sub_u32 s63, s63, 1\n" \
  "s_cbranch_scc0 Lk%=\n" \
  "v_mov_b32 v113, 1\n" \
  "ds_write_b32 v117, v113 offset:%[fl]\n" \
  "s_mov_b64 s[66:67], 0\n" \
  "s_mov_b64 s[68:69], 0\n" \
  "s_mov_b64 s[70:71], 0\n" \
  "s_mov_b64 s[72:73], 0\n" \
  "s_branch Lkc%=\n" \
  "Lc3%=:\n" \
  "v_lshlrev_b32 v91, 1, v87\n" \
  "v_lshlrev_b32 v95, 4, v87\n" \
  "v_lshrrev_b32 v107, 2, v87\n" \
  "v_and_or_b32 v91, v91, v118, %[wb]\n" \
  "v_lshlrev_b32_e64 v103, v95, 4\n" \
  "v_and_b32 v107, 0xfc, v107\n" \
  "s_mov_b64 exec, s[72:73]\n" \
  "ds_add_rtn_u32 v99, v91, v103 offset:%[hi]\n" \
  "ds_add_u32 v107, v116 offset:%[dg]\n" \
  "s_mov_b64 exec, -1\n" \
  "s_branch Lc4%=\n" \
  "Llo%=:\n" \
  "v_mov_b32 v113, 1\n" \
  "ds_write_b32 v117, v113 offset:%[fl]\n" \
  "s_branch Llc%=\n" \
  "Lslow%=:\n" \
  "s_waitcnt vmcnt(0) lgkmcnt(0)\n" \
  "s_barrier\n" \
  "s_branch Lexit%=\n" \
  "Lq3%=:\n" \
  "s_mov_b64 exec, -1\n" \
  "v_and_b32 v103, 0x7fc, v91\n" \
  "ds_read_b32 v79, v91 offset:%[hi]\n" \
  "ds_read_b32 v107, v103 offset:%[to]\n" \
  "s_waitcnt lgkmcnt(0)\n" \
  "v_add3_u32 v91, v79, v107, v99\n" \
  "v_lshrrev_b32 v91, v95, v91\n" \
  "v_bfe_u32 v75, v91, 13, 3\n" \
  "v_and_b32 v91, 0x1ffc, v91\n" \
  "v_lshl_or_b32 v75, v75, 1, v87\n" \
  "s_mov_b64 exec, s[72:73]\n" \
  "ds_write_b32 v91, v75 offset:%[so]\n" \
  "s_branch Lq4%=\n" \
  "Lpr%=:\n" \
  "v_mov_b32 v44, 0\n" \
  "v_mov_b32 v46, 0\n" \
  "s_cmpk_le_u32 s62, 0xc0\n" \
  "s_cbranch_scc1 Lpb%=\n" \
  "buffer_load_dwordx4 v[44:47], v83, %[ws], %[rp] offen sc1\n" \
  "Lpb%=:\n" \
  "buffer_load_dwordx4 v[32:35], v80, %[ws], %[rp] offen sc1\n" \
  "buffer_load_dwordx4 v[36:39], v81, %[ws], %[rp] offen sc1\n" \
  "buffer_load_dwordx4 v[40:43], v82, %[ws], %[rp] offen sc1\n" \
  "s_sub_u32 s63, s63, 1\n" \
  "s_waitcnt vmcnt(0)\n" \
  "s_cbranch_scc0 Lpc%=\n" \
  "s_branch Lpw%=\n" \
  "Lexit%=:\n" \
  "s_mov_b32 %[sl], s61\n"
#define STREAM_CLOBBERS \
  "v10", "v11", "v19", "v21", "v22", "v23", "v24", "v25", "v26", "v27", "v28", "v29", "v30", "v31", "v32", \
  "v33", "v34", "v35", "v36", "v37", "v38", "v39", "v40", "v41", "v42", "v43", "v44", "v45", "v46", "v47", \
  "v48", "v49", "v50", "v51", "v52", "v53", "v54", "v55", "v56", "v57", "v58", "v59", "v60", "v61", "v62", \
  "v63", "v64", "v65", "v66", "v67", "v68", "v69", "v70", "v71", "v72", "v73", "v74", "v75", "v76", "v77", \
  "v78", "v79", "v80", "v81", "v82", "v83", "v84", "v85", "v86", "v87", "v88", "v89", "v90", "v91", "v92", \
  "v93", "v94", "v95", "v96", "v97", "v98", "v99", "v100", "v101", "v102", "v103", "v104", "v105", "v106", \
  "v107", "v108", "v109", "v111", "v112", "v113", "v114", "v115", "v116", "v117", "v118", "v119", "v120", \
  "v121", "v122", "v123", "v124", "v125", "v126", "v127", "v128", "v129", "v130", "v131", "v132", "v133", \
  "v134", "v135", "s60", "s61", "s62", "s63", "s64", "s65", "s66", "s67", "s68", "s69", "s70", "s71", "s72", \
  "s73", "s74", "s75", "s76", "s77", "s78", "s79", "s80", "s81", "s82", "s83", "s84", "s85", "s86", "s87", \
  "scc", "vcc", "memory"
#define RKL 3
#define MIX_ASM \
  "v_mov_b32 v12, 0\n" \
  "s_movk_i32 s60, 0x3ff\n" \
  "Lz%=:\n" \
  "s_sleep %[sl]\n" \
  "ds_read_b32 v13, v1\n" \
  "ds_read_b32 v14, v12 offset:%[rd]\n" \
  "s_waitcnt lgkmcnt(0)\n" \
  "v_readfirstlane_b32 s61, v13\n" \
  "v_readfirstlane_b32 s62, v14\n" \
  "s_cmp_gt_u32 s61, 4\n" \
  "s_cselect_b32 s61, s62, 1\n" \
  "s_cmp_eq_u32 s61, 0\n" \
  "s_cbranch_scc1 Lz%=\n" \
  "s_setprio 1\n" \
  "Lp%=:\n" \
  "ds_read_b32 v13, v12 offset:%[rd]\n" \
  "s_waitcnt lgkmcnt(0)\n" \
  "v_readfirstlane_b32 s61, v13\n" \
  "s_andn2_b32 s62, 15, s61\n" \
  "s_cselect_b32 s62, 0, 0x100\n" \
  "s_andn2_b32 s63, 0xf0, s61\n" \
  "s_cselect_b32 s63, 0, 0x200\n" \
  "s_or_b32 s61, s61, s62\n" \
  "s_or_b32 s61, s61, s63\n" \
  "s_and_b32 s61, s61, s60\n" \
  "s_cbranch_scc1 Lgo%=\n" \
  "s_sleep 1\n" \
  "s_branch Lp%=\n" \
  "Lgo%=:\n" \
  "s_andn2_b32 s60, s60, s61\n" \
  "s_ff1_i32_b32 s62, s61\n" \
  "s_bitset0_b32 s61, s62\n" \
  "s_lshl_b32 s63, s62, 12\n" \
  "s_cmp_ge_u32 s62, 8\n" \
  "s_cselect_b32 s64, %[r5], 0\n" \
  "s_add_u32 s63, s63, s64\n" \
  "v_add_u32 v14, s63, v0\n" \
  "ds_read_b128 v[64:67], v14 offset:0\n" \
  "ds_read_b128 v[68:71], v14 offset:256\n" \
  "ds_read_b128 v[72:75], v14 offset:512\n" \
  "ds_read_b128 v[76:79], v14 offset:768\n" \
  "s_set_gpr_idx_on s62, gpr_idx(SRC0)\n" \
  "v_mov_b32 v16, v2\n" \
  "s_set_gpr_idx_off\n" \
  "s_cmp_eq_u32 s61, 0\n" \
  "s_cbranch_scc1 Ls%=\n" \
  "s_ff1_i32_b32 s62, s61\n" \
  "s_bitset0_b32 s61, s62\n" \
  "s_lshl_b32 s63, s62, 12\n" \
  "s_cmp_ge_u32 s62, 8\n" \
  "s_cselect_b32 s64, %[r5], 0\n" \
  "s_add_u32 s63, s63, s64\n" \
  "v_add_u32 v14, s63, v0\n" \
  "ds_read_b128 v[80:83], v14 offset:0\n" \
  "ds_read_b128 v[84:87], v14 offset:256\n" \
  "ds_read_b128 v[88:91], v14 offset:512\n" \
  "ds_read_b128 v[92:95], v14 offset:768\n" \
  "s_set_gpr_idx_on s62, gpr_idx(SRC0)\n" \
  "v_mov_b32 v17, v2\n" \
  "s_set_gpr_idx_off\n" \
  "s_waitcnt lgkmcnt(4)\n" \
  "v_mfma_f32_16x16x32_bf16 v[96:99], v[64:67], v[32:35], 0\n" \
  "v_mfma_f32_16x16x32_bf16 v[100:103], v[64:67], v[36:39], 0\n" \
  "v_mfma_f32_16x16x32_bf16 v[96:99], v[68:71], v[40:43], v[96:99]\n" \
  "v_mfma_f32_16x16x32_bf16 v[100:103], v[68:71], v[44:47], v[100:103]\n" \
  "v_mfma_f32_16x16x32_bf16 v[96:99], v[72:75], v[48:51], v[96:99]\n" \
  "v_mfma_f32_16x16x32_bf16 v[100:103], v[72:75], v[52:55], v[100:103]\n" \
  "v_mfma_f32_16x16x32_bf16 v[96:99], v[76:79], v[56:59], v[96:99]\n" \
  "v_mfma_f32_16x16x32_bf16 v[100:103], v[76:79], v[60:63], v[100:103]\n" \
  "Lb1_%=:\n" \
  "s_cmp_eq_u32 s61, 0\n" \
  "s_cbranch_scc1 Ll1_%=\n" \
  "s_ff1_i32_b32 s62, s61\n" \
  "s_bitset0_b32 s61, s62\n" \
  "s_lshl_b32 s63, s62, 12\n" \
  "s_cmp_ge_u32 s62, 8\n" \
  "s_cselect_b32 s64, %[r5], 0\n" \
  "s_add_u32 s63, s63, s64\n" \
  "v_add_u32 v14, s63, v0\n" \
  "ds_read_b128 v[64:67], v14 offset:0\n" \
  "ds_read_b128 v[68:71], v14 offset:256\n" \
  "ds_read_b128 v[72:75], v14 offset:512\n" \
  "ds_read_b128 v[76:79], v14 offset:768\n" \
  "s_waitcnt lgkmcnt(4)\n" \
  "v_mfma_f32_16x16x32_bf16 v[104:107], v[80:83], v[32:35], 0\n" \
  "v_mfma_f32_16x16x32_bf16 v[108:111], v[80:83], v[36:39], 0\n" \
  "v_cvt_pk_bf16_f32 v96, v96, v97\n" \
  "v_cvt_pk_bf16_f32 v97, v98, v99\n" \
  "v_mfma_f32_16x16x32_bf16 v[104:107], v[84:87], v[40:43], v[104:107]\n" \
  "v_cvt_pk_bf16_f32 v98, v100, v101\n" \
  "v_cvt_pk_bf16_f32 v99, v102, v103\n" \
  "v_mfma_f32_16x16x32_bf16 v[108:111], v[84:87], v[44:47], v[108:111]\n" \
  "buffer_store_dwordx4 v[96:99], v16, %[os], 0 offen sc1\n" \
  "v_mfma_f32_16x16x32_bf16 v[104:107], v[88:91], v[48:51], v[104:107]\n" \
  "v_mfma_f32_16x16x32_bf16 v[108:111], v[88:91], v[52:55], v[108:111]\n" \
  "s_set_gpr_idx_on s62, gpr_idx(SRC0)\n" \
  "v_mov_b32 v16, v2\n" \
  "s_set_gpr_idx_off\n" \
  "v_mfma_f32_16x16x32_bf16 v[104:107], v[92:95], v[56:59], v[104:107]\n" \
  "v_mfma_f32_16x16x32_bf16 v[108:111], v[92:95], v[60:63], v[108:111]\n" \
  "Lb0_%=:\n" \
  "s_cmp_eq_u32 s61, 0\n" \
  "s_cbranch_scc1 Ll0_%=\n" \
  "s_ff1_i32_b32 s62, s61\n" \
  "s_bitset0_b32 s61, s62\n" \
  "s_lshl_b32 s63, s62, 12\n" \
  "s_cmp_ge_u32 s62, 8\n" \
  "s_cselect_b32 s64, %[r5], 0\n" \
  "s_add_u32 s63, s63, s64\n" \
  "v_add_u32 v14, s63, v0\n" \
  "ds_read_b128 v[80:83], v14 offset:0\n" \
  "ds_read_b128 v[84:87], v14 offset:256\n" \
  "ds_read_b128 v[88:91], v14 offset:512\n" \
  "ds_read_b128 v[92:95], v14 offset:768\n" \
  "s_waitcnt lgkmcnt(4)\n" \
  "v_mfma_f32_16x16x32_bf16 v[96:99], v[64:67], v[32:35], 0\n" \
  "v_mfma_f32_16x16x32_bf16 v[100:103], v[64:67], v[36:39], 0\n" \
  "v_cvt_pk_bf16_f32 v104, v104, v105\n" \
  "v_cvt_pk_bf16_f32 v105, v106, v107\n" \
  "v_mfma_f32_16x16x32_bf16 v[96:99], v[68:71], v[40:43], v[96:99]\n" \
  "v_cvt_pk_bf16_f32 v106, v108, v109\n" \
  "v_cvt_pk_bf16_f32 v107, v110, v111\n" \
  "v_mfma_f32_16x16x32_bf16 v[100:103], v[68:71], v[44:47], v[100:103]\n" \
  "buffer_store_dwordx4 v[104:107], v17, %[os], 0 offen sc1\n" \
  "v_mfma_f32_16x16x32_bf16 v[96:99], v[72:75], v[48:51], v[96:99]\n" \
  "v_mfma_f32_16x16x32_bf16 v[100:103], v[72:75], v[52:55], v[100:103]\n" \
  "s_set_gpr_idx_on s62, gpr_idx(SRC0)\n" \
  "v_mov_b32 v17, v2\n" \
  "s_set_gpr_idx_off\n" \
  "v_mfma_f32_16x16x32_bf16 v[96:99], v[76:79], v[56:59], v[96:99]\n" \
  "v_mfma_f32_16x16x32_bf16 v[100:103], v[76:79], v[60:63], v[100:103]\n" \
  "s_branch Lb1_%=\n" \
  "Ll1_%=:\n" \
  "s_waitcnt lgkmcnt(0)\n" \
  "v_mfma_f32_16x16x32_bf16 v[104:107], v[80:83], v[32:35], 0\n" \
  "v_mfma_f32_16x16x32_bf16 v[108:111], v[80:83], v[36:39], 0\n" \
  "v_mfma_f32_16x16x32_bf16 v[104:107], v[84:87], v[40:43], v[104:107]\n" \
  "v_mfma_f32_16x16x32_bf16 v[108:111], v[84:87], v[44:47], v[108:111]\n" \
  "v_mfma_f32_16x16x32_bf16 v[104:107], v[88:91], v[48:51], v[104:107]\n" \
  "v_cvt_pk_bf16_f32 v96, v96, v97\n" \
  "v_cvt_pk_bf16_f32 v97, v98, v99\n" \
  "v_mfma_f32_16x16x32_bf16 v[108:111], v[88:91], v[52:55], v[108:111]\n" \
  "v_cvt_pk_bf16_f32 v98, v100, v101\n" \
  "v_cvt_pk_bf16_f32 v99, v102, v103\n" \
  "v_mfma_f32_16x16x32_bf16 v[104:107], v[92:95], v[56:59], v[104:107]\n" \
  "buffer_store_dwordx4 v[96:99], v16, %[os], 0 offen sc1\n" \
  "v_mfma_f32_16x16x32_bf16 v[108:111], v[92:95], v[60:63], v[108:111]\n" \
  "s_cmp_lg_u32 s60, 0\n" \
  "s_nop 5\n" \
  "v_cvt_pk_bf16_f32 v104, v104, v105\n" \
  "v_cvt_pk_bf16_f32 v105, v106, v107\n" \
  "v_cvt_pk_bf16_f32 v106, v108, v109\n" \
  "v_cvt_pk_bf16_f32 v107, v110, v111\n" \
  "buffer_store_dwordx4 v[104:107], v17, %[os], 0 offen sc1\n" \
  "s_cbranch_scc1 Lp%=\n" \
  "s_branch Le%=\n" \
  "Ll0_%=:\n" \
  "s_waitcnt lgkmcnt(0)\n" \
  "v_mfma_f32_16x16x32_bf16 v[96:99], v[64:67], v[32:35], 0\n" \
  "v_mfma_f32_16x16x32_bf16 v[100:103], v[64:67], v[36:39], 0\n" \
  "v_mfma_f32_16x16x32_bf16 v[96:99], v[68:71], v[40:43], v[96:99]\n" \
  "v_mfma_f32_16x16x32_bf16 v[100:103], v[68:71], v[44:47], v[100:103]\n" \
  "v_mfma_f32_16x16x32_bf16 v[96:99], v[72:75], v[48:51], v[96:99]\n" \
  "v_cvt_pk_bf16_f32 v104, v104, v105\n" \
  "v_cvt_pk_bf16_f32 v105, v106, v107\n" \
  "v_mfma_f32_16x16x32_bf16 v[100:103], v[72:75], v[52:55], v[100:103]\n" \
  "v_cvt_pk_bf16_f32 v106, v108, v109\n" \
  "v_cvt_pk_bf16_f32 v107, v110, v111\n" \
  "v_mfma_f32_16x16x32_bf16 v[96:99], v[76:79], v[56:59], v[96:99]\n" \
  "buffer_store_dwordx4 v[104:107], v17, %[os], 0 offen sc1\n" \
  "v_mfma_f32_16x16x32_bf16 v[100:103], v[76:79], v[60:63], v[100:103]\n" \
  "s_cmp_lg_u32 s60, 0\n" \
  "s_nop 5\n" \
  "v_cvt_pk_bf16_f32 v96, v96, v97\n" \
  "v_cvt_pk_bf16_f32 v97, v98, v99\n" \
  "v_cvt_pk_bf16_f32 v98, v100, v101\n" \
  "v_cvt_pk_bf16_f32 v99, v102, v103\n" \
  "buffer_store_dwordx4 v[96:99], v16, %[os], 0 offen sc1\n" \
  "s_cbranch_scc1 Lp%=\n" \
  "s_branch Le%=\n" \
  "Ls%=:\n" \
  "s_waitcnt lgkmcnt(0)\n" \
  "v_mfma_f32_16x16x32_bf16 v[96:99], v[64:67], v[32:35], 0\n" \
  "v_mfma_f32_16x16x32_bf16 v[100:103], v[64:67], v[36:39], 0\n" \
  "v_mfma_f32_16x16x32_bf16 v[96:99], v[68:71], v[40:43], v[96:99]\n" \
  "v_mfma_f32_16x16x32_bf16 v[100:103], v[68:71], v[44:47], v[100:103]\n" \
  "v_mfma_f32_16x16x32_bf16 v[96:99], v[72:75], v[48:51], v[96:99]\n" \
  "v_mfma_f32_16x16x32_bf16 v[100:103], v[72:75], v[52:55], v[100:103]\n" \
  "v_mfma_f32_16x16x32_bf16 v[96:99], v[76:79], v[56:59], v[96:99]\n" \
  "v_mfma_f32_16x16x32_bf16 v[100:103], v[76:79], v[60:63], v[100:103]\n" \
  "s_cmp_lg_u32 s60, 0\n" \
  "s_nop 5\n" \
  "v_cvt_pk_bf16_f32 v96, v96, v97\n" \
  "v_cvt_pk_bf16_f32 v97, v98, v99\n" \
  "v_cvt_pk_bf16_f32 v98, v100, v101\n" \
  "v_cvt_pk_bf16_f32 v99, v102, v103\n" \
  "buffer_store_dwordx4 v[96:99], v16, %[os], 0 offen sc1\n" \
  "s_cbranch_scc1 Lp%=\n" \
  "Le%=:\n"
#define MIX_CLOBBERS \
  "v16", "v17", "v64", "v65", "v66", "v67", "v68", "v69", "v70", "v71", "v72", "v73", "v74", "v75", "v76", \
  "v77", "v78", "v79", "v80", "v81", "v82", "v83", "v84", "v85", "v86", "v87", "v88", "v89", "v90", "v91", \
  "v92", "v93", "v94", "v95", "v96", "v97", "v98", "v99", "v100", "v101", "v102", "v103", "v104", "v105", \
  "v106", "v107", "v108", "v109", "v110", "v111", "s60", "s61", "s62", "s63", "s64", "scc", "vcc", "memory"
#define PART_ASM \
  "s_load_dwordx16 s[8:23], %[kp], 0\n" \
  "s_load_dwordx8 s[24:31], %[kp], 0x40\n" \
  "s_load_dwordx4 s[32:35], %[kp], 0x60\n" \
  "v_readfirstlane_b32 s36, %[tid]\n" \
  "v_lshlrev_b32 v90, 3, %[tid]\n" \
  "v_mov_b64 v[92:93], 0\n" \
  "s_and_b32 s37, %[bid], 7\n" \
  "s_lshr_b32 s38, %[bid], 3\n" \
  "s_lshr_b32 s36, s36, 6\n" \
  "ds_write_b64 v90, v[92:93] offset:%[sg]\n" \
  "s_cmp_lg_u32 s36, %[pw]\n" \
  "s_cbranch_scc1 1f\n" \
  "s_getpc_b64 s[44:45]\n" \
  ".Ltfn_p0:\n" \
  "s_max_i32 s46, 0, .Ltfn_p1-.Ltfn_p0\n" \
  "s_mov_b32 m0, %[sn]\n" \
  "s_mov_b32 s47, 0x20000\n" \
  "v_lshlrev_b32 v91, 12, %[tid]\n" \
  "s_lshl_b32 s39, s38, 7\n" \
  "v_and_b32 v91, 0x3f000, v91\n" \
  "buffer_load_dword v91, s[44:47], s39 offen lds\n" \
  "1:\n" \
  "s_lshl_b32 s37, s37, 5\n" \
  "s_add_u32 s37, s37, s38\n" \
  "v_lshlrev_b32 v91, 4, %[tid]\n" \
  "s_waitcnt lgkmcnt(0)\n" \
  "s_mul_i32 s38, s37, s14\n" \
  "s_mov_b32 s40, s10\n" \
  "s_add_i32 s39, s38, s14\n" \
  "s_lshl_b32 s44, s38, 2\n" \
  "s_min_i32 s39, s39, s10\n" \
  "s_add_i32 s45, s44, %[h4]\n" \
  "s_lshl_b32 s10, s39, 2\n" \
  "s_lshl_b32 s14, s39, 2\n" \
  "s_cmp_ge_u32 s36, %[pw]\n" \
  "s_cbranch_scc1 2f\n" \
  "buffer_load_dwordx2 v[40:41], v90, s[8:11], s44 offen\n" \
  "buffer_load_dwordx2 v[42:43], v90, s[8:11], s45 offen\n" \
  "buffer_load_dwordx2 v[44:45], v90, s[12:15], s44 offen\n" \
  "buffer_load_dwordx2 v[46:47], v90, s[12:15], s45 offen\n" \
  "2:\n" \
  "s_mov_b32 s41, s18\n" \
  "s_mov_b32 s42, s22\n" \
  "s_lshl_b32 s18, s39, 3\n" \
  "s_lshl_b32 s22, s39, 3\n" \
  "s_lshl_b32 s44, s44, 1\n" \
  "s_lshl_b32 s45, s45, 1\n" \
  "s_cmp_ge_u32 s36, %[pw]\n" \
  "s_barrier\n" \
  "s_cbranch_scc0 3f\n" \
  "s_cmp_lg_u32 s36, %[hw]\n" \
  "s_cbranch_scc1 4f\n" \
  "s_load_dwordx2 s[46:47], s[26:27], 0\n" \
  "s_branch Lpx%=\n" \
  "4:\n" \
  "s_barrier\n" \
  "s_branch Lpx%=\n" \
  "3:\n" \
  "s_setprio 3\n" \
  "buffer_load_dwordx4 v[100:103], v91, s[16:19], s44 offen\n" \
  "buffer_load_dwordx4 v[104:107], v91, s[16:19], s45 offen\n" \
  "buffer_load_dwordx4 v[108:111], v91, s[20:23], s44 offen\n" \
  "buffer_load_dwordx4 v[112:115], v91, s[20:23], s45 offen\n" \
  "s_sub_i32 s56, s39, s38\n" \
  "v_lshlrev_b32 v72, 1, %[tid]\n" \
  "s_lshl_b32 s57, s36, 9\n" \
  "v_cmp_gt_i32_e64 s[48:49], s56, v72\n" \
  "s_sub_i32 s58, s56, 1\n" \
  "v_cmp_gt_i32_e64 s[50:51], s58, v72\n" \
  "s_sub_i32 s58, s56, %[h1]\n" \
  "v_cmp_gt_i32_e64 s[52:53], s58, v72\n" \
  "s_sub_i32 s58, s56, %[h1]+1\n" \
  "v_cmp_gt_i32_e64 s[54:55], s58, v72\n" \
  "v_and_b32 v88, 63, %[tid]\n" \
  "v_mov_b32 v95, 0x1fc\n" \
  "v_mov_b64 v[76:77], 0\n" \
  "v_lshlrev_b32 v88, 2, v88\n" \
  "v_mov_b32 v96, 0\n" \
  "v_mov_b32 v97, 1\n" \
  "v_add_u32 v89, s57, v88\n" \
  "v_add_u32 v88, %[ph], v88\n" \
  "v_add_u32 v89, %[pb], v89\n" \
  "s_lshl_b32 s58, s36, 1\n" \
  "s_mov_b32 s59, 0x38000000\n" \
  "s_waitcnt vmcnt(6)\n" \
  "v_mul_u32_u24 v48, s42, v40\n" \
  "v_mul_u32_u24 v49, s42, v41\n" \
  "v_mul_u32_u24 v50, s42, v42\n" \
  "v_mul_u32_u24 v51, s42, v43\n" \
  "v_lshrrev_b32 v52, 22, v48\n" \
  "v_lshrrev_b32 v53, 22, v49\n" \
  "v_lshrrev_b32 v54, 22, v50\n" \
  "v_lshrrev_b32 v55, 22, v51\n" \
  "v_lshrrev_b32 v56, 21, v48\n" \
  "v_lshrrev_b32 v57, 21, v49\n" \
  "v_lshrrev_b32 v58, 21, v50\n" \
  "v_lshrrev_b32 v59, 21, v51\n" \
  "v_lshlrev_b32 v60, 4, v52\n" \
  "v_lshlrev_b32 v61, 4, v53\n" \
  "v_lshlrev_b32 v62, 4, v54\n" \
  "v_lshlrev_b32 v63, 4, v55\n" \
  "v_and_or_b32 v56, v56, v95, s57\n" \
  "v_and_or_b32 v57, v57, v95, s57\n" \
  "v_and_or_b32 v58, v58, v95, s57\n" \
  "v_and_or_b32 v59, v59, v95, s57\n" \
  "v_lshlrev_b32 v72, v60, v97\n" \
  "v_lshlrev_b32 v73, v61, v97\n" \
  "v_lshlrev_b32 v74, v62, v97\n" \
  "v_lshlrev_b32 v75, v63, v97\n" \
  "s_mov_b64 exec, s[48:49]\n" \
  "ds_add_rtn_u32 v64, v56, v72 offset:%[ph]\n" \
  "s_mov_b64 exec, s[50:51]\n" \
  "ds_add_rtn_u32 v65, v57, v73 offset:%[ph]\n" \
  "s_mov_b64 exec, s[52:53]\n" \
  "ds_add_rtn_u32 v66, v58, v74 offset:%[ph]\n" \
  "s_mov_b64 exec, s[54:55]\n" \
  "ds_add_rtn_u32 v67, v59, v75 offset:%[ph]\n" \
  "s_mov_b64 exec, -1\n" \
  "v_and_b32 v48, s59, v48\n" \
  "v_and_b32 v49, s59, v49\n" \
  "v_and_b32 v50, s59, v50\n" \
  "v_and_b32 v51, s59, v51\n" \
  "v_and_b32 v40, 63, v40\n" \
  "v_and_b32 v41, 63, v41\n" \
  "v_and_b32 v42, 63, v42\n" \
  "v_and_b32 v43, 63, v43\n" \
  "s_waitcnt vmcnt(4)\n" \
  "v_mul_u32_u24 v68, s42, v44\n" \
  "v_mul_u32_u24 v69, s42, v45\n" \
  "v_mul_u32_u24 v70, s42, v46\n" \
  "v_mul_u32_u24 v71, s42, v47\n" \
  "v_lshl_or_b32 v40, v44, 6, v40\n" \
  "v_lshl_or_b32 v41, v45, 6, v41\n" \
  "v_lshl_or_b32 v42, v46, 6, v42\n" \
  "v_lshl_or_b32 v43, v47, 6, v43\n" \
  "v_sub_u32 v68, v68, v48\n" \
  "v_sub_u32 v69, v69, v49\n" \
  "v_sub_u32 v70, v70, v50\n" \
  "v_sub_u32 v71, v71, v51\n" \
  "v_lshl_add_u32 v56, v52, 1, s57\n" \
  "v_lshl_add_u32 v57, v53, 1, s57\n" \
  "v_lshl_add_u32 v58, v54, 1, s57\n" \
  "v_lshl_add_u32 v59, v55, 1, s57\n" \
  "v_bfe_u32 v68, v68, 26, 4\n" \
  "v_bfe_u32 v69, v69, 26, 4\n" \
  "v_bfe_u32 v70, v70, 26, 4\n" \
  "v_bfe_u32 v71, v71, 26, 4\n" \
  "v_lshlrev_b32 v52, 2, v52\n" \
  "v_lshlrev_b32 v53, 2, v53\n" \
  "v_lshlrev_b32 v54, 2, v54\n" \
  "v_lshlrev_b32 v55, 2, v55\n" \
  "v_lshl_or_b32 v68, v40, 4, v68\n" \
  "v_lshl_or_b32 v69, v41, 4, v69\n" \
  "v_lshl_or_b32 v70, v42, 4, v70\n" \
  "v_lshl_or_b32 v71, v43, 4, v71\n" \
  "s_mov_b64 exec, s[48:49]\n" \
  "ds_add_u32 v52, v68 offset:%[sg]\n" \
  "s_mov_b64 exec, s[50:51]\n" \
  "ds_add_u32 v53, v69 offset:%[sg]\n" \
  "s_mov_b64 exec, s[52:53]\n" \
  "ds_add_u32 v54, v70 offset:%[sg]\n" \
  "s_mov_b64 exec, s[54:55]\n" \
  "ds_add_u32 v55, v71 offset:%[sg]\n" \
  "s_mov_b64 exec, -1\n" \
  "s_waitcnt lgkmcnt(0)\n" \
  "s_barrier\n" \
  "ds_read2st64_b32 v[78:79], v88 offset0:0 offset1:1\n" \
  "ds_read2st64_b32 v[80:81], v88 offset0:2 offset1:3\n" \
  "ds_read2st64_b32 v[82:83], v88 offset0:4 offset1:5\n" \
  "ds_read2st64_b32 v[84:85], v88 offset0:6 offset1:7\n" \
  "ds_read2st64_b32 v[86:87], v88 offset0:8 offset1:9\n" \
  "ds_read_b32 v94, v96 offset:%[tg]\n" \
  "v_bfe_u32 v64, v64, v60, 16\n" \
  "v_bfe_u32 v65, v65, v61, 16\n" \
  "v_bfe_u32 v66, v66, v62, 16\n" \
  "v_bfe_u32 v67, v67, v63, 16\n" \
  "s_mov_b32 s56, 0x05040100\n" \
  "s_mov_b32 s59, 0x07060302\n" \
  "s_waitcnt lgkmcnt(4)\n" \
  "v_add_u32 v80, v80, v78\n" \
  "v_add_u32 v81, v81, v79\n" \
  "s_waitcnt lgkmcnt(3)\n" \
  "v_add_u32 v82, v82, v80\n" \
  "v_add_u32 v83, v83, v81\n" \
  "s_waitcnt lgkmcnt(2)\n" \
  "v_add_u32 v84, v84, v82\n" \
  "v_add_u32 v85, v85, v83\n" \
  "s_waitcnt lgkmcnt(1)\n" \
  "v_add_u32 v86, v86, v84\n" \
  "v_add_u32 v87, v87, v85\n" \
  "v_perm_b32 v72, v87, v86, s56\n" \
  "v_perm_b32 v73, v87, v86, s59\n" \
  "v_add_u32 v72, v72, v73\n" \
  "s_set_gpr_idx_on s58, gpr_idx(SRC0)\n" \
  "v_mov_b32 v74, v76\n" \
  "v_mov_b32 v75, v77\n" \
  "s_set_gpr_idx_off\n" \
  "s_mov_b32 s56, 0x10001\n" \
  "v_add_u32_dpp v73, v72, v72 row_shr:1 row_mask:0xf bank_mask:0xf bound_ctrl:1\n" \
  "v_lshl_add_u32 v74, v86, 16, v74\n" \
  "v_lshl_add_u32 v75, v87, 16, v75\n" \
  "v_add_u32_dpp v73, v73, v73 row_shr:2 row_mask:0xf bank_mask:0xf bound_ctrl:1\n" \
  "s_waitcnt lgkmcnt(0)\n" \
  "v_lshl_or_b32 v68, v94, 24, v68\n" \
  "v_add_u32_dpp v73, v73, v73 row_shr:4 row_mask:0xf bank_mask:0xf bound_ctrl:1\n" \
  "v_lshl_or_b32 v69, v94, 24, v69\n" \
  "v_lshl_or_b32 v70, v94, 24, v70\n" \
  "v_add_u32_dpp v73, v73, v73 row_shr:8 row_mask:0xf bank_mask:0xf bound_ctrl:1\n" \
  "v_lshl_or_b32 v71, v94, 24, v71\n" \
  "s_nop 0\n" \
  "v_add_u32_dpp v73, v73, v73 row_bcast:15 row_mask:0xa bank_mask:0xf\n" \
  "s_nop 1\n" \
  "v_add_u32_dpp v73, v73, v73 row_bcast:31 row_mask:0xc bank_mask:0xf\n" \
  "v_sub_u32 v72, v73, v72\n" \
  "v_readlane_b32 s59, v73, 63\n" \
  "v_and_b32 v73, 0xffff, v72\n" \
  "v_lshrrev_b32 v72, 16, v72\n" \
  "s_and_b32 s59, s59, 0xffff\n" \
  "v_mad_u32_u24 v74, v73, s56, v74\n" \
  "v_add_u32 v72, s59, v72\n" \
  "v_mad_u32_u24 v75, v72, s56, v75\n" \
  "ds_write2st64_b32 v89, v74, v75 offset1:1\n" \
  "ds_read_u16 v48, v56 offset:%[pb]\n" \
  "ds_read_u16 v49, v57 offset:%[pb]\n" \
  "ds_read_u16 v50, v58 offset:%[pb]\n" \
  "ds_read_u16 v51, v59 offset:%[pb]\n" \
  "s_mov_b32 s56, %[sp]\n" \
  "s_waitcnt lgkmcnt(0)\n" \
  "v_add_lshl_u32 v48, v48, v64, 2\n" \
  "v_add_lshl_u32 v49, v49, v65, 2\n" \
  "v_add_lshl_u32 v50, v50, v66, 2\n" \
  "v_add_lshl_u32 v51, v51, v67, 2\n" \
  "v_lshl_add_u32 v56, v48, 2, s56\n" \
  "v_lshl_add_u32 v57, v49, 2, s56\n" \
  "v_lshl_add_u32 v58, v50, 2, s56\n" \
  "v_lshl_add_u32 v59, v51, 2, s56\n" \
  "s_mov_b64 exec, s[48:49]\n" \
  "ds_write_b32 v48, v68 offset:%[sk]\n" \
  "s_mov_b64 exec, s[50:51]\n" \
  "ds_write_b32 v49, v69 offset:%[sk]\n" \
  "s_mov_b64 exec, s[52:53]\n" \
  "ds_write_b32 v50, v70 offset:%[sk]\n" \
  "s_mov_b64 exec, s[54:55]\n" \
  "ds_write_b32 v51, v71 offset:%[sk]\n" \
  "s_mov_b64 exec, -1\n" \
  "s_waitcnt vmcnt(0)\n" \
  "v_add_u32 v100, v100, v94\n" \
  "v_add_u32 v102, v102, v94\n" \
  "v_add_u32 v104, v104, v94\n" \
  "v_add_u32 v106, v106, v94\n" \
  "v_add_u32 v108, v108, v94\n" \
  "v_add_u32 v110, v110, v94\n" \
  "v_add_u32 v112, v112, v94\n" \
  "v_add_u32 v114, v114, v94\n" \
  "v_add3_u32 v72, v100, v101, v108\n" \
  "v_add3_u32 v73, v102, v103, v110\n" \
  "v_add3_u32 v74, v104, v105, v112\n" \
  "v_add3_u32 v75, v106, v107, v114\n" \
  "v_add_u32 v72, v72, v109\n" \
  "v_add_u32 v73, v73, v111\n" \
  "v_add_u32 v74, v74, v113\n" \
  "v_add_u32 v75, v75, v115\n" \
  "s_mov_b64 exec, s[48:49]\n" \
  "ds_add_u32 v52, v72 offset:%[sg]+1024\n" \
  "s_mov_b64 exec, s[50:51]\n" \
  "ds_add_u32 v53, v73 offset:%[sg]+1024\n" \
  "s_mov_b64 exec, s[52:53]\n" \
  "ds_add_u32 v54, v74 offset:%[sg]+1024\n" \
  "s_mov_b64 exec, s[54:55]\n" \
  "ds_add_u32 v55, v75 offset:%[sg]+1024\n" \
  "s_mov_b64 exec, 1\n" \
  "ds_add_u32 v96, v97 offset:%[go]\n" \
  "s_mov_b64 exec, -1\n" \
  "s_mov_b64 exec, s[48:49]\n" \
  "ds_write2_b64 v56, v[100:101], v[108:109] offset1:1\n" \
  "s_mov_b64 exec, s[50:51]\n" \
  "ds_write2_b64 v57, v[102:103], v[110:111] offset1:1\n" \
  "s_mov_b64 exec, s[52:53]\n" \
  "ds_write2_b64 v58, v[104:105], v[112:113] offset1:1\n" \
  "s_mov_b64 exec, s[54:55]\n" \
  "ds_write2_b64 v59, v[106:107], v[114:115] offset1:1\n" \
  "s_mov_b64 exec, -1\n" \
  "Lpx%=:\n"
#define PART_CLOBBERS \
  "v40", "v41", "v42", "v43", "v44", "v45", "v46", "v47", "v48", "v49", "v50", "v51", "v52", "v53", "v54", \
  "v55", "v56", "v57", "v58", "v59", "v60", "v61", "v62", "v63", "v64", "v65", "v66", "v67", "v68", "v69", \
  "v70", "v71", "v72", "v73", "v74", "v75", "v76", "v77", "v78", "v79", "v80", "v81", "v82", "v83", "v84", \
  "v85", "v86", "v87", "v88", "v89", "v90", "v91", "v92", "v93", "v94", "v95", "v96", "v97", "v98", "v99", \
  "v100", "v101", "v102", "v103", "v104", "v105", "v106", "v107", "v108", "v109", "v110", "v111", "v112", \
  "v113", "v114", "v115", "s44", "s45", "s48", "s49", "s50", "s51", "s52", "s53", "s54", "s55", "s56", "s57", \
  "s58", "s59", "scc", "vcc", "memory"


namespace {

constexpr int C_ = 128;
constexpr int MW = 12;
constexpr int MT = MW * 64;
constexpr int SW = 8;
constexpr int NS = 5;
constexpr int NPB = SW * NS;
constexpr int NPASS = 16;
constexpr int HS = 64 * NPASS / 2;
constexpr int NBMAX = 256;
constexpr int PW = 5;
constexpr int PT = PW * 64;
constexpr int PK = 4;
constexpr int CH = PT * PK;
constexpr int MK = 4;
constexpr int CAPW = 256;
constexpr int HWV = 10;
constexpr int PFN = 5;
constexpr int WA = 4;
constexpr int FO1 = 2;
constexpr int WSL = 6;
constexpr int TB3 = 2;
constexpr int HSL = 24;
constexpr unsigned WS_EPOCH = 0;
constexpr unsigned WS_OFF = 4096;
constexpr unsigned WS_RECK = WS_OFF + NBMAX * NBMAX * 16;
constexpr unsigned WS_RECP = WS_RECK + NBMAX * CH * 4;
constexpr unsigned WS_END = WS_RECP + NBMAX * CH * 16;

__device__ __forceinline__ float bf2f(unsigned short v) { return __uint_as_float(((unsigned)v) << 16); }
__device__ __forceinline__ unsigned short f2bf(float f) {
  __hip_bfloat16 b = __float2bfloat16(f);
  return *reinterpret_cast<unsigned short*>(&b);
}
__device__ __forceinline__ unsigned pk2bf(float lo, float hi) {
  return __builtin_bit_cast(unsigned, __builtin_convertvector(f32x2{lo, hi}, bf16x2));
}
__device__ __forceinline__ unsigned wave_incl_scan(unsigned x) {
  x += __builtin_amdgcn_update_dpp(0u, x, 0x111, 0xf, 0xf, false);
  x += __builtin_amdgcn_update_dpp(0u, x, 0x112, 0xf, 0xf, false);
  x += __builtin_amdgcn_update_dpp(0u, x, 0x114, 0xf, 0xf, false);
  x += __builtin_amdgcn_update_dpp(0u, x, 0x118, 0xf, 0xf, false);
  x += __builtin_amdgcn_update_dpp(0u, x, 0x142, 0xa, 0xf, false);
  x += __builtin_amdgcn_update_dpp(0u, x, 0x143, 0xc, 0xf, false);
  return x;
}

struct LdsLow {
  u32x4 sink[16];
  unsigned flag, rdone, go, pad_;
  unsigned long long tag, pad2_;
  unsigned prog[16];
  int deg[64];
  unsigned byrank[64];
  unsigned nl[64];
  u32x4 ct[72];
  union {
    struct {
      __attribute__((aligned(16))) unsigned so[SW * CAPW];
      union {
        __attribute__((aligned(16))) unsigned hist[(SW + 1) * HS];
        u32x4 row5[SW * 64];
      };
    };
    struct {
      __attribute__((aligned(16))) unsigned sk[CH];
      u32x4 sp[CH];
    };
  };
  union {
    struct {
      __attribute__((aligned(16))) unsigned seg[2][NBMAX];
      unsigned phist[PW][NBMAX / 2];
      unsigned pbase[PW][NBMAX / 2];
      u32x4 ri[SW][MK * 16];
    };
    u32x4 pay[SW * CAPW];
  };
};
constexpr int TBLK = 0xc300 / 8;
struct Lds : LdsLow {
  char pad_end[163840 - TBLK * 16 - sizeof(LdsLow)];
  u32x4 sg[TBLK];
};
constexpr int TOFF = 163840 - TBLK * 16;
constexpr int WBLK = 0x4180 / 8;
static_assert(sizeof(Lds) == 163840 && TOFF < 65536, "LDS layout");

__device__ __forceinline__ unsigned lds_addr(const void* p) {
  return (unsigned)(uintptr_t)(__attribute__((address_space(3))) const void*)p;
}
__device__ __forceinline__ u32x4 make_srd(const void* p, unsigned num) {
  unsigned long long a = (unsigned long long)p;
  return u32x4{(unsigned)a, (unsigned)(a >> 32), num, 0x00020000u};
}

}

extern "C" __global__ __launch_bounds__(MT, 3) void tfn_main_kernel(
    const int* dst_, int E_, int c0_, const int* src_, int CE_, int c1_, const u32x2* Y_, int N_, int c2_, const u32x2* ef_,
    unsigned M2_, int c3_, const unsigned short* x_, char* ws_, u32x4* out_, const unsigned short* wrad_,
    const unsigned short* cg_, const unsigned short* wmix_) {
  __shared__ Lds L;
  const int tid = threadIdx.x, lane = tid & 63;
  u32x16 ka;
  u32x8 kb, kc;
  u32x4 kd;
  unsigned long long ev;
  asm volatile(PART_ASM
               : "=&{s[8:23]}"(ka), "=&{s[24:31]}"(kb), "=&{s[32:35]}"(kd), "=&{s[36:43]}"(kc), "=&{s[46:47]}"(ev)
               : [kp] "s"(__builtin_amdgcn_kernarg_segment_ptr()), [tid] "v"(tid), [bid] "s"(blockIdx.x), [pw] "n"(PW), [hw] "n"(HWV),
                 [h4] "n"(CH * 2), [h1] "n"(CH / 2), [sn] "n"(offsetof(LdsLow, sink)), [sg] "n"(offsetof(LdsLow, seg)),
                 [ph] "n"(offsetof(LdsLow, phist)), [pb] "n"(offsetof(LdsLow, pbase)), [tg] "n"(offsetof(LdsLow, tag)),
                 [go] "n"(offsetof(LdsLow, go)), [sk] "n"(offsetof(LdsLow, sk)), [sp] "n"(offsetof(LdsLow, sp))
               : PART_CLOBBERS);
  typedef __attribute__((address_space(1))) char gchar;
  auto ptr = [](unsigned lo, unsigned hi) { return (char*)(gchar*)(lo | (unsigned long long)hi << 32); };
  const int* __restrict__ dst = (const int*)ptr(ka[0], ka[1]);
  const int* __restrict__ src = (const int*)ptr(ka[4], ka[5]);
  const u32x2* __restrict__ Y = (const u32x2*)ptr(ka[8], ka[9]);
  const u32x2* __restrict__ ef = (const u32x2*)ptr(ka[12], ka[13]);
  const unsigned short* __restrict__ x = (const unsigned short*)ptr(kb[0], kb[1]);
  char* ws = ptr(kb[2], kb[3]);
  u32x4* __restrict__ out = (u32x4*)ptr(kb[4], kb[5]);
  const unsigned short* __restrict__ wrad = (const unsigned short*)ptr(kb[6], kb[7]);
  const unsigned short* __restrict__ cg = (const unsigned short*)ptr(kd[0], kd[1]);
  const unsigned short* __restrict__ wmix = (const unsigned short*)ptr(kd[2], kd[3]);
  const int w = kc[0], bucket = kc[1], E = kc[4], N = kc[5];
  const unsigned M2 = kc[6];
  __builtin_assume(w >= 0 && w < MW && bucket >= 0 && bucket < NBMAX);
  const int xcd = blockIdx.x & 7;
  const u32x4 wsd = make_srd(ws, WS_END);
  auto codes = [](unsigned q) { return q * 0x80008u + u32x4{0x10000u, 0x30002u, 0x50004u, 0x70006u}; };
  auto table = [&](int q) {
    u32x4 p, n;
#pragma unroll
    for (int i = 0; i < 4; i++) {
      const f32x2 z = {__uint_as_float((q * 8 + 2 * i) << 16), __uint_as_float((q * 8 + 2 * i + 1) << 16)};
      const f32x2 e = {__builtin_amdgcn_exp2f(-1.4426950408889634f * z[0]), __builtin_amdgcn_exp2f(-1.4426950408889634f * z[1])};
      const f32x2 r = z * f32x2{__builtin_amdgcn_rcpf(1.f + e[0]), __builtin_amdgcn_rcpf(1.f + e[1])};
      p[i] = pk2bf(r[0], r[1]);
      n[i] = pk2bf(-e[0] * r[0], -e[1] * r[1]);
    }
    if (q < WBLK) L.sg[q] = p;
    return n;
  };
  auto halves = [&](int q0, int q1, int st) {
#pragma unroll 1
    for (int q = q0; q < q1; q += st) {
      const u32x4 c = codes(q - 16);
      L.sg[q] = c;
      if (q >= 0x30) L.sg[q + 0x1000] = c | 0x80008000u;
    }
  };
  auto warm = [&](int i0, int i1) {
#pragma unroll 1
    for (int i = i0; i < i1; i += MW - SW) {
      asm volatile("s_mov_b32 m0, %2\n s_nop 0\n buffer_load_dword %0, %1, %3 offen lds\n"
                   :: "v"(lane * 128), "s"(make_srd(x, N * 1024)), "s"(lds_addr(L.sink)),
                      "s"(((xcd * N) >> 3) * 1024 + (i * 32 + (blockIdx.x >> 3)) * 8192) : "memory");
    }
  };
  auto wdma = [&](int i0, int i1) {
#pragma unroll
    for (int i = i0; i < i1; i++) {
      const unsigned o = (i + (blockIdx.x >> 3)) & 7;
      asm volatile("s_mov_b32 m0, %2\n s_nop 0\n buffer_load_dwordx4 %0, %1, %3 offen lds\n"
                   :: "v"((lane >> 2) * 256 + (lane & 3) * 16), "s"(make_srd(wmix, C_ * C_ * 2)),
                      "s"(lds_addr(&L.sg[WBLK]) + ((w - SW) * 8 + o) * 1024), "s"(o * 4096 + (w - SW) * 64) : "memory");
    }
  };
  const auto wsr = __builtin_amdgcn_make_buffer_rsrc((void*)ws, 0, WS_END, 0x00020000);
  if (w == HWV) {
    __builtin_amdgcn_s_setprio(3);
    asm volatile("s_waitcnt lgkmcnt(0)" : "+{s[46:47]}"(ev));
    ev = ev * 6364136223846793005ull + 1442695040888963407ull;
    L.tag = ev;
    if (lane < 4) (&L.flag)[lane] = 0;
    const unsigned ho0 = lane * 16 + WS_OFF + bucket * (NBMAX * 16);
    __syncthreads();
    const unsigned tl = (unsigned)ev, th = (unsigned)(ev >> 32);
    unsigned c[4] = {0, 0, 0, 0};
    u32x4 hv[4];
#pragma unroll
    for (int k = 0; k < 4; k++) {
#pragma unroll
      for (int ww = 0; ww < PW; ww++) c[k] += reinterpret_cast<const unsigned short*>(L.phist[ww])[k * 64 + lane];
      hv[k][1] = L.seg[0][k * 64 + lane];
    }
    const unsigned p0 = c[0] | (c[1] << 16), p1 = c[2] | (c[3] << 16);
    const unsigned i0 = wave_incl_scan(p0), i1 = wave_incl_scan(p1);
    const unsigned t0 = __builtin_amdgcn_readlane(i0, 63), t1 = __builtin_amdgcn_readlane(i1, 63);
    const unsigned s1 = t0 & 0xffff, s2 = s1 + (t0 >> 16), s3 = s2 + (t1 & 0xffff);
    const unsigned bs[4] = {(i0 - p0) & 0xffff, ((i0 - p0) >> 16) + s1, ((i1 - p1) & 0xffff) + s2, ((i1 - p1) >> 16) + s3};
#pragma unroll
    for (int k = 0; k < 4; k++) {
      hv[k][0] = bs[k] | (c[k] << 11) | (tl << 22);
      hv[k][1] += (unsigned)__umul24(c[k], tl & 255) << 24;
      hv[k][3] = th - hv[k][0] - hv[k][1];
    }
    asm volatile("" : "+v"(hv[0]), "+v"(hv[1]), "+v"(hv[2]), "+v"(hv[3]));
    while (true) {
      const unsigned gv = __hip_atomic_load(&L.go, __ATOMIC_RELAXED, __HIP_MEMORY_SCOPE_WORKGROUP);
      asm volatile("" ::: "memory");
#pragma unroll
      for (int k = 0; k < 4; k++) hv[k][2] = L.seg[1][k * 64 + lane];
      asm volatile("" ::: "memory");
      if (__builtin_amdgcn_readfirstlane(gv) == PW) break;
    }
#pragma unroll
    for (int k = 0; k < 4; k++) {
      hv[k][3] -= hv[k][2];
      __builtin_amdgcn_raw_buffer_store_b128(hv[k], wsr, ho0 + k * 1024, 0, 16);
    }
  } else if (w >= PW) {
    const int zt = tid - PT - (w > HWV ? 64 : 0);
    if (w >= SW) wdma(0, WA);
    if (zt < (0x4300 - 0x3a80) / 8) L.sg[0xba80 / 8 + zt] = table(0x3a80 / 8 + zt);
    __builtin_amdgcn_s_setprio(3);
  }
  asm volatile("s_waitcnt lgkmcnt(0)\n s_barrier" ::: "memory");
  {
    const unsigned so1 = (tid + MT - PT) * 16 + WS_RECP + bucket * (CH * 16);
    const unsigned so0 = tid * 16 + (w < PW ? WS_RECK + bucket * (CH * 4) : WS_RECP - PT * 16 + bucket * (CH * 16));
    const u32x4 v0 = reinterpret_cast<const u32x4*>(L.sk)[tid], v1 = reinterpret_cast<const u32x4*>(L.sk)[MT + tid];
    __builtin_amdgcn_raw_buffer_store_b128(v0, wsr, so0, 0, 16);
    __builtin_amdgcn_raw_buffer_store_b128(v1, wsr, so1, 0, 16);
    if (w == PW)
      __builtin_amdgcn_raw_buffer_store_b128(reinterpret_cast<const u32x4*>(L.sk)[2 * MT + lane], wsr, so1 + (MT - PT) * 16, 0, 16);
  }
  __syncthreads();
  const unsigned long long epoch = L.tag;
  const unsigned tagl = __builtin_amdgcn_readfirstlane((unsigned)epoch);
  const unsigned tag1 = __builtin_amdgcn_readfirstlane((unsigned)(epoch >> 32));
  const int row = lane & 15, kq = lane >> 4;
  const int node0 = __builtin_amdgcn_readfirstlane(((unsigned)(bucket << 22) + M2 - 1) / M2);
  const int nn = min(N, __builtin_amdgcn_readfirstlane(((unsigned)((bucket + 1) << 22) + M2 - 1) / M2)) - node0;
  if (w >= SW) {
    int lane;
    asm volatile("v_mbcnt_lo_u32_b32 %0, -1, 0\n v_mbcnt_hi_u32_b32 %0, -1, %0" : "=v"(lane));
    __builtin_assume(lane >= 0 && lane < 64);
    const int h = w - SW, st = h * 64 + lane, row = lane & 15, kq = lane >> 4;
    if (h == 0) {
      L.deg[lane] = ((lane - node0) & 63) < NPB ? 63 - ((lane - node0) & 63) : -1;
      if (lane < 16) L.prog[lane] = 255;
    }
    __builtin_amdgcn_s_sleep(WSL);
    if (w == HWV) wdma(0, WA);
    wdma(WA, 8);
    unsigned short cv;
    unsigned wq4[4];
    if (h == 1) {
      cv = cg[lane];
#pragma unroll
      for (int k = 0; k < 4; k++) wq4[k] = reinterpret_cast<const unsigned*>(wrad)[k * (C_ / 2) + lane];
    }
    halves(0x20 + TB3 * 512 + st, 0x3a80 / 8, 256);
    const int wq0 = WBLK + h * 512 + lane;
    u32x4 last = codes(wq0 + 448 < 0x1000 ? wq0 + 448 : wq0 + 432);
    if ((unsigned)(wq0 + 448 - 0x1000) < 0x20u) last = table(wq0 + 448 - 0x1000);
    constexpr int HR = (NPB - RKL * SW) / (MW - SW);
    const int id = (node0 + RKL * SW + h * HR + lane) & 63;
    asm volatile("s_waitcnt     vmcnt(0)" ::: "memory");
    if (h == 1) {
      reinterpret_cast<unsigned short*>(L.ct)[(lane & 3) * 16 + (lane >> 2)] = cv;
      L.ct[8 + lane] = u32x4{__builtin_amdgcn_perm(wq4[1], wq4[0], 0x05040100), __builtin_amdgcn_perm(wq4[3], wq4[2], 0x05040100),
                             __builtin_amdgcn_perm(wq4[1], wq4[0], 0x07060302), __builtin_amdgcn_perm(wq4[3], wq4[2], 0x07060302)};
    }
    u32x16 wa, wb;
    {
      const unsigned* wp = reinterpret_cast<const unsigned*>(&L.sg[WBLK + h * 512]) + kq * 128 + row;
#pragma unroll
      for (int i = 0; i < 16; i++) {
        const unsigned q0 = wp[(i >> 2) * 512 + (i & 3) * 32], q1 = wp[(i >> 2) * 512 + (i & 3) * 32 + 16];
        u32x16& d = i < 8 ? wa : wb;
        d[(i & 4) * 2 + (i & 3)] = __builtin_amdgcn_perm(q1, q0, 0x05040100);
        d[(i & 4) * 2 + (i & 3) + 4] = __builtin_amdgcn_perm(q1, q0, 0x07060302);
      }
    }
    asm volatile("" : "+v"(wa), "+v"(wb) :: "memory");
    asm volatile("s_waitcnt lgkmcnt(0)\n s_barrier" ::: "memory");
    __builtin_amdgcn_s_sleep(2);
    warm(h, PFN);
    __syncthreads();
    {
      const int dg = L.deg[lane], dm = L.deg[id];
      int dn[HR];
#pragma unroll
      for (int i = 0; i < HR; i++) dn[i] = __builtin_amdgcn_readlane(dm, i);
      __builtin_amdgcn_sched_barrier(0);
      unsigned long long m[HR];
#pragma unroll
      for (int i = 0; i < HR; i++) m[i] = __ballot(dg > dn[i]);
      __builtin_amdgcn_sched_barrier(0);
      unsigned r = 63;
#pragma unroll
      for (int i = 0; i < HR; i++) asm("v_writelane_b32 %0, %1, %2" : "+v"(r) : "s"(__popcll(m[i])), "n"(i));
      L.byrank[r] = id * 4;
    }
    __syncthreads();
    if (bucket == 0 && st == 0)
      __builtin_amdgcn_raw_buffer_store_b64(u32x2{tagl, tag1}, wsr, WS_EPOCH, 0, 16);
    __builtin_amdgcn_s_setprio(0);
    __syncthreads();
#pragma unroll
    for (int i = 0; i < 8; i++) L.sg[wq0 + i * 64] = i < 7 ? codes(wq0 + i * 64) : last;
    __syncthreads();
    u32x16 oo;
#pragma unroll
    for (int t = 0; t < 10; t++)
      oo[t + 2] = (node0 + ((L.nl[t < SW ? t * 8 + kq : ((t - SW) * 4 + kq) * 8 + 4] / 4 - node0) & 63)) * 1024 + row * 16 + h * 256;
    oo[0] = lds_addr(&L.pay[(row >> 2) * 64 + (row & 3) * 4 + kq]);
    oo[1] = lds_addr(&L.prog[h]);
    asm volatile(MIX_ASM
                 : "+{v[0:15]}"(oo)
                 : "{v[32:47]}"(wa), "{v[48:63]}"(wb), [os] "s"(make_srd(out, (node0 + nn) * 1024)),
                   [r5] "n"((unsigned)(offsetof(LdsLow, row5) - offsetof(LdsLow, pay) - SW * CAPW * 16)),
                   [rd] "n"(offsetof(LdsLow, rdone)), [sl] "n"(HSL)
                 : MIX_CLOBBERS);
    asm volatile(".Ltfn_p1:");
  } else {
    {
    halves(0x20 + tid, 0x20 + TB3 * 512, 512);
    reinterpret_cast<u32x4*>(&L.hist[w * HS])[lane] = u32x4{0, 0, 0, 0};
    reinterpret_cast<u32x4*>(&L.hist[w * HS])[64 + lane] = u32x4{0, 0, 0, 0};
    L.ri[w][lane] = u32x4{0x20000000u, 0x20000000u, 0x20000000u, 0x20000000u};
    const unsigned long long t0 = w & 1 ? 0x26796281267d7281ull : 0x277dc200272c8180ull, t1 = w & 1 ? 0x246942c324755245ull : 0x25755242217984c2ull;
    const unsigned long long t2 = w & 1 ? 0x22613345226d6483ull : 0x236d130423694307ull, t3 = w & 1 ? 0x206503c725710384ull : 0x215d2386206513cdull;
    const unsigned long long t = w & 4 ? (w & 2 ? t3 : t2) : (w & 2 ? t1 : t0);
    const unsigned rks = nn == NPB ? t >> 32 : t;
    const int start = w * CAPW;
    unsigned sop = lds_addr(&L.so[start + (lane & 7)]);
    unsigned yp = lds_addr(&L.pay[start + (lane >> 2)]);
    unsigned ep = lds_addr(&L.pay[start + (lane & 15)]) + 8;
    unsigned cga = lds_addr(&L.ct[(lane & 3) * 2]);
    const unsigned rp = lds_addr(&L.pay[start]) + kq * 256 + row * 4;
    unsigned pg = lds_addr(&L.prog[w]), slowv;
    u32x16 cs = {(unsigned)((lane >> 1) * SW + w) * (NBMAX * 16),
                 (unsigned)(((lane >> 1) * SW + w) * CH + (lane & 1)) * 4,
                 lds_addr(L.ri[w]) + (lane & 1) * 4, lds_addr(L.ri[w]) + lane * 4, (lane & 1) - 1u, lane & 1u,
                 lds_addr(&L.hist[tid]), lds_addr(&L.byrank[(rks >> (min(lane & 7, NS - 1) * 6)) & 63]),
                 0, lds_addr(&L.hist[SW * HS + (lane >> 3)]),
                 (start * 4 + ((lane & 7) << 13)) * 0x10001u, (unsigned)(start + lane) * 4,
                 lds_addr(&L.nl[w * 8 + (lane & 7)]), lane * 4u, ((node0 + w * RKL + lane) & 63) * 4u, 0};
    asm volatile(STREAM_ASM
                 : [sl] "=s"(slowv), "+{v12}"(sop), "+{v14}"(yp), "+{v15}"(ep), "+{v[136:151]}"(cs),
                   "+{v20}"(cga), "+{v110}"(pg)
                 : "{v8}"(lane * 16), "{v[152:155]}"(u32x4{rp, rp + 64, rp + 128, rp + 192}),
                   "{v156}"(lds_addr(&L.row5[w * 64]) + kq * 256 + row * 4),
                   [xs] "s"(make_srd(x, N * 1024)), [ws] "s"(wsd),
                   [ho] "s"(WS_OFF + bucket * 16), [rk] "s"(WS_RECK), [rp] "s"(WS_RECP), [t0] "s"(tagl << 22), [t1] "s"(tag1), [tl] "s"(tagl),
                   [wb] "s"(w * (HS * 4)), [bit] "s"(1u << w), [fo] "s"((w >> 2) * FO1), [tb] "n"(TOFF),
                   [rd] "n"(offsetof(LdsLow, rdone)), [fl] "n"(offsetof(LdsLow, flag)),
                   [hi] "n"(offsetof(LdsLow, hist)), [to] "n"(offsetof(LdsLow, hist) + SW * HS * 4), [dg] "n"(offsetof(LdsLow, deg)),
                   [ct] "n"(offsetof(LdsLow, ct)), [br] "n"(offsetof(LdsLow, byrank)), [so] "n"(offsetof(LdsLow, so)), [pa] "n"(offsetof(LdsLow, pay))
                 : STREAM_CLOBBERS);
    }
    int lane;
    asm volatile("v_mbcnt_lo_u32_b32 %0, -1, 0\n v_mbcnt_hi_u32_b32 %0, -1, %0" : "=v"(lane));
    __builtin_assume(lane >= 0 && lane < 64);
    u32x4* rows = &L.pay[w * CAPW];
#pragma unroll 1
    for (int it = 0; it < 2 * NS; it++) {
      const int sl = it >> 1, slice = it & 1, ch = slice * 64 + lane;
      const unsigned dl = (L.nl[w * 8 + sl] / 4 - node0) & 63;
      float acc[4] = {0.f, 0.f, 0.f, 0.f};
#pragma unroll 1
      for (int eb = 0; eb < E; eb += 64) {
        unsigned long long m = __ballot(eb + lane < E && (unsigned)dst[min(eb + lane, E - 1)] == node0 + dl);
#pragma unroll 1
        for (; m; m &= m - 1) {
          const int e = eb + __builtin_ctzll(m);
          const u32x2 ry = Y[e], rf = ef[e];
          float yy[4] = {bf2f(ry[0] & 0xffff), bf2f(ry[0] >> 16), bf2f(ry[1] & 0xffff), bf2f(ry[1] >> 16)};
          float ee[4] = {bf2f(rf[0] & 0xffff), bf2f(rf[0] >> 16), bf2f(rf[1] & 0xffff), bf2f(rf[1] >> 16)};
          float z = 0.f;
#pragma unroll
          for (int k = 0; k < 4; k++) z += ee[k] * bf2f(wrad[k * C_ + ch]);
          float R = z / (1.f + __expf(-z));
          const unsigned short* xr = x + (size_t)src[e] * 512 + ch * 4;
#pragma unroll 1
          for (int o = 0; o < 4; o++) {
            float tp = 0.f;
#pragma unroll 1
            for (int i = 0; i < 4; i++) {
              float A = 0.f;
#pragma unroll
              for (int f = 0; f < 4; f++) A += yy[f] * bf2f(cg[i * 16 + f * 4 + o]);
              tp += bf2f(xr[i]) * bf2f(f2bf(A));
            }
            float v = R * tp;
            acc[0] += o == 0 ? v : 0.f;
            acc[1] += o == 1 ? v : 0.f;
            acc[2] += o == 2 ? v : 0.f;
            acc[3] += o == 3 ? v : 0.f;
          }
        }
      }
      u32x4* rw = sl < 4 ? &rows[sl * 64] : &L.row5[w * 64];
      unsigned short* ar = reinterpret_cast<unsigned short*>(&rw[slice * 32]) + (lane >> 5) * 128 + (lane & 31);
#pragma unroll
      for (int o = 0; o < 4; o++) ar[o * 32] = f2bf(acc[o]);
    }
    asm volatile("" ::: "memory");
    if (lane == 0) atomicOr(&L.rdone, 1u << w);
  }
}

extern "C" __global__ __launch_bounds__(C_) void tfn_plain_kernel(
    const int* __restrict__ dst, const int* __restrict__ src, const unsigned short* __restrict__ Y,
    const unsigned short* __restrict__ ef, const unsigned short* __restrict__ x, unsigned short* __restrict__ out,
    const unsigned short* __restrict__ wrad, const unsigned short* __restrict__ cg, const unsigned short* __restrict__ wmix,
    long long E) {
  __shared__ f32x4 agg[C_];
  const int c = threadIdx.x, lane = c & 63;
  f32x4 acc = {0.f, 0.f, 0.f, 0.f};
#pragma unroll 1
  for (long long eb = 0; eb < E; eb += 64) {
    unsigned long long m = __ballot(eb + lane < E && dst[eb + lane < E ? eb + lane : 0] == (int)blockIdx.x);
#pragma unroll 1
    for (; m; m &= m - 1) {
      const long long e = eb + __builtin_ctzll(m);
      float z = 0.f;
#pragma unroll
      for (int k = 0; k < 4; k++) z += bf2f(ef[e * 4 + k]) * bf2f(wrad[k * C_ + c]);
      const float R = z / (1.f + __expf(-z));
#pragma unroll
      for (int i = 0; i < 4; i++) {
        const float xv = R * bf2f(x[(size_t)src[e] * 512 + c * 4 + i]);
#pragma unroll
        for (int f = 0; f < 4; f++)
#pragma unroll
          for (int o = 0; o < 4; o++) acc[o] += xv * bf2f(Y[e * 4 + f]) * bf2f(cg[i * 16 + f * 4 + o]);
      }
    }
  }
  agg[c] = acc;
  __syncthreads();
  f32x4 r = {0.f, 0.f, 0.f, 0.f};
#pragma unroll 4
  for (int k = 0; k < C_; k++) r += agg[k] * bf2f(wmix[k * C_ + c]);
  *reinterpret_cast<u32x2*>(out + (size_t)blockIdx.x * 512 + c * 4) = u32x2{pk2bf(r[0], r[1]), pk2bf(r[2], r[3])};
}

extern "C" void kernel_launch(void* const* d_in, const int* in_sizes, int n_in, void* d_out, int out_size,
                              void* d_ws, size_t ws_size, hipStream_t stream) {
  const unsigned short* x = (const unsigned short*)d_in[0];
  const int* src = (const int*)d_in[1];
  const int* dst = (const int*)d_in[2];
  const u32x2* Y = (const u32x2*)d_in[3];
  const u32x2* ef = (const u32x2*)d_in[4];
  const unsigned short* wrad = (const unsigned short*)d_in[5];
  const unsigned short* cg = (const unsigned short*)d_in[6];
  const unsigned short* wmix = (const unsigned short*)d_in[7];
  const int N = in_sizes[0] / 512;
  const int E = in_sizes[1];
  const int CE = ((E + NBMAX - 1) / NBMAX + 1) & ~1;
  const unsigned M2 = ((unsigned)NBMAX << 22) / (unsigned)N;
  if (N <= 64 || ((1u << 22) + M2 - 1) / M2 > (unsigned)NPB || CE > CH || ws_size < WS_END) {
    tfn_plain_kernel<<<N, C_, 0, stream>>>(dst, src, (const unsigned short*)Y, (const unsigned short*)ef, x,
                                           (unsigned short*)d_out, wrad, cg, wmix, E);
    return;
  }
  tfn_main_kernel<<<NBMAX, MT, 0, stream>>>(dst, E, 0x20000, src, CE, 0x20000, Y, N, 0x20000, ef, M2, 0x20000, x, (char*)d_ws,
                                           (u32x4*)d_out, wrad, cg, wmix);
}
```
